```python
import torch

def _softmax(x, axis=-1):
    return torch.softmax(x, dim=axis)
def _gelu(x):
    return torch.nn.functional.gelu(x, approximate="tanh")
def _silu(x):
    return torch.nn.functional.silu(x)
def _sigmoid(x):
    return torch.sigmoid(x)
def _layernorm(x, axis=-1, eps=1e-5):
    m = x.mean(dim=axis, keepdim=True)
    v = x.var(dim=axis, keepdim=True, unbiased=False)
    return (x - m) / torch.sqrt(v + eps)
def _scatter_add(tgt, idx, src, axis=0):
    return tgt.clone().index_add_(axis, idx, src)
def _segment_sum(src, seg, n):
    out = torch.zeros((n,) + src.shape[1:], dtype=src.dtype)
    return out.index_add_(0, seg, src)
def _pad(x, pw):
    flat = tuple(v for pair in reversed(pw) for v in pair)
    return torch.nn.functional.pad(x, flat)

kernel_name = 'recurrent_scan__sliding_local_dilated__dense_activation_gelu__prompt'

def setup_inputs():

    _w = (lambda a: (lambda t: t.to(torch.bfloat16) if t.is_floating_point() else t)(torch.as_tensor(a)))
    def _A_setup():
        B, S, D = 1, 2048, 640
        return {
            "x": _w((lambda *s: torch.randn(*s, dtype=torch.bfloat16) * 0.05)(B, S, D)),
            "wqkv": _w((lambda *s: torch.randn(*s, dtype=torch.bfloat16) * 0.2)(D, 3 * D)),
            "wo": _w((lambda *s: torch.randn(*s, dtype=torch.bfloat16) * 0.2)(D, D)),
        }
    _A_in = _A_setup()

    def _B_setup():
        B, S, D, F = 1, 2048, 640, 1728
        return {
            "x": _w((lambda *s: torch.randn(*s, dtype=torch.bfloat16) * 0.05)(B, S, D)),
            "w1": _w((lambda *s: torch.randn(*s, dtype=torch.bfloat16) * 1.6)(D, F) * (D ** -0.5)),
            "b1": _w((lambda *s: torch.randn(*s, dtype=torch.bfloat16) * 1.6)(F) * 0.01),
            "w2": _w((lambda *s: torch.randn(*s, dtype=torch.bfloat16) * 0.1)(F, D) * (F ** -0.5)),
            "b2": _w((lambda *s: torch.randn(*s, dtype=torch.bfloat16) * 0.1)(D) * 0.01),
        }
    _B_in = _B_setup()

    return {
        "A_x": _A_in["x"],
        "A_wqkv": _A_in["wqkv"],
        "A_wo": _A_in["wo"],
        "B_x": _B_in["x"],
        "B_w1": _B_in["w1"],
        "B_b1": _B_in["b1"],
        "B_w2": _B_in["w2"],
        "B_b2": _B_in["b2"],
    }

def reference(A_x, A_wqkv, A_wo, B_x, B_w1, B_b1, B_w2, B_b2):
    def _A_ref(x, wqkv, wo):
        B, S, D = x.shape
        H = 32; Dh = D // H
        W, dil = 8, 2
        qkv = (lambda x, *d: x.permute(*d))((x @ wqkv).reshape(B, S, 3, H, Dh), 2, 0, 3, 1, 4)
        q, k, v = qkv[0], qkv[1], qkv[2]
        scale = Dh ** -0.5
        pos = torch.arange(S)
        off = torch.arange(W) * dil
        idx = pos[:, None] - off[None, :]
        mask = idx >= 0
        idx_c = torch.where(mask, idx, 0).reshape(-1)
        kg = k[:, :, idx_c, :].reshape(B, H, S, W, Dh)
        vg = v[:, :, idx_c, :].reshape(B, H, S, W, Dh)
        logits = torch.einsum("bhsd,bhswd->bhsw", q, kg) * scale
        logits = torch.where(mask[None, None, :, :], logits, -1e9)
        attn = _softmax(logits, axis=-1)
        o = torch.einsum("bhsw,bhswd->bhsd", attn, vg)
        return (lambda x, *d: x.permute(*d))(o, 0, 2, 1, 3).reshape(B, S, D) @ wo

    def _B_ref(x, w1, b1, w2, b2):
        h = torch.einsum("bsd,df->bsf", x, w1) + b1
        h = _gelu(h)
        return torch.einsum("bsf,fd->bsd", h, w2) + b2

    x = A_x
    xn = _layernorm(x, axis=-1)
    s = _A_ref(xn, A_wqkv, A_wo)
    g = _silu(_B_ref(xn, B_w1, B_b1, B_w2, B_b2))
    return x + s * g
```

```cpp
#include <hip/hip_runtime.h>
#include <hip/hip_bf16.h>
typedef __hip_bfloat16 bf16;
__device__ __forceinline__ float b2f(bf16 v){return __bfloat162float(v);}
__device__ __forceinline__ bf16 f2b(float v){return __float2bfloat16(v);}
constexpr int S=2048,D=640,F=1728,H=32,DH=20;
__global__ void ln_k(const bf16* x, bf16* xn){
  int r=blockIdx.x; int t=threadIdx.x;
  float v[10]; float s=0; for(int i=0;i<10;i++){v[i]=b2f(x[r*D+t+64*i]); s+=v[i];}
  for(int o=32;o;o>>=1)s+=__shfl_xor(s,o); float m=s/D; float q=0; for(int i=0;i<10;i++){float d=v[i]-m;q+=d*d;}
  for(int o=32;o;o>>=1)q+=__shfl_xor(q,o); float rs=rsqrtf(q/D+1e-5f);
  for(int i=0;i<10;i++)xn[r*D+t+64*i]=f2b((v[i]-m)*rs);
}
template<int MODE>
__global__ void gemm_k(const bf16* A,const bf16* B,const bf16* bias,void* C,int N,int K){
  __shared__ float As[16][17],Bs[16][17];
  int tx=threadIdx.x&15,ty=threadIdx.x>>4; int row=blockIdx.y*16+ty,col=blockIdx.x*16+tx; float acc=0;
  for(int k0=0;k0<K;k0+=16){As[ty][tx]=b2f(A[row*K+k0+tx]);Bs[ty][tx]=b2f(B[(k0+ty)*N+col]);__syncthreads();
    for(int k=0;k<16;k++)acc+=As[ty][k]*Bs[k][tx];__syncthreads();}
  if(MODE==0){((bf16*)C)[row*N+col]=f2b(acc);}
  else if(MODE==1){float v=acc+b2f(bias[col]);float u=0.7978845608f*(v+0.044715f*v*v*v);((bf16*)C)[row*N+col]=f2b(0.5f*v*(1.f+tanhf(u)));}
  else if(MODE==2){((float*)C)[row*N+col]=acc;}
  else {((float*)C)[row*N+col]=acc+b2f(bias[col]);}
}
__global__ void attn_k(const bf16* qkv,bf16* o){
  int i=blockIdx.x*blockDim.x+threadIdx.x; int t=i/H,h=i%H;
  float l[8]; float mx=-1e30f;
  #pragma unroll
  for(int w=0;w<8;w++){int p=t-2*w; if(p>=0){float s=0;for(int d=0;d<DH;d++)s+=b2f(qkv[t*1920+h*DH+d])*b2f(qkv[p*1920+640+h*DH+d]); l[w]=s*0.22360679775f;}else l[w]=-1e9f; mx=fmaxf(mx,l[w]);}
  float sum=0;
  #pragma unroll
  for(int w=0;w<8;w++){l[w]=__expf(l[w]-mx);sum+=l[w];}
  for(int d=0;d<DH;d++){float a=0;
    #pragma unroll
    for(int w=0;w<8;w++){int p=t-2*w; if(p<0)p=0; a+=l[w]*b2f(qkv[p*1920+1280+h*DH+d]);}
    o[t*D+h*DH+d]=f2b(a/sum);}
}
__global__ void fin_k(const bf16* x,const float* s,const float* y,bf16* out){
  int i=blockIdx.x*blockDim.x+threadIdx.x; float g=y[i]/(1.f+__expf(-y[i])); out[i]=f2b(b2f(x[i])+s[i]*g);
}
extern "C" void kernel_launch(void* const* d_in, const int* in_sizes, int n_in, void* d_out, int out_size, void* d_ws, size_t ws_size, hipStream_t stream) {
  const bf16* x=(const bf16*)d_in[0],*wqkv=(const bf16*)d_in[1],*wo=(const bf16*)d_in[2],*w1=(const bf16*)d_in[4],*b1=(const bf16*)d_in[5],*w2=(const bf16*)d_in[6],*b2=(const bf16*)d_in[7];
  char* ws=(char*)d_ws;
  bf16* xn=(bf16*)ws; bf16* qkv=(bf16*)(ws+(4<<20)); bf16* h=(bf16*)(ws+(16<<20)); bf16* o=(bf16*)(ws+(24<<20)); float* s=(float*)(ws+(32<<20)); float* y=(float*)(ws+(40<<20));
  ln_k<<<S,64,0,stream>>>(x,xn);
  gemm_k<0><<<dim3(1920/16,S/16),256,0,stream>>>(xn,wqkv,nullptr,qkv,1920,D);
  gemm_k<1><<<dim3(F/16,S/16),256,0,stream>>>(xn,w1,b1,h,F,D);
  attn_k<<<S*H/256,256,0,stream>>>(qkv,o);
  gemm_k<2><<<dim3(D/16,S/16),256,0,stream>>>(o,wo,nullptr,s,D,D);
  gemm_k<3><<<dim3(D/16,S/16),256,0,stream>>>(h,w2,b2,y,D,F);
  fin_k<<<S*D/256,256,0,stream>>>(x,s,y,(bf16*)d_out);
}
```

```cpp
#include <hip/hip_runtime.h>
#include <hip/hip_bf16.h>
#include <type_traits>

typedef __attribute__((ext_vector_type(4))) short s16x4;
typedef __attribute__((ext_vector_type(8))) short s16x8;
typedef __attribute__((ext_vector_type(8))) __bf16 bf16x8;
typedef __attribute__((ext_vector_type(2))) __bf16 bf16x2;
typedef __attribute__((ext_vector_type(4))) float f32x4;
typedef __attribute__((ext_vector_type(2))) float f32x2;
typedef __attribute__((ext_vector_type(4))) unsigned u32x4;
typedef __attribute__((ext_vector_type(2))) unsigned u32x2;
#define LAS __attribute__((address_space(3)))
#define DEVI __device__ __forceinline__
typedef LAS char* lptr;

constexpr int S_ = 2048, D_ = 640, F_ = 1728;
constexpr int KS_T = 74;
constexpr int WSLAB = 15360, NWS = 6;
constexpr int XSLOT = 18432, NXS = 3;
constexpr int LDS_X = WSLAB * NWS;
constexpr int LDS_ST = LDS_X + XSLOT * NXS;
constexpr int LDS_CS = LDS_ST + 1152;
constexpr int LDS_B1 = LDS_CS + 1024;
constexpr int LDS_TOTAL = LDS_B1 + 1024;

#define STAMP(idx)
#define STAMPC(idx)

DEVI void bar_lgkm() { asm volatile("s_waitcnt lgkmcnt(0)\n\ts_barrier" ::: "memory"); }
template <int N> DEVI void bar_vm() { asm volatile("s_waitcnt vmcnt(%0)\n\ts_barrier" ::"i"(N) : "memory"); }
DEVI unsigned pk2(float a, float b) { f32x2 v = {a, b}; return __builtin_bit_cast(unsigned, __builtin_convertvector(v, bf16x2)); }
DEVI unsigned pk2(f32x2 v) { return __builtin_bit_cast(unsigned, __builtin_convertvector(v, bf16x2)); }
DEVI float asf(unsigned u) { return __builtin_bit_cast(float, u); }
DEVI float blo(unsigned u) { return __builtin_bit_cast(float, u << 16); }
DEVI float bhi(unsigned u) { return __builtin_bit_cast(float, u & 0xffff0000u); }
DEVI u32x2 tr_read(lptr p) { return __builtin_bit_cast(u32x2, __builtin_amdgcn_ds_read_tr16_b64_v4i16((LAS s16x4*)p)); }
DEVI f32x4 mfma(u32x4 a, u32x4 b, f32x4 c) {
  return __builtin_amdgcn_mfma_f32_16x16x32_bf16(__builtin_bit_cast(bf16x8, a), __builtin_bit_cast(bf16x8, b), c, 0, 0, 0);
}
DEVI u32x4 cat(u32x2 a, u32x2 b) { return __builtin_shufflevector(a, b, 0, 1, 2, 3); }
DEVI __amdgpu_buffer_rsrc_t mkrsrc(const void* p, int bytes) { return __builtin_amdgcn_make_buffer_rsrc((void*)p, 0, bytes, 0x00020000); }
DEVI void dma16(__amdgpu_buffer_rsrc_t r, lptr dst, int voff, int soff) { __builtin_amdgcn_raw_ptr_buffer_load_lds(r, (LAS void*)dst, 16, voff, soff, 0, 0); }
DEVI void mfma_a(f32x4& c, u32x4 a, u32x4 b) { asm volatile("v_mfma_f32_16x16x32_bf16 %0, %1, %2, %0" : "+v"(c) : "v"(a), "v"(b)); }
template <int OFF> DEVI void ds128(u32x4& d, int addr) { asm volatile("ds_read_b128 %0, %1 offset:%2" : "=v"(d) : "v"(addr), "i"(OFF)); }
template <int OFF> DEVI void dstr(u32x2& d, int addr) { asm volatile("ds_read_b64_tr_b16 %0, %1 offset:%2" : "=v"(d) : "v"(addr), "i"(OFF)); }
DEVI u32x4 mkr(const void* p, unsigned bytes) {
  unsigned long long a = (unsigned long long)p;
  return u32x4{(unsigned)a, (unsigned)(a >> 32) & 0xffffu, bytes, 0x00020000u};
}
template <int LSTEP>
DEVI void dma4(u32x4 r, unsigned long long mask, int l, int va, int vb, int s0, int s1, int s2, int s3) {
  asm volatile(
      "s_mov_b64 exec, %0\n\t"
      "s_mov_b32 m0, %1\n\ts_nop 0\n\tbuffer_load_dwordx4 %2, %4, %5 offen lds\n\t"
      "s_add_u32 m0, %1, %9\n\ts_nop 0\n\tbuffer_load_dwordx4 %3, %4, %6 offen lds\n\t"
      "s_add_u32 m0, %1, %10\n\ts_nop 0\n\tbuffer_load_dwordx4 %2, %4, %7 offen lds\n\t"
      "s_add_u32 m0, %1, %11\n\ts_nop 0\n\tbuffer_load_dwordx4 %3, %4, %8 offen lds\n\t"
      "s_mov_b64 exec, -1"
      :: "s"(mask), "s"(l), "v"(va), "v"(vb), "s"(r), "s"(s0), "s"(s1), "s"(s2), "s"(s3), "i"(LSTEP), "i"(2 * LSTEP), "i"(3 * LSTEP)
      : "memory", "scc");
}
DEVI void dma1(u32x4 r, unsigned long long mask, int l, int v, int s0) {
  asm volatile("s_mov_b64 exec, %0\n\ts_mov_b32 m0, %1\n\ts_nop 0\n\tbuffer_load_dwordx4 %2, %3, %4 offen lds\n\ts_mov_b64 exec, -1"
               :: "s"(mask), "s"(l), "v"(v), "s"(r), "s"(s0) : "memory");
}
DEVI void bload(u32x4& d, u32x4 r, int v, int s0) { asm volatile("buffer_load_dwordx4 %0, %1, %2, %3 offen" : "=v"(d) : "v"(v), "s"(r), "s"(s0)); }
template <int N> DEVI void wait_vm() { asm volatile("s_waitcnt vmcnt(%0)" ::"i"(N) : "memory"); }
DEVI int sig(int i) { return 4 * (i & 3) + (i >> 2); }

__global__ __launch_bounds__(256) void k0_stats(const unsigned* __restrict__ x, float2* __restrict__ st) {
  int row = blockIdx.x * 4 + (threadIdx.x >> 6), lane = threadIdx.x & 63;
  unsigned u[5]; float s = 0;
  for (int i = 0; i < 5; i++) { u[i] = x[row * 320 + lane + 64 * i]; s += blo(u[i]) + bhi(u[i]); }
  for (int o = 32; o; o >>= 1) s += __shfl_xor(s, o);
  float m = s * (1.f / D_), q = 0;
  for (int i = 0; i < 5; i++) { float a = blo(u[i]) - m, b = bhi(u[i]) - m; q += a * a + b * b; }
  for (int o = 32; o; o >>= 1) q += __shfl_xor(q, o);
  float rs = rsqrtf(q * (1.f / D_) + 1e-5f);
  if (lane == 0) st[row] = make_float2(rs, -m * rs);
}

DEVI int colbyte(bool att, int nw, int mw, int c, int p) {
  if (att) {
    int ct = 4 * nw + (nw < 3 ? (c ^ (2 * mw)) : c);
    int sec = ct / 5, hh = ct - 5 * sec;
    return sec * 160 + (hh < 4 ? 40 * hh + 8 * p : 40 * p + 32);
  } else {
    if (nw < 2) return 128 * nw + 32 * (c ^ (2 * mw)) + 8 * p;
    if (c < 2) return 256 + 64 * (nw - 2) + 32 * c + 8 * p;
    return 384 + 8 * ((p & 1) + 4 * (p >> 1) + 2 * (nw - 2));
  }
}

template <int NC>
struct Frags { u32x4 x[5]; u32x2 w[NC][2]; };

template <int NC, int NH, bool MMA>
DEVI void s1_step(const Frags<NC>& c, Frags<NC>& n, int xo, int xh, const int (&wa)[NC], f32x4 (&acc)[4][NC], f32x4 (&hacc)[NH]) {
  constexpr int NM = 4 * NC + NH, NR = 2 * NC + 5;
#pragma unroll
  for (int k = 0; k < NM; k++) {
    if (MMA) {
      if (k < 4 * NC) mfma_a(acc[k / NC][k % NC], cat(c.w[k % NC][0], c.w[k % NC][1]), c.x[k / NC]);
      else mfma_a(hacc[k - 4 * NC], cat(c.w[k - 4 * NC][0], c.w[k - 4 * NC][1]), c.x[4]);
    }
    if (k < NR) {
      if (k == 0) ds128<0>(n.x[0], xo);
      else if (k <= 2 * NC) { if ((k - 1) & 1) dstr<7680>(n.w[(k - 1) >> 1][1], wa[(k - 1) >> 1]); else dstr<0>(n.w[(k - 1) >> 1][0], wa[(k - 1) >> 1]); }
      else if (k == 2 * NC + 1) ds128<2048>(n.x[1], xo);
      else if (k == 2 * NC + 2) ds128<4096>(n.x[2], xo);
      else if (k == 2 * NC + 3) ds128<6144>(n.x[3], xo);
      else ds128<0>(n.x[4], xh);
    }
  }
}

DEVI f32x2 gelu2(f32x2 v) {
  const float c1 = -2.f * 0.7978845608028654f * 1.4426950408889634f, c2 = c1 * 0.044715f;
  f32x2 t = v * v;
  t = t * c2 + c1;
  t = t * v;
  f32x2 e = {__builtin_amdgcn_exp2f(t[0]), __builtin_amdgcn_exp2f(t[1])};
  e = e + 1.f;
  f32x2 r = {__builtin_amdgcn_rcpf(e[0]), __builtin_amdgcn_rcpf(e[1])};
  return v * r;
}

template <int NC>
DEVI void s1_compute(lptr shm, bool att, int w, int lane, int p_, int j_, char* __restrict__ A2, unsigned long long* dbg) {
  constexpr int NH = NC == 4 ? 2 : 3;
  const int mw = w & 1, nw = w >> 1, i = lane & 15, g = lane >> 4;
  f32x4 acc[4][NC], hacc[NH];
#pragma unroll
  for (int m = 0; m < 4; m++)
#pragma unroll
    for (int c = 0; c < NC; c++) acc[m][c] = f32x4{0, 0, 0, 0};
#pragma unroll
  for (int c = 0; c < NH; c++) hacc[c] = f32x4{0, 0, 0, 0};
  int wb[NC];
#pragma unroll
  for (int c = 0; c < NC; c++) wb[c] = (4 * g + ((lane >> 2) & 3)) * 480 + colbyte(att, nw, mw, c, lane & 3);
  const int xb = LDS_X + i * 128 + ((g ^ (i >> 1)) << 4);
  const int own = (1 + 4 * mw) * 2048;
  Frags<NC> fa, fb;
  int wa[NC];
  bar_lgkm();
  STAMP(1);
  s1_step<NC, NH, false>(fb, fa, xb + own, xb, wb, acc, hacc);
  bar_lgkm();
  int ws = WSLAB, xs = 0;
#pragma nounroll
  for (int T = 0; T < 10; T++) {
#pragma unroll
    for (int c = 0; c < NC; c++) wa[c] = wb[c] + ws;
    s1_step<NC, NH, true>(fa, fb, (xb ^ 64) + xs + own, (xb ^ 64) + xs, wa, acc, hacc);
    bar_lgkm();
    ws += WSLAB; if (ws == WSLAB * NWS) ws = 0;
    xs += XSLOT; if (xs == XSLOT * NXS) xs = 0;
#pragma unroll
    for (int c = 0; c < NC; c++) wa[c] = wb[c] + ws;
    s1_step<NC, NH, true>(fb, fa, xb + xs + own, xb + xs, wa, acc, hacc);
    ws += WSLAB;
    if (T < 9) bar_lgkm();
  }
  asm volatile("s_waitcnt lgkmcnt(0)\n\ts_nop 15" ::: "memory");
#pragma unroll
  for (int m = 0; m < 4; m++)
#pragma unroll
    for (int c = 0; c < NC; c++) asm volatile("" : "+v"(acc[m][c]));
#pragma unroll
  for (int c = 0; c < NH; c++) asm volatile("" : "+v"(hacc[c]));
  STAMP(2);
  int cb[NC];
#pragma unroll
  for (int c = 0; c < NC; c++) cb[c] = colbyte(att, nw, mw, c, g) * 2;
  if (i == 0) {
#pragma unroll
    for (int c = 0; c < NH; c++) *(LAS f32x4*)(shm + LDS_CS + cb[c]) = hacc[c];
  }
  bar_lgkm();
  f32x4 cs[NC];
#pragma unroll
  for (int c = 0; c < NC; c++) cs[c] = *(LAS f32x4*)(shm + LDS_CS + cb[c]);
  if (att) {
    const int piece = (g * 16 + (i ^ (8 * (g >> 1)))) * 8;
    const float qs = 0.22360679774997896f * 1.4426950408889634f;
#pragma unroll
    for (int m = 0; m < 5; m++) {
      const int tau = m < 4 ? 1 + 4 * mw + m : 0;
      f32x2 st = *(LAS f32x2*)(shm + LDS_ST + (tau * 16 + i) * 8);
#pragma unroll
      for (int c = 0; c < (m < 4 ? NC : NH); c++) {
        const int ct = 4 * nw + (NC == 4 ? (c ^ (2 * mw)) : c);
        const float sc = ct < 5 ? qs : 1.f;
        const float rs = st[0] * sc, nm = st[1] * sc;
        f32x4 a = m < 4 ? acc[m < 4 ? m : 0][c] : hacc[c < NH ? c : 0];
        f32x4 v = a * rs + cs[c] * nm;
        u32x2 o = {pk2(v[0], v[1]), pk2(v[2], v[3])};
        *(LAS u32x2*)(shm + (tau * 15 + ct) * 512 + piece) = o;
      }
    }
  } else {
    f32x4 b1[NC];
#pragma unroll
    for (int c = 0; c < NC; c++) b1[c] = *(LAS f32x4*)(shm + LDS_B1 + cb[c]);
    char* base = A2 + ((size_t)(8 * p_ + 4 * mw) * KS_T + 7 * j_) * 1024;
    unsigned sp[4][2];
#pragma unroll
    for (int m = 0; m < 4; m++) {
      f32x2 st = *(LAS f32x2*)(shm + LDS_ST + ((1 + 4 * mw + m) * 16 + i) * 8);
      unsigned o[NC][2];
#pragma unroll
      for (int c = 0; c < NC; c++) {
        f32x4 v = acc[m][c] * st[0] + (cs[c] * st[1] + b1[c]);
        o[c][0] = pk2(gelu2(f32x2{v[0], v[1]}));
        o[c][1] = pk2(gelu2(f32x2{v[2], v[3]}));
      }
      char* tb = base + (size_t)m * KS_T * 1024 + lane * 16;
      if (NC == 4) {
        *(u32x4*)(tb + (2 * nw + mw) * 1024) = u32x4{o[0][0], o[0][1], o[1][0], o[1][1]};
        *(u32x4*)(tb + (2 * nw + (mw ^ 1)) * 1024) = u32x4{o[2][0], o[2][1], o[NC - 1][0], o[NC - 1][1]};
      } else {
        if (j_ < 7 || nw == 2) *(u32x4*)(tb + (2 + nw) * 1024) = u32x4{o[0][0], o[0][1], o[1][0], o[1][1]};
        sp[m][0] = o[2][0]; sp[m][1] = o[2][1];
        if (m & 1) {
          auto s0 = __builtin_amdgcn_permlane32_swap(sp[m - 1][0], sp[m][0], false, false);
          auto s1 = __builtin_amdgcn_permlane32_swap(sp[m - 1][1], sp[m][1], false, false);
          char* t2 = base + (size_t)(m - 1 + (lane >> 5)) * KS_T * 1024 + 6 * 1024 + (nw - 2) * 512 + (lane & 31) * 16;
          if (j_ < 7) *(u32x4*)t2 = u32x4{s0[0], s1[0], s0[1], s1[1]};
        }
      }
    }
  }
}

DEVI float xmax16(float v) {
  unsigned u = __builtin_bit_cast(unsigned, v);
  auto r = __builtin_amdgcn_permlane16_swap(u, u, false, false);
  return fmaxf(asf(r[0]), asf(r[1]));
}
DEVI float xmax32(float v) {
  unsigned u = __builtin_bit_cast(unsigned, v);
  auto r = __builtin_amdgcn_permlane32_swap(u, u, false, false);
  return fmaxf(asf(r[0]), asf(r[1]));
}
DEVI float xadd16(float v) {
  unsigned u = __builtin_bit_cast(unsigned, v);
  auto r = __builtin_amdgcn_permlane16_swap(u, u, false, false);
  return asf(r[0]) + asf(r[1]);
}
DEVI float xadd32(float v) {
  unsigned u = __builtin_bit_cast(unsigned, v);
  auto r = __builtin_amdgcn_permlane32_swap(u, u, false, false);
  return asf(r[0]) + asf(r[1]);
}

DEVI void s1_attention(lptr shm, int w, int lane, int p_, int j_, char* __restrict__ A2) {
  const int i = lane & 15, g = lane >> 4;
  const int tau = w + 1, taup = w >= 2 ? tau - 2 : 0;
  const int flip = (w == 1) ? 2 : 0;
  const bool start = (p_ == 0) && (w < 2);
  const int qi = sig(i);
  f32x4 mO, mP;
  unsigned M1 = 0, M2 = 0, M3 = 0;
  {
    bool vo[4], vp[4];
#pragma unroll
    for (int r = 0; r < 4; r++) {
      int jj = 4 * r + g;
      vo[r] = (jj <= qi) && (jj >= qi - 7);
      vp[r] = (jj - 16 >= qi - 7) && !start;
      mO[r] = vo[r] ? 0.f : -1e30f;
      mP[r] = vp[r] ? 0.f : -1e30f;
    }
    M1 = (vp[2] ? 0xffffu : 0u) | (vp[3] ? 0xffff0000u : 0u);
    M2 = (vo[0] ? 0xffffu : 0u) | (vo[1] ? 0xffff0000u : 0u);
    M3 = (vo[2] ? 0xffffu : 0u) | (vo[3] ? 0xffff0000u : 0u);
  }
  lptr po = shm + tau * 15 * 512 + (g * 16 + (i ^ (8 * (g >> 1)))) * 8;
  lptr pp = shm + taup * 15 * 512 + (g * 16 + ((i ^ flip) ^ (8 * (g >> 1)))) * 8;
  const int q = (lane >> 2) & 3, p = lane & 3;
  lptr to = shm + tau * 15 * 512 + (p * 16 + ((4 * g + q) ^ (8 * (p >> 1)))) * 8;
  lptr tp = shm + taup * 15 * 512 + (p * 16 + ((4 * g + (q ^ flip)) ^ (8 * (p >> 1)))) * 8;
  const u32x2 q4 = *(LAS u32x2*)(po + 4 * 512), ko4 = *(LAS u32x2*)(po + 9 * 512), kp4 = *(LAS u32x2*)(pp + 9 * 512);
  const u32x4 v4 = cat(tr_read(tp + 14 * 512), tr_read(to + 14 * 512));
  f32x4 o4 = {0, 0, 0, 0};
  char* base = A2 + ((size_t)(8 * p_ + w) * KS_T + 54 + 5 * (j_ >> 1)) * 1024;
#pragma unroll
  for (int hp = 0; hp < 2; hp++) {
    unsigned out[4];
#pragma unroll
    for (int e = 0; e < 2; e++) {
      const int h = 2 * hp + e;
      u32x2 qa = *(LAS u32x2*)(po + h * 512), ko = *(LAS u32x2*)(po + (5 + h) * 512), kp = *(LAS u32x2*)(pp + (5 + h) * 512);
      u32x4 vf = cat(tr_read(tp + (10 + h) * 512), tr_read(to + (10 + h) * 512));
      const unsigned mq = g == h ? 0xffffffffu : 0u, mv = (i >> 2) == h ? 0xffffffffu : 0u;
      u32x4 qf = {qa[0], qa[1], q4[0] & mq, q4[1] & mq};
      f32x4 sp = mfma(cat(kp, kp4), qf, mP), so = mfma(cat(ko, ko4), qf, mO);
      float ee = fmaxf(fmaxf(sp[2], so[0]), so[2]), eo = fmaxf(fmaxf(sp[3], so[1]), so[3]);
      float mx = xmax32(xmax16(fmaxf(ee, eo)));
      float pe = __builtin_amdgcn_exp2f(ee - mx), pod = __builtin_amdgcn_exp2f(eo - mx);
      float rl = __builtin_amdgcn_rcpf(xadd32(xadd16(pe + pod)));
      unsigned pk = pk2(pe * rl, pod * rl);
      u32x4 pf = {0u, pk & M1, pk & M2, pk & M3};
      f32x4 oh = mfma(vf, pf, f32x4{0, 0, 0, 0});
      o4 = mfma(v4 & mv, pf, o4);
      out[2 * e] = pk2(oh[0], oh[1]); out[2 * e + 1] = pk2(oh[2], oh[3]);
    }
    *(u32x4*)(base + ((j_ & 1) * 3 + hp) * 1024 + lane * 16) = u32x4{out[0], out[1], out[2], out[3]};
  }
  unsigned a0 = pk2(o4[0], o4[1]), a1 = pk2(o4[2], o4[3]);
  auto s0 = __builtin_amdgcn_permlane32_swap(a0, a0, false, false);
  auto s1 = __builtin_amdgcn_permlane32_swap(a1, a1, false, false);
  if (lane < 32) *(u32x4*)(base + 2 * 1024 + (j_ & 1) * 512 + lane * 16) = u32x4{s0[0], s1[0], s0[1], s1[1]};
}

template <bool ATT>
DEVI void s1_loader(lptr shm, int lw, int lane, int p_, int j_, const void* x, const void* wsrc) {
  constexpr int NX = ATT ? 5 : 4;
  constexpr int RS = ATT ? 3840 : 3456;
  const u32x4 rx = mkr(x, S_ * D_ * 2), rw = mkr(wsrc, D_ * RS);
  int wv;
  {
    int hi = lane >= 30, ch = lane - 30 * hi;
    int cbyte;
    if (ATT) { int sec = ch / 10; cbyte = sec * 1280 + j_ * 160 + (ch - 10 * sec) * 16; }
    else { int lim = j_ < 7 ? 27 : 19; cbyte = j_ * 448 + (ch < lim ? ch : lim) * 16; }
    wv = hi * RS + cbyte;
  }
  const int wrow = 16 * (lw & 1) + 4 * (lw >> 1);
  const int r8 = lane >> 3, cp = lane & 7, t0 = p_ * 128;
  int xv[2], hv;
#pragma unroll
  for (int hf = 0; hf < 2; hf++)
    xv[hf] = (t0 + 32 * lw + 2 * (4 * (r8 & 3) + 2 * hf + (r8 >> 2))) * 1280 + ((cp ^ (4 * hf + (r8 >> 1))) << 4);
  {
    int tok = t0 - 16 + 8 * (r8 & 1) + 2 * lw + ((r8 & 3) < 2);
    tok = tok < 0 ? 0 : tok;
    hv = tok * 1280 + ((cp ^ ((2 * lw + ((r8 & 3) >> 1)) & 7)) << 4);
  }
  const unsigned long long hmask = lw == 0 ? 0xffffff00ull : 0xffffffffull;
  auto ldw = [&](int s, int slot) {
    const int so = (32 * s + wrow) * RS;
    dma4<960>(rw, 0x0fffffffffffffffull, slot * WSLAB + 4 * lw * 960, wv, wv, so, so + 2 * RS, so + 8 * RS, so + 10 * RS);
  };
  auto ldx = [&](int T, int slot) {
    const int d = LDS_X + slot * XSLOT;
    if (ATT) dma1(rx, hmask, d + lw * 512, hv, T * 128);
    dma4<1024>(rx, ~0ull, d + 2048 + 4 * lw * 1024, xv[0], xv[1], T * 128, T * 128, T * 128 + 1280, T * 128 + 1280);
  };
  ldx(0, 0); ldw(0, 0); ldw(1, 1); ldx(1, 1); ldw(2, 2); ldw(3, 3); ldx(2, 2); ldw(4, 4); ldw(5, 5);
  bar_vm<2 * NX + 20>();
  bar_vm<2 * NX + 16>();
  int wslot = 0, xslot = 0;
#pragma nounroll
  for (int T = 0; T < 7; T++) {
    ldw(2 * T + 6, wslot);
    bar_vm<NX + 16>();
    ldx(T + 3, xslot); ldw(2 * T + 7, wslot + 1);
    bar_vm<2 * NX + 16>();
    wslot += 2; if (wslot == NWS) wslot = 0;
    xslot += 1; if (xslot == NXS) xslot = 0;
  }
  bar_vm<NX + 12>(); bar_vm<NX + 8>(); bar_vm<4>(); bar_vm<0>();
  bar_vm<0>();
  bar_vm<0>();
}

__global__ __launch_bounds__(768) void k1_stage1(const void* __restrict__ x, const void* __restrict__ wqkv, const void* __restrict__ w1,
                                                 const unsigned short* __restrict__ b1, const float2* __restrict__ stats,
                                                 char* __restrict__ A2, unsigned long long* dbg) {
  extern __shared__ __attribute__((aligned(16))) char shm_[];
  lptr shm = (lptr)shm_;
  const int tid = threadIdx.x, lane = tid & 63, w = __builtin_amdgcn_readfirstlane(tid >> 6);
  const int xcd = blockIdx.x & 7, idx = blockIdx.x >> 3;
  const int p_ = (xcd & 1) * 8 + (idx & 7), sub = idx >> 3;
  const bool att = !(sub & 1);
  const int j_ = (xcd >> 1) * 2 + (sub >> 1);
  STAMP(0); STAMPC(14);
  if (w >= 8) {
    if (att) { s1_loader<true>(shm, w - 8, lane, p_, j_, x, wqkv); asm volatile("s_barrier" ::: "memory"); }
    else s1_loader<false>(shm, w - 8, lane, p_, j_, x, w1);
  } else {
    if (tid < 144) {
      int tau = tid >> 4, i = tid & 15, tok;
      if (tau == 0) { tok = p_ * 128 - 16 + 8 * (i & 1) + 2 * (i >> 2) + ((i & 3) < 2); tok = tok < 0 ? 0 : tok; }
      else tok = p_ * 128 + 32 * ((tau - 1) >> 1) + 2 * sig(i) + ((tau - 1) & 1);
      float2 s = stats[tok];
      *(LAS f32x2*)(shm + LDS_ST + tid * 8) = f32x2{s.x, s.y};
    } else if (tid < 144 + 96) {
      int t = tid - 144;
      *(LAS unsigned*)(shm + LDS_X + (t >> 5) * XSLOT + (t & 31) * 4) = 0x3f803f80u;
    } else if (tid >= 256 && tid < 256 + 224 && !att) {
      int c = tid - 256, n = j_ * 224 + c;
      *(LAS float*)(shm + LDS_B1 + c * 4) = blo(b1[n < F_ ? n : F_ - 1]);
    }
    const bool light = att ? (w >= 6) : (w >= 4);
    if (light) s1_compute<3>(shm, att, w, lane, p_, j_, A2, dbg);
    else s1_compute<4>(shm, att, w, lane, p_, j_, A2, dbg);
    STAMP(3);
    if (att) { bar_lgkm(); s1_attention(shm, w, lane, p_, j_, A2); }
  }
  STAMP(4); STAMPC(15);
}

constexpr int LDS2_TOTAL = 131072;

__global__ __launch_bounds__(768) void k2_stage2(const char* __restrict__ A2, const void* __restrict__ wo, const void* __restrict__ w2,
                                                 const char* __restrict__ b2, const char* __restrict__ x, char* __restrict__ out,
                                                 unsigned long long* dbg) {
  extern __shared__ __attribute__((aligned(16))) char shm_[];
  lptr shm = (lptr)shm_;
  const int tid = threadIdx.x, lane = tid & 63, w = __builtin_amdgcn_readfirstlane(tid >> 6);
  const int xcd = blockIdx.x & 7, idx = blockIdx.x >> 3;
  const int unit = (xcd >> 1) * 28 + idx;
  if (unit >= 110) return;
  const int mt = unit / 5, n_ = (xcd & 1) * 5 + (unit - 5 * mt);
  const int G0 = 6 * mt;
  STAMP(8);
  const int frow = 8 * w + (lane >> 3), fG = G0 + (frow >> 4);
  const bool fvalid = fG < 128;
  const int ftok = 32 * ((fvalid ? fG : 0) >> 1) + 2 * sig(frow & 15) + (fG & 1);
  const int foff = ftok * 1280 + n_ * 128 + (lane & 7) * 16;
  const int kq = w & 3;
  const int nsteps = kq == 0 ? 20 : 18;
  if (w >= 8) {
    const u32x4 rw = mkr(kq == 0 ? wo : w2, kq == 0 ? D_ * 1280 : F_ * 1280);
    const int l3 = lane >> 3, cbyte = n_ * 128 + (((lane & 7) ^ ((lane >> 4) << 1)) << 4);
    const int v1 = l3 * 1280 + cbyte, v2 = (20 * (l3 >> 2) + (l3 & 3)) * 1280 + cbyte;
    auto ld = [&](int s) {
      int rb, sa = 8, sb = 16, v = v1;
      if (kq == 0) {
        int P = s / 5, kp = s - 5 * P;
        if (kp == 2) { rb = 160 * P + 16; sa = 80; sb = 40; v = v2; }
        else { rb = 160 * P + 40 * (kp > 2 ? kp - 1 : kp); sb = 20; }
      } else rb = 32 * (18 * (kq - 1) + s);
      dma4<1024>(rw, ~0ull, kq * 32768 + (s & 7) * 4096, v, v, rb * 1280, (rb + sa) * 1280, (rb + sb) * 1280, (rb + sa + sb) * 1280);
    };
#pragma nounroll
    for (int s = 0; s < 8; s++) ld(s);
    bar_vm<28>();
    bar_vm<24>();
#pragma nounroll
    for (int s = 0; s < 20; s++) {
      if (s + 8 < nsteps) ld(s + 8);
      int rem = nsteps - (s + 3);
      if (rem >= 6) bar_vm<24>();
      else if (rem == 5) bar_vm<20>();
      else if (rem == 4) bar_vm<16>();
      else if (rem == 3) bar_vm<12>();
      else if (rem == 2) bar_vm<8>();
      else if (rem == 1) bar_vm<4>();
      else bar_vm<0>();
    }
  } else {
    const int mh = w >> 2, i = lane & 15, g = lane >> 4, q = (lane >> 2) & 3, p = lane & 3;
    const int ks0 = kq == 0 ? 54 : 18 * (kq - 1);
    const u32x4 ra = mkr(A2, 128 * KS_T * 1024);
    int av[3];
#pragma unroll
    for (int m = 0; m < 3; m++) { int G = G0 + 3 * mh + m; G = G < 128 ? G : 127; av[m] = (G * KS_T + ks0) * 1024 + lane * 16; }
    u32x4 a[4][3];
#pragma unroll
    for (int u = 0; u < 4; u++)
#pragma unroll
      for (int m = 0; m < 3; m++) bload(a[u][m], ra, av[m], u * 1024);
    int ba[4];
#pragma unroll
    for (int c = 0; c < 4; c++) ba[c] = kq * 32768 + (4 * g + q) * 128 + ((c ^ ((2 * g + (q >> 1)) & 3)) << 5) + 8 * p;
    f32x4 acc[3][4];
#pragma unroll
    for (int m = 0; m < 3; m++)
#pragma unroll
      for (int c = 0; c < 4; c++) acc[m][c] = f32x4{0, 0, 0, 0};
    u32x2 b0[4][2], b1[4][2];
    bar_lgkm();
    STAMP(9);
#pragma unroll
    for (int c = 0; c < 4; c++) { dstr<0>(b0[c][0], ba[c]); dstr<2048>(b0[c][1], ba[c]); }
    bar_lgkm();
    auto step = [&](auto U, u32x2 (&bc)[4][2], u32x2 (&bn)[4][2], int s) {
      constexpr int u = decltype(U)::value, off = ((u + 1) & 3) * 4096;
      const int so = s + 4 < nsteps ? (s + 4) * 1024 : 0x7ff00000;
#pragma unroll
      for (int m = 0; m < 3; m++) {
        wait_vm<11>();
#pragma unroll
        for (int c = 0; c < 4; c++) {
          mfma_a(acc[m][c], cat(bc[c][0], bc[c][1]), a[u][m]);
          if (m < 2) { if (c & 1) dstr<off + 2048>(bn[2 * m + (c >> 1)][1], ba[2 * m + (c >> 1)]); else dstr<off>(bn[2 * m + (c >> 1)][0], ba[2 * m + (c >> 1)]); }
        }
        bload(a[u][m], ra, av[m], so);
      }
    };
    auto dump = [&]() {
      asm volatile("s_nop 15" ::: "memory");
#pragma unroll
      for (int m = 0; m < 3; m++)
#pragma unroll
        for (int c = 0; c < 4; c++) {
          asm volatile("" : "+v"(acc[m][c]));
          *(LAS f32x4*)(shm + kq * 32768 + ((3 * mh + m) * 16 + i) * 256 + (((4 * c + g) ^ (i & 7)) << 4)) = acc[m][c];
        }
    };
#pragma nounroll
    for (int it = 0; it < 5; it++) {
      const int s = 4 * it;
      step(std::integral_constant<int, 0>{}, b0, b1, s);
      bar_lgkm();
      step(std::integral_constant<int, 1>{}, b1, b0, s + 1);
      bar_lgkm();
      if (s + 2 == nsteps) break;
      step(std::integral_constant<int, 2>{}, b0, b1, s + 2);
      bar_lgkm();
#pragma unroll
      for (int c = 0; c < 4; c++) ba[c] ^= 16384;
      step(std::integral_constant<int, 3>{}, b1, b0, s + 3);
      if (it < 4) bar_lgkm();
    }
    wait_vm<0>();
    asm volatile("s_waitcnt lgkmcnt(0)" ::: "memory");
    dump();
    if (kq != 0) bar_lgkm();
    bar_lgkm();
  }
  STAMP(10);
  {
    const u32x4 xv = *(const u32x4*)(x + foff);
    const u32x4 bv = *(const u32x4*)(b2 + n_ * 128 + (lane & 7) * 16);
    f32x4 s[2], y[2];
#pragma unroll
    for (int e = 0; e < 2; e++) {
      lptr pr = shm + frow * 256 + (((2 * (lane & 7) + e) ^ (frow & 7)) << 4);
      s[e] = *(LAS f32x4*)pr;
      y[e] = *(LAS f32x4*)(pr + 32768) + *(LAS f32x4*)(pr + 65536) + *(LAS f32x4*)(pr + 98304);
    }
    u32x4 o;
#pragma unroll
    for (int k = 0; k < 4; k++) {
      f32x2 yy = f32x2{y[k >> 1][2 * (k & 1)], y[k >> 1][2 * (k & 1) + 1]} + f32x2{blo(bv[k]), bhi(bv[k])};
      f32x2 t = yy * -1.4426950408889634f;
      f32x2 e = f32x2{__builtin_amdgcn_exp2f(t[0]), __builtin_amdgcn_exp2f(t[1])} + 1.f;
      f32x2 gt = yy * f32x2{__builtin_amdgcn_rcpf(e[0]), __builtin_amdgcn_rcpf(e[1])};
      f32x2 r = f32x2{s[k >> 1][2 * (k & 1)], s[k >> 1][2 * (k & 1) + 1]} * gt + f32x2{blo(xv[k]), bhi(xv[k])};
      o[k] = pk2(r);
    }
    if (fvalid) *(u32x4*)(out + foff) = o;
  }
  STAMP(11);
}

extern "C" void kernel_launch(void* const* d_in, const int* in_sizes, int n_in, void* d_out, int out_size, void* d_ws,
                              size_t ws_size, hipStream_t stream) {
  char* ws = (char*)d_ws;
  float2* stats = (float2*)ws;
  char* A2 = ws + (1 << 20);
  unsigned long long* dbg = (unsigned long long*)(ws + (32 << 20));
  (void)hipFuncSetAttribute((const void*)k1_stage1, hipFuncAttributeMaxDynamicSharedMemorySize, LDS_TOTAL);
  (void)hipFuncSetAttribute((const void*)k2_stage2, hipFuncAttributeMaxDynamicSharedMemorySize, LDS2_TOTAL);
  k0_stats<<<S_ / 4, 256, 0, stream>>>((const unsigned*)d_in[0], stats);
  k1_stage1<<<256, 768, LDS_TOTAL, stream>>>(d_in[0], d_in[1], d_in[4], (const unsigned short*)d_in[5], stats, A2, dbg);
  k2_stage2<<<224, 768, LDS2_TOTAL, stream>>>(A2, d_in[2], d_in[6], (const char*)d_in[7], (const char*)d_in[0], (char*)d_out, dbg);
}
```

```cpp
#include <hip/hip_runtime.h>
#include <hip/hip_bf16.h>
#include <type_traits>

typedef __attribute__((ext_vector_type(4))) short s16x4;
typedef __attribute__((ext_vector_type(8))) __bf16 bf16x8;
typedef __attribute__((ext_vector_type(2))) __bf16 bf16x2;
typedef __attribute__((ext_vector_type(4))) float f32x4;
typedef __attribute__((ext_vector_type(2))) float f32x2;
typedef __attribute__((ext_vector_type(4))) unsigned u32x4;
typedef __attribute__((ext_vector_type(2))) unsigned u32x2;
#define LAS __attribute__((address_space(3)))
#define GAS __attribute__((address_space(1)))
#define DEVI __device__ __forceinline__
typedef LAS char* lptr;
typedef unsigned long long u64;

constexpr int S_ = 2048, D_ = 640, F_ = 1728;
constexpr int KS_T = 74;
constexpr int WSLAB = 15360, NWS = 6;
constexpr int XSLOT = 18432, NXS = 3;
constexpr int LDS_X = WSLAB * NWS;
constexpr int LDS_TB = 86016;
constexpr int LDS_ST = 155648;
constexpr int LDS_CS = LDS_ST + 1152;
constexpr int LDS_B1 = LDS_CS + 1024;
constexpr int LDS_CNT = LDS_B1 + 1024;
constexpr int LDS_TOTAL = 163840;
constexpr int WS_FLAG = 4096, WS_STAT = 8192, WS_A2 = 1 << 20;
constexpr int A2_BYTES = 128 * KS_T * 1024;
constexpr unsigned SPIN_CAP = 1u << 20;

#define STAMP(idx)

DEVI void bar_lgkm() { asm volatile("s_waitcnt lgkmcnt(0)\n\ts_barrier" ::: "memory"); }
DEVI void bar() { asm volatile("s_barrier" ::: "memory"); }
template <int N> DEVI void bar_vm() { asm volatile("s_waitcnt vmcnt(%0)\n\ts_barrier" ::"i"(N) : "memory"); }
template <int N> DEVI void wait_vm() { asm volatile("s_waitcnt vmcnt(%0)" ::"i"(N) : "memory"); }
DEVI unsigned pk2(float a, float b) { f32x2 v = {a, b}; return __builtin_bit_cast(unsigned, __builtin_convertvector(v, bf16x2)); }
DEVI unsigned pk2(f32x2 v) { return __builtin_bit_cast(unsigned, __builtin_convertvector(v, bf16x2)); }
DEVI float asf(unsigned u) { return __builtin_bit_cast(float, u); }
DEVI unsigned asu(float f) { return __builtin_bit_cast(unsigned, f); }
DEVI float blo(unsigned u) { return __builtin_bit_cast(float, u << 16); }
DEVI float bhi(unsigned u) { return __builtin_bit_cast(float, u & 0xffff0000u); }
DEVI u32x2 tr_read(lptr p) { return __builtin_bit_cast(u32x2, __builtin_amdgcn_ds_read_tr16_b64_v4i16((LAS s16x4*)p)); }
DEVI f32x4 mfma(u32x4 a, u32x4 b, f32x4 c) {
  return __builtin_amdgcn_mfma_f32_16x16x32_bf16(__builtin_bit_cast(bf16x8, a), __builtin_bit_cast(bf16x8, b), c, 0, 0, 0);
}
DEVI u32x4 cat(u32x2 a, u32x2 b) { return __builtin_shufflevector(a, b, 0, 1, 2, 3); }
DEVI void mfma_a(f32x4& c, u32x4 a, u32x4 b) { asm volatile("v_mfma_f32_16x16x32_bf16 %0, %1, %2, %0" : "+v"(c) : "v"(a), "v"(b)); }
template <int OFF> DEVI void ds128(u32x4& d, int addr) { asm volatile("ds_read_b128 %0, %1 offset:%2" : "=v"(d) : "v"(addr), "i"(OFF)); }
template <int OFF> DEVI void dstr(u32x2& d, int addr) { asm volatile("ds_read_b64_tr_b16 %0, %1 offset:%2" : "=v"(d) : "v"(addr), "i"(OFF)); }
template <int OFF> DEVI void dsw128(int addr, u32x4 d) { asm volatile("ds_write_b128 %0, %1 offset:%2" ::"v"(addr), "v"(d), "i"(OFF) : "memory"); }
DEVI u32x4 mkr(const void* p, unsigned bytes) {
  u64 a = (u64)p;
  return u32x4{(unsigned)a, (unsigned)(a >> 32) & 0xffffu, bytes, 0x00020000u};
}
DEVI void st_wt(u32x4 r, int voff, int soff, u32x4 v) {
  asm volatile("buffer_store_dwordx4 %0, %1, %2, %3 offen sc1\n\ts_nop 1" ::"v"(v), "v"(voff), "s"(r), "s"(soff) : "memory");
}
template <int LSTEP>
DEVI void dma4(u32x4 r, u64 mask, int l, int va, int vb, int s0, int s1, int s2, int s3) {
  asm volatile(
      "s_mov_b64 exec, %0\n\t"
      "s_mov_b32 m0, %1\n\ts_nop 0\n\tbuffer_load_dwordx4 %2, %4, %5 offen lds\n\t"
      "s_add_u32 m0, %1, %9\n\ts_nop 0\n\tbuffer_load_dwordx4 %3, %4, %6 offen lds\n\t"
      "s_add_u32 m0, %1, %10\n\ts_nop 0\n\tbuffer_load_dwordx4 %2, %4, %7 offen lds\n\t"
      "s_add_u32 m0, %1, %11\n\ts_nop 0\n\tbuffer_load_dwordx4 %3, %4, %8 offen lds\n\t"
      "s_mov_b64 exec, -1"
      :: "s"(mask), "s"(l), "v"(va), "v"(vb), "s"(r), "s"(s0), "s"(s1), "s"(s2), "s"(s3), "i"(LSTEP), "i"(2 * LSTEP), "i"(3 * LSTEP)
      : "memory", "scc");
}
DEVI void dma1(u32x4 r, u64 mask, int l, int v, int s0) {
  asm volatile("s_mov_b64 exec, %0\n\ts_mov_b32 m0, %1\n\ts_nop 0\n\tbuffer_load_dwordx4 %2, %3, %4 offen lds\n\ts_mov_b64 exec, -1"
               :: "s"(mask), "s"(l), "v"(v), "s"(r), "s"(s0) : "memory");
}
DEVI void bload(u32x4& d, u32x4 r, int v, int s0) { asm volatile("buffer_load_dwordx4 %0, %1, %2, %3 offen" : "=v"(d) : "v"(v), "s"(r), "s"(s0)); }
DEVI void bload_sc1(u32x4& d, u32x4 r, int v, int s0) { asm volatile("buffer_load_dwordx4 %0, %1, %2, %3 offen sc1" : "=v"(d) : "v"(v), "s"(r), "s"(s0) : "memory"); }
DEVI int lane_id() { int l; asm volatile("v_mbcnt_lo_u32_b32 %0, -1, 0\n\tv_mbcnt_hi_u32_b32 %0, -1, %0" : "=v"(l)); return l; }
DEVI int sig(int i) { return 4 * (i & 3) + (i >> 2); }
DEVI u64 flagval(unsigned tag) { return ((u64)(tag ^ 0x5bd1e995u) << 32) | tag; }

DEVI float xmax16(float v) { auto r = __builtin_amdgcn_permlane16_swap(asu(v), asu(v), false, false); return fmaxf(asf(r[0]), asf(r[1])); }
DEVI float xmax32(float v) { auto r = __builtin_amdgcn_permlane32_swap(asu(v), asu(v), false, false); return fmaxf(asf(r[0]), asf(r[1])); }
DEVI float xadd16(float v) { auto r = __builtin_amdgcn_permlane16_swap(asu(v), asu(v), false, false); return asf(r[0]) + asf(r[1]); }
DEVI float xadd32(float v) { auto r = __builtin_amdgcn_permlane32_swap(asu(v), asu(v), false, false); return asf(r[0]) + asf(r[1]); }
template <int CTRL> DEVI float dpp(float v) { return asf(__builtin_amdgcn_mov_dpp(asu(v), CTRL, 0xf, 0xf, true)); }
DEVI float wave_sum(float v) {
  v += dpp<0xb1>(v); v += dpp<0x4e>(v); v += dpp<0x141>(v); v += dpp<0x140>(v);
  return xadd32(xadd16(v));
}

DEVI int row_token(int p_, int pos) {
  const int tau = pos >> 4, i = pos & 15;
  int tok = tau == 0 ? p_ * 128 - 16 + 8 * (i & 1) + 2 * (i >> 2) + ((i & 3) < 2) : p_ * 128 + 32 * ((tau - 1) >> 1) + 2 * sig(i) + ((tau - 1) & 1);
  return tok < 0 ? 0 : tok;
}

DEVI int colbyte(bool att, int nw, int mw, int c, int p) {
  if (att) {
    int ct = 4 * nw + (nw < 3 ? (c ^ (2 * mw)) : c);
    int sec = ct / 5, hh = ct - 5 * sec;
    return sec * 160 + (hh < 4 ? 40 * hh + 8 * p : 40 * p + 32);
  } else {
    if (nw < 2) return 128 * nw + 32 * (c ^ (2 * mw)) + 8 * p;
    if (c < 2) return 256 + 64 * (nw - 2) + 32 * c + 8 * p;
    return 384 + 8 * ((p & 1) + 4 * (p >> 1) + 2 * (nw - 2));
  }
}

template <int NC>
struct Frags { u32x4 x[5]; u32x2 w[NC][2]; };

template <int NC, int NH, bool MMA>
DEVI void s1_step(const Frags<NC>& c, Frags<NC>& n, int xo, int xh, const int (&wa)[NC], f32x4 (&acc)[4][NC], f32x4 (&hacc)[NH]) {
  constexpr int NM = 4 * NC + NH, NR = 2 * NC + 5;
#pragma unroll
  for (int k = 0; k < NM; k++) {
    if (MMA) {
      if (k < 4 * NC) mfma_a(acc[k / NC][k % NC], cat(c.w[k % NC][0], c.w[k % NC][1]), c.x[k / NC]);
      else mfma_a(hacc[k - 4 * NC], cat(c.w[k - 4 * NC][0], c.w[k - 4 * NC][1]), c.x[4]);
    }
    if (k < NR) {
      if (k == 0) ds128<0>(n.x[0], xo);
      else if (k <= 2 * NC) { if ((k - 1) & 1) dstr<7680>(n.w[(k - 1) >> 1][1], wa[(k - 1) >> 1]); else dstr<0>(n.w[(k - 1) >> 1][0], wa[(k - 1) >> 1]); }
      else if (k == 2 * NC + 1) ds128<2048>(n.x[1], xo);
      else if (k == 2 * NC + 2) ds128<4096>(n.x[2], xo);
      else if (k == 2 * NC + 3) ds128<6144>(n.x[3], xo);
      else ds128<0>(n.x[4], xh);
    }
  }
}

DEVI f32x2 gelu2(f32x2 v) {
  const float c1 = -2.f * 0.7978845608028654f * 1.4426950408889634f, c2 = c1 * 0.044715f;
  f32x2 t = v * v;
  t = t * c2 + c1;
  t = t * v;
  f32x2 e = {__builtin_amdgcn_exp2f(t[0]), __builtin_amdgcn_exp2f(t[1])};
  e = e + 1.f;
  f32x2 r = {__builtin_amdgcn_rcpf(e[0]), __builtin_amdgcn_rcpf(e[1])};
  return v * r;
}

template <int NC>
DEVI void s1_compute(lptr shm, bool att, int w, int lane, int p_, int j_, char* ws) {
  constexpr int NH = NC == 4 ? 2 : 3;
  const int mw = w & 1, nw = w >> 1, i = lane & 15, g = lane >> 4;
  f32x4 acc[4][NC], hacc[NH];
#pragma unroll
  for (int m = 0; m < 4; m++)
#pragma unroll
    for (int c = 0; c < NC; c++) { acc[m][c] = f32x4{0, 0, 0, 0}; asm volatile("" : "+v"(acc[m][c])); }
#pragma unroll
  for (int c = 0; c < NH; c++) { hacc[c] = f32x4{0, 0, 0, 0}; asm volatile("" : "+v"(hacc[c])); }
  int wb[NC], cb[NC];
#pragma unroll
  for (int c = 0; c < NC; c++) {
    wb[c] = (4 * g + ((lane >> 2) & 3)) * 480 + colbyte(att, nw, mw, c, lane & 3);
    cb[c] = colbyte(att, nw, mw, c, g) * 2;
    asm volatile("" : "+v"(cb[c]));
  }
  const int xb = LDS_X + i * 128 + ((g ^ (i >> 1)) << 4);
  const int own = (1 + 4 * mw) * 2048;
  Frags<NC> fa, fb;
  int wa[NC];
  bar_lgkm();
  STAMP(1);
  s1_step<NC, NH, false>(fb, fa, xb + own, xb, wb, acc, hacc);
  int ws_ = WSLAB, xs = 0;
#pragma nounroll
  for (int T = 0; T < 10; T++) {
#pragma unroll
    for (int c = 0; c < NC; c++) wa[c] = wb[c] + ws_;
    asm volatile("s_waitcnt lgkmcnt(0)" ::: "memory");
    s1_step<NC, NH, true>(fa, fb, (xb ^ 64) + xs + own, (xb ^ 64) + xs, wa, acc, hacc);
    ws_ += WSLAB; if (ws_ == WSLAB * NWS) ws_ = 0;
    xs += XSLOT; if (xs == XSLOT * NXS) xs = 0;
#pragma unroll
    for (int c = 0; c < NC; c++) wa[c] = wb[c] + ws_;
    bar_lgkm();
    s1_step<NC, NH, true>(fb, fa, xb + xs + own, xb + xs, wa, acc, hacc);
    ws_ += WSLAB;
  }
  asm volatile("s_waitcnt lgkmcnt(0)\n\ts_nop 15" ::: "memory");
#pragma unroll
  for (int m = 0; m < 4; m++)
#pragma unroll
    for (int c = 0; c < NC; c++) asm volatile("" : "+v"(acc[m][c]));
#pragma unroll
  for (int c = 0; c < NH; c++) asm volatile("" : "+v"(hacc[c]));
  STAMP(2);
  if (i == 0) {
#pragma unroll
    for (int c = 0; c < NH; c++) *(LAS f32x4*)(shm + LDS_CS + cb[c]) = hacc[c];
  }
  bar_lgkm();
  f32x4 cs[NC];
#pragma unroll
  for (int c = 0; c < NC; c++) cs[c] = *(LAS f32x4*)(shm + LDS_CS + cb[c]);
  if (att) {
    const int piece = LDS_TB + (g * 16 + (i ^ (8 * (g >> 1)))) * 8;
    const float qs = 0.22360679774997896f * 1.4426950408889634f;
#pragma unroll
    for (int m = 0; m < 5; m++) {
      const int tau = m < 4 ? 1 + 4 * mw + m : 0;
      f32x2 st = *(LAS f32x2*)(shm + LDS_ST + (tau * 16 + i) * 8);
#pragma unroll
      for (int c = 0; c < (m < 4 ? NC : NH); c++) {
        const int ct = 4 * nw + (NC == 4 ? (c ^ (2 * mw)) : c);
        const float sc = ct < 5 ? qs : 1.f;
        const float rs = st[0] * sc, nm = st[1] * sc;
        f32x4 a = m < 4 ? acc[m < 4 ? m : 0][c] : hacc[c < NH ? c : 0];
        f32x4 v = a * rs + cs[c] * nm;
        u32x2 o = {pk2(v[0], v[1]), pk2(v[2], v[3])};
        *(LAS u32x2*)(shm + (tau * 15 + ct) * 512 + piece) = o;
      }
    }
  } else {
    f32x4 b1[NC];
#pragma unroll
    for (int c = 0; c < NC; c++) b1[c] = *(LAS f32x4*)(shm + LDS_B1 + cb[c]);
    const u32x4 ra = mkr(ws + WS_A2, A2_BYTES);
    const int base = ((8 * p_ + 4 * mw) * KS_T + 7 * j_) * 1024;
    unsigned sp[4][2];
#pragma unroll
    for (int m = 0; m < 4; m++) {
      f32x2 st = *(LAS f32x2*)(shm + LDS_ST + ((1 + 4 * mw + m) * 16 + i) * 8);
      unsigned o[NC][2];
#pragma unroll
      for (int c = 0; c < NC; c++) {
        f32x4 v = acc[m][c] * st[0] + (cs[c] * st[1] + b1[c]);
        o[c][0] = pk2(gelu2(f32x2{v[0], v[1]}));
        o[c][1] = pk2(gelu2(f32x2{v[2], v[3]}));
      }
      const int tb = base + m * KS_T * 1024;
      if (NC == 4) {
        st_wt(ra, lane * 16, tb + (2 * nw + mw) * 1024, u32x4{o[0][0], o[0][1], o[1][0], o[1][1]});
        st_wt(ra, lane * 16, tb + (2 * nw + (mw ^ 1)) * 1024, u32x4{o[2][0], o[2][1], o[NC - 1][0], o[NC - 1][1]});
      } else {
        if (j_ < 7 || nw == 2) st_wt(ra, lane * 16, tb + (2 + nw) * 1024, u32x4{o[0][0], o[0][1], o[1][0], o[1][1]});
        sp[m][0] = o[2][0]; sp[m][1] = o[2][1];
        if (m & 1) {
          auto s0 = __builtin_amdgcn_permlane32_swap(sp[m - 1][0], sp[m][0], false, false);
          auto s1 = __builtin_amdgcn_permlane32_swap(sp[m - 1][1], sp[m][1], false, false);
          if (j_ < 7) st_wt(ra, (lane >> 5) * (KS_T * 1024) + (lane & 31) * 16, tb - KS_T * 1024 + 6 * 1024 + (nw - 2) * 512, u32x4{s0[0], s1[0], s0[1], s1[1]});
        }
      }
    }
  }
}

DEVI void s1_attention(lptr shm, int w, int lane, int p_, int j_, char* ws) {
  const int i = lane & 15, g = lane >> 4;
  const int tau = w + 1, taup = w >= 2 ? tau - 2 : 0;
  const int flip = (w == 1) ? 2 : 0;
  const bool start = (p_ == 0) && (w < 2);
  const int qi = sig(i);
  f32x4 mO, mP;
  unsigned M1, M2, M3;
  {
    bool vo[4], vp[4];
#pragma unroll
    for (int r = 0; r < 4; r++) {
      int jj = 4 * r + g;
      vo[r] = (jj <= qi) && (jj >= qi - 7);
      vp[r] = (jj - 16 >= qi - 7) && !start;
      mO[r] = vo[r] ? 0.f : -1e30f;
      mP[r] = vp[r] ? 0.f : -1e30f;
    }
    M1 = (vp[2] ? 0xffffu : 0u) | (vp[3] ? 0xffff0000u : 0u);
    M2 = (vo[0] ? 0xffffu : 0u) | (vo[1] ? 0xffff0000u : 0u);
    M3 = (vo[2] ? 0xffffu : 0u) | (vo[3] ? 0xffff0000u : 0u);
  }
  lptr tb = shm + LDS_TB;
  lptr po = tb + tau * 15 * 512 + (g * 16 + (i ^ (8 * (g >> 1)))) * 8;
  lptr pp = tb + taup * 15 * 512 + (g * 16 + ((i ^ flip) ^ (8 * (g >> 1)))) * 8;
  const int q = (lane >> 2) & 3, p = lane & 3;
  lptr to = tb + tau * 15 * 512 + (p * 16 + ((4 * g + q) ^ (8 * (p >> 1)))) * 8;
  lptr tp = tb + taup * 15 * 512 + (p * 16 + ((4 * g + (q ^ flip)) ^ (8 * (p >> 1)))) * 8;
  asm volatile("" :: "v"(M1), "v"(M2), "v"(M3), "v"(mO), "v"(mP), "v"(po), "v"(pp), "v"(to), "v"(tp));
  bar_lgkm();
  const u32x2 q4 = *(LAS u32x2*)(po + 4 * 512), ko4 = *(LAS u32x2*)(po + 9 * 512), kp4 = *(LAS u32x2*)(pp + 9 * 512);
  u32x2 qa[4], ko[4], kp[4];
#pragma unroll
  for (int h = 0; h < 4; h++) { qa[h] = *(LAS u32x2*)(po + h * 512); ko[h] = *(LAS u32x2*)(po + (5 + h) * 512); kp[h] = *(LAS u32x2*)(pp + (5 + h) * 512); }
  u32x4 vf[4];
#pragma unroll
  for (int h = 0; h < 4; h++) vf[h] = cat(tr_read(tp + (10 + h) * 512), tr_read(to + (10 + h) * 512));
  const u32x4 v4 = cat(tr_read(tp + 14 * 512), tr_read(to + 14 * 512));
  f32x4 sp[4], so[4];
#pragma unroll
  for (int h = 0; h < 4; h++) {
    const unsigned mq = g == h ? 0xffffffffu : 0u;
    u32x4 qf = {qa[h][0], qa[h][1], q4[0] & mq, q4[1] & mq};
    sp[h] = mfma(cat(kp[h], kp4), qf, mP);
    so[h] = mfma(cat(ko[h], ko4), qf, mO);
  }
  float ee[4], eo[4], mx[4], l[4];
#pragma unroll
  for (int h = 0; h < 4; h++) { ee[h] = fmaxf(fmaxf(sp[h][2], so[h][0]), so[h][2]); eo[h] = fmaxf(fmaxf(sp[h][3], so[h][1]), so[h][3]); mx[h] = fmaxf(ee[h], eo[h]); }
  __builtin_amdgcn_sched_barrier(0);
#pragma unroll
  for (int h = 0; h < 4; h++) mx[h] = xmax16(mx[h]);
#pragma unroll
  for (int h = 0; h < 4; h++) mx[h] = xmax32(mx[h]);
  __builtin_amdgcn_sched_barrier(0);
#pragma unroll
  for (int h = 0; h < 4; h++) { ee[h] = __builtin_amdgcn_exp2f(ee[h] - mx[h]); eo[h] = __builtin_amdgcn_exp2f(eo[h] - mx[h]); l[h] = ee[h] + eo[h]; }
  __builtin_amdgcn_sched_barrier(0);
#pragma unroll
  for (int h = 0; h < 4; h++) l[h] = xadd16(l[h]);
#pragma unroll
  for (int h = 0; h < 4; h++) l[h] = xadd32(l[h]);
  __builtin_amdgcn_sched_barrier(0);
  f32x4 o4 = {0, 0, 0, 0}, oh[4];
#pragma unroll
  for (int h = 0; h < 4; h++) {
    const float rl = __builtin_amdgcn_rcpf(l[h]);
    const unsigned pk = pk2(ee[h] * rl, eo[h] * rl), mv = (i >> 2) == h ? 0xffffffffu : 0u;
    u32x4 pf = {0u, pk & M1, pk & M2, pk & M3};
    oh[h] = mfma(vf[h], pf, f32x4{0, 0, 0, 0});
    o4 = mfma(v4 & mv, pf, o4);
  }
  const u32x4 ra = mkr(ws + WS_A2, A2_BYTES);
  const int base = ((8 * p_ + w) * KS_T + 54 + 5 * (j_ >> 1)) * 1024;
#pragma unroll
  for (int hp = 0; hp < 2; hp++)
    st_wt(ra, lane * 16, base + ((j_ & 1) * 3 + hp) * 1024,
          u32x4{pk2(oh[2 * hp][0], oh[2 * hp][1]), pk2(oh[2 * hp][2], oh[2 * hp][3]), pk2(oh[2 * hp + 1][0], oh[2 * hp + 1][1]), pk2(oh[2 * hp + 1][2], oh[2 * hp + 1][3])});
  unsigned a0 = pk2(o4[0], o4[1]), a1 = pk2(o4[2], o4[3]);
  auto s0 = __builtin_amdgcn_permlane32_swap(a0, a0, false, false);
  auto s1 = __builtin_amdgcn_permlane32_swap(a1, a1, false, false);
  if (lane < 32) st_wt(ra, lane * 16, base + 2 * 1024 + (j_ & 1) * 512, u32x4{s0[0], s1[0], s0[1], s1[1]});
}

template <bool ATT>
DEVI void s1_loader(int lw, int lane, int p_, int j_, const void* x, const void* wsrc) {
  constexpr int NX = ATT ? 5 : 4;
  constexpr int RS = ATT ? 3840 : 3456;
  const u32x4 rx = mkr(x, S_ * D_ * 2), rw = mkr(wsrc, D_ * RS);
  int wv;
  {
    int hi = lane >= 30, ch = lane - 30 * hi;
    int cbyte;
    if (ATT) { int sec = (ch >= 10) + (ch >= 20); cbyte = sec * 1120 + j_ * 160 + ch * 16; }
    else { int lim = j_ < 7 ? 27 : 19; cbyte = j_ * 448 + (ch < lim ? ch : lim) * 16; }
    wv = hi * RS + cbyte;
  }
  const int wrow = 16 * (lw & 1) + 4 * (lw >> 1);
  const int r8 = lane >> 3, cp = lane & 7, t0 = p_ * 128;
  int xv[2], hv;
#pragma unroll
  for (int hf = 0; hf < 2; hf++)
    xv[hf] = (t0 + 32 * lw + 2 * (4 * (r8 & 3) + 2 * hf + (r8 >> 2))) * 1280 + ((cp ^ (4 * hf + (r8 >> 1))) << 4);
  {
    int tok = t0 - 16 + 8 * (r8 & 1) + 2 * lw + ((r8 & 3) < 2);
    tok = tok < 0 ? 0 : tok;
    hv = tok * 1280 + ((cp ^ ((2 * lw + ((r8 & 3) >> 1)) & 7)) << 4);
  }
  const u64 hmask = lw == 0 ? 0xffffff00ull : 0xffffffffull;
  auto ldw = [&](int s, int slot) {
    const int so = (32 * s + wrow) * RS;
    dma4<960>(rw, 0x0fffffffffffffffull, slot * WSLAB + 4 * lw * 960, wv, wv, so, so + 2 * RS, so + 8 * RS, so + 10 * RS);
  };
  auto ldx = [&](int T, int slot) {
    const int d = LDS_X + slot * XSLOT;
    if (ATT) dma1(rx, hmask, d + lw * 512, hv, T * 128);
    dma4<1024>(rx, ~0ull, d + 2048 + 4 * lw * 1024, xv[0], xv[1], T * 128, T * 128, T * 128 + 1280, T * 128 + 1280);
  };
  ldx(0, 0); ldw(0, 0); ldw(1, 1);
  int slot = 1;
#pragma nounroll
  for (int T = 1; T < 10; T++) {
    ldx(T, slot); ldw(2 * T, 2 * slot); ldw(2 * T + 1, 2 * slot + 1);
    bar_vm<NX + 8>();
    slot += 1; if (slot == NXS) slot = 0;
  }
}

__global__ __launch_bounds__(768, 3) void fused_block(const void* __restrict__ x, const void* __restrict__ wqkv, const void* __restrict__ wo,
                                                      const void* __restrict__ w1, const unsigned short* __restrict__ b1,
                                                      const void* __restrict__ w2, const char* __restrict__ b2, char* __restrict__ out,
                                                      char* ws) {
  extern __shared__ __attribute__((aligned(16))) char shm_[];
  lptr shm = (lptr)shm_;
  const int tid = threadIdx.x, lane = tid & 63, w = __builtin_amdgcn_readfirstlane(tid >> 6);
  const int xcd = blockIdx.x & 7, idx = blockIdx.x >> 3;
  const int p_ = (xcd & 1) * 8 + (idx & 7), sub = idx >> 3;
  const bool att = !(sub & 1);
  const int j_ = (xcd >> 1) * 2 + (sub >> 1);
  const int unit = (xcd >> 1) * 28 + idx;
  const bool has2 = idx < 28 && unit < 110;
  const int mt = unit / 5, n_ = (xcd & 1) * 5 + (unit - 5 * mt);
  const int G0 = 6 * mt;
  const int kq = w & 3;
  STAMP(0);
  if (w >= 8) {
    const int lw = w - 8;
    if (att) s1_loader<true>(lw, lane, p_, j_, x, wqkv);
    else s1_loader<false>(lw, lane, p_, j_, x, w1);
    const unsigned tag = __builtin_amdgcn_readfirstlane(*(LAS unsigned*)(shm + LDS_CNT + 4));
    const u32x4 rst = mkr(ws + WS_STAT, S_ * 16);
    const int spos = 36 * lw + (lane < 36 ? lane : 35), sv = row_token(p_, spos) * 16;
    u32x4 sg;
    bload_sc1(sg, rst, sv, 0);
    const u32x4 rw = mkr(kq == 0 ? wo : w2, has2 ? (kq == 0 ? D_ * 1280 : F_ * 1280) : 0);
    const int l3 = lane >> 3, cbyte = n_ * 128 + (((lane & 7) ^ ((lane >> 4) << 1)) << 4);
    const int v1 = l3 * 1280 + cbyte, v2 = (20 * (l3 >> 2) + (l3 & 3)) * 1280 + cbyte;
    auto geom = [&](int s, int& rb, int& sa, int& sb, int& v) {
      sa = 8; sb = 16; v = v1;
      if (kq == 0) {
        int P = (s * 13) >> 6, kp = s - 5 * P;
        if (kp == 2) { rb = 160 * P + 16; sa = 80; sb = 40; v = v2; }
        else { rb = 160 * P + 40 * (kp > 2 ? kp - 1 : kp); sb = 20; }
      } else rb = 32 * (18 * (kq - 1) + s);
      rb *= 1280; sa *= 1280; sb *= 1280;
    };
    u32x4 pre[9][4];
    auto ldr = [&](auto R) {
      constexpr int r = decltype(R)::value;
      int rb, sa, sb, v; geom(9 + r, rb, sa, sb, v);
      bload(pre[r][0], rw, v, rb); bload(pre[r][1], rw, v, rb + sa); bload(pre[r][2], rw, v, rb + sb); bload(pre[r][3], rw, v, rb + sa + sb);
    };
    auto ldl = [&](int s) {
      int rb, sa, sb, v; geom(s, rb, sa, sb, v);
      dma4<1024>(rw, ~0ull, s < 9 ? s * 16384 + kq * 4096 : 147456 + (s - 18) * 4096, v, v, rb, rb + sa, rb + sb, rb + sa + sb);
    };
#define IC(n) std::integral_constant<int, n>{}
    ldr(IC(0)); ldr(IC(1)); ldr(IC(2));
    asm volatile("s_waitcnt vmcnt(12)" : "+v"(sg) :: "memory");
#pragma nounroll
    for (unsigned spins = 0; !__all(sg[1] == tag && sg[3] == tag) && spins < SPIN_CAP; spins++) {
      __builtin_amdgcn_s_sleep(2);
      bload_sc1(sg, rst, sv, 0);
      asm volatile("s_waitcnt vmcnt(0)" : "+v"(sg) :: "memory");
    }
    *(LAS u32x2*)(shm + LDS_ST + spos * 8) = u32x2{sg[0], sg[2]};
    bar_lgkm();
    ldr(IC(3)); ldr(IC(4)); ldr(IC(5));
    bar();
    ldr(IC(6)); ldr(IC(7));
    bar();
    ldr(IC(8)); ldl(0); ldl(1);
    if (att) bar();
    ldl(2); ldl(3); ldl(4);
    if (att) bar();
    STAMP(5);
    if (!has2) return;
    ldl(5); ldl(6); ldl(7); ldl(8);
    if (kq == 0) { ldl(18); ldl(19); wait_vm<24>(); } else wait_vm<16>();
    STAMP(6);
    bar();
    bar_vm<0>();
    const int wa = kq * 4096 + lane * 16;
#define PUT(r, off) dsw128<off>(wa + (r / 3) * 49152, pre[r][0]); dsw128<off + 1024>(wa + (r / 3) * 49152, pre[r][1]); dsw128<off + 2048>(wa + (r / 3) * 49152, pre[r][2]); dsw128<off + 3072>(wa + (r / 3) * 49152, pre[r][3]);
    PUT(0, 0) PUT(1, 16384) PUT(2, 32768)
    bar_lgkm();
    PUT(3, 0) PUT(4, 16384) PUT(5, 32768)
    bar_lgkm();
    PUT(6, 0) PUT(7, 16384) PUT(8, 32768)
    bar_lgkm();
    bar();
    bar();
  } else {
    const int rid = (att ? 0 : 8) + j_;
    const int srow = p_ * 128 + 8 * rid + w;
    unsigned u[5];
#pragma unroll
    for (int k = 0; k < 5; k++) u[k] = ((const unsigned*)x)[srow * 320 + lane + 64 * k];
    const unsigned tag = __builtin_amdgcn_readfirstlane(__hip_atomic_load((GAS unsigned*)ws, __ATOMIC_RELAXED, __HIP_MEMORY_SCOPE_AGENT)) + 1u;
    if (!att && tid < 224) {
      int n = j_ * 224 + tid;
      *(LAS float*)(shm + LDS_B1 + tid * 4) = blo(b1[n < F_ ? n : F_ - 1]);
    }
    if (tid < 384) {
      const int sl = tid >> 7, o = tid & 127;
      const unsigned v = o < 8 ? 0x3f803f80u : 0u;
      if (o < 8 || !att) *(LAS u32x4*)(shm + LDS_X + sl * XSLOT + o * 16) = u32x4{v, v, v, v};
    }
    if (tid == 448) *(LAS u32x2*)(shm + LDS_CNT) = u32x2{0u, tag};
    {
      float s = 0;
#pragma unroll
      for (int k = 0; k < 5; k++) s += blo(u[k]) + bhi(u[k]);
      const float m = wave_sum(s) * (1.f / D_);
      float q = 0;
#pragma unroll
      for (int k = 0; k < 5; k++) { float a = blo(u[k]) - m, b = bhi(u[k]) - m; q += a * a + b * b; }
      const float rs = rsqrtf(wave_sum(q) * (1.f / D_) + 1e-5f);
      if (lane == 0) st_wt(mkr(ws + WS_STAT, S_ * 16), 0, srow * 16, u32x4{asu(rs), tag, asu(-m * rs), tag});
    }
    const bool light = att ? (w >= 6) : (w >= 4);
    if (light) s1_compute<3>(shm, att, w, lane, p_, j_, ws);
    else s1_compute<4>(shm, att, w, lane, p_, j_, ws);
    STAMP(3);
    if (att) { s1_attention(shm, w, lane, p_, j_, ws); bar_lgkm(); }
    STAMP(4);
    wait_vm<0>();
    GAS u64* flags = (GAS u64*)(ws + WS_FLAG);
    const u64 fv = flagval(tag);
    if (lane == 0) {
      unsigned old = __hip_atomic_fetch_add((LAS unsigned*)(shm + LDS_CNT), 1u, __ATOMIC_RELAXED, __HIP_MEMORY_SCOPE_WORKGROUP);
      if (old == 7) __hip_atomic_store(flags + p_ * 16 + rid, fv, __ATOMIC_RELAXED, __HIP_MEMORY_SCOPE_AGENT);
    }
    STAMP(5);
    if (!has2) {
      if (blockIdx.x == 255 && w == 0) {
#pragma nounroll
        for (unsigned spins = 0; spins < SPIN_CAP; spins++) {
          bool ok = true;
#pragma unroll
          for (int k = 0; k < 4; k++) ok &= __hip_atomic_load(flags + lane + 64 * k, __ATOMIC_RELAXED, __HIP_MEMORY_SCOPE_AGENT) == fv;
          if (__all(ok)) break;
          __builtin_amdgcn_s_sleep(8);
        }
        if (lane == 0) __hip_atomic_store((GAS unsigned*)ws, tag, __ATOMIC_RELAXED, __HIP_MEMORY_SCOPE_AGENT);
      }
      return;
    }
    const int mh = w >> 2, g = lane >> 4, q = (lane >> 2) & 3, p = lane & 3;
    const int ks0 = kq == 0 ? 54 : 18 * (kq - 1), nsteps = kq == 0 ? 20 : 18;
    const u32x4 ra = mkr(ws + WS_A2, A2_BYTES);
    int ao[3];
    const int av = lane * 16;
#pragma unroll
    for (int m = 0; m < 3; m++) { int G = G0 + 3 * mh + m; G = G < 128 ? G : 127; ao[m] = (G * KS_T + ks0) * 1024; }
    int ba[4], bs[4];
#pragma unroll
    for (int c = 0; c < 4; c++) ba[c] = kq * 4096 + (4 * g + q) * 128 + ((c ^ ((2 * g + (q >> 1)) & 3)) << 5) + 8 * p;
    f32x4 acc[3][4];
#pragma unroll
    for (int m = 0; m < 3; m++)
#pragma unroll
      for (int c = 0; c < 4; c++) { acc[m][c] = f32x4{0, 0, 0, 0}; asm volatile("" : "+v"(acc[m][c])); }
    {
      int Ga = G0 + 3 * mh, Gb = Ga + 2;
      Ga = Ga < 128 ? Ga : 127; Gb = Gb < 128 ? Gb : 127;
      GAS u64* fp = flags + (((lane & 8) ? Gb : Ga) >> 3) * 16 + (kq == 0 ? 0 : 8) + (lane & 7);
#pragma nounroll
      for (unsigned spins = 0; spins < SPIN_CAP; spins++) {
        if (__all(__hip_atomic_load(fp, __ATOMIC_RELAXED, __HIP_MEMORY_SCOPE_AGENT) == fv)) break;
        __builtin_amdgcn_s_sleep(2);
      }
      __builtin_amdgcn_fence(__ATOMIC_ACQUIRE, "wavefront");
    }
    STAMP(6);
    u32x4 a[6][3];
#pragma unroll
    for (int u = 0; u < 6; u++)
#pragma unroll
      for (int m = 0; m < 3; m++) bload_sc1(a[u][m], ra, av, ao[m] + u * 1024);
    u32x2 b0[4][2], b1f[4][2];
    bar();
    STAMP(7);
#pragma unroll
    for (int c = 0; c < 4; c++) { dstr<0>(b0[c][0], ba[c]); dstr<2048>(b0[c][1], ba[c]); }
    int sl = 0;
    auto step = [&](auto U, u32x2 (&bc)[4][2], u32x2 (&bn)[4][2], int s) {
      constexpr int u = decltype(U)::value;
      const int so = s + 6 < nsteps ? (s + 6) * 1024 : 0x70000000;
      sl = min(sl + 16384, 151552);
      if (u == 2 && s == 8) sl = 0;
#pragma unroll
      for (int c = 0; c < 4; c++) bs[c] = ba[c] + sl;
#pragma unroll
      for (int m = 0; m < 3; m++) {
        wait_vm<17>();
#pragma unroll
        for (int c = 0; c < 4; c++) {
          mfma_a(acc[m][c], cat(bc[c][0], bc[c][1]), a[u][m]);
          if (m < 2) { if (c & 1) dstr<2048>(bn[2 * m + (c >> 1)][1], bs[2 * m + (c >> 1)]); else dstr<0>(bn[2 * m + (c >> 1)][0], bs[2 * m + (c >> 1)]); }
        }
        bload_sc1(a[u][m], ra, av, ao[m] + so);
      }
    };
#define LG0 asm volatile("s_waitcnt lgkmcnt(0)" ::: "memory")
    LG0;
#pragma nounroll
    for (int s = 0; s < nsteps; s += 6) {
      step(IC(0), b0, b1f, s); LG0;
      step(IC(1), b1f, b0, s + 1); LG0;
      if (s == 18) break;
      step(IC(2), b0, b1f, s + 2); bar_lgkm();
      step(IC(3), b1f, b0, s + 3); LG0;
      step(IC(4), b0, b1f, s + 4); LG0;
      step(IC(5), b1f, b0, s + 5); bar_lgkm();
    }
    asm volatile("s_waitcnt vmcnt(0)\n\ts_nop 15" ::: "memory");
    STAMP(9);
    const int l2 = lane_id(), i2 = l2 & 15, g2 = l2 >> 4;
#pragma unroll
    for (int m = 0; m < 3; m++)
#pragma unroll
      for (int c = 0; c < 4; c++) {
        asm volatile("" : "+v"(acc[m][c]));
        *(LAS f32x4*)(shm + (3 * mh + m) * 16384 + kq * 4096 + i2 * 256 + (((4 * c + g2) ^ (i2 & 7)) << 4)) = acc[m][c];
      }
  }
  {
    const int lane = lane_id();
    const int frow = 8 * w + (lane >> 3), fG = G0 + (frow >> 4);
    const bool fvalid = fG < 128;
    const int ftok = 32 * ((fvalid ? fG : 0) >> 1) + 2 * sig(frow & 15) + (fG & 1);
    const int foff = ftok * 1280 + n_ * 128 + (lane & 7) * 16;
    const u32x4 xv = *(const u32x4*)((const char*)x + foff);
    const u32x4 bv = *(const u32x4*)(b2 + n_ * 128 + (lane & 7) * 16);
    bar_lgkm();
    STAMP(10);
    f32x4 s[2], y[2];
#pragma unroll
    for (int e = 0; e < 2; e++) {
      lptr pr = shm + (frow >> 4) * 16384 + (frow & 15) * 256 + (((2 * (lane & 7) + e) ^ (frow & 7)) << 4);
      s[e] = *(LAS f32x4*)pr;
      y[e] = *(LAS f32x4*)(pr + 4096) + *(LAS f32x4*)(pr + 8192) + *(LAS f32x4*)(pr + 12288);
    }
    u32x4 o;
#pragma unroll
    for (int k = 0; k < 4; k++) {
      f32x2 yy = f32x2{y[k >> 1][2 * (k & 1)], y[k >> 1][2 * (k & 1) + 1]} + f32x2{blo(bv[k]), bhi(bv[k])};
      f32x2 t = yy * -1.4426950408889634f;
      f32x2 e = f32x2{__builtin_amdgcn_exp2f(t[0]), __builtin_amdgcn_exp2f(t[1])} + 1.f;
      f32x2 gt = yy * f32x2{__builtin_amdgcn_rcpf(e[0]), __builtin_amdgcn_rcpf(e[1])};
      f32x2 r = f32x2{s[k >> 1][2 * (k & 1)], s[k >> 1][2 * (k & 1) + 1]} * gt + f32x2{blo(xv[k]), bhi(xv[k])};
      o[k] = pk2(r);
    }
    if (fvalid) *(u32x4*)(out + foff) = o;
  }
  STAMP(11);
}

extern "C" void kernel_launch(void* const* d_in, const int* in_sizes, int n_in, void* d_out, int out_size, void* d_ws,
                              size_t ws_size, hipStream_t stream) {
  (void)hipFuncSetAttribute((const void*)fused_block, hipFuncAttributeMaxDynamicSharedMemorySize, LDS_TOTAL);
  fused_block<<<256, 768, LDS_TOTAL, stream>>>(d_in[0], d_in[1], d_in[2], d_in[4], (const unsigned short*)d_in[5], d_in[6],
                                               (const char*)d_in[7], (char*)d_out, (char*)d_ws);
}
```

```cpp
#define PF_CODE 1
#include <hip/hip_runtime.h>
#include <hip/hip_bf16.h>
#include <type_traits>

typedef __attribute__((ext_vector_type(4))) short s16x4;
typedef __attribute__((ext_vector_type(8))) __bf16 bf16x8;
typedef __attribute__((ext_vector_type(2))) __bf16 bf16x2;
typedef __attribute__((ext_vector_type(4))) float f32x4;
typedef __attribute__((ext_vector_type(2))) float f32x2;
typedef __attribute__((ext_vector_type(4))) unsigned u32x4;
typedef __attribute__((ext_vector_type(2))) unsigned u32x2;
#define LAS __attribute__((address_space(3)))
#define GAS __attribute__((address_space(1)))
#define DEVI __device__ __forceinline__
typedef LAS char* lptr;
typedef unsigned long long u64;

constexpr int S_ = 2048, D_ = 640, F_ = 1728;
constexpr int KS_T = 74;
constexpr int WSLAB = 15360, NWS = 6;
constexpr int XSLOT = 18432, NXS = 3;
constexpr int LDS_X = WSLAB * NWS;
constexpr int LDS_TB = 86016;
constexpr int LDS_ST = 155648;
constexpr int LDS_CS = LDS_ST + 1152;
constexpr int LDS_B1 = LDS_CS + 1024;
constexpr int LDS_CNT = LDS_B1 + 1024;
constexpr int LDS_TOTAL = 163840;
constexpr int WS_FLAG = 4096, WS_STAT = 8192, WS_A2 = 1 << 20;
constexpr int A2_BYTES = 128 * KS_T * 1024;
#define PF_TLB 0
constexpr int CODE_BYTES = 24960;
constexpr unsigned SPIN_CAP = 1u << 20;

#define STAMP(idx)

DEVI void bar_lgkm() { asm volatile("s_waitcnt lgkmcnt(0)\n\ts_barrier" ::: "memory"); }
DEVI void bar() { asm volatile("s_barrier" ::: "memory"); }
template <int N> DEVI void bar_vm() { asm volatile("s_waitcnt vmcnt(%0)\n\ts_barrier" ::"i"(N) : "memory"); }
template <int N> DEVI void wait_vm() { asm volatile("s_waitcnt vmcnt(%0)" ::"i"(N) : "memory"); }
DEVI unsigned pk2(float a, float b) { f32x2 v = {a, b}; return __builtin_bit_cast(unsigned, __builtin_convertvector(v, bf16x2)); }
DEVI unsigned pk2(f32x2 v) { return __builtin_bit_cast(unsigned, __builtin_convertvector(v, bf16x2)); }
DEVI float asf(unsigned u) { return __builtin_bit_cast(float, u); }
DEVI unsigned asu(float f) { return __builtin_bit_cast(unsigned, f); }
DEVI float blo(unsigned u) { return __builtin_bit_cast(float, u << 16); }
DEVI float bhi(unsigned u) { return __builtin_bit_cast(float, u & 0xffff0000u); }
DEVI u32x2 tr_read(lptr p) { return __builtin_bit_cast(u32x2, __builtin_amdgcn_ds_read_tr16_b64_v4i16((LAS s16x4*)p)); }
DEVI f32x4 mfma(u32x4 a, u32x4 b, f32x4 c) {
  return __builtin_amdgcn_mfma_f32_16x16x32_bf16(__builtin_bit_cast(bf16x8, a), __builtin_bit_cast(bf16x8, b), c, 0, 0, 0);
}
DEVI u32x4 cat(u32x2 a, u32x2 b) { return __builtin_shufflevector(a, b, 0, 1, 2, 3); }
DEVI void mfma_a(f32x4& c, u32x4 a, u32x4 b) { asm volatile("v_mfma_f32_16x16x32_bf16 %0, %1, %2, %0" : "+v"(c) : "v"(a), "v"(b)); }
template <int OFF> DEVI void ds128(u32x4& d, int addr) { asm volatile("ds_read_b128 %0, %1 offset:%2" : "=v"(d) : "v"(addr), "i"(OFF)); }
template <int OFF> DEVI void dstr(u32x2& d, int addr) { asm volatile("ds_read_b64_tr_b16 %0, %1 offset:%2" : "=v"(d) : "v"(addr), "i"(OFF)); }
template <int OFF> DEVI void dsw128(int addr, u32x4 d) { asm volatile("ds_write_b128 %0, %1 offset:%2" ::"v"(addr), "v"(d), "i"(OFF) : "memory"); }
DEVI u32x4 mkr(const void* p, unsigned bytes) {
  u64 a = (u64)p;
  return u32x4{(unsigned)a, (unsigned)(a >> 32) & 0xffffu, bytes, 0x00020000u};
}
DEVI void st_wt(u32x4 r, int voff, int soff, u32x4 v) {
  asm volatile("buffer_store_dwordx4 %0, %1, %2, %3 offen sc1\n\ts_nop 1" ::"v"(v), "v"(voff), "s"(r), "s"(soff) : "memory");
}
template <int LSTEP>
DEVI void dma4(u32x4 r, u64 mask, int l, int va, int vb, int s0, int s1, int s2, int s3) {
  asm volatile(
      "s_mov_b64 exec, %0\n\t"
      "s_mov_b32 m0, %1\n\ts_nop 0\n\tbuffer_load_dwordx4 %2, %4, %5 offen lds\n\t"
      "s_add_u32 m0, %1, %9\n\ts_nop 0\n\tbuffer_load_dwordx4 %3, %4, %6 offen lds\n\t"
      "s_add_u32 m0, %1, %10\n\ts_nop 0\n\tbuffer_load_dwordx4 %2, %4, %7 offen lds\n\t"
      "s_add_u32 m0, %1, %11\n\ts_nop 0\n\tbuffer_load_dwordx4 %3, %4, %8 offen lds\n\t"
      "s_mov_b64 exec, -1"
      :: "s"(mask), "s"(l), "v"(va), "v"(vb), "s"(r), "s"(s0), "s"(s1), "s"(s2), "s"(s3), "i"(LSTEP), "i"(2 * LSTEP), "i"(3 * LSTEP)
      : "memory", "scc");
}
DEVI void dma1(u32x4 r, u64 mask, int l, int v, int s0) {
  asm volatile("s_mov_b64 exec, %0\n\ts_mov_b32 m0, %1\n\ts_nop 0\n\tbuffer_load_dwordx4 %2, %3, %4 offen lds\n\ts_mov_b64 exec, -1"
               :: "s"(mask), "s"(l), "v"(v), "s"(r), "s"(s0) : "memory");
}
DEVI void touch(u32x4 r, int v) {
  asm volatile("s_mov_b32 m0, %2\n\ts_nop 0\n\tbuffer_load_dword %0, %1, 0 offen lds" ::"v"(v), "s"(r), "i"(LDS_TOTAL - 256) : "memory");
}
DEVI void bload(u32x4& d, u32x4 r, int v, int s0) { asm volatile("buffer_load_dwordx4 %0, %1, %2, %3 offen" : "=v"(d) : "v"(v), "s"(r), "s"(s0)); }
DEVI void bload_sc1(u32x4& d, u32x4 r, int v, int s0) { asm volatile("buffer_load_dwordx4 %0, %1, %2, %3 offen sc1" : "=v"(d) : "v"(v), "s"(r), "s"(s0) : "memory"); }
DEVI int lane_id() { int l; asm volatile("v_mbcnt_lo_u32_b32 %0, -1, 0\n\tv_mbcnt_hi_u32_b32 %0, -1, %0" : "=v"(l)); return l; }
DEVI int sig(int i) { return 4 * (i & 3) + (i >> 2); }
DEVI u64 flagval(unsigned tag) { return ((u64)(tag ^ 0x5bd1e995u) << 32) | tag; }

DEVI float xmax16(float v) { auto r = __builtin_amdgcn_permlane16_swap(asu(v), asu(v), false, false); return fmaxf(asf(r[0]), asf(r[1])); }
DEVI float xmax32(float v) { auto r = __builtin_amdgcn_permlane32_swap(asu(v), asu(v), false, false); return fmaxf(asf(r[0]), asf(r[1])); }
DEVI float xadd16(float v) { auto r = __builtin_amdgcn_permlane16_swap(asu(v), asu(v), false, false); return asf(r[0]) + asf(r[1]); }
DEVI float xadd32(float v) { auto r = __builtin_amdgcn_permlane32_swap(asu(v), asu(v), false, false); return asf(r[0]) + asf(r[1]); }
template <int CTRL> DEVI float dpp(float v) { return asf(__builtin_amdgcn_mov_dpp(asu(v), CTRL, 0xf, 0xf, true)); }
DEVI float wave_sum(float v) {
  v += dpp<0xb1>(v); v += dpp<0x4e>(v); v += dpp<0x141>(v); v += dpp<0x140>(v);
  return xadd32(xadd16(v));
}

DEVI int row_token(int p_, int pos) {
  const int tau = pos >> 4, i = pos & 15;
  int tok = tau == 0 ? p_ * 128 - 16 + 8 * (i & 1) + 2 * (i >> 2) + ((i & 3) < 2) : p_ * 128 + 32 * ((tau - 1) >> 1) + 2 * sig(i) + ((tau - 1) & 1);
  return tok < 0 ? 0 : tok;
}

DEVI int colbyte(bool att, int nw, int mw, int c, int p) {
  if (att) {
    int ct = 4 * nw + (nw < 3 ? (c ^ (2 * mw)) : c);
    int sec = ct / 5, hh = ct - 5 * sec;
    return sec * 160 + (hh < 4 ? 40 * hh + 8 * p : 40 * p + 32);
  } else {
    if (nw < 2) return 128 * nw + 32 * (c ^ (2 * mw)) + 8 * p;
    if (c < 2) return 256 + 64 * (nw - 2) + 32 * c + 8 * p;
    return 384 + 8 * ((p & 1) + 4 * (p >> 1) + 2 * (nw - 2));
  }
}

template <int NC>
struct Frags { u32x4 x[5]; u32x2 w[NC][2]; };

template <int NC, int NH, bool MMA>
DEVI void s1_step(const Frags<NC>& c, Frags<NC>& n, int xo, int xh, const int (&wa)[NC], f32x4 (&acc)[4][NC], f32x4 (&hacc)[NH]) {
  constexpr int NM = 4 * NC + NH, NR = 2 * NC + 5;
#pragma unroll
  for (int k = 0; k < NM; k++) {
    if (MMA) {
      if (k < 4 * NC) mfma_a(acc[k / NC][k % NC], cat(c.w[k % NC][0], c.w[k % NC][1]), c.x[k / NC]);
      else mfma_a(hacc[k - 4 * NC], cat(c.w[k - 4 * NC][0], c.w[k - 4 * NC][1]), c.x[4]);
    }
    if (k < NR) {
      if (k == 0) ds128<0>(n.x[0], xo);
      else if (k <= 2 * NC) { if ((k - 1) & 1) dstr<7680>(n.w[(k - 1) >> 1][1], wa[(k - 1) >> 1]); else dstr<0>(n.w[(k - 1) >> 1][0], wa[(k - 1) >> 1]); }
      else if (k == 2 * NC + 1) ds128<2048>(n.x[1], xo);
      else if (k == 2 * NC + 2) ds128<4096>(n.x[2], xo);
      else if (k == 2 * NC + 3) ds128<6144>(n.x[3], xo);
      else ds128<0>(n.x[4], xh);
    }
  }
}

DEVI f32x2 gelu2(f32x2 v) {
  const float c1 = -2.f * 0.7978845608028654f * 1.4426950408889634f, c2 = c1 * 0.044715f;
  f32x2 t = v * v;
  t = t * c2 + c1;
  t = t * v;
  f32x2 e = {__builtin_amdgcn_exp2f(t[0]), __builtin_amdgcn_exp2f(t[1])};
  e = e + 1.f;
  f32x2 r = {__builtin_amdgcn_rcpf(e[0]), __builtin_amdgcn_rcpf(e[1])};
  return v * r;
}

template <int NC>
DEVI void s1_compute(lptr shm, bool att, int w, int lane, int p_, int j_, char* ws) {
  constexpr int NH = NC == 4 ? 2 : 3;
  const int mw = w & 1, nw = w >> 1, i = lane & 15, g = lane >> 4;
  f32x4 acc[4][NC], hacc[NH];
#pragma unroll
  for (int m = 0; m < 4; m++)
#pragma unroll
    for (int c = 0; c < NC; c++) { acc[m][c] = f32x4{0, 0, 0, 0}; asm volatile("" : "+v"(acc[m][c])); }
#pragma unroll
  for (int c = 0; c < NH; c++) { hacc[c] = f32x4{0, 0, 0, 0}; asm volatile("" : "+v"(hacc[c])); }
  int wb[NC], cb[NC];
#pragma unroll
  for (int c = 0; c < NC; c++) {
    wb[c] = (4 * g + ((lane >> 2) & 3)) * 480 + colbyte(att, nw, mw, c, lane & 3);
    cb[c] = colbyte(att, nw, mw, c, g) * 2;
    asm volatile("" : "+v"(cb[c]));
  }
  const int xb = LDS_X + i * 128 + ((g ^ (i >> 1)) << 4);
  const int own = (1 + 4 * mw) * 2048;
  Frags<NC> fa, fb;
  int wa[NC];
  bar_lgkm();
  STAMP(1);
  s1_step<NC, NH, false>(fb, fa, xb + own, xb, wb, acc, hacc);
  int ws_ = WSLAB, xs = 0;
#pragma nounroll
  for (int T = 0; T < 10; T++) {
#pragma unroll
    for (int c = 0; c < NC; c++) wa[c] = wb[c] + ws_;
    asm volatile("s_waitcnt lgkmcnt(0)" ::: "memory");
    s1_step<NC, NH, true>(fa, fb, (xb ^ 64) + xs + own, (xb ^ 64) + xs, wa, acc, hacc);
    ws_ += WSLAB; if (ws_ == WSLAB * NWS) ws_ = 0;
    xs += XSLOT; if (xs == XSLOT * NXS) xs = 0;
#pragma unroll
    for (int c = 0; c < NC; c++) wa[c] = wb[c] + ws_;
    bar_lgkm();
    s1_step<NC, NH, true>(fb, fa, xb + xs + own, xb + xs, wa, acc, hacc);
    ws_ += WSLAB;
  }
  asm volatile("s_waitcnt lgkmcnt(0)\n\ts_nop 15" ::: "memory");
#pragma unroll
  for (int m = 0; m < 4; m++)
#pragma unroll
    for (int c = 0; c < NC; c++) asm volatile("" : "+v"(acc[m][c]));
#pragma unroll
  for (int c = 0; c < NH; c++) asm volatile("" : "+v"(hacc[c]));
  STAMP(2);
  if (i == 0) {
#pragma unroll
    for (int c = 0; c < NH; c++) *(LAS f32x4*)(shm + LDS_CS + cb[c]) = hacc[c];
  }
  bar_lgkm();
  f32x4 cs[NC];
#pragma unroll
  for (int c = 0; c < NC; c++) cs[c] = *(LAS f32x4*)(shm + LDS_CS + cb[c]);
  if (att) {
    const int piece = LDS_TB + (g * 16 + (i ^ (8 * (g >> 1)))) * 8;
    const float qs = 0.22360679774997896f * 1.4426950408889634f;
#pragma unroll
    for (int m = 0; m < 5; m++) {
      const int tau = m < 4 ? 1 + 4 * mw + m : 0;
      f32x2 st = *(LAS f32x2*)(shm + LDS_ST + (tau * 16 + i) * 8);
#pragma unroll
      for (int c = 0; c < (m < 4 ? NC : NH); c++) {
        const int ct = 4 * nw + (NC == 4 ? (c ^ (2 * mw)) : c);
        const float sc = ct < 5 ? qs : 1.f;
        const float rs = st[0] * sc, nm = st[1] * sc;
        f32x4 a = m < 4 ? acc[m < 4 ? m : 0][c] : hacc[c < NH ? c : 0];
        f32x4 v = a * rs + cs[c] * nm;
        u32x2 o = {pk2(v[0], v[1]), pk2(v[2], v[3])};
        *(LAS u32x2*)(shm + (tau * 15 + ct) * 512 + piece) = o;
      }
    }
  } else {
    f32x4 b1[NC];
#pragma unroll
    for (int c = 0; c < NC; c++) b1[c] = *(LAS f32x4*)(shm + LDS_B1 + cb[c]);
    const u32x4 ra = mkr(ws + WS_A2, A2_BYTES);
    const int base = ((8 * p_ + 4 * mw) * KS_T + 7 * j_) * 1024;
    unsigned sp[4][2];
#pragma unroll
    for (int m = 0; m < 4; m++) {
      f32x2 st = *(LAS f32x2*)(shm + LDS_ST + ((1 + 4 * mw + m) * 16 + i) * 8);
      unsigned o[NC][2];
#pragma unroll
      for (int c = 0; c < NC; c++) {
        f32x4 v = acc[m][c] * st[0] + (cs[c] * st[1] + b1[c]);
        o[c][0] = pk2(gelu2(f32x2{v[0], v[1]}));
        o[c][1] = pk2(gelu2(f32x2{v[2], v[3]}));
      }
      const int tb = base + m * KS_T * 1024;
      if (NC == 4) {
        st_wt(ra, lane * 16, tb + (2 * nw + mw) * 1024, u32x4{o[0][0], o[0][1], o[1][0], o[1][1]});
        st_wt(ra, lane * 16, tb + (2 * nw + (mw ^ 1)) * 1024, u32x4{o[2][0], o[2][1], o[NC - 1][0], o[NC - 1][1]});
      } else {
        if (j_ < 7 || nw == 2) st_wt(ra, lane * 16, tb + (2 + nw) * 1024, u32x4{o[0][0], o[0][1], o[1][0], o[1][1]});
        sp[m][0] = o[2][0]; sp[m][1] = o[2][1];
        if (m & 1) {
          auto s0 = __builtin_amdgcn_permlane32_swap(sp[m - 1][0], sp[m][0], false, false);
          auto s1 = __builtin_amdgcn_permlane32_swap(sp[m - 1][1], sp[m][1], false, false);
          if (j_ < 7) st_wt(ra, (lane >> 5) * (KS_T * 1024) + (lane & 31) * 16, tb - KS_T * 1024 + 6 * 1024 + (nw - 2) * 512, u32x4{s0[0], s1[0], s0[1], s1[1]});
        }
      }
    }
  }
}

DEVI void s1_attention(lptr shm, int w, int lane, int p_, int j_, char* ws) {
  const int i = lane & 15, g = lane >> 4;
  const int tau = w + 1, taup = w >= 2 ? tau - 2 : 0;
  const int flip = (w == 1) ? 2 : 0;
  const bool start = (p_ == 0) && (w < 2);
  const int qi = sig(i);
  f32x4 mO, mP;
  unsigned M1, M2, M3;
  {
    bool vo[4], vp[4];
#pragma unroll
    for (int r = 0; r < 4; r++) {
      int jj = 4 * r + g;
      vo[r] = (jj <= qi) && (jj >= qi - 7);
      vp[r] = (jj - 16 >= qi - 7) && !start;
      mO[r] = vo[r] ? 0.f : -1e30f;
      mP[r] = vp[r] ? 0.f : -1e30f;
    }
    M1 = (vp[2] ? 0xffffu : 0u) | (vp[3] ? 0xffff0000u : 0u);
    M2 = (vo[0] ? 0xffffu : 0u) | (vo[1] ? 0xffff0000u : 0u);
    M3 = (vo[2] ? 0xffffu : 0u) | (vo[3] ? 0xffff0000u : 0u);
  }
  lptr tb = shm + LDS_TB;
  lptr po = tb + tau * 15 * 512 + (g * 16 + (i ^ (8 * (g >> 1)))) * 8;
  lptr pp = tb + taup * 15 * 512 + (g * 16 + ((i ^ flip) ^ (8 * (g >> 1)))) * 8;
  const int q = (lane >> 2) & 3, p = lane & 3;
  lptr to = tb + tau * 15 * 512 + (p * 16 + ((4 * g + q) ^ (8 * (p >> 1)))) * 8;
  lptr tp = tb + taup * 15 * 512 + (p * 16 + ((4 * g + (q ^ flip)) ^ (8 * (p >> 1)))) * 8;
  asm volatile("" :: "v"(M1), "v"(M2), "v"(M3), "v"(mO), "v"(mP), "v"(po), "v"(pp), "v"(to), "v"(tp));
  bar_lgkm();
  const u32x2 q4 = *(LAS u32x2*)(po + 4 * 512), ko4 = *(LAS u32x2*)(po + 9 * 512), kp4 = *(LAS u32x2*)(pp + 9 * 512);
  u32x2 qa[4], ko[4], kp[4];
#pragma unroll
  for (int h = 0; h < 4; h++) { qa[h] = *(LAS u32x2*)(po + h * 512); ko[h] = *(LAS u32x2*)(po + (5 + h) * 512); kp[h] = *(LAS u32x2*)(pp + (5 + h) * 512); }
  u32x4 vf[4];
#pragma unroll
  for (int h = 0; h < 4; h++) vf[h] = cat(tr_read(tp + (10 + h) * 512), tr_read(to + (10 + h) * 512));
  const u32x4 v4 = cat(tr_read(tp + 14 * 512), tr_read(to + 14 * 512));
  f32x4 sp[4], so[4];
#pragma unroll
  for (int h = 0; h < 4; h++) {
    const unsigned mq = g == h ? 0xffffffffu : 0u;
    u32x4 qf = {qa[h][0], qa[h][1], q4[0] & mq, q4[1] & mq};
    sp[h] = mfma(cat(kp[h], kp4), qf, mP);
    so[h] = mfma(cat(ko[h], ko4), qf, mO);
  }
  float ee[4], eo[4], mx[4], l[4];
#pragma unroll
  for (int h = 0; h < 4; h++) { ee[h] = fmaxf(fmaxf(sp[h][2], so[h][0]), so[h][2]); eo[h] = fmaxf(fmaxf(sp[h][3], so[h][1]), so[h][3]); mx[h] = fmaxf(ee[h], eo[h]); }
  __builtin_amdgcn_sched_barrier(0);
#pragma unroll
  for (int h = 0; h < 4; h++) mx[h] = xmax16(mx[h]);
#pragma unroll
  for (int h = 0; h < 4; h++) mx[h] = xmax32(mx[h]);
  __builtin_amdgcn_sched_barrier(0);
#pragma unroll
  for (int h = 0; h < 4; h++) { ee[h] = __builtin_amdgcn_exp2f(ee[h] - mx[h]); eo[h] = __builtin_amdgcn_exp2f(eo[h] - mx[h]); l[h] = ee[h] + eo[h]; }
  __builtin_amdgcn_sched_barrier(0);
#pragma unroll
  for (int h = 0; h < 4; h++) l[h] = xadd16(l[h]);
#pragma unroll
  for (int h = 0; h < 4; h++) l[h] = xadd32(l[h]);
  __builtin_amdgcn_sched_barrier(0);
  f32x4 o4 = {0, 0, 0, 0}, oh[4];
#pragma unroll
  for (int h = 0; h < 4; h++) {
    const float rl = __builtin_amdgcn_rcpf(l[h]);
    const unsigned pk = pk2(ee[h] * rl, eo[h] * rl), mv = (i >> 2) == h ? 0xffffffffu : 0u;
    u32x4 pf = {0u, pk & M1, pk & M2, pk & M3};
    oh[h] = mfma(vf[h], pf, f32x4{0, 0, 0, 0});
    o4 = mfma(v4 & mv, pf, o4);
  }
  const u32x4 ra = mkr(ws + WS_A2, A2_BYTES);
  const int base = ((8 * p_ + w) * KS_T + 54 + 5 * (j_ >> 1)) * 1024;
#pragma unroll
  for (int hp = 0; hp < 2; hp++)
    st_wt(ra, lane * 16, base + ((j_ & 1) * 3 + hp) * 1024,
          u32x4{pk2(oh[2 * hp][0], oh[2 * hp][1]), pk2(oh[2 * hp][2], oh[2 * hp][3]), pk2(oh[2 * hp + 1][0], oh[2 * hp + 1][1]), pk2(oh[2 * hp + 1][2], oh[2 * hp + 1][3])});
  unsigned a0 = pk2(o4[0], o4[1]), a1 = pk2(o4[2], o4[3]);
  auto s0 = __builtin_amdgcn_permlane32_swap(a0, a0, false, false);
  auto s1 = __builtin_amdgcn_permlane32_swap(a1, a1, false, false);
  if (lane < 32) st_wt(ra, lane * 16, base + 2 * 1024 + (j_ & 1) * 512, u32x4{s0[0], s1[0], s0[1], s1[1]});
}

template <bool ATT>
DEVI void s1_loader(int lw, int lane, int p_, int j_, const void* x, const void* wsrc) {
  constexpr int NX = ATT ? 5 : 4;
  constexpr int RS = ATT ? 3840 : 3456;
  const u32x4 rx = mkr(x, S_ * D_ * 2), rw = mkr(wsrc, D_ * RS);
  int wv;
  {
    int hi = lane >= 30, ch = lane - 30 * hi;
    int cbyte;
    if (ATT) { int sec = (ch >= 10) + (ch >= 20); cbyte = sec * 1120 + j_ * 160 + ch * 16; }
    else { int lim = j_ < 7 ? 27 : 19; cbyte = j_ * 448 + (ch < lim ? ch : lim) * 16; }
    wv = hi * RS + cbyte;
  }
  const int wrow = 16 * (lw & 1) + 4 * (lw >> 1);
  const int r8 = lane >> 3, cp = lane & 7, t0 = p_ * 128;
  int xv[2], hv;
#pragma unroll
  for (int hf = 0; hf < 2; hf++)
    xv[hf] = (t0 + 32 * lw + 2 * (4 * (r8 & 3) + 2 * hf + (r8 >> 2))) * 1280 + ((cp ^ (4 * hf + (r8 >> 1))) << 4);
  {
    int tok = t0 - 16 + 8 * (r8 & 1) + 2 * lw + ((r8 & 3) < 2);
    tok = tok < 0 ? 0 : tok;
    hv = tok * 1280 + ((cp ^ ((2 * lw + ((r8 & 3) >> 1)) & 7)) << 4);
  }
  const u64 hmask = lw == 0 ? 0xffffff00ull : 0xffffffffull;
  auto ldw = [&](int s, int slot) {
    const int so = (32 * s + wrow) * RS;
    dma4<960>(rw, 0x0fffffffffffffffull, slot * WSLAB + 4 * lw * 960, wv, wv, so, so + 2 * RS, so + 8 * RS, so + 10 * RS);
  };
  auto ldx = [&](int T, int slot) {
    const int d = LDS_X + slot * XSLOT;
    if (ATT) dma1(rx, hmask, d + lw * 512, hv, T * 128);
    dma4<1024>(rx, ~0ull, d + 2048 + 4 * lw * 1024, xv[0], xv[1], T * 128, T * 128, T * 128 + 1280, T * 128 + 1280);
  };
  ldx(0, 0); ldw(0, 0); ldw(1, 1);
  int slot = 1;
#pragma nounroll
  for (int T = 1; T < 10; T++) {
    ldx(T, slot); ldw(2 * T, 2 * slot); ldw(2 * T + 1, 2 * slot + 1);
    bar_vm<NX + 8>();
    slot += 1; if (slot == NXS) slot = 0;
  }
}

__global__ __launch_bounds__(768, 3) void fused_block(const void* __restrict__ x, const void* __restrict__ wqkv, const void* __restrict__ wo,
                                                      const void* __restrict__ w1, const unsigned short* __restrict__ b1,
                                                      const void* __restrict__ w2, const char* __restrict__ b2, char* __restrict__ out,
                                                      char* ws) {
  extern __shared__ __attribute__((aligned(16))) char shm_[];
  lptr shm = (lptr)shm_;
  const int tid = threadIdx.x, lane = tid & 63, w = __builtin_amdgcn_readfirstlane(tid >> 6);
  const int xcd = blockIdx.x & 7, idx = blockIdx.x >> 3;
  const int p_ = (xcd & 1) * 8 + (idx & 7), sub = idx >> 3;
  const bool att = !(sub & 1);
  const int j_ = (xcd >> 1) * 2 + (sub >> 1);
  const int unit = (xcd >> 1) * 28 + idx;
  const bool has2 = idx < 28 && unit < 110;
  const int mt = unit / 5, n_ = (xcd & 1) * 5 + (unit - 5 * mt);
  const int G0 = 6 * mt;
  const int kq = w & 3;
  STAMP(0);
  if (w == 7) {
    u64 pc; asm volatile("s_getpc_b64 %0" : "=s"(pc));
    touch(mkr((const void*)(pc & ~255ull), CODE_BYTES), (idx + 32 * lane) * 128);
  }
  if (w >= 8) {
    const int lw = w - 8;
    if (att) s1_loader<true>(lw, lane, p_, j_, x, wqkv);
    else s1_loader<false>(lw, lane, p_, j_, x, w1);
    const unsigned tag = __builtin_amdgcn_readfirstlane(*(LAS unsigned*)(shm + LDS_CNT + 4));
    const u32x4 rst = mkr(ws + WS_STAT, S_ * 16);
    const int spos = 36 * lw + (lane < 36 ? lane : 35), sv = row_token(p_, spos) * 16;
    u32x4 sg;
    bload_sc1(sg, rst, sv, 0);
    const u32x4 rw = mkr(kq == 0 ? wo : w2, has2 ? (kq == 0 ? D_ * 1280 : F_ * 1280) : 0);
    const int l3 = lane >> 3, cbyte = n_ * 128 + (((lane & 7) ^ ((lane >> 4) << 1)) << 4);
    const int v1 = l3 * 1280 + cbyte, v2 = (20 * (l3 >> 2) + (l3 & 3)) * 1280 + cbyte;
    auto geom = [&](int s, int& rb, int& sa, int& sb, int& v) {
      sa = 8; sb = 16; v = v1;
      if (kq == 0) {
        int P = (s * 13) >> 6, kp = s - 5 * P;
        if (kp == 2) { rb = 160 * P + 16; sa = 80; sb = 40; v = v2; }
        else { rb = 160 * P + 40 * (kp > 2 ? kp - 1 : kp); sb = 20; }
      } else rb = 32 * (18 * (kq - 1) + s);
      rb *= 1280; sa *= 1280; sb *= 1280;
    };
    u32x4 pre[9][4];
    auto ldr = [&](auto R) {
      constexpr int r = decltype(R)::value;
      int rb, sa, sb, v; geom(9 + r, rb, sa, sb, v);
      bload(pre[r][0], rw, v, rb); bload(pre[r][1], rw, v, rb + sa); bload(pre[r][2], rw, v, rb + sb); bload(pre[r][3], rw, v, rb + sa + sb);
    };
    auto ldl = [&](int s) {
      int rb, sa, sb, v; geom(s, rb, sa, sb, v);
      dma4<1024>(rw, ~0ull, s < 9 ? s * 16384 + kq * 4096 : 147456 + (s - 18) * 4096, v, v, rb, rb + sa, rb + sb, rb + sa + sb);
    };
#define IC(n) std::integral_constant<int, n>{}
    ldr(IC(0)); ldr(IC(1)); ldr(IC(2));
    asm volatile("s_waitcnt vmcnt(12)" : "+v"(sg) :: "memory");
#pragma nounroll
    for (unsigned spins = 0; !__all(sg[1] == tag && sg[3] == tag) && spins < SPIN_CAP; spins++) {
      __builtin_amdgcn_s_sleep(2);
      bload_sc1(sg, rst, sv, 0);
      asm volatile("s_waitcnt vmcnt(0)" : "+v"(sg) :: "memory");
    }
    *(LAS u32x2*)(shm + LDS_ST + spos * 8) = u32x2{sg[0], sg[2]};
    bar_lgkm();
    ldr(IC(3)); ldr(IC(4)); ldr(IC(5));
    bar();
    ldr(IC(6)); ldr(IC(7));
    bar();
    ldr(IC(8)); ldl(0); ldl(1);
    if (att) bar();
    ldl(2); ldl(3); ldl(4);
    if (att) bar();
    STAMP(5);
    if (!has2) return;
    ldl(5); ldl(6); ldl(7); ldl(8);
    if (kq == 0) { ldl(18); ldl(19); wait_vm<24>(); } else wait_vm<16>();
    STAMP(6);
    bar();
    bar_vm<0>();
    const int wa = kq * 4096 + lane * 16;
#define PUT(r, off) dsw128<off>(wa + (r / 3) * 49152, pre[r][0]); dsw128<off + 1024>(wa + (r / 3) * 49152, pre[r][1]); dsw128<off + 2048>(wa + (r / 3) * 49152, pre[r][2]); dsw128<off + 3072>(wa + (r / 3) * 49152, pre[r][3]);
    PUT(0, 0) PUT(1, 16384) PUT(2, 32768)
    bar_lgkm();
    PUT(3, 0) PUT(4, 16384) PUT(5, 32768)
    bar_lgkm();
    PUT(6, 0) PUT(7, 16384) PUT(8, 32768)
    bar_lgkm();
    bar();
    bar();
  } else {
    const int rid = (att ? 0 : 8) + j_;
    const int srow = p_ * 128 + 8 * rid + w;
    unsigned u[5];
#pragma unroll
    for (int k = 0; k < 5; k++) u[k] = ((const unsigned*)x)[srow * 320 + lane + 64 * k];
    const unsigned tag = __builtin_amdgcn_readfirstlane(__hip_atomic_load((GAS unsigned*)ws, __ATOMIC_RELAXED, __HIP_MEMORY_SCOPE_AGENT)) + 1u;
    if (!att && tid < 224) {
      int n = j_ * 224 + tid;
      *(LAS float*)(shm + LDS_B1 + tid * 4) = blo(b1[n < F_ ? n : F_ - 1]);
    }
    if (tid < 384) {
      const int sl = tid >> 7, o = tid & 127;
      const unsigned v = o < 8 ? 0x3f803f80u : 0u;
      if (o < 8 || !att) *(LAS u32x4*)(shm + LDS_X + sl * XSLOT + o * 16) = u32x4{v, v, v, v};
    }
    if (tid == 448) *(LAS u32x2*)(shm + LDS_CNT) = u32x2{0u, tag};
    {
      float s = 0;
#pragma unroll
      for (int k = 0; k < 5; k++) s += blo(u[k]) + bhi(u[k]);
      const float m = wave_sum(s) * (1.f / D_);
      float q = 0;
#pragma unroll
      for (int k = 0; k < 5; k++) { float a = blo(u[k]) - m, b = bhi(u[k]) - m; q += a * a + b * b; }
      const float rs = rsqrtf(wave_sum(q) * (1.f / D_) + 1e-5f);
      if (lane == 0) st_wt(mkr(ws + WS_STAT, S_ * 16), 0, srow * 16, u32x4{asu(rs), tag, asu(-m * rs), tag});
    }
    const bool light = att ? (w >= 6) : (w >= 4);
    if (light) s1_compute<3>(shm, att, w, lane, p_, j_, ws);
    else s1_compute<4>(shm, att, w, lane, p_, j_, ws);
    STAMP(3);
    if (att) { s1_attention(shm, w, lane, p_, j_, ws); bar_lgkm(); }
    STAMP(4);
    wait_vm<0>();
    GAS u64* flags = (GAS u64*)(ws + WS_FLAG);
    const u64 fv = flagval(tag);
    if (lane == 0) {
      unsigned old = __hip_atomic_fetch_add((LAS unsigned*)(shm + LDS_CNT), 1u, __ATOMIC_RELAXED, __HIP_MEMORY_SCOPE_WORKGROUP);
      if (old == 7) __hip_atomic_store(flags + p_ * 16 + rid, fv, __ATOMIC_RELAXED, __HIP_MEMORY_SCOPE_AGENT);
    }
    STAMP(5);
    if (!has2) {
      if (blockIdx.x == 255 && w == 0) {
#pragma nounroll
        for (unsigned spins = 0; spins < SPIN_CAP; spins++) {
          bool ok = true;
#pragma unroll
          for (int k = 0; k < 4; k++) ok &= __hip_atomic_load(flags + lane + 64 * k, __ATOMIC_RELAXED, __HIP_MEMORY_SCOPE_AGENT) == fv;
          if (__all(ok)) break;
          __builtin_amdgcn_s_sleep(8);
        }
        if (lane == 0) __hip_atomic_store((GAS unsigned*)ws, tag, __ATOMIC_RELAXED, __HIP_MEMORY_SCOPE_AGENT);
      }
      return;
    }
    const int mh = w >> 2, g = lane >> 4, q = (lane >> 2) & 3, p = lane & 3;
    const int ks0 = kq == 0 ? 54 : 18 * (kq - 1), nsteps = kq == 0 ? 20 : 18;
    const u32x4 ra = mkr(ws + WS_A2, A2_BYTES);
    int ao[3];
    const int av = lane * 16;
#pragma unroll
    for (int m = 0; m < 3; m++) { int G = G0 + 3 * mh + m; G = G < 128 ? G : 127; ao[m] = (G * KS_T + ks0) * 1024; }
    int ba[4], bs[4];
#pragma unroll
    for (int c = 0; c < 4; c++) ba[c] = kq * 4096 + (4 * g + q) * 128 + ((c ^ ((2 * g + (q >> 1)) & 3)) << 5) + 8 * p;
    f32x4 acc[3][4];
#pragma unroll
    for (int m = 0; m < 3; m++)
#pragma unroll
      for (int c = 0; c < 4; c++) { acc[m][c] = f32x4{0, 0, 0, 0}; asm volatile("" : "+v"(acc[m][c])); }
    {
      int Ga = G0 + 3 * mh, Gb = Ga + 2;
      Ga = Ga < 128 ? Ga : 127; Gb = Gb < 128 ? Gb : 127;
      GAS u64* fp = flags + (((lane & 8) ? Gb : Ga) >> 3) * 16 + (kq == 0 ? 0 : 8) + (lane & 7);
#pragma nounroll
      for (unsigned spins = 0; spins < SPIN_CAP; spins++) {
        if (__all(__hip_atomic_load(fp, __ATOMIC_RELAXED, __HIP_MEMORY_SCOPE_AGENT) == fv)) break;
        __builtin_amdgcn_s_sleep(2);
      }
      __builtin_amdgcn_fence(__ATOMIC_ACQUIRE, "wavefront");
    }
    STAMP(6);
    u32x4 a[6][3];
#pragma unroll
    for (int u = 0; u < 6; u++)
#pragma unroll
      for (int m = 0; m < 3; m++) bload_sc1(a[u][m], ra, av, ao[m] + u * 1024);
    u32x2 b0[4][2], b1f[4][2];
    bar();
    STAMP(7);
#pragma unroll
    for (int c = 0; c < 4; c++) { dstr<0>(b0[c][0], ba[c]); dstr<2048>(b0[c][1], ba[c]); }
    int sl = 0;
    auto step = [&](auto U, u32x2 (&bc)[4][2], u32x2 (&bn)[4][2], int s) {
      constexpr int u = decltype(U)::value;
      const int so = s + 6 < nsteps ? (s + 6) * 1024 : 0x70000000;
      sl = min(sl + 16384, 151552);
      if (u == 2 && s == 8) sl = 0;
#pragma unroll
      for (int c = 0; c < 4; c++) bs[c] = ba[c] + sl;
#pragma unroll
      for (int m = 0; m < 3; m++) {
        wait_vm<17>();
#pragma unroll
        for (int c = 0; c < 4; c++) {
          mfma_a(acc[m][c], cat(bc[c][0], bc[c][1]), a[u][m]);
          if (m < 2) { if (c & 1) dstr<2048>(bn[2 * m + (c >> 1)][1], bs[2 * m + (c >> 1)]); else dstr<0>(bn[2 * m + (c >> 1)][0], bs[2 * m + (c >> 1)]); }
        }
        bload_sc1(a[u][m], ra, av, ao[m] + so);
      }
    };
#define LG0 asm volatile("s_waitcnt lgkmcnt(0)" ::: "memory")
    LG0;
#pragma nounroll
    for (int s = 0; s < nsteps; s += 6) {
      step(IC(0), b0, b1f, s); LG0;
      step(IC(1), b1f, b0, s + 1); LG0;
      if (s == 18) break;
      step(IC(2), b0, b1f, s + 2); bar_lgkm();
      step(IC(3), b1f, b0, s + 3); LG0;
      step(IC(4), b0, b1f, s + 4); LG0;
      step(IC(5), b1f, b0, s + 5); bar_lgkm();
    }
    asm volatile("s_waitcnt vmcnt(0)\n\ts_nop 15" ::: "memory");
    STAMP(9);
    const int l2 = lane_id(), i2 = l2 & 15, g2 = l2 >> 4;
#pragma unroll
    for (int m = 0; m < 3; m++)
#pragma unroll
      for (int c = 0; c < 4; c++) {
        asm volatile("" : "+v"(acc[m][c]));
        *(LAS f32x4*)(shm + (3 * mh + m) * 16384 + kq * 4096 + i2 * 256 + (((4 * c + g2) ^ (i2 & 7)) << 4)) = acc[m][c];
      }
  }
  {
    const int lane = lane_id();
    const int frow = 8 * w + (lane >> 3), fG = G0 + (frow >> 4);
    const bool fvalid = fG < 128;
    const int ftok = 32 * ((fvalid ? fG : 0) >> 1) + 2 * sig(frow & 15) + (fG & 1);
    const int foff = ftok * 1280 + n_ * 128 + (lane & 7) * 16;
    const u32x4 xv = *(const u32x4*)((const char*)x + foff);
    const u32x4 bv = *(const u32x4*)(b2 + n_ * 128 + (lane & 7) * 16);
    bar_lgkm();
    STAMP(10);
    f32x4 s[2], y[2];
#pragma unroll
    for (int e = 0; e < 2; e++) {
      lptr pr = shm + (frow >> 4) * 16384 + (frow & 15) * 256 + (((2 * (lane & 7) + e) ^ (frow & 7)) << 4);
      s[e] = *(LAS f32x4*)pr;
      y[e] = *(LAS f32x4*)(pr + 4096) + *(LAS f32x4*)(pr + 8192) + *(LAS f32x4*)(pr + 12288);
    }
    u32x4 o;
#pragma unroll
    for (int k = 0; k < 4; k++) {
      f32x2 yy = f32x2{y[k >> 1][2 * (k & 1)], y[k >> 1][2 * (k & 1) + 1]} + f32x2{blo(bv[k]), bhi(bv[k])};
      f32x2 t = yy * -1.4426950408889634f;
      f32x2 e = f32x2{__builtin_amdgcn_exp2f(t[0]), __builtin_amdgcn_exp2f(t[1])} + 1.f;
      f32x2 gt = yy * f32x2{__builtin_amdgcn_rcpf(e[0]), __builtin_amdgcn_rcpf(e[1])};
      f32x2 r = f32x2{s[k >> 1][2 * (k & 1)], s[k >> 1][2 * (k & 1) + 1]} * gt + f32x2{blo(xv[k]), bhi(xv[k])};
      o[k] = pk2(r);
    }
    if (fvalid) *(u32x4*)(out + foff) = o;
  }
  STAMP(11);
}

extern "C" void kernel_launch(void* const* d_in, const int* in_sizes, int n_in, void* d_out, int out_size, void* d_ws,
                              size_t ws_size, hipStream_t stream) {
  (void)hipFuncSetAttribute((const void*)fused_block, hipFuncAttributeMaxDynamicSharedMemorySize, LDS_TOTAL);
  fused_block<<<256, 768, LDS_TOTAL, stream>>>(d_in[0], d_in[1], d_in[2], d_in[4], (const unsigned short*)d_in[5], d_in[6],
                                               (const char*)d_in[7], (char*)d_out, (char*)d_ws);
}
```

```cpp
#define PROBE 0
#define PROBE_SEL true
#include <hip/hip_runtime.h>
#include <hip/hip_bf16.h>
#include <type_traits>

typedef __attribute__((ext_vector_type(4))) short s16x4;
typedef __attribute__((ext_vector_type(8))) __bf16 bf16x8;
typedef __attribute__((ext_vector_type(2))) __bf16 bf16x2;
typedef __attribute__((ext_vector_type(4))) float f32x4;
typedef __attribute__((ext_vector_type(2))) float f32x2;
typedef __attribute__((ext_vector_type(4))) unsigned u32x4;
typedef __attribute__((ext_vector_type(2))) unsigned u32x2;
#define LAS __attribute__((address_space(3)))
#define GAS __attribute__((address_space(1)))
#define DEVI __device__ __forceinline__
typedef LAS char* lptr;
typedef unsigned long long u64;

constexpr int S_ = 2048, D_ = 640, F_ = 1728;
constexpr int KS_T = 74;
constexpr int WSLAB = 15360, NWS = 6;
constexpr int XSLOT = 18432, NXS = 3;
constexpr int LDS_X = WSLAB * NWS;
constexpr int LDS_TB = 86016;
constexpr int LDS_ST = 155648;
constexpr int LDS_CS = LDS_ST + 1152;
constexpr int LDS_B1 = LDS_CS + 1024;
constexpr int LDS_CNT = LDS_B1 + 1024;
constexpr int LDS_TOTAL = 163840;
constexpr int WS_FLAG = 4096, WS_STAT = 8192, WS_A2 = 1 << 20;
constexpr int A2_BYTES = 128 * KS_T * 1024;
#define PF_CODE 1
#define PF_TLB 0
constexpr int CODE_BYTES = 24960;
constexpr unsigned SPIN_CAP = 1u << 20;

#define STAMP(idx) do{ if((idx)==0) pr_t0=__builtin_amdgcn_s_memrealtime(); if((idx)==PROBE && (PROBE_SEL)) pr_tau=__builtin_amdgcn_s_memrealtime()-pr_t0; }while(0)
#define FINISH() do{ while(__builtin_amdgcn_s_memrealtime()-pr_t0 < 6000+10*pr_tau) __builtin_amdgcn_s_sleep(8); }while(0)
#define PR_ARGS , u64& pr_t0, u64& pr_tau
#define PR_PASS , pr_t0, pr_tau

DEVI void bar_lgkm() { asm volatile("s_waitcnt lgkmcnt(0)\n\ts_barrier" ::: "memory"); }
DEVI void bar() { asm volatile("s_barrier" ::: "memory"); }
template <int N> DEVI void bar_vm() { asm volatile("s_waitcnt vmcnt(%0)\n\ts_barrier" ::"i"(N) : "memory"); }
template <int N> DEVI void wait_vm() { asm volatile("s_waitcnt vmcnt(%0)" ::"i"(N) : "memory"); }
DEVI unsigned pk2(float a, float b) { f32x2 v = {a, b}; return __builtin_bit_cast(unsigned, __builtin_convertvector(v, bf16x2)); }
DEVI unsigned pk2(f32x2 v) { return __builtin_bit_cast(unsigned, __builtin_convertvector(v, bf16x2)); }
DEVI float asf(unsigned u) { return __builtin_bit_cast(float, u); }
DEVI unsigned asu(float f) { return __builtin_bit_cast(unsigned, f); }
DEVI float blo(unsigned u) { return __builtin_bit_cast(float, u << 16); }
DEVI float bhi(unsigned u) { return __builtin_bit_cast(float, u & 0xffff0000u); }
DEVI u32x2 tr_read(lptr p) { return __builtin_bit_cast(u32x2, __builtin_amdgcn_ds_read_tr16_b64_v4i16((LAS s16x4*)p)); }
DEVI f32x4 mfma(u32x4 a, u32x4 b, f32x4 c) {
  return __builtin_amdgcn_mfma_f32_16x16x32_bf16(__builtin_bit_cast(bf16x8, a), __builtin_bit_cast(bf16x8, b), c, 0, 0, 0);
}
DEVI u32x4 cat(u32x2 a, u32x2 b) { return __builtin_shufflevector(a, b, 0, 1, 2, 3); }
DEVI void mfma_a(f32x4& c, u32x4 a, u32x4 b) { asm volatile("v_mfma_f32_16x16x32_bf16 %0, %1, %2, %0" : "+v"(c) : "v"(a), "v"(b)); }
template <int OFF> DEVI void ds128(u32x4& d, int addr) { asm volatile("ds_read_b128 %0, %1 offset:%2" : "=v"(d) : "v"(addr), "i"(OFF)); }
template <int OFF> DEVI void dstr(u32x2& d, int addr) { asm volatile("ds_read_b64_tr_b16 %0, %1 offset:%2" : "=v"(d) : "v"(addr), "i"(OFF)); }
template <int OFF> DEVI void dsw128(int addr, u32x4 d) { asm volatile("ds_write_b128 %0, %1 offset:%2" ::"v"(addr), "v"(d), "i"(OFF) : "memory"); }
DEVI u32x4 mkr(const void* p, unsigned bytes) {
  u64 a = (u64)p;
  return u32x4{(unsigned)a, (unsigned)(a >> 32) & 0xffffu, bytes, 0x00020000u};
}
DEVI void st_wt(u32x4 r, int voff, int soff, u32x4 v) {
  asm volatile("buffer_store_dwordx4 %0, %1, %2, %3 offen sc1\n\ts_nop 1" ::"v"(v), "v"(voff), "s"(r), "s"(soff) : "memory");
}
template <int LSTEP>
DEVI void dma4(u32x4 r, u64 mask, int l, int va, int vb, int s0, int s1, int s2, int s3) {
  asm volatile(
      "s_mov_b64 exec, %0\n\t"
      "s_mov_b32 m0, %1\n\ts_nop 0\n\tbuffer_load_dwordx4 %2, %4, %5 offen lds\n\t"
      "s_add_u32 m0, %1, %9\n\ts_nop 0\n\tbuffer_load_dwordx4 %3, %4, %6 offen lds\n\t"
      "s_add_u32 m0, %1, %10\n\ts_nop 0\n\tbuffer_load_dwordx4 %2, %4, %7 offen lds\n\t"
      "s_add_u32 m0, %1, %11\n\ts_nop 0\n\tbuffer_load_dwordx4 %3, %4, %8 offen lds\n\t"
      "s_mov_b64 exec, -1"
      :: "s"(mask), "s"(l), "v"(va), "v"(vb), "s"(r), "s"(s0), "s"(s1), "s"(s2), "s"(s3), "i"(LSTEP), "i"(2 * LSTEP), "i"(3 * LSTEP)
      : "memory", "scc");
}
DEVI void dma1(u32x4 r, u64 mask, int l, int v, int s0) {
  asm volatile("s_mov_b64 exec, %0\n\ts_mov_b32 m0, %1\n\ts_nop 0\n\tbuffer_load_dwordx4 %2, %3, %4 offen lds\n\ts_mov_b64 exec, -1"
               :: "s"(mask), "s"(l), "v"(v), "s"(r), "s"(s0) : "memory");
}
DEVI void touch(u32x4 r, int v) {
  asm volatile("s_mov_b32 m0, %2\n\ts_nop 0\n\tbuffer_load_dword %0, %1, 0 offen lds" ::"v"(v), "s"(r), "i"(LDS_TOTAL - 256) : "memory");
}
DEVI void bload(u32x4& d, u32x4 r, int v, int s0) { asm volatile("buffer_load_dwordx4 %0, %1, %2, %3 offen" : "=v"(d) : "v"(v), "s"(r), "s"(s0)); }
DEVI void bload_sc1(u32x4& d, u32x4 r, int v, int s0) { asm volatile("buffer_load_dwordx4 %0, %1, %2, %3 offen sc1" : "=v"(d) : "v"(v), "s"(r), "s"(s0) : "memory"); }
DEVI int lane_id() { int l; asm volatile("v_mbcnt_lo_u32_b32 %0, -1, 0\n\tv_mbcnt_hi_u32_b32 %0, -1, %0" : "=v"(l)); return l; }
DEVI int sig(int i) { return 4 * (i & 3) + (i >> 2); }
DEVI u64 flagval(unsigned tag) { return ((u64)(tag ^ 0x5bd1e995u) << 32) | tag; }

DEVI float xmax16(float v) { auto r = __builtin_amdgcn_permlane16_swap(asu(v), asu(v), false, false); return fmaxf(asf(r[0]), asf(r[1])); }
DEVI float xmax32(float v) { auto r = __builtin_amdgcn_permlane32_swap(asu(v), asu(v), false, false); return fmaxf(asf(r[0]), asf(r[1])); }
DEVI float xadd16(float v) { auto r = __builtin_amdgcn_permlane16_swap(asu(v), asu(v), false, false); return asf(r[0]) + asf(r[1]); }
DEVI float xadd32(float v) { auto r = __builtin_amdgcn_permlane32_swap(asu(v), asu(v), false, false); return asf(r[0]) + asf(r[1]); }
template <int CTRL> DEVI float dpp(float v) { return asf(__builtin_amdgcn_mov_dpp(asu(v), CTRL, 0xf, 0xf, true)); }
DEVI float wave_sum(float v) {
  v += dpp<0xb1>(v); v += dpp<0x4e>(v); v += dpp<0x141>(v); v += dpp<0x140>(v);
  return xadd32(xadd16(v));
}

DEVI int row_token(int p_, int pos) {
  const int tau = pos >> 4, i = pos & 15;
  int tok = tau == 0 ? p_ * 128 - 16 + 8 * (i & 1) + 2 * (i >> 2) + ((i & 3) < 2) : p_ * 128 + 32 * ((tau - 1) >> 1) + 2 * sig(i) + ((tau - 1) & 1);
  return tok < 0 ? 0 : tok;
}

DEVI int colbyte(bool att, int nw, int mw, int c, int p) {
  if (att) {
    int ct = 4 * nw + (nw < 3 ? (c ^ (2 * mw)) : c);
    int sec = ct / 5, hh = ct - 5 * sec;
    return sec * 160 + (hh < 4 ? 40 * hh + 8 * p : 40 * p + 32);
  } else {
    if (nw < 2) return 128 * nw + 32 * (c ^ (2 * mw)) + 8 * p;
    if (c < 2) return 256 + 64 * (nw - 2) + 32 * c + 8 * p;
    return 384 + 8 * ((p & 1) + 4 * (p >> 1) + 2 * (nw - 2));
  }
}

template <int NC>
struct Frags { u32x4 x[5]; u32x2 w[NC][2]; };

template <int NC, int NH, bool MMA>
DEVI void s1_step(const Frags<NC>& c, Frags<NC>& n, int xo, int xh, const int (&wa)[NC], f32x4 (&acc)[4][NC], f32x4 (&hacc)[NH]) {
  constexpr int NM = 4 * NC + NH, NR = 2 * NC + 5;
#pragma unroll
  for (int k = 0; k < NM; k++) {
    if (MMA) {
      if (k < 4 * NC) mfma_a(acc[k / NC][k % NC], cat(c.w[k % NC][0], c.w[k % NC][1]), c.x[k / NC]);
      else mfma_a(hacc[k - 4 * NC], cat(c.w[k - 4 * NC][0], c.w[k - 4 * NC][1]), c.x[4]);
    }
    if (k < NR) {
      if (k == 0) ds128<0>(n.x[0], xo);
      else if (k <= 2 * NC) { if ((k - 1) & 1) dstr<7680>(n.w[(k - 1) >> 1][1], wa[(k - 1) >> 1]); else dstr<0>(n.w[(k - 1) >> 1][0], wa[(k - 1) >> 1]); }
      else if (k == 2 * NC + 1) ds128<2048>(n.x[1], xo);
      else if (k == 2 * NC + 2) ds128<4096>(n.x[2], xo);
      else if (k == 2 * NC + 3) ds128<6144>(n.x[3], xo);
      else ds128<0>(n.x[4], xh);
    }
  }
}

DEVI f32x2 gelu2(f32x2 v) {
  const float c1 = -2.f * 0.7978845608028654f * 1.4426950408889634f, c2 = c1 * 0.044715f;
  f32x2 t = v * v;
  t = t * c2 + c1;
  t = t * v;
  f32x2 e = {__builtin_amdgcn_exp2f(t[0]), __builtin_amdgcn_exp2f(t[1])};
  e = e + 1.f;
  f32x2 r = {__builtin_amdgcn_rcpf(e[0]), __builtin_amdgcn_rcpf(e[1])};
  return v * r;
}

template <int NC>
DEVI void s1_compute(lptr shm, bool att, int w, int lane, int p_, int j_, char* ws PR_ARGS) {
  constexpr int NH = NC == 4 ? 2 : 3;
  const int mw = w & 1, nw = w >> 1, i = lane & 15, g = lane >> 4;
  f32x4 acc[4][NC], hacc[NH];
#pragma unroll
  for (int m = 0; m < 4; m++)
#pragma unroll
    for (int c = 0; c < NC; c++) { acc[m][c] = f32x4{0, 0, 0, 0}; asm volatile("" : "+v"(acc[m][c])); }
#pragma unroll
  for (int c = 0; c < NH; c++) { hacc[c] = f32x4{0, 0, 0, 0}; asm volatile("" : "+v"(hacc[c])); }
  int wb[NC], cb[NC];
#pragma unroll
  for (int c = 0; c < NC; c++) {
    wb[c] = (4 * g + ((lane >> 2) & 3)) * 480 + colbyte(att, nw, mw, c, lane & 3);
    cb[c] = colbyte(att, nw, mw, c, g) * 2;
    asm volatile("" : "+v"(cb[c]));
  }
  const int xb = LDS_X + i * 128 + ((g ^ (i >> 1)) << 4);
  const int own = (1 + 4 * mw) * 2048;
  Frags<NC> fa, fb;
  int wa[NC];
  bar_lgkm();
  STAMP(1);
  s1_step<NC, NH, false>(fb, fa, xb + own, xb, wb, acc, hacc);
  int ws_ = WSLAB, xs = 0;
#pragma nounroll
  for (int T = 0; T < 10; T++) {
#pragma unroll
    for (int c = 0; c < NC; c++) wa[c] = wb[c] + ws_;
    asm volatile("s_waitcnt lgkmcnt(0)" ::: "memory");
    s1_step<NC, NH, true>(fa, fb, (xb ^ 64) + xs + own, (xb ^ 64) + xs, wa, acc, hacc);
    ws_ += WSLAB; if (ws_ == WSLAB * NWS) ws_ = 0;
    xs += XSLOT; if (xs == XSLOT * NXS) xs = 0;
#pragma unroll
    for (int c = 0; c < NC; c++) wa[c] = wb[c] + ws_;
    bar_lgkm();
    s1_step<NC, NH, true>(fb, fa, xb + xs + own, xb + xs, wa, acc, hacc);
    ws_ += WSLAB;
  }
  asm volatile("s_waitcnt lgkmcnt(0)\n\ts_nop 15" ::: "memory");
#pragma unroll
  for (int m = 0; m < 4; m++)
#pragma unroll
    for (int c = 0; c < NC; c++) asm volatile("" : "+v"(acc[m][c]));
#pragma unroll
  for (int c = 0; c < NH; c++) asm volatile("" : "+v"(hacc[c]));
  STAMP(2);
  if (i == 0) {
#pragma unroll
    for (int c = 0; c < NH; c++) *(LAS f32x4*)(shm + LDS_CS + cb[c]) = hacc[c];
  }
  bar_lgkm();
  f32x4 cs[NC];
#pragma unroll
  for (int c = 0; c < NC; c++) cs[c] = *(LAS f32x4*)(shm + LDS_CS + cb[c]);
  if (att) {
    const int piece = LDS_TB + (g * 16 + (i ^ (8 * (g >> 1)))) * 8;
    const float qs = 0.22360679774997896f * 1.4426950408889634f;
#pragma unroll
    for (int m = 0; m < 5; m++) {
      const int tau = m < 4 ? 1 + 4 * mw + m : 0;
      f32x2 st = *(LAS f32x2*)(shm + LDS_ST + (tau * 16 + i) * 8);
#pragma unroll
      for (int c = 0; c < (m < 4 ? NC : NH); c++) {
        const int ct = 4 * nw + (NC == 4 ? (c ^ (2 * mw)) : c);
        const float sc = ct < 5 ? qs : 1.f;
        const float rs = st[0] * sc, nm = st[1] * sc;
        f32x4 a = m < 4 ? acc[m < 4 ? m : 0][c] : hacc[c < NH ? c : 0];
        f32x4 v = a * rs + cs[c] * nm;
        u32x2 o = {pk2(v[0], v[1]), pk2(v[2], v[3])};
        *(LAS u32x2*)(shm + (tau * 15 + ct) * 512 + piece) = o;
      }
    }
  } else {
    f32x4 b1[NC];
#pragma unroll
    for (int c = 0; c < NC; c++) b1[c] = *(LAS f32x4*)(shm + LDS_B1 + cb[c]);
    const u32x4 ra = mkr(ws + WS_A2, A2_BYTES);
    const int base = ((8 * p_ + 4 * mw) * KS_T + 7 * j_) * 1024;
    unsigned sp[4][2];
#pragma unroll
    for (int m = 0; m < 4; m++) {
      f32x2 st = *(LAS f32x2*)(shm + LDS_ST + ((1 + 4 * mw + m) * 16 + i) * 8);
      unsigned o[NC][2];
#pragma unroll
      for (int c = 0; c < NC; c++) {
        f32x4 v = acc[m][c] * st[0] + (cs[c] * st[1] + b1[c]);
        o[c][0] = pk2(gelu2(f32x2{v[0], v[1]}));
        o[c][1] = pk2(gelu2(f32x2{v[2], v[3]}));
      }
      const int tb = base + m * KS_T * 1024;
      if (NC == 4) {
        st_wt(ra, lane * 16, tb + (2 * nw + mw) * 1024, u32x4{o[0][0], o[0][1], o[1][0], o[1][1]});
        st_wt(ra, lane * 16, tb + (2 * nw + (mw ^ 1)) * 1024, u32x4{o[2][0], o[2][1], o[NC - 1][0], o[NC - 1][1]});
      } else {
        if (j_ < 7 || nw == 2) st_wt(ra, lane * 16, tb + (2 + nw) * 1024, u32x4{o[0][0], o[0][1], o[1][0], o[1][1]});
        sp[m][0] = o[2][0]; sp[m][1] = o[2][1];
        if (m & 1) {
          auto s0 = __builtin_amdgcn_permlane32_swap(sp[m - 1][0], sp[m][0], false, false);
          auto s1 = __builtin_amdgcn_permlane32_swap(sp[m - 1][1], sp[m][1], false, false);
          if (j_ < 7) st_wt(ra, (lane >> 5) * (KS_T * 1024) + (lane & 31) * 16, tb - KS_T * 1024 + 6 * 1024 + (nw - 2) * 512, u32x4{s0[0], s1[0], s0[1], s1[1]});
        }
      }
    }
  }
}

DEVI void s1_attention(lptr shm, int w, int lane, int p_, int j_, char* ws) {
  const int i = lane & 15, g = lane >> 4;
  const int tau = w + 1, taup = w >= 2 ? tau - 2 : 0;
  const int flip = (w == 1) ? 2 : 0;
  const bool start = (p_ == 0) && (w < 2);
  const int qi = sig(i);
  f32x4 mO, mP;
  unsigned M1, M2, M3;
  {
    bool vo[4], vp[4];
#pragma unroll
    for (int r = 0; r < 4; r++) {
      int jj = 4 * r + g;
      vo[r] = (jj <= qi) && (jj >= qi - 7);
      vp[r] = (jj - 16 >= qi - 7) && !start;
      mO[r] = vo[r] ? 0.f : -1e30f;
      mP[r] = vp[r] ? 0.f : -1e30f;
    }
    M1 = (vp[2] ? 0xffffu : 0u) | (vp[3] ? 0xffff0000u : 0u);
    M2 = (vo[0] ? 0xffffu : 0u) | (vo[1] ? 0xffff0000u : 0u);
    M3 = (vo[2] ? 0xffffu : 0u) | (vo[3] ? 0xffff0000u : 0u);
  }
  lptr tb = shm + LDS_TB;
  lptr po = tb + tau * 15 * 512 + (g * 16 + (i ^ (8 * (g >> 1)))) * 8;
  lptr pp = tb + taup * 15 * 512 + (g * 16 + ((i ^ flip) ^ (8 * (g >> 1)))) * 8;
  const int q = (lane >> 2) & 3, p = lane & 3;
  lptr to = tb + tau * 15 * 512 + (p * 16 + ((4 * g + q) ^ (8 * (p >> 1)))) * 8;
  lptr tp = tb + taup * 15 * 512 + (p * 16 + ((4 * g + (q ^ flip)) ^ (8 * (p >> 1)))) * 8;
  asm volatile("" :: "v"(M1), "v"(M2), "v"(M3), "v"(mO), "v"(mP), "v"(po), "v"(pp), "v"(to), "v"(tp));
  bar_lgkm();
  const u32x2 q4 = *(LAS u32x2*)(po + 4 * 512), ko4 = *(LAS u32x2*)(po + 9 * 512), kp4 = *(LAS u32x2*)(pp + 9 * 512);
  u32x2 qa[4], ko[4], kp[4];
#pragma unroll
  for (int h = 0; h < 4; h++) { qa[h] = *(LAS u32x2*)(po + h * 512); ko[h] = *(LAS u32x2*)(po + (5 + h) * 512); kp[h] = *(LAS u32x2*)(pp + (5 + h) * 512); }
  u32x4 vf[4];
#pragma unroll
  for (int h = 0; h < 4; h++) vf[h] = cat(tr_read(tp + (10 + h) * 512), tr_read(to + (10 + h) * 512));
  const u32x4 v4 = cat(tr_read(tp + 14 * 512), tr_read(to + 14 * 512));
  f32x4 sp[4], so[4];
#pragma unroll
  for (int h = 0; h < 4; h++) {
    const unsigned mq = g == h ? 0xffffffffu : 0u;
    u32x4 qf = {qa[h][0], qa[h][1], q4[0] & mq, q4[1] & mq};
    sp[h] = mfma(cat(kp[h], kp4), qf, mP);
    so[h] = mfma(cat(ko[h], ko4), qf, mO);
  }
  float ee[4], eo[4], mx[4], l[4];
#pragma unroll
  for (int h = 0; h < 4; h++) { ee[h] = fmaxf(fmaxf(sp[h][2], so[h][0]), so[h][2]); eo[h] = fmaxf(fmaxf(sp[h][3], so[h][1]), so[h][3]); mx[h] = fmaxf(ee[h], eo[h]); }
  __builtin_amdgcn_sched_barrier(0);
#pragma unroll
  for (int h = 0; h < 4; h++) mx[h] = xmax16(mx[h]);
#pragma unroll
  for (int h = 0; h < 4; h++) mx[h] = xmax32(mx[h]);
  __builtin_amdgcn_sched_barrier(0);
#pragma unroll
  for (int h = 0; h < 4; h++) { ee[h] = __builtin_amdgcn_exp2f(ee[h] - mx[h]); eo[h] = __builtin_amdgcn_exp2f(eo[h] - mx[h]); l[h] = ee[h] + eo[h]; }
  __builtin_amdgcn_sched_barrier(0);
#pragma unroll
  for (int h = 0; h < 4; h++) l[h] = xadd16(l[h]);
#pragma unroll
  for (int h = 0; h < 4; h++) l[h] = xadd32(l[h]);
  __builtin_amdgcn_sched_barrier(0);
  f32x4 o4 = {0, 0, 0, 0}, oh[4];
#pragma unroll
  for (int h = 0; h < 4; h++) {
    const float rl = __builtin_amdgcn_rcpf(l[h]);
    const unsigned pk = pk2(ee[h] * rl, eo[h] * rl), mv = (i >> 2) == h ? 0xffffffffu : 0u;
    u32x4 pf = {0u, pk & M1, pk & M2, pk & M3};
    oh[h] = mfma(vf[h], pf, f32x4{0, 0, 0, 0});
    o4 = mfma(v4 & mv, pf, o4);
  }
  const u32x4 ra = mkr(ws + WS_A2, A2_BYTES);
  const int base = ((8 * p_ + w) * KS_T + 54 + 5 * (j_ >> 1)) * 1024;
#pragma unroll
  for (int hp = 0; hp < 2; hp++)
    st_wt(ra, lane * 16, base + ((j_ & 1) * 3 + hp) * 1024,
          u32x4{pk2(oh[2 * hp][0], oh[2 * hp][1]), pk2(oh[2 * hp][2], oh[2 * hp][3]), pk2(oh[2 * hp + 1][0], oh[2 * hp + 1][1]), pk2(oh[2 * hp + 1][2], oh[2 * hp + 1][3])});
  unsigned a0 = pk2(o4[0], o4[1]), a1 = pk2(o4[2], o4[3]);
  auto s0 = __builtin_amdgcn_permlane32_swap(a0, a0, false, false);
  auto s1 = __builtin_amdgcn_permlane32_swap(a1, a1, false, false);
  if (lane < 32) st_wt(ra, lane * 16, base + 2 * 1024 + (j_ & 1) * 512, u32x4{s0[0], s1[0], s0[1], s1[1]});
}

template <bool ATT>
DEVI void s1_loader(int lw, int lane, int p_, int j_, const void* x, const void* wsrc) {
  constexpr int NX = ATT ? 5 : 4;
  constexpr int RS = ATT ? 3840 : 3456;
  const u32x4 rx = mkr(x, S_ * D_ * 2), rw = mkr(wsrc, D_ * RS);
  int wv;
  {
    int hi = lane >= 30, ch = lane - 30 * hi;
    int cbyte;
    if (ATT) { int sec = (ch >= 10) + (ch >= 20); cbyte = sec * 1120 + j_ * 160 + ch * 16; }
    else { int lim = j_ < 7 ? 27 : 19; cbyte = j_ * 448 + (ch < lim ? ch : lim) * 16; }
    wv = hi * RS + cbyte;
  }
  const int wrow = 16 * (lw & 1) + 4 * (lw >> 1);
  const int r8 = lane >> 3, cp = lane & 7, t0 = p_ * 128;
  int xv[2], hv;
#pragma unroll
  for (int hf = 0; hf < 2; hf++)
    xv[hf] = (t0 + 32 * lw + 2 * (4 * (r8 & 3) + 2 * hf + (r8 >> 2))) * 1280 + ((cp ^ (4 * hf + (r8 >> 1))) << 4);
  {
    int tok = t0 - 16 + 8 * (r8 & 1) + 2 * lw + ((r8 & 3) < 2);
    tok = tok < 0 ? 0 : tok;
    hv = tok * 1280 + ((cp ^ ((2 * lw + ((r8 & 3) >> 1)) & 7)) << 4);
  }
  const u64 hmask = lw == 0 ? 0xffffff00ull : 0xffffffffull;
  auto ldw = [&](int s, int slot) {
    const int so = (32 * s + wrow) * RS;
    dma4<960>(rw, 0x0fffffffffffffffull, slot * WSLAB + 4 * lw * 960, wv, wv, so, so + 2 * RS, so + 8 * RS, so + 10 * RS);
  };
  auto ldx = [&](int T, int slot) {
    const int d = LDS_X + slot * XSLOT;
    if (ATT) dma1(rx, hmask, d + lw * 512, hv, T * 128);
    dma4<1024>(rx, ~0ull, d + 2048 + 4 * lw * 1024, xv[0], xv[1], T * 128, T * 128, T * 128 + 1280, T * 128 + 1280);
  };
  ldx(0, 0); ldw(0, 0); ldw(1, 1);
  int slot = 1;
#pragma nounroll
  for (int T = 1; T < 10; T++) {
    ldx(T, slot); ldw(2 * T, 2 * slot); ldw(2 * T + 1, 2 * slot + 1);
    bar_vm<NX + 8>();
    slot += 1; if (slot == NXS) slot = 0;
  }
}

__global__ __launch_bounds__(768, 3) void fused_block(const void* __restrict__ x, const void* __restrict__ wqkv, const void* __restrict__ wo,
                                                      const void* __restrict__ w1, const unsigned short* __restrict__ b1,
                                                      const void* __restrict__ w2, const char* __restrict__ b2, char* __restrict__ out,
                                                      char* ws) {
  extern __shared__ __attribute__((aligned(16))) char shm_[];
  lptr shm = (lptr)shm_;
  const int tid = threadIdx.x, lane = tid & 63, w = __builtin_amdgcn_readfirstlane(tid >> 6);
  const int xcd = blockIdx.x & 7, idx = blockIdx.x >> 3;
  const int p_ = (xcd & 1) * 8 + (idx & 7), sub = idx >> 3;
  const bool att = !(sub & 1);
  const int j_ = (xcd >> 1) * 2 + (sub >> 1);
  const int unit = (xcd >> 1) * 28 + idx;
  const bool has2 = idx < 28 && unit < 110;
  const int mt = unit / 5, n_ = (xcd & 1) * 5 + (unit - 5 * mt);
  const int G0 = 6 * mt;
  const int kq = w & 3;
  u64 pr_t0 = 0, pr_tau = 0;
  STAMP(0);
  if (w == 7) {
    u64 pc; asm volatile("s_getpc_b64 %0" : "=s"(pc));
    touch(mkr((const void*)(pc & ~255ull), CODE_BYTES), (idx + 32 * lane) * 128);
  }
  if (w >= 8) {
    const int lw = w - 8;
    if (att) s1_loader<true>(lw, lane, p_, j_, x, wqkv);
    else s1_loader<false>(lw, lane, p_, j_, x, w1);
    const unsigned tag = __builtin_amdgcn_readfirstlane(*(LAS unsigned*)(shm + LDS_CNT + 4));
    const u32x4 rst = mkr(ws + WS_STAT, S_ * 16);
    const int spos = 36 * lw + (lane < 36 ? lane : 35), sv = row_token(p_, spos) * 16;
    u32x4 sg;
    bload_sc1(sg, rst, sv, 0);
    const u32x4 rw = mkr(kq == 0 ? wo : w2, has2 ? (kq == 0 ? D_ * 1280 : F_ * 1280) : 0);
    const int l3 = lane >> 3, cbyte = n_ * 128 + (((lane & 7) ^ ((lane >> 4) << 1)) << 4);
    const int v1 = l3 * 1280 + cbyte, v2 = (20 * (l3 >> 2) + (l3 & 3)) * 1280 + cbyte;
    auto geom = [&](int s, int& rb, int& sa, int& sb, int& v) {
      sa = 8; sb = 16; v = v1;
      if (kq == 0) {
        int P = (s * 13) >> 6, kp = s - 5 * P;
        if (kp == 2) { rb = 160 * P + 16; sa = 80; sb = 40; v = v2; }
        else { rb = 160 * P + 40 * (kp > 2 ? kp - 1 : kp); sb = 20; }
      } else rb = 32 * (18 * (kq - 1) + s);
      rb *= 1280; sa *= 1280; sb *= 1280;
    };
    u32x4 pre[9][4];
    auto ldr = [&](auto R) {
      constexpr int r = decltype(R)::value;
      int rb, sa, sb, v; geom(9 + r, rb, sa, sb, v);
      bload(pre[r][0], rw, v, rb); bload(pre[r][1], rw, v, rb + sa); bload(pre[r][2], rw, v, rb + sb); bload(pre[r][3], rw, v, rb + sa + sb);
    };
    auto ldl = [&](int s) {
      int rb, sa, sb, v; geom(s, rb, sa, sb, v);
      dma4<1024>(rw, ~0ull, s < 9 ? s * 16384 + kq * 4096 : 147456 + (s - 18) * 4096, v, v, rb, rb + sa, rb + sb, rb + sa + sb);
    };
#define IC(n) std::integral_constant<int, n>{}
    ldr(IC(0)); ldr(IC(1)); ldr(IC(2));
    asm volatile("s_waitcnt vmcnt(12)" : "+v"(sg) :: "memory");
#pragma nounroll
    for (unsigned spins = 0; !__all(sg[1] == tag && sg[3] == tag) && spins < SPIN_CAP; spins++) {
      __builtin_amdgcn_s_sleep(2);
      bload_sc1(sg, rst, sv, 0);
      asm volatile("s_waitcnt vmcnt(0)" : "+v"(sg) :: "memory");
    }
    *(LAS u32x2*)(shm + LDS_ST + spos * 8) = u32x2{sg[0], sg[2]};
    bar_lgkm();
    ldr(IC(3)); ldr(IC(4)); ldr(IC(5));
    bar();
    ldr(IC(6)); ldr(IC(7));
    bar();
    ldr(IC(8)); ldl(0); ldl(1);
    if (att) bar();
    ldl(2); ldl(3); ldl(4);
    if (att) bar();
    STAMP(5);
    if (!has2) { FINISH(); return; }
    ldl(5); ldl(6); ldl(7); ldl(8);
    if (kq == 0) { ldl(18); ldl(19); wait_vm<24>(); } else wait_vm<16>();
    STAMP(6);
    bar();
    bar_vm<0>();
    const int wa = kq * 4096 + lane * 16;
#define PUT(r, off) dsw128<off>(wa + (r / 3) * 49152, pre[r][0]); dsw128<off + 1024>(wa + (r / 3) * 49152, pre[r][1]); dsw128<off + 2048>(wa + (r / 3) * 49152, pre[r][2]); dsw128<off + 3072>(wa + (r / 3) * 49152, pre[r][3]);
    PUT(0, 0) PUT(1, 16384) PUT(2, 32768)
    bar_lgkm();
    PUT(3, 0) PUT(4, 16384) PUT(5, 32768)
    bar_lgkm();
    PUT(6, 0) PUT(7, 16384) PUT(8, 32768)
    bar_lgkm();
    bar();
    bar();
  } else {
    const int rid = (att ? 0 : 8) + j_;
    const int srow = p_ * 128 + 8 * rid + w;
    unsigned u[5];
#pragma unroll
    for (int k = 0; k < 5; k++) u[k] = ((const unsigned*)x)[srow * 320 + lane + 64 * k];
    const unsigned tag = __builtin_amdgcn_readfirstlane(__hip_atomic_load((GAS unsigned*)ws, __ATOMIC_RELAXED, __HIP_MEMORY_SCOPE_AGENT)) + 1u;
    if (!att && tid < 224) {
      int n = j_ * 224 + tid;
      *(LAS float*)(shm + LDS_B1 + tid * 4) = blo(b1[n < F_ ? n : F_ - 1]);
    }
    if (tid < 384) {
      const int sl = tid >> 7, o = tid & 127;
      const unsigned v = o < 8 ? 0x3f803f80u : 0u;
      if (o < 8 || !att) *(LAS u32x4*)(shm + LDS_X + sl * XSLOT + o * 16) = u32x4{v, v, v, v};
    }
    if (tid == 448) *(LAS u32x2*)(shm + LDS_CNT) = u32x2{0u, tag};
    {
      float s = 0;
#pragma unroll
      for (int k = 0; k < 5; k++) s += blo(u[k]) + bhi(u[k]);
      const float m = wave_sum(s) * (1.f / D_);
      float q = 0;
#pragma unroll
      for (int k = 0; k < 5; k++) { float a = blo(u[k]) - m, b = bhi(u[k]) - m; q += a * a + b * b; }
      const float rs = rsqrtf(wave_sum(q) * (1.f / D_) + 1e-5f);
      if (lane == 0) st_wt(mkr(ws + WS_STAT, S_ * 16), 0, srow * 16, u32x4{asu(rs), tag, asu(-m * rs), tag});
    }
    const bool light = att ? (w >= 6) : (w >= 4);
    if (light) s1_compute<3>(shm, att, w, lane, p_, j_, ws PR_PASS);
    else s1_compute<4>(shm, att, w, lane, p_, j_, ws PR_PASS);
    STAMP(3);
    if (att) { s1_attention(shm, w, lane, p_, j_, ws); bar_lgkm(); }
    STAMP(4);
    wait_vm<0>();
    GAS u64* flags = (GAS u64*)(ws + WS_FLAG);
    const u64 fv = flagval(tag);
    if (lane == 0) {
      unsigned old = __hip_atomic_fetch_add((LAS unsigned*)(shm + LDS_CNT), 1u, __ATOMIC_RELAXED, __HIP_MEMORY_SCOPE_WORKGROUP);
      if (old == 7) __hip_atomic_store(flags + p_ * 16 + rid, fv, __ATOMIC_RELAXED, __HIP_MEMORY_SCOPE_AGENT);
    }
    STAMP(5);
    if (!has2) {
      if (blockIdx.x == 255 && w == 0) {
#pragma nounroll
        for (unsigned spins = 0; spins < SPIN_CAP; spins++) {
          bool ok = true;
#pragma unroll
          for (int k = 0; k < 4; k++) ok &= __hip_atomic_load(flags + lane + 64 * k, __ATOMIC_RELAXED, __HIP_MEMORY_SCOPE_AGENT) == fv;
          if (__all(ok)) break;
          __builtin_amdgcn_s_sleep(8);
        }
        if (lane == 0) __hip_atomic_store((GAS unsigned*)ws, tag, __ATOMIC_RELAXED, __HIP_MEMORY_SCOPE_AGENT);
      }
      FINISH();
      return;
    }
    const int mh = w >> 2, g = lane >> 4, q = (lane >> 2) & 3, p = lane & 3;
    const int ks0 = kq == 0 ? 54 : 18 * (kq - 1), nsteps = kq == 0 ? 20 : 18;
    const u32x4 ra = mkr(ws + WS_A2, A2_BYTES);
    int ao[3];
    const int av = lane * 16;
#pragma unroll
    for (int m = 0; m < 3; m++) { int G = G0 + 3 * mh + m; G = G < 128 ? G : 127; ao[m] = (G * KS_T + ks0) * 1024; }
    int ba[4], bs[4];
#pragma unroll
    for (int c = 0; c < 4; c++) ba[c] = kq * 4096 + (4 * g + q) * 128 + ((c ^ ((2 * g + (q >> 1)) & 3)) << 5) + 8 * p;
    f32x4 acc[3][4];
#pragma unroll
    for (int m = 0; m < 3; m++)
#pragma unroll
      for (int c = 0; c < 4; c++) { acc[m][c] = f32x4{0, 0, 0, 0}; asm volatile("" : "+v"(acc[m][c])); }
    {
      int Ga = G0 + 3 * mh, Gb = Ga + 2;
      Ga = Ga < 128 ? Ga : 127; Gb = Gb < 128 ? Gb : 127;
      GAS u64* fp = flags + (((lane & 8) ? Gb : Ga) >> 3) * 16 + (kq == 0 ? 0 : 8) + (lane & 7);
#pragma nounroll
      for (unsigned spins = 0; spins < SPIN_CAP; spins++) {
        if (__all(__hip_atomic_load(fp, __ATOMIC_RELAXED, __HIP_MEMORY_SCOPE_AGENT) == fv)) break;
        __builtin_amdgcn_s_sleep(2);
      }
      __builtin_amdgcn_fence(__ATOMIC_ACQUIRE, "wavefront");
    }
    STAMP(6);
    u32x4 a[6][3];
#pragma unroll
    for (int u = 0; u < 6; u++)
#pragma unroll
      for (int m = 0; m < 3; m++) bload_sc1(a[u][m], ra, av, ao[m] + u * 1024);
    u32x2 b0[4][2], b1f[4][2];
    bar();
    STAMP(7);
#pragma unroll
    for (int c = 0; c < 4; c++) { dstr<0>(b0[c][0], ba[c]); dstr<2048>(b0[c][1], ba[c]); }
    int sl = 0;
    auto step = [&](auto U, u32x2 (&bc)[4][2], u32x2 (&bn)[4][2], int s) {
      constexpr int u = decltype(U)::value;
      const int so = s + 6 < nsteps ? (s + 6) * 1024 : 0x70000000;
      sl = min(sl + 16384, 151552);
      if (u == 2 && s == 8) sl = 0;
#pragma unroll
      for (int c = 0; c < 4; c++) bs[c] = ba[c] + sl;
#pragma unroll
      for (int m = 0; m < 3; m++) {
        wait_vm<17>();
#pragma unroll
        for (int c = 0; c < 4; c++) {
          mfma_a(acc[m][c], cat(bc[c][0], bc[c][1]), a[u][m]);
          if (m < 2) { if (c & 1) dstr<2048>(bn[2 * m + (c >> 1)][1], bs[2 * m + (c >> 1)]); else dstr<0>(bn[2 * m + (c >> 1)][0], bs[2 * m + (c >> 1)]); }
        }
        bload_sc1(a[u][m], ra, av, ao[m] + so);
      }
    };
#define LG0 asm volatile("s_waitcnt lgkmcnt(0)" ::: "memory")
    LG0;
#pragma nounroll
    for (int s = 0; s < nsteps; s += 6) {
      step(IC(0), b0, b1f, s); LG0;
      step(IC(1), b1f, b0, s + 1); LG0;
      if (s == 18) break;
      step(IC(2), b0, b1f, s + 2); bar_lgkm();
      step(IC(3), b1f, b0, s + 3); LG0;
      step(IC(4), b0, b1f, s + 4); LG0;
      step(IC(5), b1f, b0, s + 5); bar_lgkm();
    }
    asm volatile("s_waitcnt vmcnt(0)\n\ts_nop 15" ::: "memory");
    STAMP(9);
    const int l2 = lane_id(), i2 = l2 & 15, g2 = l2 >> 4;
#pragma unroll
    for (int m = 0; m < 3; m++)
#pragma unroll
      for (int c = 0; c < 4; c++) {
        asm volatile("" : "+v"(acc[m][c]));
        *(LAS f32x4*)(shm + (3 * mh + m) * 16384 + kq * 4096 + i2 * 256 + (((4 * c + g2) ^ (i2 & 7)) << 4)) = acc[m][c];
      }
  }
  {
    const int lane = lane_id();
    const int frow = 8 * w + (lane >> 3), fG = G0 + (frow >> 4);
    const bool fvalid = fG < 128;
    const int ftok = 32 * ((fvalid ? fG : 0) >> 1) + 2 * sig(frow & 15) + (fG & 1);
    const int foff = ftok * 1280 + n_ * 128 + (lane & 7) * 16;
    const u32x4 xv = *(const u32x4*)((const char*)x + foff);
    const u32x4 bv = *(const u32x4*)(b2 + n_ * 128 + (lane & 7) * 16);
    bar_lgkm();
    STAMP(10);
    f32x4 s[2], y[2];
#pragma unroll
    for (int e = 0; e < 2; e++) {
      lptr pr = shm + (frow >> 4) * 16384 + (frow & 15) * 256 + (((2 * (lane & 7) + e) ^ (frow & 7)) << 4);
      s[e] = *(LAS f32x4*)pr;
      y[e] = *(LAS f32x4*)(pr + 4096) + *(LAS f32x4*)(pr + 8192) + *(LAS f32x4*)(pr + 12288);
    }
    u32x4 o;
#pragma unroll
    for (int k = 0; k < 4; k++) {
      f32x2 yy = f32x2{y[k >> 1][2 * (k & 1)], y[k >> 1][2 * (k & 1) + 1]} + f32x2{blo(bv[k]), bhi(bv[k])};
      f32x2 t = yy * -1.4426950408889634f;
      f32x2 e = f32x2{__builtin_amdgcn_exp2f(t[0]), __builtin_amdgcn_exp2f(t[1])} + 1.f;
      f32x2 gt = yy * f32x2{__builtin_amdgcn_rcpf(e[0]), __builtin_amdgcn_rcpf(e[1])};
      f32x2 r = f32x2{s[k >> 1][2 * (k & 1)], s[k >> 1][2 * (k & 1) + 1]} * gt + f32x2{blo(xv[k]), bhi(xv[k])};
      o[k] = pk2(r);
    }
    if (fvalid) *(u32x4*)(out + foff) = o;
  }
  STAMP(11);
  FINISH();
}

extern "C" void kernel_launch(void* const* d_in, const int* in_sizes, int n_in, void* d_out, int out_size, void* d_ws,
                              size_t ws_size, hipStream_t stream) {
  (void)hipFuncSetAttribute((const void*)fused_block, hipFuncAttributeMaxDynamicSharedMemorySize, LDS_TOTAL);
  fused_block<<<256, 768, LDS_TOTAL, stream>>>(d_in[0], d_in[1], d_in[2], d_in[4], (const unsigned short*)d_in[5], d_in[6],
                                               (const char*)d_in[7], (char*)d_out, (char*)d_ws);
}
```

```cpp
#define XP 2
#define XPB 0
#include <hip/hip_runtime.h>
#include <hip/hip_bf16.h>
#include <type_traits>

typedef __attribute__((ext_vector_type(4))) short s16x4;
typedef __attribute__((ext_vector_type(8))) __bf16 bf16x8;
typedef __attribute__((ext_vector_type(2))) __bf16 bf16x2;
typedef __attribute__((ext_vector_type(4))) float f32x4;
typedef __attribute__((ext_vector_type(2))) float f32x2;
typedef __attribute__((ext_vector_type(4))) unsigned u32x4;
typedef __attribute__((ext_vector_type(2))) unsigned u32x2;
#define LAS __attribute__((address_space(3)))
#define GAS __attribute__((address_space(1)))
#define DEVI __device__ __forceinline__
typedef LAS char* lptr;
typedef unsigned long long u64;

constexpr int S_ = 2048, D_ = 640, F_ = 1728;
constexpr int KS_T = 74;
constexpr int WSLAB = 15360, NWS = 6;
constexpr int XSLOT = 18432, NXS = 3;
constexpr int LDS_X = WSLAB * NWS;
constexpr int LDS_TB = 86016;
constexpr int LDS_ST = 155648;
constexpr int LDS_CS = LDS_ST + 1152;
constexpr int LDS_B1 = LDS_CS + 1024;
constexpr int LDS_CNT = LDS_B1 + 1024;
constexpr int LDS_TOTAL = 163840;
constexpr int WS_FLAG = 4096, WS_STAT = 8192, WS_A2 = 1 << 20;
constexpr int A2_BYTES = 128 * KS_T * 1024;
#define PF_CODE 1
#define PF_TLB 0
constexpr int CODE_BYTES = 24960;
constexpr unsigned SPIN_CAP = 1u << 20;

#define STAMP(idx) do{ if((idx)==0 && !(XP&1) && blockIdx.x==XPB && threadIdx.x==0) __hip_atomic_store((GAS u64*)(ws+256), (u64)__builtin_amdgcn_s_memrealtime(), __ATOMIC_RELAXED, __HIP_MEMORY_SCOPE_AGENT); }while(0)
#define FINISH() do{ if(!(XP&2)){ u64 t0_=__hip_atomic_load((GAS u64*)(ws+256), __ATOMIC_RELAXED, __HIP_MEMORY_SCOPE_AGENT); while(__builtin_amdgcn_s_memrealtime()-t0_ < 10000) __builtin_amdgcn_s_sleep(2);} }while(0)
__global__ void tiny_first(char* ws){ if(threadIdx.x==0) *(u64*)(ws+256)=__builtin_amdgcn_s_memrealtime(); }
__global__ void tiny_last(char* ws){ u64 t0_=*(u64*)(ws+256); while(__builtin_amdgcn_s_memrealtime()-t0_ < 10000) __builtin_amdgcn_s_sleep(2); }
#define PR_ARGS
#define PR_PASS

DEVI void bar_lgkm() { asm volatile("s_waitcnt lgkmcnt(0)\n\ts_barrier" ::: "memory"); }
DEVI void bar() { asm volatile("s_barrier" ::: "memory"); }
template <int N> DEVI void bar_vm() { asm volatile("s_waitcnt vmcnt(%0)\n\ts_barrier" ::"i"(N) : "memory"); }
template <int N> DEVI void wait_vm() { asm volatile("s_waitcnt vmcnt(%0)" ::"i"(N) : "memory"); }
DEVI unsigned pk2(float a, float b) { f32x2 v = {a, b}; return __builtin_bit_cast(unsigned, __builtin_convertvector(v, bf16x2)); }
DEVI unsigned pk2(f32x2 v) { return __builtin_bit_cast(unsigned, __builtin_convertvector(v, bf16x2)); }
DEVI float asf(unsigned u) { return __builtin_bit_cast(float, u); }
DEVI unsigned asu(float f) { return __builtin_bit_cast(unsigned, f); }
DEVI float blo(unsigned u) { return __builtin_bit_cast(float, u << 16); }
DEVI float bhi(unsigned u) { return __builtin_bit_cast(float, u & 0xffff0000u); }
DEVI u32x2 tr_read(lptr p) { return __builtin_bit_cast(u32x2, __builtin_amdgcn_ds_read_tr16_b64_v4i16((LAS s16x4*)p)); }
DEVI f32x4 mfma(u32x4 a, u32x4 b, f32x4 c) {
  return __builtin_amdgcn_mfma_f32_16x16x32_bf16(__builtin_bit_cast(bf16x8, a), __builtin_bit_cast(bf16x8, b), c, 0, 0, 0);
}
DEVI u32x4 cat(u32x2 a, u32x2 b) { return __builtin_shufflevector(a, b, 0, 1, 2, 3); }
DEVI void mfma_a(f32x4& c, u32x4 a, u32x4 b) { asm volatile("v_mfma_f32_16x16x32_bf16 %0, %1, %2, %0" : "+v"(c) : "v"(a), "v"(b)); }
template <int OFF> DEVI void ds128(u32x4& d, int addr) { asm volatile("ds_read_b128 %0, %1 offset:%2" : "=v"(d) : "v"(addr), "i"(OFF)); }
template <int OFF> DEVI void dstr(u32x2& d, int addr) { asm volatile("ds_read_b64_tr_b16 %0, %1 offset:%2" : "=v"(d) : "v"(addr), "i"(OFF)); }
template <int OFF> DEVI void dsw128(int addr, u32x4 d) { asm volatile("ds_write_b128 %0, %1 offset:%2" ::"v"(addr), "v"(d), "i"(OFF) : "memory"); }
DEVI u32x4 mkr(const void* p, unsigned bytes) {
  u64 a = (u64)p;
  return u32x4{(unsigned)a, (unsigned)(a >> 32) & 0xffffu, bytes, 0x00020000u};
}
DEVI void st_wt(u32x4 r, int voff, int soff, u32x4 v) {
  asm volatile("buffer_store_dwordx4 %0, %1, %2, %3 offen sc1\n\ts_nop 1" ::"v"(v), "v"(voff), "s"(r), "s"(soff) : "memory");
}
template <int LSTEP>
DEVI void dma4(u32x4 r, u64 mask, int l, int va, int vb, int s0, int s1, int s2, int s3) {
  asm volatile(
      "s_mov_b64 exec, %0\n\t"
      "s_mov_b32 m0, %1\n\ts_nop 0\n\tbuffer_load_dwordx4 %2, %4, %5 offen lds\n\t"
      "s_add_u32 m0, %1, %9\n\ts_nop 0\n\tbuffer_load_dwordx4 %3, %4, %6 offen lds\n\t"
      "s_add_u32 m0, %1, %10\n\ts_nop 0\n\tbuffer_load_dwordx4 %2, %4, %7 offen lds\n\t"
      "s_add_u32 m0, %1, %11\n\ts_nop 0\n\tbuffer_load_dwordx4 %3, %4, %8 offen lds\n\t"
      "s_mov_b64 exec, -1"
      :: "s"(mask), "s"(l), "v"(va), "v"(vb), "s"(r), "s"(s0), "s"(s1), "s"(s2), "s"(s3), "i"(LSTEP), "i"(2 * LSTEP), "i"(3 * LSTEP)
      : "memory", "scc");
}
DEVI void dma1(u32x4 r, u64 mask, int l, int v, int s0) {
  asm volatile("s_mov_b64 exec, %0\n\ts_mov_b32 m0, %1\n\ts_nop 0\n\tbuffer_load_dwordx4 %2, %3, %4 offen lds\n\ts_mov_b64 exec, -1"
               :: "s"(mask), "s"(l), "v"(v), "s"(r), "s"(s0) : "memory");
}
DEVI void touch(u32x4 r, int v) {
  asm volatile("s_mov_b32 m0, %2\n\ts_nop 0\n\tbuffer_load_dword %0, %1, 0 offen lds" ::"v"(v), "s"(r), "i"(LDS_TOTAL - 256) : "memory");
}
DEVI void bload(u32x4& d, u32x4 r, int v, int s0) { asm volatile("buffer_load_dwordx4 %0, %1, %2, %3 offen" : "=v"(d) : "v"(v), "s"(r), "s"(s0)); }
DEVI void bload_sc1(u32x4& d, u32x4 r, int v, int s0) { asm volatile("buffer_load_dwordx4 %0, %1, %2, %3 offen sc1" : "=v"(d) : "v"(v), "s"(r), "s"(s0) : "memory"); }
DEVI int lane_id() { int l; asm volatile("v_mbcnt_lo_u32_b32 %0, -1, 0\n\tv_mbcnt_hi_u32_b32 %0, -1, %0" : "=v"(l)); return l; }
DEVI int sig(int i) { return 4 * (i & 3) + (i >> 2); }
DEVI u64 flagval(unsigned tag) { return ((u64)(tag ^ 0x5bd1e995u) << 32) | tag; }

DEVI float xmax16(float v) { auto r = __builtin_amdgcn_permlane16_swap(asu(v), asu(v), false, false); return fmaxf(asf(r[0]), asf(r[1])); }
DEVI float xmax32(float v) { auto r = __builtin_amdgcn_permlane32_swap(asu(v), asu(v), false, false); return fmaxf(asf(r[0]), asf(r[1])); }
DEVI float xadd16(float v) { auto r = __builtin_amdgcn_permlane16_swap(asu(v), asu(v), false, false); return asf(r[0]) + asf(r[1]); }
DEVI float xadd32(float v) { auto r = __builtin_amdgcn_permlane32_swap(asu(v), asu(v), false, false); return asf(r[0]) + asf(r[1]); }
template <int CTRL> DEVI float dpp(float v) { return asf(__builtin_amdgcn_mov_dpp(asu(v), CTRL, 0xf, 0xf, true)); }
DEVI float wave_sum(float v) {
  v += dpp<0xb1>(v); v += dpp<0x4e>(v); v += dpp<0x141>(v); v += dpp<0x140>(v);
  return xadd32(xadd16(v));
}

DEVI int row_token(int p_, int pos) {
  const int tau = pos >> 4, i = pos & 15;
  int tok = tau == 0 ? p_ * 128 - 16 + 8 * (i & 1) + 2 * (i >> 2) + ((i & 3) < 2) : p_ * 128 + 32 * ((tau - 1) >> 1) + 2 * sig(i) + ((tau - 1) & 1);
  return tok < 0 ? 0 : tok;
}

DEVI int colbyte(bool att, int nw, int mw, int c, int p) {
  if (att) {
    int ct = 4 * nw + (nw < 3 ? (c ^ (2 * mw)) : c);
    int sec = ct / 5, hh = ct - 5 * sec;
    return sec * 160 + (hh < 4 ? 40 * hh + 8 * p : 40 * p + 32);
  } else {
    if (nw < 2) return 128 * nw + 32 * (c ^ (2 * mw)) + 8 * p;
    if (c < 2) return 256 + 64 * (nw - 2) + 32 * c + 8 * p;
    return 384 + 8 * ((p & 1) + 4 * (p >> 1) + 2 * (nw - 2));
  }
}

template <int NC>
struct Frags { u32x4 x[5]; u32x2 w[NC][2]; };

template <int NC, int NH, bool MMA>
DEVI void s1_step(const Frags<NC>& c, Frags<NC>& n, int xo, int xh, const int (&wa)[NC], f32x4 (&acc)[4][NC], f32x4 (&hacc)[NH]) {
  constexpr int NM = 4 * NC + NH, NR = 2 * NC + 5;
#pragma unroll
  for (int k = 0; k < NM; k++) {
    if (MMA) {
      if (k < 4 * NC) mfma_a(acc[k / NC][k % NC], cat(c.w[k % NC][0], c.w[k % NC][1]), c.x[k / NC]);
      else mfma_a(hacc[k - 4 * NC], cat(c.w[k - 4 * NC][0], c.w[k - 4 * NC][1]), c.x[4]);
    }
    if (k < NR) {
      if (k == 0) ds128<0>(n.x[0], xo);
      else if (k <= 2 * NC) { if ((k - 1) & 1) dstr<7680>(n.w[(k - 1) >> 1][1], wa[(k - 1) >> 1]); else dstr<0>(n.w[(k - 1) >> 1][0], wa[(k - 1) >> 1]); }
      else if (k == 2 * NC + 1) ds128<2048>(n.x[1], xo);
      else if (k == 2 * NC + 2) ds128<4096>(n.x[2], xo);
      else if (k == 2 * NC + 3) ds128<6144>(n.x[3], xo);
      else ds128<0>(n.x[4], xh);
    }
  }
}

DEVI f32x2 gelu2(f32x2 v) {
  const float c1 = -2.f * 0.7978845608028654f * 1.4426950408889634f, c2 = c1 * 0.044715f;
  f32x2 t = v * v;
  t = t * c2 + c1;
  t = t * v;
  f32x2 e = {__builtin_amdgcn_exp2f(t[0]), __builtin_amdgcn_exp2f(t[1])};
  e = e + 1.f;
  f32x2 r = {__builtin_amdgcn_rcpf(e[0]), __builtin_amdgcn_rcpf(e[1])};
  return v * r;
}

template <int NC>
DEVI void s1_compute(lptr shm, bool att, int w, int lane, int p_, int j_, char* ws PR_ARGS) {
  constexpr int NH = NC == 4 ? 2 : 3;
  const int mw = w & 1, nw = w >> 1, i = lane & 15, g = lane >> 4;
  f32x4 acc[4][NC], hacc[NH];
#pragma unroll
  for (int m = 0; m < 4; m++)
#pragma unroll
    for (int c = 0; c < NC; c++) { acc[m][c] = f32x4{0, 0, 0, 0}; asm volatile("" : "+v"(acc[m][c])); }
#pragma unroll
  for (int c = 0; c < NH; c++) { hacc[c] = f32x4{0, 0, 0, 0}; asm volatile("" : "+v"(hacc[c])); }
  int wb[NC], cb[NC];
#pragma unroll
  for (int c = 0; c < NC; c++) {
    wb[c] = (4 * g + ((lane >> 2) & 3)) * 480 + colbyte(att, nw, mw, c, lane & 3);
    cb[c] = colbyte(att, nw, mw, c, g) * 2;
    asm volatile("" : "+v"(cb[c]));
  }
  const int xb = LDS_X + i * 128 + ((g ^ (i >> 1)) << 4);
  const int own = (1 + 4 * mw) * 2048;
  Frags<NC> fa, fb;
  int wa[NC];
  bar_lgkm();
  STAMP(1);
  s1_step<NC, NH, false>(fb, fa, xb + own, xb, wb, acc, hacc);
  int ws_ = WSLAB, xs = 0;
#pragma nounroll
  for (int T = 0; T < 10; T++) {
#pragma unroll
    for (int c = 0; c < NC; c++) wa[c] = wb[c] + ws_;
    asm volatile("s_waitcnt lgkmcnt(0)" ::: "memory");
    s1_step<NC, NH, true>(fa, fb, (xb ^ 64) + xs + own, (xb ^ 64) + xs, wa, acc, hacc);
    ws_ += WSLAB; if (ws_ == WSLAB * NWS) ws_ = 0;
    xs += XSLOT; if (xs == XSLOT * NXS) xs = 0;
#pragma unroll
    for (int c = 0; c < NC; c++) wa[c] = wb[c] + ws_;
    bar_lgkm();
    s1_step<NC, NH, true>(fb, fa, xb + xs + own, xb + xs, wa, acc, hacc);
    ws_ += WSLAB;
  }
  asm volatile("s_waitcnt lgkmcnt(0)\n\ts_nop 15" ::: "memory");
#pragma unroll
  for (int m = 0; m < 4; m++)
#pragma unroll
    for (int c = 0; c < NC; c++) asm volatile("" : "+v"(acc[m][c]));
#pragma unroll
  for (int c = 0; c < NH; c++) asm volatile("" : "+v"(hacc[c]));
  STAMP(2);
  if (i == 0) {
#pragma unroll
    for (int c = 0; c < NH; c++) *(LAS f32x4*)(shm + LDS_CS + cb[c]) = hacc[c];
  }
  bar_lgkm();
  f32x4 cs[NC];
#pragma unroll
  for (int c = 0; c < NC; c++) cs[c] = *(LAS f32x4*)(shm + LDS_CS + cb[c]);
  if (att) {
    const int piece = LDS_TB + (g * 16 + (i ^ (8 * (g >> 1)))) * 8;
    const float qs = 0.22360679774997896f * 1.4426950408889634f;
#pragma unroll
    for (int m = 0; m < 5; m++) {
      const int tau = m < 4 ? 1 + 4 * mw + m : 0;
      f32x2 st = *(LAS f32x2*)(shm + LDS_ST + (tau * 16 + i) * 8);
#pragma unroll
      for (int c = 0; c < (m < 4 ? NC : NH); c++) {
        const int ct = 4 * nw + (NC == 4 ? (c ^ (2 * mw)) : c);
        const float sc = ct < 5 ? qs : 1.f;
        const float rs = st[0] * sc, nm = st[1] * sc;
        f32x4 a = m < 4 ? acc[m < 4 ? m : 0][c] : hacc[c < NH ? c : 0];
        f32x4 v = a * rs + cs[c] * nm;
        u32x2 o = {pk2(v[0], v[1]), pk2(v[2], v[3])};
        *(LAS u32x2*)(shm + (tau * 15 + ct) * 512 + piece) = o;
      }
    }
  } else {
    f32x4 b1[NC];
#pragma unroll
    for (int c = 0; c < NC; c++) b1[c] = *(LAS f32x4*)(shm + LDS_B1 + cb[c]);
    const u32x4 ra = mkr(ws + WS_A2, A2_BYTES);
    const int base = ((8 * p_ + 4 * mw) * KS_T + 7 * j_) * 1024;
    unsigned sp[4][2];
#pragma unroll
    for (int m = 0; m < 4; m++) {
      f32x2 st = *(LAS f32x2*)(shm + LDS_ST + ((1 + 4 * mw + m) * 16 + i) * 8);
      unsigned o[NC][2];
#pragma unroll
      for (int c = 0; c < NC; c++) {
        f32x4 v = acc[m][c] * st[0] + (cs[c] * st[1] + b1[c]);
        o[c][0] = pk2(gelu2(f32x2{v[0], v[1]}));
        o[c][1] = pk2(gelu2(f32x2{v[2], v[3]}));
      }
      const int tb = base + m * KS_T * 1024;
      if (NC == 4) {
        st_wt(ra, lane * 16, tb + (2 * nw + mw) * 1024, u32x4{o[0][0], o[0][1], o[1][0], o[1][1]});
        st_wt(ra, lane * 16, tb + (2 * nw + (mw ^ 1)) * 1024, u32x4{o[2][0], o[2][1], o[NC - 1][0], o[NC - 1][1]});
      } else {
        if (j_ < 7 || nw == 2) st_wt(ra, lane * 16, tb + (2 + nw) * 1024, u32x4{o[0][0], o[0][1], o[1][0], o[1][1]});
        sp[m][0] = o[2][0]; sp[m][1] = o[2][1];
        if (m & 1) {
          auto s0 = __builtin_amdgcn_permlane32_swap(sp[m - 1][0], sp[m][0], false, false);
          auto s1 = __builtin_amdgcn_permlane32_swap(sp[m - 1][1], sp[m][1], false, false);
          if (j_ < 7) st_wt(ra, (lane >> 5) * (KS_T * 1024) + (lane & 31) * 16, tb - KS_T * 1024 + 6 * 1024 + (nw - 2) * 512, u32x4{s0[0], s1[0], s0[1], s1[1]});
        }
      }
    }
  }
}

DEVI void s1_attention(lptr shm, int w, int lane, int p_, int j_, char* ws) {
  const int i = lane & 15, g = lane >> 4;
  const int tau = w + 1, taup = w >= 2 ? tau - 2 : 0;
  const int flip = (w == 1) ? 2 : 0;
  const bool start = (p_ == 0) && (w < 2);
  const int qi = sig(i);
  f32x4 mO, mP;
  unsigned M1, M2, M3;
  {
    bool vo[4], vp[4];
#pragma unroll
    for (int r = 0; r < 4; r++) {
      int jj = 4 * r + g;
      vo[r] = (jj <= qi) && (jj >= qi - 7);
      vp[r] = (jj - 16 >= qi - 7) && !start;
      mO[r] = vo[r] ? 0.f : -1e30f;
      mP[r] = vp[r] ? 0.f : -1e30f;
    }
    M1 = (vp[2] ? 0xffffu : 0u) | (vp[3] ? 0xffff0000u : 0u);
    M2 = (vo[0] ? 0xffffu : 0u) | (vo[1] ? 0xffff0000u : 0u);
    M3 = (vo[2] ? 0xffffu : 0u) | (vo[3] ? 0xffff0000u : 0u);
  }
  lptr tb = shm + LDS_TB;
  lptr po = tb + tau * 15 * 512 + (g * 16 + (i ^ (8 * (g >> 1)))) * 8;
  lptr pp = tb + taup * 15 * 512 + (g * 16 + ((i ^ flip) ^ (8 * (g >> 1)))) * 8;
  const int q = (lane >> 2) & 3, p = lane & 3;
  lptr to = tb + tau * 15 * 512 + (p * 16 + ((4 * g + q) ^ (8 * (p >> 1)))) * 8;
  lptr tp = tb + taup * 15 * 512 + (p * 16 + ((4 * g + (q ^ flip)) ^ (8 * (p >> 1)))) * 8;
  asm volatile("" :: "v"(M1), "v"(M2), "v"(M3), "v"(mO), "v"(mP), "v"(po), "v"(pp), "v"(to), "v"(tp));
  bar_lgkm();
  const u32x2 q4 = *(LAS u32x2*)(po + 4 * 512), ko4 = *(LAS u32x2*)(po + 9 * 512), kp4 = *(LAS u32x2*)(pp + 9 * 512);
  u32x2 qa[4], ko[4], kp[4];
#pragma unroll
  for (int h = 0; h < 4; h++) { qa[h] = *(LAS u32x2*)(po + h * 512); ko[h] = *(LAS u32x2*)(po + (5 + h) * 512); kp[h] = *(LAS u32x2*)(pp + (5 + h) * 512); }
  u32x4 vf[4];
#pragma unroll
  for (int h = 0; h < 4; h++) vf[h] = cat(tr_read(tp + (10 + h) * 512), tr_read(to + (10 + h) * 512));
  const u32x4 v4 = cat(tr_read(tp + 14 * 512), tr_read(to + 14 * 512));
  f32x4 sp[4], so[4];
#pragma unroll
  for (int h = 0; h < 4; h++) {
    const unsigned mq = g == h ? 0xffffffffu : 0u;
    u32x4 qf = {qa[h][0], qa[h][1], q4[0] & mq, q4[1] & mq};
    sp[h] = mfma(cat(kp[h], kp4), qf, mP);
    so[h] = mfma(cat(ko[h], ko4), qf, mO);
  }
  float ee[4], eo[4], mx[4], l[4];
#pragma unroll
  for (int h = 0; h < 4; h++) { ee[h] = fmaxf(fmaxf(sp[h][2], so[h][0]), so[h][2]); eo[h] = fmaxf(fmaxf(sp[h][3], so[h][1]), so[h][3]); mx[h] = fmaxf(ee[h], eo[h]); }
  __builtin_amdgcn_sched_barrier(0);
#pragma unroll
  for (int h = 0; h < 4; h++) mx[h] = xmax16(mx[h]);
#pragma unroll
  for (int h = 0; h < 4; h++) mx[h] = xmax32(mx[h]);
  __builtin_amdgcn_sched_barrier(0);
#pragma unroll
  for (int h = 0; h < 4; h++) { ee[h] = __builtin_amdgcn_exp2f(ee[h] - mx[h]); eo[h] = __builtin_amdgcn_exp2f(eo[h] - mx[h]); l[h] = ee[h] + eo[h]; }
  __builtin_amdgcn_sched_barrier(0);
#pragma unroll
  for (int h = 0; h < 4; h++) l[h] = xadd16(l[h]);
#pragma unroll
  for (int h = 0; h < 4; h++) l[h] = xadd32(l[h]);
  __builtin_amdgcn_sched_barrier(0);
  f32x4 o4 = {0, 0, 0, 0}, oh[4];
#pragma unroll
  for (int h = 0; h < 4; h++) {
    const float rl = __builtin_amdgcn_rcpf(l[h]);
    const unsigned pk = pk2(ee[h] * rl, eo[h] * rl), mv = (i >> 2) == h ? 0xffffffffu : 0u;
    u32x4 pf = {0u, pk & M1, pk & M2, pk & M3};
    oh[h] = mfma(vf[h], pf, f32x4{0, 0, 0, 0});
    o4 = mfma(v4 & mv, pf, o4);
  }
  const u32x4 ra = mkr(ws + WS_A2, A2_BYTES);
  const int base = ((8 * p_ + w) * KS_T + 54 + 5 * (j_ >> 1)) * 1024;
#pragma unroll
  for (int hp = 0; hp < 2; hp++)
    st_wt(ra, lane * 16, base + ((j_ & 1) * 3 + hp) * 1024,
          u32x4{pk2(oh[2 * hp][0], oh[2 * hp][1]), pk2(oh[2 * hp][2], oh[2 * hp][3]), pk2(oh[2 * hp + 1][0], oh[2 * hp + 1][1]), pk2(oh[2 * hp + 1][2], oh[2 * hp + 1][3])});
  unsigned a0 = pk2(o4[0], o4[1]), a1 = pk2(o4[2], o4[3]);
  auto s0 = __builtin_amdgcn_permlane32_swap(a0, a0, false, false);
  auto s1 = __builtin_amdgcn_permlane32_swap(a1, a1, false, false);
  if (lane < 32) st_wt(ra, lane * 16, base + 2 * 1024 + (j_ & 1) * 512, u32x4{s0[0], s1[0], s0[1], s1[1]});
}

template <bool ATT>
DEVI void s1_loader(int lw, int lane, int p_, int j_, const void* x, const void* wsrc) {
  constexpr int NX = ATT ? 5 : 4;
  constexpr int RS = ATT ? 3840 : 3456;
  const u32x4 rx = mkr(x, S_ * D_ * 2), rw = mkr(wsrc, D_ * RS);
  int wv;
  {
    int hi = lane >= 30, ch = lane - 30 * hi;
    int cbyte;
    if (ATT) { int sec = (ch >= 10) + (ch >= 20); cbyte = sec * 1120 + j_ * 160 + ch * 16; }
    else { int lim = j_ < 7 ? 27 : 19; cbyte = j_ * 448 + (ch < lim ? ch : lim) * 16; }
    wv = hi * RS + cbyte;
  }
  const int wrow = 16 * (lw & 1) + 4 * (lw >> 1);
  const int r8 = lane >> 3, cp = lane & 7, t0 = p_ * 128;
  int xv[2], hv;
#pragma unroll
  for (int hf = 0; hf < 2; hf++)
    xv[hf] = (t0 + 32 * lw + 2 * (4 * (r8 & 3) + 2 * hf + (r8 >> 2))) * 1280 + ((cp ^ (4 * hf + (r8 >> 1))) << 4);
  {
    int tok = t0 - 16 + 8 * (r8 & 1) + 2 * lw + ((r8 & 3) < 2);
    tok = tok < 0 ? 0 : tok;
    hv = tok * 1280 + ((cp ^ ((2 * lw + ((r8 & 3) >> 1)) & 7)) << 4);
  }
  const u64 hmask = lw == 0 ? 0xffffff00ull : 0xffffffffull;
  auto ldw = [&](int s, int slot) {
    const int so = (32 * s + wrow) * RS;
    dma4<960>(rw, 0x0fffffffffffffffull, slot * WSLAB + 4 * lw * 960, wv, wv, so, so + 2 * RS, so + 8 * RS, so + 10 * RS);
  };
  auto ldx = [&](int T, int slot) {
    const int d = LDS_X + slot * XSLOT;
    if (ATT) dma1(rx, hmask, d + lw * 512, hv, T * 128);
    dma4<1024>(rx, ~0ull, d + 2048 + 4 * lw * 1024, xv[0], xv[1], T * 128, T * 128, T * 128 + 1280, T * 128 + 1280);
  };
  ldx(0, 0); ldw(0, 0); ldw(1, 1);
  int slot = 1;
#pragma nounroll
  for (int T = 1; T < 10; T++) {
    ldx(T, slot); ldw(2 * T, 2 * slot); ldw(2 * T + 1, 2 * slot + 1);
    bar_vm<NX + 8>();
    slot += 1; if (slot == NXS) slot = 0;
  }
}

__global__ __launch_bounds__(768, 3) void fused_block(const void* __restrict__ x, const void* __restrict__ wqkv, const void* __restrict__ wo,
                                                      const void* __restrict__ w1, const unsigned short* __restrict__ b1,
                                                      const void* __restrict__ w2, const char* __restrict__ b2, char* __restrict__ out,
                                                      char* ws) {
  extern __shared__ __attribute__((aligned(16))) char shm_[];
  lptr shm = (lptr)shm_;
  const int tid = threadIdx.x, lane = tid & 63, w = __builtin_amdgcn_readfirstlane(tid >> 6);
  const int xcd = blockIdx.x & 7, idx = blockIdx.x >> 3;
  const int p_ = (xcd & 1) * 8 + (idx & 7), sub = idx >> 3;
  const bool att = !(sub & 1);
  const int j_ = (xcd >> 1) * 2 + (sub >> 1);
  const int unit = (xcd >> 1) * 28 + idx;
  const bool has2 = idx < 28 && unit < 110;
  const int mt = unit / 5, n_ = (xcd & 1) * 5 + (unit - 5 * mt);
  const int G0 = 6 * mt;
  const int kq = w & 3;
  STAMP(0);
  if (w == 7) {
    u64 pc; asm volatile("s_getpc_b64 %0" : "=s"(pc));
    touch(mkr((const void*)(pc & ~255ull), CODE_BYTES), (idx + 32 * lane) * 128);
  }
  if (w >= 8) {
    const int lw = w - 8;
    if (att) s1_loader<true>(lw, lane, p_, j_, x, wqkv);
    else s1_loader<false>(lw, lane, p_, j_, x, w1);
    const unsigned tag = __builtin_amdgcn_readfirstlane(*(LAS unsigned*)(shm + LDS_CNT + 4));
    const u32x4 rst = mkr(ws + WS_STAT, S_ * 16);
    const int spos = 36 * lw + (lane < 36 ? lane : 35), sv = row_token(p_, spos) * 16;
    u32x4 sg;
    bload_sc1(sg, rst, sv, 0);
    const u32x4 rw = mkr(kq == 0 ? wo : w2, has2 ? (kq == 0 ? D_ * 1280 : F_ * 1280) : 0);
    const int l3 = lane >> 3, cbyte = n_ * 128 + (((lane & 7) ^ ((lane >> 4) << 1)) << 4);
    const int v1 = l3 * 1280 + cbyte, v2 = (20 * (l3 >> 2) + (l3 & 3)) * 1280 + cbyte;
    auto geom = [&](int s, int& rb, int& sa, int& sb, int& v) {
      sa = 8; sb = 16; v = v1;
      if (kq == 0) {
        int P = (s * 13) >> 6, kp = s - 5 * P;
        if (kp == 2) { rb = 160 * P + 16; sa = 80; sb = 40; v = v2; }
        else { rb = 160 * P + 40 * (kp > 2 ? kp - 1 : kp); sb = 20; }
      } else rb = 32 * (18 * (kq - 1) + s);
      rb *= 1280; sa *= 1280; sb *= 1280;
    };
    u32x4 pre[9][4];
    auto ldr = [&](auto R) {
      constexpr int r = decltype(R)::value;
      int rb, sa, sb, v; geom(9 + r, rb, sa, sb, v);
      bload(pre[r][0], rw, v, rb); bload(pre[r][1], rw, v, rb + sa); bload(pre[r][2], rw, v, rb + sb); bload(pre[r][3], rw, v, rb + sa + sb);
    };
    auto ldl = [&](int s) {
      int rb, sa, sb, v; geom(s, rb, sa, sb, v);
      dma4<1024>(rw, ~0ull, s < 9 ? s * 16384 + kq * 4096 : 147456 + (s - 18) * 4096, v, v, rb, rb + sa, rb + sb, rb + sa + sb);
    };
#define IC(n) std::integral_constant<int, n>{}
    ldr(IC(0)); ldr(IC(1)); ldr(IC(2));
    asm volatile("s_waitcnt vmcnt(12)" : "+v"(sg) :: "memory");
#pragma nounroll
    for (unsigned spins = 0; !__all(sg[1] == tag && sg[3] == tag) && spins < SPIN_CAP; spins++) {
      __builtin_amdgcn_s_sleep(2);
      bload_sc1(sg, rst, sv, 0);
      asm volatile("s_waitcnt vmcnt(0)" : "+v"(sg) :: "memory");
    }
    *(LAS u32x2*)(shm + LDS_ST + spos * 8) = u32x2{sg[0], sg[2]};
    bar_lgkm();
    ldr(IC(3)); ldr(IC(4)); ldr(IC(5));
    bar();
    ldr(IC(6)); ldr(IC(7));
    bar();
    ldr(IC(8)); ldl(0); ldl(1);
    if (att) bar();
    ldl(2); ldl(3); ldl(4);
    if (att) bar();
    STAMP(5);
    if (!has2) { FINISH(); return; }
    ldl(5); ldl(6); ldl(7); ldl(8);
    if (kq == 0) { ldl(18); ldl(19); wait_vm<24>(); } else wait_vm<16>();
    STAMP(6);
    bar();
    bar_vm<0>();
    const int wa = kq * 4096 + lane * 16;
#define PUT(r, off) dsw128<off>(wa + (r / 3) * 49152, pre[r][0]); dsw128<off + 1024>(wa + (r / 3) * 49152, pre[r][1]); dsw128<off + 2048>(wa + (r / 3) * 49152, pre[r][2]); dsw128<off + 3072>(wa + (r / 3) * 49152, pre[r][3]);
    PUT(0, 0) PUT(1, 16384) PUT(2, 32768)
    bar_lgkm();
    PUT(3, 0) PUT(4, 16384) PUT(5, 32768)
    bar_lgkm();
    PUT(6, 0) PUT(7, 16384) PUT(8, 32768)
    bar_lgkm();
    bar();
    bar();
  } else {
    const int rid = (att ? 0 : 8) + j_;
    const int srow = p_ * 128 + 8 * rid + w;
    unsigned u[5];
#pragma unroll
    for (int k = 0; k < 5; k++) u[k] = ((const unsigned*)x)[srow * 320 + lane + 64 * k];
    const unsigned tag = __builtin_amdgcn_readfirstlane(__hip_atomic_load((GAS unsigned*)ws, __ATOMIC_RELAXED, __HIP_MEMORY_SCOPE_AGENT)) + 1u;
    if (!att && tid < 224) {
      int n = j_ * 224 + tid;
      *(LAS float*)(shm + LDS_B1 + tid * 4) = blo(b1[n < F_ ? n : F_ - 1]);
    }
    if (tid < 384) {
      const int sl = tid >> 7, o = tid & 127;
      const unsigned v = o < 8 ? 0x3f803f80u : 0u;
      if (o < 8 || !att) *(LAS u32x4*)(shm + LDS_X + sl * XSLOT + o * 16) = u32x4{v, v, v, v};
    }
    if (tid == 448) *(LAS u32x2*)(shm + LDS_CNT) = u32x2{0u, tag};
    {
      float s = 0;
#pragma unroll
      for (int k = 0; k < 5; k++) s += blo(u[k]) + bhi(u[k]);
      const float m = wave_sum(s) * (1.f / D_);
      float q = 0;
#pragma unroll
      for (int k = 0; k < 5; k++) { float a = blo(u[k]) - m, b = bhi(u[k]) - m; q += a * a + b * b; }
      const float rs = rsqrtf(wave_sum(q) * (1.f / D_) + 1e-5f);
      if (lane == 0) st_wt(mkr(ws + WS_STAT, S_ * 16), 0, srow * 16, u32x4{asu(rs), tag, asu(-m * rs), tag});
    }
    const bool light = att ? (w >= 6) : (w >= 4);
    if (light) s1_compute<3>(shm, att, w, lane, p_, j_, ws PR_PASS);
    else s1_compute<4>(shm, att, w, lane, p_, j_, ws PR_PASS);
    STAMP(3);
    if (att) { s1_attention(shm, w, lane, p_, j_, ws); bar_lgkm(); }
    STAMP(4);
    wait_vm<0>();
    GAS u64* flags = (GAS u64*)(ws + WS_FLAG);
    const u64 fv = flagval(tag);
    if (lane == 0) {
      unsigned old = __hip_atomic_fetch_add((LAS unsigned*)(shm + LDS_CNT), 1u, __ATOMIC_RELAXED, __HIP_MEMORY_SCOPE_WORKGROUP);
      if (old == 7) __hip_atomic_store(flags + p_ * 16 + rid, fv, __ATOMIC_RELAXED, __HIP_MEMORY_SCOPE_AGENT);
    }
    STAMP(5);
    if (!has2) {
      if (blockIdx.x == 255 && w == 0) {
#pragma nounroll
        for (unsigned spins = 0; spins < SPIN_CAP; spins++) {
          bool ok = true;
#pragma unroll
          for (int k = 0; k < 4; k++) ok &= __hip_atomic_load(flags + lane + 64 * k, __ATOMIC_RELAXED, __HIP_MEMORY_SCOPE_AGENT) == fv;
          if (__all(ok)) break;
          __builtin_amdgcn_s_sleep(8);
        }
        if (lane == 0) __hip_atomic_store((GAS unsigned*)ws, tag, __ATOMIC_RELAXED, __HIP_MEMORY_SCOPE_AGENT);
      }
      FINISH();
      return;
    }
    const int mh = w >> 2, g = lane >> 4, q = (lane >> 2) & 3, p = lane & 3;
    const int ks0 = kq == 0 ? 54 : 18 * (kq - 1), nsteps = kq == 0 ? 20 : 18;
    const u32x4 ra = mkr(ws + WS_A2, A2_BYTES);
    int ao[3];
    const int av = lane * 16;
#pragma unroll
    for (int m = 0; m < 3; m++) { int G = G0 + 3 * mh + m; G = G < 128 ? G : 127; ao[m] = (G * KS_T + ks0) * 1024; }
    int ba[4], bs[4];
#pragma unroll
    for (int c = 0; c < 4; c++) ba[c] = kq * 4096 + (4 * g + q) * 128 + ((c ^ ((2 * g + (q >> 1)) & 3)) << 5) + 8 * p;
    f32x4 acc[3][4];
#pragma unroll
    for (int m = 0; m < 3; m++)
#pragma unroll
      for (int c = 0; c < 4; c++) { acc[m][c] = f32x4{0, 0, 0, 0}; asm volatile("" : "+v"(acc[m][c])); }
    {
      int Ga = G0 + 3 * mh, Gb = Ga + 2;
      Ga = Ga < 128 ? Ga : 127; Gb = Gb < 128 ? Gb : 127;
      GAS u64* fp = flags + (((lane & 8) ? Gb : Ga) >> 3) * 16 + (kq == 0 ? 0 : 8) + (lane & 7);
#pragma nounroll
      for (unsigned spins = 0; spins < SPIN_CAP; spins++) {
        if (__all(__hip_atomic_load(fp, __ATOMIC_RELAXED, __HIP_MEMORY_SCOPE_AGENT) == fv)) break;
        __builtin_amdgcn_s_sleep(2);
      }
      __builtin_amdgcn_fence(__ATOMIC_ACQUIRE, "wavefront");
    }
    STAMP(6);
    u32x4 a[6][3];
#pragma unroll
    for (int u = 0; u < 6; u++)
#pragma unroll
      for (int m = 0; m < 3; m++) bload_sc1(a[u][m], ra, av, ao[m] + u * 1024);
    u32x2 b0[4][2], b1f[4][2];
    bar();
    STAMP(7);
#pragma unroll
    for (int c = 0; c < 4; c++) { dstr<0>(b0[c][0], ba[c]); dstr<2048>(b0[c][1], ba[c]); }
    int sl = 0;
    auto step = [&](auto U, u32x2 (&bc)[4][2], u32x2 (&bn)[4][2], int s) {
      constexpr int u = decltype(U)::value;
      const int so = s + 6 < nsteps ? (s + 6) * 1024 : 0x70000000;
      sl = min(sl + 16384, 151552);
      if (u == 2 && s == 8) sl = 0;
#pragma unroll
      for (int c = 0; c < 4; c++) bs[c] = ba[c] + sl;
#pragma unroll
      for (int m = 0; m < 3; m++) {
        wait_vm<17>();
#pragma unroll
        for (int c = 0; c < 4; c++) {
          mfma_a(acc[m][c], cat(bc[c][0], bc[c][1]), a[u][m]);
          if (m < 2) { if (c & 1) dstr<2048>(bn[2 * m + (c >> 1)][1], bs[2 * m + (c >> 1)]); else dstr<0>(bn[2 * m + (c >> 1)][0], bs[2 * m + (c >> 1)]); }
        }
        bload_sc1(a[u][m], ra, av, ao[m] + so);
      }
    };
#define LG0 asm volatile("s_waitcnt lgkmcnt(0)" ::: "memory")
    LG0;
#pragma nounroll
    for (int s = 0; s < nsteps; s += 6) {
      step(IC(0), b0, b1f, s); LG0;
      step(IC(1), b1f, b0, s + 1); LG0;
      if (s == 18) break;
      step(IC(2), b0, b1f, s + 2); bar_lgkm();
      step(IC(3), b1f, b0, s + 3); LG0;
      step(IC(4), b0, b1f, s + 4); LG0;
      step(IC(5), b1f, b0, s + 5); bar_lgkm();
    }
    asm volatile("s_waitcnt vmcnt(0)\n\ts_nop 15" ::: "memory");
    STAMP(9);
    const int l2 = lane_id(), i2 = l2 & 15, g2 = l2 >> 4;
#pragma unroll
    for (int m = 0; m < 3; m++)
#pragma unroll
      for (int c = 0; c < 4; c++) {
        asm volatile("" : "+v"(acc[m][c]));
        *(LAS f32x4*)(shm + (3 * mh + m) * 16384 + kq * 4096 + i2 * 256 + (((4 * c + g2) ^ (i2 & 7)) << 4)) = acc[m][c];
      }
  }
  {
    const int lane = lane_id();
    const int frow = 8 * w + (lane >> 3), fG = G0 + (frow >> 4);
    const bool fvalid = fG < 128;
    const int ftok = 32 * ((fvalid ? fG : 0) >> 1) + 2 * sig(frow & 15) + (fG & 1);
    const int foff = ftok * 1280 + n_ * 128 + (lane & 7) * 16;
    const u32x4 xv = *(const u32x4*)((const char*)x + foff);
    const u32x4 bv = *(const u32x4*)(b2 + n_ * 128 + (lane & 7) * 16);
    bar_lgkm();
    STAMP(10);
    f32x4 s[2], y[2];
#pragma unroll
    for (int e = 0; e < 2; e++) {
      lptr pr = shm + (frow >> 4) * 16384 + (frow & 15) * 256 + (((2 * (lane & 7) + e) ^ (frow & 7)) << 4);
      s[e] = *(LAS f32x4*)pr;
      y[e] = *(LAS f32x4*)(pr + 4096) + *(LAS f32x4*)(pr + 8192) + *(LAS f32x4*)(pr + 12288);
    }
    u32x4 o;
#pragma unroll
    for (int k = 0; k < 4; k++) {
      f32x2 yy = f32x2{y[k >> 1][2 * (k & 1)], y[k >> 1][2 * (k & 1) + 1]} + f32x2{blo(bv[k]), bhi(bv[k])};
      f32x2 t = yy * -1.4426950408889634f;
      f32x2 e = f32x2{__builtin_amdgcn_exp2f(t[0]), __builtin_amdgcn_exp2f(t[1])} + 1.f;
      f32x2 gt = yy * f32x2{__builtin_amdgcn_rcpf(e[0]), __builtin_amdgcn_rcpf(e[1])};
      f32x2 r = f32x2{s[k >> 1][2 * (k & 1)], s[k >> 1][2 * (k & 1) + 1]} * gt + f32x2{blo(xv[k]), bhi(xv[k])};
      o[k] = pk2(r);
    }
    if (fvalid) *(u32x4*)(out + foff) = o;
  }
  STAMP(11);
  FINISH();
}

extern "C" void kernel_launch(void* const* d_in, const int* in_sizes, int n_in, void* d_out, int out_size, void* d_ws,
                              size_t ws_size, hipStream_t stream) {
  (void)hipFuncSetAttribute((const void*)fused_block, hipFuncAttributeMaxDynamicSharedMemorySize, LDS_TOTAL);
  fused_block<<<256, 768, LDS_TOTAL, stream>>>(d_in[0], d_in[1], d_in[2], d_in[4], (const unsigned short*)d_in[5], d_in[6],
                                               (const char*)d_in[7], (char*)d_out, (char*)d_ws);
  tiny_last<<<1,64,0,stream>>>((char*)d_ws);
}
```

```cpp
#include <hip/hip_runtime.h>
#include <hip/hip_bf16.h>
#include <type_traits>
#include <utility>

typedef __attribute__((ext_vector_type(4))) short s16x4;
typedef __attribute__((ext_vector_type(8))) __bf16 bf16x8;
typedef __attribute__((ext_vector_type(2))) __bf16 bf16x2;
typedef __attribute__((ext_vector_type(4))) float f32x4;
typedef __attribute__((ext_vector_type(2))) float f32x2;
typedef __attribute__((ext_vector_type(4))) unsigned u32x4;
typedef __attribute__((ext_vector_type(2))) unsigned u32x2;
#define LAS __attribute__((address_space(3)))
#define GAS __attribute__((address_space(1)))
#define DEVI __device__ __forceinline__
typedef LAS char* lptr;
typedef unsigned long long u64;
#define IC(n) std::integral_constant<int, n>{}

constexpr int S_ = 2048, D_ = 640, F_ = 1728;
constexpr int KS_T = 74;
constexpr int WSLAB = 15360;
constexpr int XOFF = 2 * WSLAB;
constexpr int SLOT = 49152;
constexpr int LDS_TB = 86016;
constexpr int LDS_ST = 155648;
constexpr int LDS_CS = LDS_ST + 1152;
constexpr int LDS_B1 = LDS_CS + 1024;
constexpr int LDS_CNT = LDS_B1 + 1024;
constexpr int LDS_ZERO = LDS_CNT + 24;
constexpr int LDS_TAB = LDS_CNT + 64;
constexpr int LDS_TOTAL = 163840;
constexpr int WS_FLAG = 4096, WS_STAT = 8192, WS_BOX = 65536, WS_A2 = 1 << 20;
constexpr int A2_BYTES = 128 * KS_T * 1024;
#define PF_CODE 1
#define PF_MALL 0
#define MAP2 0
#define S1MODE 0
#define S2MODE 0
#define THR 4
#define PSL 4
#define ABL 0
#define ROT1 0
#define ROT2 0
constexpr int CODE_BYTES = 29056;
constexpr unsigned SPIN_CAP = 1u << 20;

#define STAMP(idx)
#define FINISH()
#define STAMPA(k) STAMP(k)
#define STAMPB(k)
#define PR_ARGS
#define PR_PASS

DEVI void bar_lgkm() { asm volatile("s_waitcnt lgkmcnt(0)\n\ts_barrier" ::: "memory"); }
DEVI void bar() { asm volatile("s_barrier" ::: "memory"); }
template <int N> DEVI void bar_vm() { asm volatile("s_waitcnt vmcnt(%0)\n\ts_barrier" ::"i"(N) : "memory"); }
template <int N> DEVI void wait_vm() { asm volatile("s_waitcnt vmcnt(%0)" ::"i"(N) : "memory"); }
DEVI unsigned pk2(float a, float b) { f32x2 v = {a, b}; return __builtin_bit_cast(unsigned, __builtin_convertvector(v, bf16x2)); }
DEVI unsigned pk2(f32x2 v) { return __builtin_bit_cast(unsigned, __builtin_convertvector(v, bf16x2)); }
DEVI float asf(unsigned u) { return __builtin_bit_cast(float, u); }
DEVI unsigned asu(float f) { return __builtin_bit_cast(unsigned, f); }
DEVI float blo(unsigned u) { return __builtin_bit_cast(float, u << 16); }
DEVI float bhi(unsigned u) { return __builtin_bit_cast(float, u & 0xffff0000u); }
DEVI u32x2 tr_read(lptr p) { return __builtin_bit_cast(u32x2, __builtin_amdgcn_ds_read_tr16_b64_v4i16((LAS s16x4*)p)); }
DEVI f32x4 mfma(u32x4 a, u32x4 b, f32x4 c) {
  return __builtin_amdgcn_mfma_f32_16x16x32_bf16(__builtin_bit_cast(bf16x8, a), __builtin_bit_cast(bf16x8, b), c, 0, 0, 0);
}
DEVI u32x4 cat(u32x2 a, u32x2 b) { return __builtin_shufflevector(a, b, 0, 1, 2, 3); }
DEVI void mfma_a(f32x4& c, u32x4 a, u32x4 b) { asm volatile("v_mfma_f32_16x16x32_bf16 %0, %1, %2, %0" : "+v"(c) : "v"(a), "v"(b)); }
template <int OFF> DEVI void ds128(u32x4& d, int addr) { asm volatile("ds_read_b128 %0, %1 offset:%2" : "=v"(d) : "v"(addr), "i"(OFF)); }
template <int OFF> DEVI void dstr(u32x2& d, int addr) { asm volatile("ds_read_b64_tr_b16 %0, %1 offset:%2" : "=v"(d) : "v"(addr), "i"(OFF)); }
template <int OFF> DEVI void dsw128(int addr, u32x4 d) { asm volatile("ds_write_b128 %0, %1 offset:%2" ::"v"(addr), "v"(d), "i"(OFF) : "memory"); }
DEVI u32x4 mkr(const void* p, unsigned bytes) {
  u64 a = (u64)p;
  return u32x4{(unsigned)a, (unsigned)(a >> 32) & 0xffffu, bytes, 0x00020000u};
}
DEVI void st_wt(u32x4 r, int voff, int soff, u32x4 v) {
  asm volatile("buffer_store_dwordx4 %0, %1, %2, %3 offen sc1\n\ts_nop 1" ::"v"(v), "v"(voff), "s"(r), "s"(soff) : "memory");
}
template <int LSTEP>
DEVI void dma4(u32x4 r, u64 mask, int l, int va, int vb, int s0, int s1, int s2, int s3) {
  asm volatile(
      "s_mov_b64 exec, %0\n\t"
      "s_mov_b32 m0, %1\n\ts_nop 0\n\tbuffer_load_dwordx4 %2, %4, %5 offen lds\n\t"
      "s_add_u32 m0, %1, %9\n\ts_nop 0\n\tbuffer_load_dwordx4 %3, %4, %6 offen lds\n\t"
      "s_add_u32 m0, %1, %10\n\ts_nop 0\n\tbuffer_load_dwordx4 %2, %4, %7 offen lds\n\t"
      "s_add_u32 m0, %1, %11\n\ts_nop 0\n\tbuffer_load_dwordx4 %3, %4, %8 offen lds\n\t"
      "s_mov_b64 exec, -1"
      :: "s"(mask), "s"(l), "v"(va), "v"(vb), "s"(r), "s"(s0), "s"(s1), "s"(s2), "s"(s3), "i"(LSTEP), "i"(2 * LSTEP), "i"(3 * LSTEP)
      : "memory", "scc");
}
DEVI void dma1(u32x4 r, u64 mask, int l, int v, int s0) {
  asm volatile("s_mov_b64 exec, %0\n\ts_mov_b32 m0, %1\n\ts_nop 0\n\tbuffer_load_dwordx4 %2, %3, %4 offen lds\n\ts_mov_b64 exec, -1"
               :: "s"(mask), "s"(l), "v"(v), "s"(r), "s"(s0) : "memory");
}
template <int WHEN>
DEVI void bload4_if(u32x4 (&d)[4], u32x4 r, int v, int s0, int s1, int s2, int s3, int flag) {
#define LDR_BODY "s_cmp_lg_u32 %10, %11\n\ts_cbranch_scc1 .Lr%=\n\tbuffer_load_dwordx4 %0, %4, %5, %6 offen\n\tbuffer_load_dwordx4 %1, %4, %5, %7 offen\n\t" \
                 "buffer_load_dwordx4 %2, %4, %5, %8 offen\n\tbuffer_load_dwordx4 %3, %4, %5, %9 offen\n.Lr%=:"
  if (WHEN) asm volatile(LDR_BODY : "+v"(d[0]), "+v"(d[1]), "+v"(d[2]), "+v"(d[3]) : "v"(v), "s"(r), "s"(s0), "s"(s1), "s"(s2), "s"(s3), "s"(flag), "i"(WHEN) : "scc");
  else asm volatile(LDR_BODY : "=&v"(d[0]), "=&v"(d[1]), "=&v"(d[2]), "=&v"(d[3]) : "v"(v), "s"(r), "s"(s0), "s"(s1), "s"(s2), "s"(s3), "s"(flag), "i"(WHEN) : "scc");
}
template <int N0, int N1>
DEVI void bar_vm_sel(int flag) {
  asm volatile("s_cmp_lg_u32 %0, 0\n\ts_cbranch_scc1 .La%=\n\ts_waitcnt vmcnt(%1)\n\ts_branch .Lb%=\n.La%=:\n\ts_waitcnt vmcnt(%2)\n.Lb%=:\n\ts_barrier"
               ::"s"(flag), "i"(N0), "i"(N1) : "memory", "scc");
}
template <class T> DEVI void pin(T& v) { asm volatile("" : "+v"(v)); }
DEVI void dsw_col0(int a0, f32x4 d0, int a1, f32x4 d1, int a2, f32x4 d2) {
  asm volatile("s_mov_b64 exec, %6\n\tds_write_b128 %0, %1\n\tds_write_b128 %2, %3\n\tds_write_b128 %4, %5\n\ts_mov_b64 exec, -1"
               ::"v"(a0), "v"(d0), "v"(a1), "v"(d1), "v"(a2), "v"(d2), "s"(0x0001000100010001ull) : "memory");
}
DEVI void vadd(int& d, int a, int s) { asm volatile("v_add_u32 %0, %2, %1" : "=v"(d) : "v"(a), "s"(s)); }
DEVI void touch(u32x4 r, int v) {
  asm volatile("s_mov_b32 m0, %2\n\ts_nop 0\n\tbuffer_load_dword %0, %1, 0 offen lds" ::"v"(v), "s"(r), "i"(LDS_TOTAL - 256) : "memory");
}
DEVI void bload(u32x4& d, u32x4 r, int v, int s0) { asm volatile("buffer_load_dwordx4 %0, %1, %2, %3 offen" : "=v"(d) : "v"(v), "s"(r), "s"(s0)); }
DEVI void bload_sc1(u32x4& d, u32x4 r, int v, int s0) { if (ABL & 4) { bload(d, r, v, s0); return; } asm volatile("buffer_load_dwordx4 %0, %1, %2, %3 offen sc1" : "=v"(d) : "v"(v), "s"(r), "s"(s0) : "memory"); }
DEVI void arrive(int k) {
  asm volatile("s_mov_b64 exec, 1\n\tds_add_u32 %0, %1\n\ts_mov_b64 exec, -1" ::"v"(LDS_CNT + 4 * k), "v"(1) : "memory");
}
template <int SLEEP, unsigned N>
DEVI void await(int k) {
  unsigned v;
  for (;;) {
    asm volatile("ds_read_b32 %0, %1\n\ts_waitcnt lgkmcnt(0)" : "=v"(v) : "v"(LDS_CNT + 4 * k) : "memory");
    if (__builtin_amdgcn_readfirstlane(v) >= N) break;
    if (SLEEP) __builtin_amdgcn_s_sleep(SLEEP);
  }
}
DEVI int lane_id() { int l; asm volatile("v_mbcnt_lo_u32_b32 %0, -1, 0\n\tv_mbcnt_hi_u32_b32 %0, -1, %0" : "=v"(l)); return l; }
DEVI int sig(int i) { return 4 * (i & 3) + (i >> 2); }
DEVI u64 flagval(unsigned tag) { return ((u64)(tag ^ 0x5bd1e995u) << 32) | tag; }

DEVI float xmax16(float v) { auto r = __builtin_amdgcn_permlane16_swap(asu(v), asu(v), false, false); return fmaxf(asf(r[0]), asf(r[1])); }
DEVI float xmax32(float v) { auto r = __builtin_amdgcn_permlane32_swap(asu(v), asu(v), false, false); return fmaxf(asf(r[0]), asf(r[1])); }
DEVI float xadd16(float v) { auto r = __builtin_amdgcn_permlane16_swap(asu(v), asu(v), false, false); return asf(r[0]) + asf(r[1]); }
DEVI float xadd32(float v) { auto r = __builtin_amdgcn_permlane32_swap(asu(v), asu(v), false, false); return asf(r[0]) + asf(r[1]); }
template <int CTRL> DEVI float dpp(float v) { return asf(__builtin_amdgcn_mov_dpp(asu(v), CTRL, 0xf, 0xf, true)); }
DEVI float wave_sum(float v) {
  v += dpp<0xb1>(v); v += dpp<0x4e>(v); v += dpp<0x141>(v); v += dpp<0x140>(v);
  return xadd32(xadd16(v));
}

DEVI int row_token(int p_, int pos) {
  const int tau = pos >> 4, i = pos & 15;
  int tok = tau == 0 ? p_ * 128 - 16 + 8 * (i & 1) + 2 * (i >> 2) + ((i & 3) < 2) : p_ * 128 + 32 * ((tau - 1) >> 1) + 2 * sig(i) + ((tau - 1) & 1);
  return tok < 0 ? 0 : tok;
}

struct ColGeo { int A, B, C, ct; };
DEVI ColGeo colgeo(bool att, int nw, int mw, int c, bool heavy) {
  const int cc = heavy ? (c ^ (2 * mw)) : c;
  const int ct = 4 * nw + cc, sec = (ct * 13) >> 6, hh = ct - 5 * sec;
  const bool q4 = hh == 4, sp = !heavy && c == 2;
  ColGeo r;
  r.A = att ? sec * 160 + (q4 ? 32 : 40 * hh) : heavy ? 128 * nw + 32 * cc : (sp ? 352 + 16 * nw : 128 + 64 * nw + 32 * c);
  r.B = att && q4 ? 40 : 8;
  r.C = att ? (q4 ? 80 : 16) : (sp ? 32 : 16);
  r.ct = ct;
  return r;
}

template <int NC>
struct Frags { u32x4 x[5]; u32x2 w[NC][2]; };

template <class F, int... I> DEVI void sfor_(F f, std::integer_sequence<int, I...>) { (f(std::integral_constant<int, I>{}), ...); }
template <int N, class F> DEVI void sfor(F f) { sfor_(f, std::make_integer_sequence<int, N>{}); }
template <int N> DEVI void wait_lg() { asm volatile("s_waitcnt lgkmcnt(%0)" ::"i"(N) : "memory"); }

template <int NC, int K>
DEVI void s1_rd(Frags<NC>& n, int xo, int xh, const int (&wa)[NC]) {
  if constexpr (K == 0) ds128<0>(n.x[0], xo);
  else if constexpr (K <= 2 * NC) { if ((K - 1) & 1) dstr<7680>(n.w[(K - 1) >> 1][1], wa[(K - 1) >> 1]); else dstr<0>(n.w[(K - 1) >> 1][0], wa[(K - 1) >> 1]); }
  else if constexpr (K < 2 * NC + 4) ds128<(K - 2 * NC) * 2048>(n.x[K - 2 * NC], xo);
  else if constexpr (K == 2 * NC + 4) ds128<0>(n.x[4], xh);
}
template <int NC, int NH, int K>
DEVI void s1_mm(const Frags<NC>& c, f32x4 (&acc)[4][NC], f32x4 (&hacc)[NH]) {
  if constexpr (K < 4 * NC) mfma_a(acc[K / NC][K % NC], cat(c.w[K % NC][0], c.w[K % NC][1]), c.x[K / NC]);
  else mfma_a(hacc[K - 4 * NC], cat(c.w[K - 4 * NC][0], c.w[K - 4 * NC][1]), c.x[4]);
}
template <int NC, int NH, bool BAR>
DEVI void s1_step(const Frags<NC>& c, Frags<NC>& n, int xo, int xh, const int (&wa)[NC], f32x4 (&acc)[4][NC], f32x4 (&hacc)[NH]) {
  constexpr int NM = 4 * NC + NH, NR = 2 * NC + 5, D = BAR ? NM - NR : 0;
  sfor<NM>([&](auto KK) {
    constexpr int k = decltype(KK)::value;
    constexpr int need = k < NC ? 2 * k + 2 : (k % NC == 0 && k <= 4 * NC) ? 2 * NC + k / NC : -1;
    if constexpr (BAR && k == D) bar_lgkm();
    if constexpr (need >= 0 && (!BAR || k < D)) wait_lg<NR - 1 - need + (BAR ? 0 : (k < NR ? k : NR))>();
    s1_mm<NC, NH, k>(c, acc, hacc);
    if constexpr (k >= D) s1_rd<NC, k - D>(n, xo, xh, wa);
  });
}

template <int OFF> DEVI void dsw64(int addr, u32x2 d) { asm volatile("ds_write_b64 %0, %1 offset:%2" ::"v"(addr), "v"(d), "i"(OFF) : "memory"); }
template <int OFF> DEVI void ds64(u32x2& d, int addr) { asm volatile("ds_read_b64 %0, %1 offset:%2" : "=v"(d) : "v"(addr), "i"(OFF)); }

template <int NC, class PRE>
DEVI void s1_compute(lptr shm, bool att, int w, int lane, int p_, int j_, char* ws, PRE pre PR_ARGS) {
  constexpr int NH = NC == 4 ? 2 : 3;
  const int mw = w & 1, nw = w >> 1, i = lane & 15, g = lane >> 4;
  f32x4 acc[4][NC], hacc[NH];
#pragma unroll
  for (int m = 0; m < 4; m++)
#pragma unroll
    for (int c = 0; c < NC; c++) { acc[m][c] = f32x4{0, 0, 0, 0}; asm volatile("" : "+v"(acc[m][c])); }
#pragma unroll
  for (int c = 0; c < NH; c++) { hacc[c] = f32x4{0, 0, 0, 0}; asm volatile("" : "+v"(hacc[c])); }
  int wb[NC], cb[NC], ct[NC];
#pragma unroll
  for (int c = 0; c < NC; c++) {
    const ColGeo cg = colgeo(att, nw, mw, c, NC == 4);
    wb[c] = (lane >> 2) * 480 + cg.A + cg.B * (lane & 1) + cg.C * ((lane >> 1) & 1);
    cb[c] = LDS_CS + 2 * (cg.A + cg.B * (g & 1) + cg.C * (g >> 1));
    ct[c] = cg.ct;
    asm volatile("" : "+v"(cb[c]), "+v"(wb[c]));
  }
  int xb = XOFF + i * 128 + ((g ^ (i >> 1)) << 4);
  int sta = LDS_ST + i * 8;
  asm volatile("" : "+v"(xb), "+v"(sta));
  const int own = (1 + 4 * mw) * 2048;
  Frags<NC> fa, fb;
  int wa[NC];
  pre();
  bar_lgkm();
  STAMP(1);
  sfor<2 * NC + 5>([&](auto K) { s1_rd<NC, decltype(K)::value>(fa, xb + own, xb, wb); });
#pragma unroll
  for (int c = 0; c < NC; c++) wa[c] = wb[c] + WSLAB;
  s1_step<NC, NH, false>(fa, fb, (xb ^ 64) + own, xb ^ 64, wa, acc, hacc);
  int sl = 0;
#pragma nounroll
  for (int T = 1; T < 10; T++) {
    sl = sl == 2 * SLOT ? 0 : sl + SLOT;
#pragma unroll
    for (int c = 0; c < NC; c++) wa[c] = wb[c] + sl;
    s1_step<NC, NH, true>(fb, fa, xb + sl + own, xb + sl, wa, acc, hacc);
#pragma unroll
    for (int c = 0; c < NC; c++) wa[c] = wb[c] + sl + WSLAB;
    s1_step<NC, NH, false>(fa, fb, (xb ^ 64) + sl + own, (xb ^ 64) + sl, wa, acc, hacc);
  }
  wait_lg<0>();
  arrive(2);
  sfor<NH>([&](auto K) { s1_mm<NC, NH, 4 * NC + decltype(K)::value>(fb, acc, hacc); });
  u32x2 stu[5];
  u32x4 b1u[NC], csu[NC];
  sfor<4 * NC>([&](auto KK) {
    constexpr int k = decltype(KK)::value;
    s1_mm<NC, NH, k>(fb, acc, hacc);
    if constexpr (k < 4) ds64<(1 + k) * 128>(stu[k], sta + mw * 512);
    if constexpr (k == 4) ds64<0>(stu[4], sta);
    if constexpr (k == 5) {
      dsw_col0(cb[0], hacc[0], cb[1], hacc[1], cb[NH - 1], hacc[NH - 1]);
      if (NC == 4) arrive(8 + nw);
    }
    if constexpr (k >= 6 && k < 6 + NC) ds128<LDS_B1 - LDS_CS>(b1u[k - 6], cb[k - 6]);
  });
  STAMP(2);
  if (NC == 4) await<0, 2>(8 + nw);
#pragma unroll
  for (int c = 0; c < NC; c++) ds128<0>(csu[c], cb[c]);
  asm volatile("s_waitcnt lgkmcnt(0)\n\ts_nop 7" ::: "memory");
  f32x2 st[5];
  f32x4 cs[NC], b1[NC];
#pragma unroll
  for (int m = 0; m < 5; m++) { asm volatile("" : "+v"(stu[m])); st[m] = __builtin_bit_cast(f32x2, stu[m]); }
#pragma unroll
  for (int c = 0; c < NC; c++) { asm volatile("" : "+v"(csu[c]), "+v"(b1u[c])); cs[c] = __builtin_bit_cast(f32x4, csu[c]); b1[c] = __builtin_bit_cast(f32x4, b1u[c]); }
#pragma unroll
  for (int m = 0; m < 4; m++)
#pragma unroll
    for (int c = 0; c < NC; c++) pin(acc[m][c]);
#pragma unroll
  for (int c = 0; c < NH; c++) pin(hacc[c]);
  STAMPA(12);
#define SB __builtin_amdgcn_sched_barrier(0)
  const int l2 = lane_id();
  if (att) {
    const int g2 = l2 >> 4, piece = LDS_TB + ((l2 & 48) + ((l2 & 15) ^ (8 * (g2 >> 1)))) * 8;
    sfor<NC>([&](auto C) {
      constexpr int c = decltype(C)::value;
      const int ah = piece + ct[c] * 4608, ao = ah + (1 + 4 * mw) * 512;
      u32x2 o[5];
      SB;
#pragma unroll
      for (int m = 0; m < (c < NH ? 5 : 4); m++) {
        f32x4 a = m < 4 ? acc[m < 4 ? m : 0][c] : hacc[c < NH ? c : 0];
        f32x4 v = a * st[m][0] + cs[c] * st[m][1];
        o[m] = u32x2{pk2(v[0], v[1]), pk2(v[2], v[3])};
      }
      dsw64<0>(ao, o[0]); dsw64<512>(ao, o[1]); dsw64<1024>(ao, o[2]); dsw64<1536>(ao, o[3]);
      if (c < NH) dsw64<0>(ah, o[4]);
    });
  } else {
    const u32x4 ra = mkr(ws + WS_A2, A2_BYTES);
    const int base = ((8 * p_ + 4 * mw) * KS_T + 7 * j_) * 1024;
    const int o0 = base + (NC == 4 ? (2 * nw + mw) * 1024 : (j_ < 7 || nw == 2) ? (2 + nw) * 1024 : 0x70000000);
    const int o1 = base + (NC == 4 ? (2 * nw + (mw ^ 1)) * 1024 : j_ < 7 ? 6 * 1024 + (nw - 2) * 512 : 0x70000000);
    const float c1 = -2.f * 0.7978845608028654f * 1.4426950408889634f, c2 = c1 * 0.044715f;
    unsigned sp[2];
#pragma unroll
    for (int m = 0; m < 4; m++) {
      f32x4 v[NC], t[NC];
      SB;
#pragma unroll
      for (int c = 0; c < NC; c++) t[c] = cs[c] * st[m][1] + b1[c];
      SB;
#pragma unroll
      for (int c = 0; c < NC; c++) v[c] = acc[m][c] * st[m][0] + t[c];
      SB;
#pragma unroll
      for (int c = 0; c < NC; c++) t[c] = v[c] * v[c];
      SB;
#pragma unroll
      for (int c = 0; c < NC; c++) t[c] = t[c] * c2 + c1;
      SB;
#pragma unroll
      for (int c = 0; c < NC; c++) t[c] = t[c] * v[c];
      SB;
#pragma unroll
      for (int c = 0; c < NC; c++)
#pragma unroll
        for (int e = 0; e < 4; e++) t[c][e] = __builtin_amdgcn_exp2f(t[c][e]);
      SB;
#pragma unroll
      for (int c = 0; c < NC; c++) t[c] = t[c] + 1.f;
      SB;
#pragma unroll
      for (int c = 0; c < NC; c++)
#pragma unroll
        for (int e = 0; e < 4; e++) t[c][e] = __builtin_amdgcn_rcpf(t[c][e]);
      SB;
#pragma unroll
      for (int c = 0; c < NC; c++) v[c] = v[c] * t[c];
      SB;
      unsigned o[NC][2];
#pragma unroll
      for (int c = 0; c < NC; c++) { o[c][0] = pk2(v[c][0], v[c][1]); o[c][1] = pk2(v[c][2], v[c][3]); }
      st_wt(ra, l2 * 16, o0 + m * KS_T * 1024, u32x4{o[0][0], o[0][1], o[1][0], o[1][1]});
      if (NC == 4) st_wt(ra, l2 * 16, o1 + m * KS_T * 1024, u32x4{o[2][0], o[2][1], o[NC - 1][0], o[NC - 1][1]});
      else if (m & 1) {
        auto s0 = __builtin_amdgcn_permlane32_swap(sp[0], o[2][0], false, false);
        auto s1 = __builtin_amdgcn_permlane32_swap(sp[1], o[2][1], false, false);
        st_wt(ra, (l2 >> 5) * (KS_T * 1024) + (l2 & 31) * 16, o1 + (m - 1) * KS_T * 1024, u32x4{s0[0], s1[0], s0[1], s1[1]});
      } else { sp[0] = o[2][0]; sp[1] = o[2][1]; }
    }
  }
}

template <bool MAX> DEVI float red4(const float (&v)[4]) {
  auto op = [](unsigned a, unsigned b) { float r; if (MAX) asm("v_max_f32 %0, %1, %2" : "=v"(r) : "v"(a), "v"(b)); else r = asf(a) + asf(b); return r; };
  auto a = __builtin_amdgcn_permlane32_swap(asu(v[0]), asu(v[1]), false, false);
  auto b = __builtin_amdgcn_permlane32_swap(asu(v[2]), asu(v[3]), false, false);
  auto c = __builtin_amdgcn_permlane16_swap(asu(op(a[0], a[1])), asu(op(b[0], b[1])), false, false);
  return op(c[0], c[1]);
}
DEVI void bcast4(float r, float (&v)[4]) {
  auto e = __builtin_amdgcn_permlane16_swap(asu(r), asu(r), false, false);
  unsigned e0 = e[0], e1 = e[1];
  auto a = __builtin_amdgcn_permlane32_swap(e0, e0, false, false);
  auto b = __builtin_amdgcn_permlane32_swap(e1, e1, false, false);
  v[0] = asf(a[0]); v[1] = asf(a[1]); v[2] = asf(b[0]); v[3] = asf(b[1]);
}

DEVI void att_table(lptr shm, int lane) {
  const int i = lane & 15, g = lane >> 4, p = lane & 3;
  const int d = sig(i) - g;
  const unsigned NEG = 0xf149f2cau;
  bool vo[4], vp[4];
#pragma unroll
  for (int r = 0; r < 4; r++) { vo[r] = (unsigned)(d - 4 * r) <= 7u; vp[r] = d + 16 - 4 * r <= 7; }
  auto pm = [](bool lo, bool hi) { return (lo ? 0xffffu : 0u) | (hi ? 0xffff0000u : 0u); };
  LAS u32x4* t = (LAS u32x4*)(shm + LDS_TAB) + lane;
  t[0] = u32x4{vo[0] ? 0u : NEG, vo[1] ? 0u : NEG, vo[2] ? 0u : NEG, vo[3] ? 0u : NEG};
  t[64] = u32x4{pm(vp[2], vp[3]), pm(vo[0], vo[1]), vp[2] ? 0u : NEG, vp[3] ? 0u : NEG};
  t[128] = u32x4{pm(vo[2], vo[3]), (unsigned)(LDS_TB + (g * 16 + (i ^ (8 * (g >> 1)))) * 8), (unsigned)(LDS_TB + (p * 16 + ((lane >> 2) ^ (8 * (p >> 1)))) * 8), 0u};
}
DEVI int sel(u64 mask, int a, int b) { int r; asm("v_cndmask_b32_e64 %0, %2, %1, %3" : "=v"(r) : "v"(a), "v"(b), "s"(mask)); return r; }

DEVI void s1_attention(lptr shm, int w, int p_, int j_, char* ws PR_ARGS) {
  arrive(3);
  const int lane = lane_id();
  const int tau = (w + 1) * 512, taup = w >= 2 ? tau - 1024 : 0, flip = w == 1 ? 16 : 0;
  const LAS u32x4* t = (const LAS u32x4*)(shm + LDS_TAB) + lane;
  const u32x4 t0 = t[0], t2 = t[128];
  u32x4 t1 = t[64];
  if (p_ == 0 && w < 2) { t1[0] = 0u; t1[2] = 0xf149f2cau; t1[3] = 0xf149f2cau; }
  const f32x4 mO = __builtin_bit_cast(f32x4, t0), mP = __builtin_bit_cast(f32x4, t1);
  const unsigned M1 = t1[0], M2 = t1[1], M3 = t2[0];
  const int po = t2[1] + tau, pp = (t2[1] ^ flip) + taup, to = t2[2] + tau, tp = (t2[2] ^ flip) + taup;
  int aq[4], avo[4], avp[4];
#pragma unroll
  for (int h = 0; h < 4; h++) {
    aq[h] = sel(0xffffull << (16 * h), po, LDS_ZERO - 4 * 4608);
    avo[h] = sel(0x1111111111111111ull << h, to, LDS_ZERO - 14 * 4608);
    avp[h] = sel(0x1111111111111111ull << h, tp, LDS_ZERO - 14 * 4608);
    asm volatile("" : "+v"(aq[h]), "+v"(avo[h]), "+v"(avp[h]));
  }
  STAMPA(14);
  await<0, 8>(3);
  STAMPA(15);
  u32x2 qa[4][2], ko[4][2], kp[4][2], va[4][2], vb[4][2];
  f32x4 sp[4], so[4];
  auto rd = [&](auto H) {
    constexpr int h = decltype(H)::value;
    ds64<(5 + h) * 4608>(ko[h][0], po); ds64<9 * 4608>(ko[h][1], po); ds64<h * 4608>(qa[h][0], po); ds64<4 * 4608>(qa[h][1], aq[h]);
    ds64<(5 + h) * 4608>(kp[h][0], pp); ds64<9 * 4608>(kp[h][1], pp);
  };
  auto rv = [&](auto H) {
    constexpr int h = decltype(H)::value;
    dstr<(10 + h) * 4608>(va[h][0], tp); dstr<(10 + h) * 4608>(va[h][1], to); dstr<14 * 4608>(vb[h][0], avp[h]); dstr<14 * 4608>(vb[h][1], avo[h]);
  };
  auto qk = [&](auto H, auto N) {
    constexpr int h = decltype(H)::value;
    asm volatile("s_waitcnt lgkmcnt(%6)" : "+v"(ko[h][0]), "+v"(ko[h][1]), "+v"(qa[h][0]), "+v"(qa[h][1]), "+v"(kp[h][0]), "+v"(kp[h][1]) : "i"(decltype(N)::value));
    so[h] = mfma(cat(ko[h][0], ko[h][1]), cat(qa[h][0], qa[h][1]), mO);
    sp[h] = mfma(cat(kp[h][0], kp[h][1]), cat(qa[h][0], qa[h][1]), mP);
  };
  rd(IC(0)); rd(IC(1));
  qk(IC(0), IC(6)); rd(IC(2));
  qk(IC(1), IC(6)); rd(IC(3));
  qk(IC(2), IC(6)); rv(IC(0)); rv(IC(1));
  qk(IC(3), IC(8)); rv(IC(2));
  asm volatile("s_waitcnt lgkmcnt(8)");
  rv(IC(3));
  const float qs = 0.22360679774997896f * 1.4426950408889634f;
  float ee[4], eo[4], mx[4], l[4];
#pragma unroll
  for (int h = 0; h < 4; h++) { ee[h] = fmaxf(fmaxf(sp[h][2], so[h][0]), so[h][2]); eo[h] = fmaxf(fmaxf(sp[h][3], so[h][1]), so[h][3]); mx[h] = fmaxf(ee[h], eo[h]); }
  bcast4(red4<true>(mx) * -qs, mx);
#pragma unroll
  for (int h = 0; h < 4; h++) {
    f32x2 t = f32x2{ee[h], eo[h]} * qs + mx[h];
    ee[h] = __builtin_amdgcn_exp2f(t[0]); eo[h] = __builtin_amdgcn_exp2f(t[1]); l[h] = ee[h] + eo[h];
  }
  bcast4(__builtin_amdgcn_rcpf(red4<false>(l)), l);
  asm volatile("s_waitcnt lgkmcnt(0)" : "+v"(va[0][0]), "+v"(va[0][1]), "+v"(va[1][0]), "+v"(va[1][1]), "+v"(va[2][0]), "+v"(va[2][1]), "+v"(va[3][0]), "+v"(va[3][1]),
               "+v"(vb[0][0]), "+v"(vb[0][1]), "+v"(vb[1][0]), "+v"(vb[1][1]), "+v"(vb[2][0]), "+v"(vb[2][1]), "+v"(vb[3][0]), "+v"(vb[3][1]));
  f32x4 o4 = {0, 0, 0, 0}, oh[4];
#pragma unroll
  for (int h = 0; h < 4; h++) {
    const unsigned pk = pk2(f32x2{ee[h], eo[h]} * l[h]);
    u32x4 pf = {0u, pk & M1, pk & M2, pk & M3};
    o4 = mfma(cat(vb[h][0], vb[h][1]), pf, o4);
    oh[h] = mfma(cat(va[h][0], va[h][1]), pf, f32x4{0, 0, 0, 0});
  }
  const u32x4 ra = mkr(ws + WS_A2, A2_BYTES);
  const int base = ((8 * p_ + w) * KS_T + 54 + 5 * (j_ >> 1)) * 1024;
#pragma unroll
  for (int hp = 0; hp < 2; hp++)
    st_wt(ra, lane * 16, base + ((j_ & 1) * 3 + hp) * 1024,
          u32x4{pk2(oh[2 * hp][0], oh[2 * hp][1]), pk2(oh[2 * hp][2], oh[2 * hp][3]), pk2(oh[2 * hp + 1][0], oh[2 * hp + 1][1]), pk2(oh[2 * hp + 1][2], oh[2 * hp + 1][3])});
  unsigned a0 = pk2(o4[0], o4[1]), a1 = pk2(o4[2], o4[3]);
  auto s0 = __builtin_amdgcn_permlane32_swap(a0, a0, false, false);
  auto s1 = __builtin_amdgcn_permlane32_swap(a1, a1, false, false);
  st_wt(ra, lane < 32 ? lane * 16 : 0x70000000, base + 2 * 1024 + (j_ & 1) * 512, u32x4{s0[0], s1[0], s0[1], s1[1]});
  arrive(4);
}

__global__ __launch_bounds__(768, 3) void fused_block(const void* __restrict__ x, const void* __restrict__ wqkv, const void* __restrict__ wo,
                                                      const void* __restrict__ w1, const unsigned short* __restrict__ b1,
                                                      const void* __restrict__ w2, const char* __restrict__ b2, char* __restrict__ out,
                                                      char* ws) {
  extern __shared__ __attribute__((aligned(16))) char shm_[];
  lptr shm = (lptr)shm_;
  asm volatile("" : "+s"(x), "+s"(wqkv), "+s"(wo), "+s"(w1), "+s"(b1), "+s"(w2), "+s"(b2), "+s"(out), "+s"(ws));
  const int tid = threadIdx.x, lane = tid & 63, w = __builtin_amdgcn_readfirstlane(tid >> 6);
  const int xcd = blockIdx.x & 7, idx = blockIdx.x >> 3;
  const int p_ = (xcd & 1) * 8 + (idx & 7), sub = idx >> 3;
  const bool att = !(sub & 1);
  const int j_ = (xcd >> 1) * 2 + (sub >> 1);
  const int unit = (xcd >> 1) * 28 + idx;
  const bool has2 = idx < 28 && unit < 110;
  const int mt = unit / 5, nl = unit - 5 * mt, n_ = (xcd & 1) * 5 + nl;
  const int G0 = 6 * mt;
  const int kq = w < 8 ? (w + (w >> 2)) & 3 : w & 3;
  const int rot = ROT2 ? (nl + mt) % 6 : 0;
  STAMP(0);
  if (w == 7) {
    u64 pc; asm volatile("s_getpc_b64 %0" : "=s"(pc));
    touch(mkr((const void*)(pc & ~255ull), CODE_BYTES), (idx + 32 * lane) * 128);
  }
  if (w >= 8) {
    const int lw = w - 8;
    const int RS = att ? 3840 : 3456;
    const u32x4 rx = mkr(x, S_ * D_ * 2), rw1 = mkr(att ? wqkv : w1, D_ * RS);
    const int r8 = lane >> 3, cp = lane & 7, t0 = p_ * 128;
    int xv[2], hv, wv;
#pragma unroll
    for (int hf = 0; hf < 2; hf++)
      xv[hf] = (t0 + 32 * lw + 2 * (4 * (r8 & 3) + 2 * hf + (r8 >> 2))) * 1280 + ((cp ^ (4 * hf + (r8 >> 1))) << 4);
    auto ldx = [&](int T, int slot) { dma4<1024>(rx, ~0ull, slot * SLOT + XOFF + 2048 + 4 * lw * 1024, xv[0], xv[1], T * 128, T * 128, T * 128 + 1280, T * 128 + 1280); };
    ldx(0, 0);
    {
      const int hi = lane >= 30, ch = lane - 30 * hi, lim = j_ < 7 ? 27 : 19;
      wv = hi * RS + (att ? ((ch >= 10) + (ch >= 20)) * 1120 + j_ * 160 + ch * 16 : j_ * 448 + (ch < lim ? ch : lim) * 16);
    }
    const int wrow = (16 * (lw & 1) + 4 * (lw >> 1)) * RS;
    auto ldw = [&](int s, int slot) {
      const int so = 32 * s * RS + wrow;
      dma4<960>(rw1, 0x0fffffffffffffffull, slot * SLOT + (s & 1) * WSLAB + 4 * lw * 960, wv, wv, so, so + 2 * RS, so + 8 * RS, so + 10 * RS);
    };
    ldw(0, 0);
    {
      int tok = t0 - 16 + 8 * (r8 & 1) + 2 * lw + ((r8 & 3) < 2);
      tok = tok < 0 ? 0 : tok;
      hv = att ? tok * 1280 + ((cp ^ ((2 * lw + ((r8 & 3) >> 1)) & 7)) << 4) : 0x70000000;
    }
    const u64 hmask = lw == 0 ? 0xffffff00ull : 0xffffffffull;
    auto ldh = [&](int T, int slot) { dma1(rx, hmask, slot * SLOT + XOFF + lw * 512, hv, T * 128); };
    ldh(0, 0); ldw(1, 0);
    const u32x4 rw = mkr(kq == 0 ? wo : w2, has2 ? (kq == 0 ? D_ * 1280 : F_ * 1280) : 0);
    const int cbyte = n_ * 128 + ((cp ^ ((lane >> 4) << 1)) << 4);
    const int v1 = r8 * 1280 + cbyte, v2 = kq == 0 ? (20 * (r8 >> 2) + (r8 & 3)) * 1280 + cbyte : v1;
    auto geom = [&](int s, int& rb, int& sa, int& sb, int& v) {
      const int P = s / 5, kp = s - 5 * P;
      v = kp == 2 ? v2 : v1;
      rb = 1280 * (kq == 0 ? (kp == 2 ? 160 * P + 16 : 160 * P + 40 * (kp > 2 ? kp - 1 : kp)) : 32 * (18 * (kq - 1) + s));
      sa = 1280 * (kq == 0 && kp == 2 ? 80 : 8);
      sb = 1280 * (kq == 0 ? (kp == 2 ? 40 : 20) : 16);
    };
    u32x4 pre[9][4];
    const int attf = att;
    auto ldr = [&](auto R, auto LATE) {
      constexpr int r = decltype(R)::value;
      int rb, sa, sb, v; geom(9 + r, rb, sa, sb, v);
      bload4_if<decltype(LATE)::value>(pre[r], rw, v, rb, rb + sa, rb + sb, rb + sa + sb, attf);
    };
    auto ldl = [&](auto S) {
      constexpr int s = decltype(S)::value;
      int rb, sa, sb, v; geom(s, rb, sa, sb, v);
      dma4<1024>(rw, ~0ull, s < 9 ? s * 16384 + kq * 4096 : 147456 + (s - 18) * 4096, v, v, rb, rb + sa, rb + sb, rb + sa + sb);
    };
    const u32x4 rst = mkr(ws + WS_STAT, S_ * 16);
    const int spos = 36 * lw + (lane < 36 ? lane : 35), sv = row_token(p_, spos) * 16;
    u32x4 sg;
    sfor<9>([&](auto TT) {
      constexpr int T = decltype(TT)::value + 1, slot = T % 3;
      if (T == 9) bload_sc1(sg, rst, sv, 0);
      ldx(T, slot); ldw(2 * T, slot); ldh(T, slot); ldw(2 * T + 1, slot);
      ldr(IC(T - 1), IC(0));
      bar_vm_sel<(T == 1 || T == 9 ? 17 : 21), 13>(attf);
    });
    STAMP(1);
    const unsigned tag = __builtin_amdgcn_readfirstlane(*(LAS unsigned*)(shm + LDS_CNT + 4));
    asm volatile("" : "+v"(sg));
#pragma nounroll
    for (unsigned spins = 0; !__all(sg[1] == tag && sg[3] == tag) && spins < SPIN_CAP; spins++) {
      __builtin_amdgcn_s_sleep(2);
      bload_sc1(sg, rst, sv, 0);
      asm volatile("s_waitcnt vmcnt(0)" : "+v"(sg) :: "memory");
    }
    *(LAS u32x2*)(shm + LDS_ST + spos * 8) = u32x2{sg[0], sg[2]};
    ldr(IC(0), IC(1));
    wait_vm<4>();
    STAMP(2);
    bar_lgkm();
    STAMP(3);
    if (!has2) { FINISH(); return; }
    sfor<8>([&](auto R) { ldr(IC(decltype(R)::value + 1), IC(1)); });
    ldl(IC(3)); ldl(IC(4));
    STAMP(4);
    await<1, 8>(2);
    ldl(IC(0)); wait_vm<THR>(); ldl(IC(1)); wait_vm<THR>(); ldl(IC(2)); wait_vm<THR>();
    if (att) await<1, 8>(4);
    STAMP(5);
    ldl(IC(5)); wait_vm<THR>(); ldl(IC(6)); wait_vm<THR>(); ldl(IC(7)); wait_vm<THR>(); ldl(IC(8));
    if (kq == 0) { ldl(IC(18)); ldl(IC(19)); }
    STAMP(6);
    bar();
    bar_vm<0>();
    const int wa = kq * 4096 + lane * 16;
#define NAP __builtin_amdgcn_s_sleep(PSL);
#define PUT(r, off) if (!(ABL & 32)) { dsw128<off>(wa + (r / 3) * 49152, pre[r][0]); dsw128<off + 1024>(wa + (r / 3) * 49152, pre[r][1]); dsw128<off + 2048>(wa + (r / 3) * 49152, pre[r][2]); dsw128<off + 3072>(wa + (r / 3) * 49152, pre[r][3]); } else asm volatile("" :: "v"(pre[r][0]), "v"(pre[r][1]), "v"(pre[r][2]), "v"(pre[r][3]));
    PUT(0, 0) NAP PUT(1, 16384) NAP PUT(2, 32768)
    bar_lgkm();
    PUT(3, 0) NAP PUT(4, 16384) NAP PUT(5, 32768)
    bar_lgkm();
    PUT(6, 0) NAP PUT(7, 16384) NAP PUT(8, 32768)
    bar_lgkm();
    bar();
  } else {
    const int rid = (att ? 0 : 8) + j_;
    const int srow = p_ * 128 + 8 * rid + w;
    unsigned u[5];
#pragma unroll
    for (int k = 0; k < 5; k++) u[k] = ((const GAS unsigned*)x)[srow * 320 + lane + 64 * k];
    const unsigned ep = __hip_atomic_load((GAS unsigned*)ws, __ATOMIC_RELAXED, __HIP_MEMORY_SCOPE_AGENT);
    const int bn = j_ * 224 + tid;
    const unsigned short bv = ((const GAS unsigned short*)b1)[bn < F_ ? bn : F_ - 1];
    if (tid < 24) *(LAS u32x4*)(shm + (tid >> 3) * SLOT + XOFF + (tid & 7) * 16) = u32x4{0x3f803f80u, 0x3f803f80u, 0x3f803f80u, 0x3f803f80u};
    if (w == 5 && att) att_table(shm, lane);
    unsigned tag;
    auto pre = [&]() {
      tag = __builtin_amdgcn_readfirstlane(ep) + 1u;
      float s = 0;
#pragma unroll
      for (int k = 0; k < 5; k++) s += blo(u[k]) + bhi(u[k]);
      const float m = wave_sum(s) * (1.f / D_);
      float q = 0;
#pragma unroll
      for (int k = 0; k < 5; k++) { float a = blo(u[k]) - m, b = bhi(u[k]) - m; q += a * a + b * b; }
      const float rs = __builtin_amdgcn_rsqf(wave_sum(q) * (1.f / D_) + 1e-5f);
      if (lane == 0) st_wt(mkr(ws + WS_STAT, S_ * 16), 0, srow * 16, u32x4{asu(rs), tag, asu(-m * rs), tag});
      if (tid < 256) *(LAS float*)(shm + LDS_B1 + tid * 4) = blo(bv);
      if (tid >= 448 && tid < 464) *(LAS unsigned*)(shm + LDS_CNT + 4 * (tid - 448)) = tid == 449 ? tag : 0u;
    };
    const bool light = att ? (w >= 6) : (w >= 4);
    if (light) s1_compute<3>(shm, att, w, lane, p_, j_, ws, pre PR_PASS);
    else s1_compute<4>(shm, att, w, lane, p_, j_, ws, pre PR_PASS);
    STAMP(3);
    if (att) s1_attention(shm, w, p_, j_, ws PR_PASS);
    STAMP(4);
    wait_vm<0>();
    const int lane = lane_id();
    GAS u64* flags = (GAS u64*)(ws + WS_FLAG);
    const u64 fv = flagval(tag);
    {
      unsigned old = 0;
      if (lane == 0) old = __hip_atomic_fetch_add((LAS unsigned*)(shm + LDS_CNT), 1u, __ATOMIC_RELAXED, __HIP_MEMORY_SCOPE_WORKGROUP);
      if (__builtin_amdgcn_readfirstlane(old) == 7) {
        const int dm = (lane * 26) >> 8, cm = (8 * p_) / 6 + dm;
        GAS u64* dst = lane == 30 ? flags + p_ * 16 + rid : (GAS u64*)(ws + WS_BOX) + (cm * 10 + lane - 10 * dm) * 32 + (p_ - ((6 * cm) >> 3)) * 16 + rid;
        if (lane == 30 || (lane < 30 && 6 * cm < 8 * p_ + 8)) __hip_atomic_store(dst, fv, __ATOMIC_RELAXED, __HIP_MEMORY_SCOPE_AGENT);
      }
    }
    STAMP(5);
    if (!has2) {
      if (blockIdx.x == 255 && w == 0) {
#pragma nounroll
        for (unsigned spins = 0; spins < SPIN_CAP; spins++) {
          bool ok = true;
#pragma unroll
          for (int k = 0; k < 4; k++) ok &= __hip_atomic_load(flags + lane + 64 * k, __ATOMIC_RELAXED, __HIP_MEMORY_SCOPE_AGENT) == fv;
          if (__all(ok)) break;
          __builtin_amdgcn_s_sleep(8);
        }
        if (lane == 0) __hip_atomic_store((GAS unsigned*)ws, tag, __ATOMIC_RELAXED, __HIP_MEMORY_SCOPE_AGENT);
      }
      FINISH();
      return;
    }
    const int mh = w >> 2, g = lane >> 4, q = (lane >> 2) & 3, p = lane & 3;
    const int ks0 = kq == 0 ? 54 : 18 * (kq - 1);
    const u32x4 ra = mkr(ws + WS_A2, A2_BYTES);
    int ao[3];
    const int av = lane * 16;
#pragma unroll
    for (int m = 0; m < 3; m++) { int G = G0 + 3 * mh + m; G = G < 128 ? G : 127; if (ABL & 16) G = 3 * mh + m; ao[m] = (G * KS_T + ks0) * 1024; if (ABL & 2) ao[m] = 0x70000000; }
    int ba[4], bs[4];
#pragma unroll
    for (int c = 0; c < 4; c++) ba[c] = kq * 4096 + (4 * g + q) * 128 + ((c ^ ((2 * g + (q >> 1)) & 3)) << 5) + 8 * p;
    f32x4 acc[3][4];
#pragma unroll
    for (int m = 0; m < 3; m++)
#pragma unroll
      for (int c = 0; c < 4; c++) { acc[m][c] = f32x4{0, 0, 0, 0}; asm volatile("" : "+v"(acc[m][c])); }
    if (w == 0) {
      const int Gb = G0 + 5 < 128 ? G0 + 5 : 127;
      const int nfl = (Gb >> 3) != (G0 >> 3) ? 31 : 15;
      GAS u64* fp = (GAS u64*)(ws + WS_BOX) + (mt * 10 + n_) * 32 + (lane & nfl);
#pragma nounroll
      for (unsigned spins = 0; spins < SPIN_CAP; spins++) {
        if (__all(__hip_atomic_load(fp, __ATOMIC_RELAXED, __HIP_MEMORY_SCOPE_AGENT) == fv)) break;
        __builtin_amdgcn_s_sleep(1);
      }
    }
    STAMP(6);
    bar();
    STAMP(7);
    u32x4 a[3][3];
    int ro[6];
#pragma unroll
    for (int u = 0; u < 6; u++) ro[u] = (u + rot >= 6 ? u + rot - 6 : u + rot) * 1024;
#pragma unroll
    for (int m = 0; m < 3; m++) bload_sc1(a[0][m], ra, av, ao[m] + ro[0]);
    u32x2 b0[4][2], b1f[4][2];
#pragma unroll
    for (int c = 0; c < 4; c++) { dstr<0>(b0[c][0], ba[c]); dstr<2048>(b0[c][1], ba[c]); }
#define LG0 asm volatile("s_waitcnt lgkmcnt(0)" ::: "memory")
    if (ABL & 1) { for (int c = 0; c < 4; c++) { dstr<0>(b1f[c][0], ba[c]); dstr<2048>(b1f[c][1], ba[c]); } }
    LG0;
#pragma unroll
    for (int m = 0; m < 3; m++) bload_sc1(a[1][m], ra, av, ao[m] + ro[1]);
    auto step = [&](auto U, auto VV, u32x2 (&bc)[4][2], u32x2 (&bn)[4][2], int sl, int so) {
      constexpr int u = decltype(U)::value % 3, V = decltype(VV)::value;
      sfor<3>([&](auto M) {
        constexpr int m = decltype(M)::value;
        if (V < 0) bload_sc1(a[(u + 2) % 3][m], ra, av, ao[m] + so);
        if (V < 10) wait_vm<(V < 0 ? 6 : V < 10 ? V + 2 - m : 0)>();
        else
          asm volatile("s_cmp_lg_u32 %4, 0\n\ts_cbranch_scc1 .Lt%=\n\tbuffer_load_dwordx4 %0, %1, %2, %3 offen sc1\n\ts_waitcnt vmcnt(6)\n\ts_branch .Le%=\n"
                       ".Lt%=:\n\ts_waitcnt vmcnt(%5)\n.Le%=:"
                       : "=v"(a[(u + 2) % 3][m]) : "v"(av), "s"(ra), "s"(ao[m] + so), "s"(kq), "i"(V - 10 + 2 - m) : "memory", "scc");
#pragma unroll
        for (int c = 0; c < 4; c++) {
          if (!(ABL & 8)) mfma_a(acc[m][c], cat(bc[c][0], bc[c][1]), a[u][m]); else asm volatile("" :: "v"(bc[c][0]), "v"(bc[c][1]), "v"(a[u][m]));
          if (m < 2 && !(ABL & 1)) { if (c & 1) dstr<2048>(bn[2 * m + (c >> 1)][1], bs[2 * m + (c >> 1)]); else { vadd(bs[2 * m + (c >> 1)], ba[2 * m + (c >> 1)], sl); dstr<0>(bn[2 * m + (c >> 1)][0], bs[2 * m + (c >> 1)]); } }
        }
      });
    };
    STAMP(8);
#pragma nounroll
    for (int it = 0; it < 2; it++) {
      const int sb = it * 6 * 16384, sw = it * -3 * 16384, so = it * 6144;
      step(IC(0), IC(-1), b0, b1f, sb + 16384, so + ro[2]); LG0;
      step(IC(1), IC(-1), b1f, b0, sb + 2 * 16384, so + ro[3]); LG0;
      step(IC(2), IC(-1), b0, b1f, sw + 3 * 16384, so + ro[4]); bar_lgkm();
      if (it == 0) { STAMPB(12); } else { STAMPB(14); }
      step(IC(3), IC(-1), b1f, b0, sw + 4 * 16384, so + ro[5]); LG0;
      step(IC(4), IC(-1), b0, b1f, sw + 5 * 16384, so + 6144 + ro[0]); LG0;
      step(IC(5), IC(-1), b1f, b0, sw + 6 * 16384, so + 6144 + ro[1]); bar_lgkm();
      if (it == 0) { STAMPB(13); } else { STAMPB(15); }
    }
    step(IC(0), IC(-1), b0, b1f, 4 * 16384, 12288 + ro[2]); LG0;
    step(IC(1), IC(-1), b1f, b0, 5 * 16384, 12288 + ro[3]); LG0;
    step(IC(2), IC(-1), b0, b1f, 6 * 16384, 12288 + ro[4]); bar_lgkm();
    step(IC(3), IC(-1), b1f, b0, 7 * 16384, 12288 + ro[5]); LG0;
    if (kq == 0) __builtin_amdgcn_s_setprio(3);
    step(IC(4), IC(13), b0, b1f, 8 * 16384, 18 * 1024); LG0;
    step(IC(5), IC(10), b1f, b0, 147456, 19 * 1024); LG0;
    if (kq == 0) {
      step(IC(0), IC(3), b0, b1f, 151552, 0); LG0;
      step(IC(1), IC(0), b1f, b0, 151552, 0); LG0;
    }
    asm volatile("s_nop 7" ::: "memory");
    STAMP(9);
    const int l2 = lane_id(), i2 = l2 & 15, g2 = l2 >> 4;
#pragma unroll
    for (int m = 0; m < 3; m++)
#pragma unroll
      for (int c = 0; c < 4; c++) {
        asm volatile("" : "+v"(acc[m][c]));
        *(LAS f32x4*)(shm + (3 * mh + m) * 16384 + kq * 4096 + i2 * 256 + (((4 * c + g2) ^ (i2 & 7)) << 4)) = acc[m][c];
      }
  }
  {
    const int lane = lane_id();
    const int frow = 8 * w + (lane >> 3), fG = G0 + (frow >> 4);
    const bool fvalid = fG < 128;
    const int ftok = 32 * ((fvalid ? fG : 0) >> 1) + 2 * sig(frow & 15) + (fG & 1);
    const int foff = ftok * 1280 + n_ * 128 + (lane & 7) * 16;
    const u32x4 xv = *(const u32x4*)((const char*)x + foff);
    const u32x4 bv = *(const u32x4*)(b2 + n_ * 128 + (lane & 7) * 16);
    bar_lgkm();
    STAMP(10);
    f32x4 s[2], y[2];
#pragma unroll
    for (int e = 0; e < 2; e++) {
      lptr pr = shm + (frow >> 4) * 16384 + (frow & 15) * 256 + (((2 * (lane & 7) + e) ^ (frow & 7)) << 4);
      s[e] = *(LAS f32x4*)pr;
      y[e] = *(LAS f32x4*)(pr + 4096) + *(LAS f32x4*)(pr + 8192) + *(LAS f32x4*)(pr + 12288);
    }
    u32x4 o;
#pragma unroll
    for (int k = 0; k < 4; k++) {
      f32x2 yy = f32x2{y[k >> 1][2 * (k & 1)], y[k >> 1][2 * (k & 1) + 1]} + f32x2{blo(bv[k]), bhi(bv[k])};
      f32x2 t = yy * -1.4426950408889634f;
      f32x2 e = f32x2{__builtin_amdgcn_exp2f(t[0]), __builtin_amdgcn_exp2f(t[1])} + 1.f;
      f32x2 gt = yy * f32x2{__builtin_amdgcn_rcpf(e[0]), __builtin_amdgcn_rcpf(e[1])};
      f32x2 r = f32x2{s[k >> 1][2 * (k & 1)], s[k >> 1][2 * (k & 1) + 1]} * gt + f32x2{blo(xv[k]), bhi(xv[k])};
      o[k] = pk2(r);
    }
    if (fvalid) *(u32x4*)(out + foff) = o;
  }
  STAMP(11);
  FINISH();
}

extern "C" void kernel_launch(void* const* d_in, const int* in_sizes, int n_in, void* d_out, int out_size, void* d_ws,
                              size_t ws_size, hipStream_t stream) {
  (void)hipFuncSetAttribute((const void*)fused_block, hipFuncAttributeMaxDynamicSharedMemorySize, LDS_TOTAL);
  fused_block<<<256, 768, LDS_TOTAL, stream>>>(d_in[0], d_in[1], d_in[2], d_in[4], (const unsigned short*)d_in[5], d_in[6],
                                               (const char*)d_in[7], (char*)d_out, (char*)d_ws);
}
```

```cpp
#define PF_TLB 12
#define PF_MALL 1
#define INLOOP 0
#include <hip/hip_runtime.h>
#include <hip/hip_bf16.h>
#include <type_traits>
#include <utility>

typedef __attribute__((ext_vector_type(4))) short s16x4;
typedef __attribute__((ext_vector_type(8))) __bf16 bf16x8;
typedef __attribute__((ext_vector_type(2))) __bf16 bf16x2;
typedef __attribute__((ext_vector_type(4))) float f32x4;
typedef __attribute__((ext_vector_type(2))) float f32x2;
typedef __attribute__((ext_vector_type(4))) unsigned u32x4;
typedef __attribute__((ext_vector_type(2))) unsigned u32x2;
#define LAS __attribute__((address_space(3)))
#define GAS __attribute__((address_space(1)))
#define DEVI __device__ __forceinline__
typedef LAS char* lptr;
typedef unsigned long long u64;
#define IC(n) std::integral_constant<int, n>{}

constexpr int S_ = 2048, D_ = 640, F_ = 1728;
constexpr int KS_T = 74;
constexpr int WSLAB = 15360;
constexpr int XOFF = 2 * WSLAB;
constexpr int SLOT = 49152;
constexpr int LDS_TB = 86016;
constexpr int LDS_ST = 155648;
constexpr int LDS_CS = LDS_ST + 1152;
constexpr int LDS_B1 = LDS_CS + 1024;
constexpr int LDS_CNT = LDS_B1 + 1024;
constexpr int LDS_ZERO = LDS_CNT + 24;
constexpr int LDS_TAB = LDS_CNT + 64;
constexpr int LDS_TOTAL = 163840;
constexpr int WS_FLAG = 4096, WS_STAT = 8192, WS_BOX = 65536, WS_A2 = 1 << 20;
constexpr int A2_BYTES = 128 * KS_T * 1024;
#define PF_CODE 1
#define MAP2 0
#define S1MODE 0
#define S2MODE 0
#define THR 4
#define PSL 4
#define ABL 0
#define ROT1 0
#define ROT2 0
constexpr int CODE_BYTES = 29056;
constexpr unsigned SPIN_CAP = 1u << 20;

#define STAMP(idx)
#define FINISH()
#define STAMPA(k) STAMP(k)
#define STAMPB(k)
#define PR_ARGS
#define PR_PASS

DEVI void bar_lgkm() { asm volatile("s_waitcnt lgkmcnt(0)\n\ts_barrier" ::: "memory"); }
DEVI void bar() { asm volatile("s_barrier" ::: "memory"); }
template <int N> DEVI void bar_vm() { asm volatile("s_waitcnt vmcnt(%0)\n\ts_barrier" ::"i"(N) : "memory"); }
template <int N> DEVI void wait_vm() { asm volatile("s_waitcnt vmcnt(%0)" ::"i"(N) : "memory"); }
DEVI unsigned pk2(float a, float b) { f32x2 v = {a, b}; return __builtin_bit_cast(unsigned, __builtin_convertvector(v, bf16x2)); }
DEVI unsigned pk2(f32x2 v) { return __builtin_bit_cast(unsigned, __builtin_convertvector(v, bf16x2)); }
DEVI float asf(unsigned u) { return __builtin_bit_cast(float, u); }
DEVI unsigned asu(float f) { return __builtin_bit_cast(unsigned, f); }
DEVI float blo(unsigned u) { return __builtin_bit_cast(float, u << 16); }
DEVI float bhi(unsigned u) { return __builtin_bit_cast(float, u & 0xffff0000u); }
DEVI u32x2 tr_read(lptr p) { return __builtin_bit_cast(u32x2, __builtin_amdgcn_ds_read_tr16_b64_v4i16((LAS s16x4*)p)); }
DEVI f32x4 mfma(u32x4 a, u32x4 b, f32x4 c) {
  return __builtin_amdgcn_mfma_f32_16x16x32_bf16(__builtin_bit_cast(bf16x8, a), __builtin_bit_cast(bf16x8, b), c, 0, 0, 0);
}
DEVI u32x4 cat(u32x2 a, u32x2 b) { return __builtin_shufflevector(a, b, 0, 1, 2, 3); }
DEVI void mfma_a(f32x4& c, u32x4 a, u32x4 b) { asm volatile("v_mfma_f32_16x16x32_bf16 %0, %1, %2, %0" : "+v"(c) : "v"(a), "v"(b)); }
template <int OFF> DEVI void ds128(u32x4& d, int addr) { asm volatile("ds_read_b128 %0, %1 offset:%2" : "=v"(d) : "v"(addr), "i"(OFF)); }
template <int OFF> DEVI void dstr(u32x2& d, int addr) { asm volatile("ds_read_b64_tr_b16 %0, %1 offset:%2" : "=v"(d) : "v"(addr), "i"(OFF)); }
template <int OFF> DEVI void dsw128(int addr, u32x4 d) { asm volatile("ds_write_b128 %0, %1 offset:%2" ::"v"(addr), "v"(d), "i"(OFF) : "memory"); }
DEVI u32x4 mkr(const void* p, unsigned bytes) {
  u64 a = (u64)p;
  return u32x4{(unsigned)a, (unsigned)(a >> 32) & 0xffffu, bytes, 0x00020000u};
}
DEVI void st_wt(u32x4 r, int voff, int soff, u32x4 v) {
  asm volatile("buffer_store_dwordx4 %0, %1, %2, %3 offen sc1\n\ts_nop 1" ::"v"(v), "v"(voff), "s"(r), "s"(soff) : "memory");
}
template <int LSTEP>
DEVI void dma4(u32x4 r, u64 mask, int l, int va, int vb, int s0, int s1, int s2, int s3) {
  asm volatile(
      "s_mov_b64 exec, %0\n\t"
      "s_mov_b32 m0, %1\n\ts_nop 0\n\tbuffer_load_dwordx4 %2, %4, %5 offen lds\n\t"
      "s_add_u32 m0, %1, %9\n\ts_nop 0\n\tbuffer_load_dwordx4 %3, %4, %6 offen lds\n\t"
      "s_add_u32 m0, %1, %10\n\ts_nop 0\n\tbuffer_load_dwordx4 %2, %4, %7 offen lds\n\t"
      "s_add_u32 m0, %1, %11\n\ts_nop 0\n\tbuffer_load_dwordx4 %3, %4, %8 offen lds\n\t"
      "s_mov_b64 exec, -1"
      :: "s"(mask), "s"(l), "v"(va), "v"(vb), "s"(r), "s"(s0), "s"(s1), "s"(s2), "s"(s3), "i"(LSTEP), "i"(2 * LSTEP), "i"(3 * LSTEP)
      : "memory", "scc");
}
DEVI void dma1(u32x4 r, u64 mask, int l, int v, int s0) {
  asm volatile("s_mov_b64 exec, %0\n\ts_mov_b32 m0, %1\n\ts_nop 0\n\tbuffer_load_dwordx4 %2, %3, %4 offen lds\n\ts_mov_b64 exec, -1"
               :: "s"(mask), "s"(l), "v"(v), "s"(r), "s"(s0) : "memory");
}
template <int WHEN>
DEVI void bload4_if(u32x4 (&d)[4], u32x4 r, int v, int s0, int s1, int s2, int s3, int flag) {
#define LDR_BODY "s_cmp_lg_u32 %10, %11\n\ts_cbranch_scc1 .Lr%=\n\tbuffer_load_dwordx4 %0, %4, %5, %6 offen\n\tbuffer_load_dwordx4 %1, %4, %5, %7 offen\n\t" \
                 "buffer_load_dwordx4 %2, %4, %5, %8 offen\n\tbuffer_load_dwordx4 %3, %4, %5, %9 offen\n.Lr%=:"
  if (WHEN) asm volatile(LDR_BODY : "+v"(d[0]), "+v"(d[1]), "+v"(d[2]), "+v"(d[3]) : "v"(v), "s"(r), "s"(s0), "s"(s1), "s"(s2), "s"(s3), "s"(flag), "i"(WHEN) : "scc");
  else asm volatile(LDR_BODY : "=&v"(d[0]), "=&v"(d[1]), "=&v"(d[2]), "=&v"(d[3]) : "v"(v), "s"(r), "s"(s0), "s"(s1), "s"(s2), "s"(s3), "s"(flag), "i"(WHEN) : "scc");
}
template <int N0, int N1>
DEVI void bar_vm_sel(int flag) {
  asm volatile("s_cmp_lg_u32 %0, 0\n\ts_cbranch_scc1 .La%=\n\ts_waitcnt vmcnt(%1)\n\ts_branch .Lb%=\n.La%=:\n\ts_waitcnt vmcnt(%2)\n.Lb%=:\n\ts_barrier"
               ::"s"(flag), "i"(N0), "i"(N1) : "memory", "scc");
}
template <class T> DEVI void pin(T& v) { asm volatile("" : "+v"(v)); }
DEVI void dsw_col0(int a0, f32x4 d0, int a1, f32x4 d1, int a2, f32x4 d2) {
  asm volatile("s_mov_b64 exec, %6\n\tds_write_b128 %0, %1\n\tds_write_b128 %2, %3\n\tds_write_b128 %4, %5\n\ts_mov_b64 exec, -1"
               ::"v"(a0), "v"(d0), "v"(a1), "v"(d1), "v"(a2), "v"(d2), "s"(0x0001000100010001ull) : "memory");
}
DEVI void vadd(int& d, int a, int s) { asm volatile("v_add_u32 %0, %2, %1" : "=v"(d) : "v"(a), "s"(s)); }
DEVI void touch(u32x4 r, int v) {
  asm volatile("s_mov_b32 m0, %2\n\ts_nop 0\n\tbuffer_load_dword %0, %1, 0 offen lds" ::"v"(v), "s"(r), "i"(LDS_TOTAL - 256) : "memory");
}
DEVI void bload(u32x4& d, u32x4 r, int v, int s0) { asm volatile("buffer_load_dwordx4 %0, %1, %2, %3 offen" : "=v"(d) : "v"(v), "s"(r), "s"(s0)); }
DEVI void bload_sc1(u32x4& d, u32x4 r, int v, int s0) { if (ABL & 4) { bload(d, r, v, s0); return; } asm volatile("buffer_load_dwordx4 %0, %1, %2, %3 offen sc1" : "=v"(d) : "v"(v), "s"(r), "s"(s0) : "memory"); }
DEVI void arrive(int k) {
  asm volatile("s_mov_b64 exec, 1\n\tds_add_u32 %0, %1\n\ts_mov_b64 exec, -1" ::"v"(LDS_CNT + 4 * k), "v"(1) : "memory");
}
template <int SLEEP, unsigned N>
DEVI void await(int k) {
  unsigned v;
  for (;;) {
    asm volatile("ds_read_b32 %0, %1\n\ts_waitcnt lgkmcnt(0)" : "=v"(v) : "v"(LDS_CNT + 4 * k) : "memory");
    if (__builtin_amdgcn_readfirstlane(v) >= N) break;
    if (SLEEP) __builtin_amdgcn_s_sleep(SLEEP);
  }
}
DEVI int lane_id() { int l; asm volatile("v_mbcnt_lo_u32_b32 %0, -1, 0\n\tv_mbcnt_hi_u32_b32 %0, -1, %0" : "=v"(l)); return l; }
DEVI int sig(int i) { return 4 * (i & 3) + (i >> 2); }
DEVI u64 flagval(unsigned tag) { return ((u64)(tag ^ 0x5bd1e995u) << 32) | tag; }

DEVI float xmax16(float v) { auto r = __builtin_amdgcn_permlane16_swap(asu(v), asu(v), false, false); return fmaxf(asf(r[0]), asf(r[1])); }
DEVI float xmax32(float v) { auto r = __builtin_amdgcn_permlane32_swap(asu(v), asu(v), false, false); return fmaxf(asf(r[0]), asf(r[1])); }
DEVI float xadd16(float v) { auto r = __builtin_amdgcn_permlane16_swap(asu(v), asu(v), false, false); return asf(r[0]) + asf(r[1]); }
DEVI float xadd32(float v) { auto r = __builtin_amdgcn_permlane32_swap(asu(v), asu(v), false, false); return asf(r[0]) + asf(r[1]); }
template <int CTRL> DEVI float dpp(float v) { return asf(__builtin_amdgcn_mov_dpp(asu(v), CTRL, 0xf, 0xf, true)); }
DEVI float wave_sum(float v) {
  v += dpp<0xb1>(v); v += dpp<0x4e>(v); v += dpp<0x141>(v); v += dpp<0x140>(v);
  return xadd32(xadd16(v));
}

DEVI int row_token(int p_, int pos) {
  const int tau = pos >> 4, i = pos & 15;
  int tok = tau == 0 ? p_ * 128 - 16 + 8 * (i & 1) + 2 * (i >> 2) + ((i & 3) < 2) : p_ * 128 + 32 * ((tau - 1) >> 1) + 2 * sig(i) + ((tau - 1) & 1);
  return tok < 0 ? 0 : tok;
}

struct ColGeo { int A, B, C, ct; };
DEVI ColGeo colgeo(bool att, int nw, int mw, int c, bool heavy) {
  const int cc = heavy ? (c ^ (2 * mw)) : c;
  const int ct = 4 * nw + cc, sec = (ct * 13) >> 6, hh = ct - 5 * sec;
  const bool q4 = hh == 4, sp = !heavy && c == 2;
  ColGeo r;
  r.A = att ? sec * 160 + (q4 ? 32 : 40 * hh) : heavy ? 128 * nw + 32 * cc : (sp ? 352 + 16 * nw : 128 + 64 * nw + 32 * c);
  r.B = att && q4 ? 40 : 8;
  r.C = att ? (q4 ? 80 : 16) : (sp ? 32 : 16);
  r.ct = ct;
  return r;
}

template <int NC>
struct Frags { u32x4 x[5]; u32x2 w[NC][2]; };

template <class F, int... I> DEVI void sfor_(F f, std::integer_sequence<int, I...>) { (f(std::integral_constant<int, I>{}), ...); }
template <int N, class F> DEVI void sfor(F f) { sfor_(f, std::make_integer_sequence<int, N>{}); }
template <int N> DEVI void wait_lg() { asm volatile("s_waitcnt lgkmcnt(%0)" ::"i"(N) : "memory"); }

template <int NC, int K>
DEVI void s1_rd(Frags<NC>& n, int xo, int xh, const int (&wa)[NC]) {
  if constexpr (K == 0) ds128<0>(n.x[0], xo);
  else if constexpr (K <= 2 * NC) { if ((K - 1) & 1) dstr<7680>(n.w[(K - 1) >> 1][1], wa[(K - 1) >> 1]); else dstr<0>(n.w[(K - 1) >> 1][0], wa[(K - 1) >> 1]); }
  else if constexpr (K < 2 * NC + 4) ds128<(K - 2 * NC) * 2048>(n.x[K - 2 * NC], xo);
  else if constexpr (K == 2 * NC + 4) ds128<0>(n.x[4], xh);
}
template <int NC, int NH, int K>
DEVI void s1_mm(const Frags<NC>& c, f32x4 (&acc)[4][NC], f32x4 (&hacc)[NH]) {
  if constexpr (K < 4 * NC) mfma_a(acc[K / NC][K % NC], cat(c.w[K % NC][0], c.w[K % NC][1]), c.x[K / NC]);
  else mfma_a(hacc[K - 4 * NC], cat(c.w[K - 4 * NC][0], c.w[K - 4 * NC][1]), c.x[4]);
}
template <int NC, int NH, bool BAR>
DEVI void s1_step(const Frags<NC>& c, Frags<NC>& n, int xo, int xh, const int (&wa)[NC], f32x4 (&acc)[4][NC], f32x4 (&hacc)[NH]) {
  constexpr int NM = 4 * NC + NH, NR = 2 * NC + 5, D = BAR ? NM - NR : 0;
  sfor<NM>([&](auto KK) {
    constexpr int k = decltype(KK)::value;
    constexpr int need = k < NC ? 2 * k + 2 : (k % NC == 0 && k <= 4 * NC) ? 2 * NC + k / NC : -1;
    if constexpr (BAR && k == D) bar_lgkm();
    if constexpr (need >= 0 && (!BAR || k < D)) wait_lg<NR - 1 - need + (BAR ? 0 : (k < NR ? k : NR))>();
    s1_mm<NC, NH, k>(c, acc, hacc);
    if constexpr (k >= D) s1_rd<NC, k - D>(n, xo, xh, wa);
  });
}

template <int OFF> DEVI void dsw64(int addr, u32x2 d) { asm volatile("ds_write_b64 %0, %1 offset:%2" ::"v"(addr), "v"(d), "i"(OFF) : "memory"); }
template <int OFF> DEVI void ds64(u32x2& d, int addr) { asm volatile("ds_read_b64 %0, %1 offset:%2" : "=v"(d) : "v"(addr), "i"(OFF)); }

template <int NC, class PRE>
DEVI void s1_compute(lptr shm, bool att, int w, int lane, int p_, int j_, char* ws, PRE pre PR_ARGS) {
  constexpr int NH = NC == 4 ? 2 : 3;
  const int mw = w & 1, nw = w >> 1, i = lane & 15, g = lane >> 4;
  f32x4 acc[4][NC], hacc[NH];
#pragma unroll
  for (int m = 0; m < 4; m++)
#pragma unroll
    for (int c = 0; c < NC; c++) { acc[m][c] = f32x4{0, 0, 0, 0}; asm volatile("" : "+v"(acc[m][c])); }
#pragma unroll
  for (int c = 0; c < NH; c++) { hacc[c] = f32x4{0, 0, 0, 0}; asm volatile("" : "+v"(hacc[c])); }
  int wb[NC], cb[NC], ct[NC];
#pragma unroll
  for (int c = 0; c < NC; c++) {
    const ColGeo cg = colgeo(att, nw, mw, c, NC == 4);
    wb[c] = (lane >> 2) * 480 + cg.A + cg.B * (lane & 1) + cg.C * ((lane >> 1) & 1);
    cb[c] = LDS_CS + 2 * (cg.A + cg.B * (g & 1) + cg.C * (g >> 1));
    ct[c] = cg.ct;
    asm volatile("" : "+v"(cb[c]), "+v"(wb[c]));
  }
  int xb = XOFF + i * 128 + ((g ^ (i >> 1)) << 4);
  int sta = LDS_ST + i * 8;
  asm volatile("" : "+v"(xb), "+v"(sta));
  const int own = (1 + 4 * mw) * 2048;
  Frags<NC> fa, fb;
  int wa[NC];
  pre();
  bar_lgkm();
  STAMP(1);
  sfor<2 * NC + 5>([&](auto K) { s1_rd<NC, decltype(K)::value>(fa, xb + own, xb, wb); });
#pragma unroll
  for (int c = 0; c < NC; c++) wa[c] = wb[c] + WSLAB;
  s1_step<NC, NH, false>(fa, fb, (xb ^ 64) + own, xb ^ 64, wa, acc, hacc);
  int sl = 0;
#pragma nounroll
  for (int T = 1; T < 10; T++) {
    sl = sl == 2 * SLOT ? 0 : sl + SLOT;
#pragma unroll
    for (int c = 0; c < NC; c++) wa[c] = wb[c] + sl;
    s1_step<NC, NH, true>(fb, fa, xb + sl + own, xb + sl, wa, acc, hacc);
#pragma unroll
    for (int c = 0; c < NC; c++) wa[c] = wb[c] + sl + WSLAB;
    s1_step<NC, NH, false>(fa, fb, (xb ^ 64) + sl + own, (xb ^ 64) + sl, wa, acc, hacc);
  }
  wait_lg<0>();
  arrive(2);
  sfor<NH>([&](auto K) { s1_mm<NC, NH, 4 * NC + decltype(K)::value>(fb, acc, hacc); });
  u32x2 stu[5];
  u32x4 b1u[NC], csu[NC];
  sfor<4 * NC>([&](auto KK) {
    constexpr int k = decltype(KK)::value;
    s1_mm<NC, NH, k>(fb, acc, hacc);
    if constexpr (k < 4) ds64<(1 + k) * 128>(stu[k], sta + mw * 512);
    if constexpr (k == 4) ds64<0>(stu[4], sta);
    if constexpr (k == 5) {
      dsw_col0(cb[0], hacc[0], cb[1], hacc[1], cb[NH - 1], hacc[NH - 1]);
      if (NC == 4) arrive(8 + nw);
    }
    if constexpr (k >= 6 && k < 6 + NC) ds128<LDS_B1 - LDS_CS>(b1u[k - 6], cb[k - 6]);
  });
  STAMP(2);
  if (NC == 4) await<0, 2>(8 + nw);
#pragma unroll
  for (int c = 0; c < NC; c++) ds128<0>(csu[c], cb[c]);
  asm volatile("s_waitcnt lgkmcnt(0)\n\ts_nop 7" ::: "memory");
  f32x2 st[5];
  f32x4 cs[NC], b1[NC];
#pragma unroll
  for (int m = 0; m < 5; m++) { asm volatile("" : "+v"(stu[m])); st[m] = __builtin_bit_cast(f32x2, stu[m]); }
#pragma unroll
  for (int c = 0; c < NC; c++) { asm volatile("" : "+v"(csu[c]), "+v"(b1u[c])); cs[c] = __builtin_bit_cast(f32x4, csu[c]); b1[c] = __builtin_bit_cast(f32x4, b1u[c]); }
#pragma unroll
  for (int m = 0; m < 4; m++)
#pragma unroll
    for (int c = 0; c < NC; c++) pin(acc[m][c]);
#pragma unroll
  for (int c = 0; c < NH; c++) pin(hacc[c]);
  STAMPA(12);
#define SB __builtin_amdgcn_sched_barrier(0)
  const int l2 = lane_id();
  if (att) {
    const int g2 = l2 >> 4, piece = LDS_TB + ((l2 & 48) + ((l2 & 15) ^ (8 * (g2 >> 1)))) * 8;
    sfor<NC>([&](auto C) {
      constexpr int c = decltype(C)::value;
      const int ah = piece + ct[c] * 4608, ao = ah + (1 + 4 * mw) * 512;
      u32x2 o[5];
      SB;
#pragma unroll
      for (int m = 0; m < (c < NH ? 5 : 4); m++) {
        f32x4 a = m < 4 ? acc[m < 4 ? m : 0][c] : hacc[c < NH ? c : 0];
        f32x4 v = a * st[m][0] + cs[c] * st[m][1];
        o[m] = u32x2{pk2(v[0], v[1]), pk2(v[2], v[3])};
      }
      dsw64<0>(ao, o[0]); dsw64<512>(ao, o[1]); dsw64<1024>(ao, o[2]); dsw64<1536>(ao, o[3]);
      if (c < NH) dsw64<0>(ah, o[4]);
    });
  } else {
    const u32x4 ra = mkr(ws + WS_A2, A2_BYTES);
    const int base = ((8 * p_ + 4 * mw) * KS_T + 7 * j_) * 1024;
    const int o0 = base + (NC == 4 ? (2 * nw + mw) * 1024 : (j_ < 7 || nw == 2) ? (2 + nw) * 1024 : 0x70000000);
    const int o1 = base + (NC == 4 ? (2 * nw + (mw ^ 1)) * 1024 : j_ < 7 ? 6 * 1024 + (nw - 2) * 512 : 0x70000000);
    const float c1 = -2.f * 0.7978845608028654f * 1.4426950408889634f, c2 = c1 * 0.044715f;
    unsigned sp[2];
#pragma unroll
    for (int m = 0; m < 4; m++) {
      f32x4 v[NC], t[NC];
      SB;
#pragma unroll
      for (int c = 0; c < NC; c++) t[c] = cs[c] * st[m][1] + b1[c];
      SB;
#pragma unroll
      for (int c = 0; c < NC; c++) v[c] = acc[m][c] * st[m][0] + t[c];
      SB;
#pragma unroll
      for (int c = 0; c < NC; c++) t[c] = v[c] * v[c];
      SB;
#pragma unroll
      for (int c = 0; c < NC; c++) t[c] = t[c] * c2 + c1;
      SB;
#pragma unroll
      for (int c = 0; c < NC; c++) t[c] = t[c] * v[c];
      SB;
#pragma unroll
      for (int c = 0; c < NC; c++)
#pragma unroll
        for (int e = 0; e < 4; e++) t[c][e] = __builtin_amdgcn_exp2f(t[c][e]);
      SB;
#pragma unroll
      for (int c = 0; c < NC; c++) t[c] = t[c] + 1.f;
      SB;
#pragma unroll
      for (int c = 0; c < NC; c++)
#pragma unroll
        for (int e = 0; e < 4; e++) t[c][e] = __builtin_amdgcn_rcpf(t[c][e]);
      SB;
#pragma unroll
      for (int c = 0; c < NC; c++) v[c] = v[c] * t[c];
      SB;
      unsigned o[NC][2];
#pragma unroll
      for (int c = 0; c < NC; c++) { o[c][0] = pk2(v[c][0], v[c][1]); o[c][1] = pk2(v[c][2], v[c][3]); }
      st_wt(ra, l2 * 16, o0 + m * KS_T * 1024, u32x4{o[0][0], o[0][1], o[1][0], o[1][1]});
      if (NC == 4) st_wt(ra, l2 * 16, o1 + m * KS_T * 1024, u32x4{o[2][0], o[2][1], o[NC - 1][0], o[NC - 1][1]});
      else if (m & 1) {
        auto s0 = __builtin_amdgcn_permlane32_swap(sp[0], o[2][0], false, false);
        auto s1 = __builtin_amdgcn_permlane32_swap(sp[1], o[2][1], false, false);
        st_wt(ra, (l2 >> 5) * (KS_T * 1024) + (l2 & 31) * 16, o1 + (m - 1) * KS_T * 1024, u32x4{s0[0], s1[0], s0[1], s1[1]});
      } else { sp[0] = o[2][0]; sp[1] = o[2][1]; }
    }
  }
}

template <bool MAX> DEVI float red4(const float (&v)[4]) {
  auto op = [](unsigned a, unsigned b) { float r; if (MAX) asm("v_max_f32 %0, %1, %2" : "=v"(r) : "v"(a), "v"(b)); else r = asf(a) + asf(b); return r; };
  auto a = __builtin_amdgcn_permlane32_swap(asu(v[0]), asu(v[1]), false, false);
  auto b = __builtin_amdgcn_permlane32_swap(asu(v[2]), asu(v[3]), false, false);
  auto c = __builtin_amdgcn_permlane16_swap(asu(op(a[0], a[1])), asu(op(b[0], b[1])), false, false);
  return op(c[0], c[1]);
}
DEVI void bcast4(float r, float (&v)[4]) {
  auto e = __builtin_amdgcn_permlane16_swap(asu(r), asu(r), false, false);
  unsigned e0 = e[0], e1 = e[1];
  auto a = __builtin_amdgcn_permlane32_swap(e0, e0, false, false);
  auto b = __builtin_amdgcn_permlane32_swap(e1, e1, false, false);
  v[0] = asf(a[0]); v[1] = asf(a[1]); v[2] = asf(b[0]); v[3] = asf(b[1]);
}

DEVI void att_table(lptr shm, int lane) {
  const int i = lane & 15, g = lane >> 4, p = lane & 3;
  const int d = sig(i) - g;
  const unsigned NEG = 0xf149f2cau;
  bool vo[4], vp[4];
#pragma unroll
  for (int r = 0; r < 4; r++) { vo[r] = (unsigned)(d - 4 * r) <= 7u; vp[r] = d + 16 - 4 * r <= 7; }
  auto pm = [](bool lo, bool hi) { return (lo ? 0xffffu : 0u) | (hi ? 0xffff0000u : 0u); };
  LAS u32x4* t = (LAS u32x4*)(shm + LDS_TAB) + lane;
  t[0] = u32x4{vo[0] ? 0u : NEG, vo[1] ? 0u : NEG, vo[2] ? 0u : NEG, vo[3] ? 0u : NEG};
  t[64] = u32x4{pm(vp[2], vp[3]), pm(vo[0], vo[1]), vp[2] ? 0u : NEG, vp[3] ? 0u : NEG};
  t[128] = u32x4{pm(vo[2], vo[3]), (unsigned)(LDS_TB + (g * 16 + (i ^ (8 * (g >> 1)))) * 8), (unsigned)(LDS_TB + (p * 16 + ((lane >> 2) ^ (8 * (p >> 1)))) * 8), 0u};
}
DEVI int sel(u64 mask, int a, int b) { int r; asm("v_cndmask_b32_e64 %0, %2, %1, %3" : "=v"(r) : "v"(a), "v"(b), "s"(mask)); return r; }

DEVI void s1_attention(lptr shm, int w, int p_, int j_, char* ws PR_ARGS) {
  arrive(3);
  const int lane = lane_id();
  const int tau = (w + 1) * 512, taup = w >= 2 ? tau - 1024 : 0, flip = w == 1 ? 16 : 0;
  const LAS u32x4* t = (const LAS u32x4*)(shm + LDS_TAB) + lane;
  const u32x4 t0 = t[0], t2 = t[128];
  u32x4 t1 = t[64];
  if (p_ == 0 && w < 2) { t1[0] = 0u; t1[2] = 0xf149f2cau; t1[3] = 0xf149f2cau; }
  const f32x4 mO = __builtin_bit_cast(f32x4, t0), mP = __builtin_bit_cast(f32x4, t1);
  const unsigned M1 = t1[0], M2 = t1[1], M3 = t2[0];
  const int po = t2[1] + tau, pp = (t2[1] ^ flip) + taup, to = t2[2] + tau, tp = (t2[2] ^ flip) + taup;
  int aq[4], avo[4], avp[4];
#pragma unroll
  for (int h = 0; h < 4; h++) {
    aq[h] = sel(0xffffull << (16 * h), po, LDS_ZERO - 4 * 4608);
    avo[h] = sel(0x1111111111111111ull << h, to, LDS_ZERO - 14 * 4608);
    avp[h] = sel(0x1111111111111111ull << h, tp, LDS_ZERO - 14 * 4608);
    asm volatile("" : "+v"(aq[h]), "+v"(avo[h]), "+v"(avp[h]));
  }
  STAMPA(14);
  await<0, 8>(3);
  STAMPA(15);
  u32x2 qa[4][2], ko[4][2], kp[4][2], va[4][2], vb[4][2];
  f32x4 sp[4], so[4];
  auto rd = [&](auto H) {
    constexpr int h = decltype(H)::value;
    ds64<(5 + h) * 4608>(ko[h][0], po); ds64<9 * 4608>(ko[h][1], po); ds64<h * 4608>(qa[h][0], po); ds64<4 * 4608>(qa[h][1], aq[h]);
    ds64<(5 + h) * 4608>(kp[h][0], pp); ds64<9 * 4608>(kp[h][1], pp);
  };
  auto rv = [&](auto H) {
    constexpr int h = decltype(H)::value;
    dstr<(10 + h) * 4608>(va[h][0], tp); dstr<(10 + h) * 4608>(va[h][1], to); dstr<14 * 4608>(vb[h][0], avp[h]); dstr<14 * 4608>(vb[h][1], avo[h]);
  };
  auto qk = [&](auto H, auto N) {
    constexpr int h = decltype(H)::value;
    asm volatile("s_waitcnt lgkmcnt(%6)" : "+v"(ko[h][0]), "+v"(ko[h][1]), "+v"(qa[h][0]), "+v"(qa[h][1]), "+v"(kp[h][0]), "+v"(kp[h][1]) : "i"(decltype(N)::value));
    so[h] = mfma(cat(ko[h][0], ko[h][1]), cat(qa[h][0], qa[h][1]), mO);
    sp[h] = mfma(cat(kp[h][0], kp[h][1]), cat(qa[h][0], qa[h][1]), mP);
  };
  rd(IC(0)); rd(IC(1));
  qk(IC(0), IC(6)); rd(IC(2));
  qk(IC(1), IC(6)); rd(IC(3));
  qk(IC(2), IC(6)); rv(IC(0)); rv(IC(1));
  qk(IC(3), IC(8)); rv(IC(2));
  asm volatile("s_waitcnt lgkmcnt(8)");
  rv(IC(3));
  const float qs = 0.22360679774997896f * 1.4426950408889634f;
  float ee[4], eo[4], mx[4], l[4];
#pragma unroll
  for (int h = 0; h < 4; h++) { ee[h] = fmaxf(fmaxf(sp[h][2], so[h][0]), so[h][2]); eo[h] = fmaxf(fmaxf(sp[h][3], so[h][1]), so[h][3]); mx[h] = fmaxf(ee[h], eo[h]); }
  bcast4(red4<true>(mx) * -qs, mx);
#pragma unroll
  for (int h = 0; h < 4; h++) {
    f32x2 t = f32x2{ee[h], eo[h]} * qs + mx[h];
    ee[h] = __builtin_amdgcn_exp2f(t[0]); eo[h] = __builtin_amdgcn_exp2f(t[1]); l[h] = ee[h] + eo[h];
  }
  bcast4(__builtin_amdgcn_rcpf(red4<false>(l)), l);
  asm volatile("s_waitcnt lgkmcnt(0)" : "+v"(va[0][0]), "+v"(va[0][1]), "+v"(va[1][0]), "+v"(va[1][1]), "+v"(va[2][0]), "+v"(va[2][1]), "+v"(va[3][0]), "+v"(va[3][1]),
               "+v"(vb[0][0]), "+v"(vb[0][1]), "+v"(vb[1][0]), "+v"(vb[1][1]), "+v"(vb[2][0]), "+v"(vb[2][1]), "+v"(vb[3][0]), "+v"(vb[3][1]));
  f32x4 o4 = {0, 0, 0, 0}, oh[4];
#pragma unroll
  for (int h = 0; h < 4; h++) {
    const unsigned pk = pk2(f32x2{ee[h], eo[h]} * l[h]);
    u32x4 pf = {0u, pk & M1, pk & M2, pk & M3};
    o4 = mfma(cat(vb[h][0], vb[h][1]), pf, o4);
    oh[h] = mfma(cat(va[h][0], va[h][1]), pf, f32x4{0, 0, 0, 0});
  }
  const u32x4 ra = mkr(ws + WS_A2, A2_BYTES);
  const int base = ((8 * p_ + w) * KS_T + 54 + 5 * (j_ >> 1)) * 1024;
#pragma unroll
  for (int hp = 0; hp < 2; hp++)
    st_wt(ra, lane * 16, base + ((j_ & 1) * 3 + hp) * 1024,
          u32x4{pk2(oh[2 * hp][0], oh[2 * hp][1]), pk2(oh[2 * hp][2], oh[2 * hp][3]), pk2(oh[2 * hp + 1][0], oh[2 * hp + 1][1]), pk2(oh[2 * hp + 1][2], oh[2 * hp + 1][3])});
  unsigned a0 = pk2(o4[0], o4[1]), a1 = pk2(o4[2], o4[3]);
  auto s0 = __builtin_amdgcn_permlane32_swap(a0, a0, false, false);
  auto s1 = __builtin_amdgcn_permlane32_swap(a1, a1, false, false);
  st_wt(ra, lane < 32 ? lane * 16 : 0x70000000, base + 2 * 1024 + (j_ & 1) * 512, u32x4{s0[0], s1[0], s0[1], s1[1]});
  arrive(4);
}

__global__ __launch_bounds__(768, 3) void fused_block(const void* __restrict__ x, const void* __restrict__ wqkv, const void* __restrict__ wo,
                                                      const void* __restrict__ w1, const unsigned short* __restrict__ b1,
                                                      const void* __restrict__ w2, const char* __restrict__ b2, char* __restrict__ out,
                                                      char* ws) {
  extern __shared__ __attribute__((aligned(16))) char shm_[];
  lptr shm = (lptr)shm_;
  asm volatile("" : "+s"(x), "+s"(wqkv), "+s"(wo), "+s"(w1), "+s"(b1), "+s"(w2), "+s"(b2), "+s"(out), "+s"(ws));
  const int tid = threadIdx.x, lane = tid & 63, w = __builtin_amdgcn_readfirstlane(tid >> 6);
  const int xcd = blockIdx.x & 7, idx = blockIdx.x >> 3;
  const int p_ = (xcd & 1) * 8 + (idx & 7), sub = idx >> 3;
  const bool att = !(sub & 1);
  const int j_ = (xcd >> 1) * 2 + (sub >> 1);
  const int unit = (xcd >> 1) * 28 + idx;
  const bool has2 = idx < 28 && unit < 110;
  const int mt = unit / 5, nl = unit - 5 * mt, n_ = (xcd & 1) * 5 + nl;
  const int G0 = 6 * mt;
  const int kq = w < 8 ? (w + (w >> 2)) & 3 : w & 3;
  const int rot = ROT2 ? (nl + mt) % 6 : 0;
  STAMP(0);
  if (w == 7) {
    u64 pc; asm volatile("s_getpc_b64 %0" : "=s"(pc));
    touch(mkr((const void*)(pc & ~255ull), CODE_BYTES), (idx + 32 * lane) * 128);
  }
  if (w == 6) {
    const int o = (idx + 32 * lane) << PF_TLB;
    touch(mkr(wqkv, D_ * 3840), o); touch(mkr(w1, D_ * 3456), o);
  }
  if (w >= 8) {
    const int lw = w - 8;
    int kt0 = ROT1 == 1 ? (((idx & 7) * 5) >> 2) + ((sub * 5 + 1) >> 1) : ROT1 == 2 ? xcd : 0;
    kt0 = kt0 >= 10 ? kt0 - 10 : kt0;
    const int RS = att ? 3840 : 3456;
    const u32x4 rx = mkr(x, S_ * D_ * 2), rw1 = mkr(att ? wqkv : w1, D_ * RS);
    const int r8 = lane >> 3, cp = lane & 7, t0 = p_ * 128;
    int xv[2], hv, wv;
#pragma unroll
    for (int hf = 0; hf < 2; hf++)
      xv[hf] = (t0 + 32 * lw + 2 * (4 * (r8 & 3) + 2 * hf + (r8 >> 2))) * 1280 + ((cp ^ (4 * hf + (r8 >> 1))) << 4);
    auto ldx = [&](int T, int slot) { dma4<1024>(rx, ~0ull, slot * SLOT + XOFF + 2048 + 4 * lw * 1024, xv[0], xv[1], T * 128, T * 128, T * 128 + 1280, T * 128 + 1280); };
    ldx(kt0, 0);
    {
      const int hi = lane >= 30, ch = lane - 30 * hi, lim = j_ < 7 ? 27 : 19;
      wv = hi * RS + (att ? ((ch >= 10) + (ch >= 20)) * 1120 + j_ * 160 + ch * 16 : j_ * 448 + (ch < lim ? ch : lim) * 16);
    }
    const int wrow = (16 * (lw & 1) + 4 * (lw >> 1)) * RS;
    auto ldw = [&](int T, int h, int slot) {
      const int so = (64 * T + 32 * h) * RS + wrow;
      dma4<960>(rw1, 0x0fffffffffffffffull, slot * SLOT + h * WSLAB + 4 * lw * 960, wv, wv, so, so + 2 * RS, so + 8 * RS, so + 10 * RS);
    };
    ldw(kt0, 0, 0);
    {
      int tok = t0 - 16 + 8 * (r8 & 1) + 2 * lw + ((r8 & 3) < 2);
      tok = tok < 0 ? 0 : tok;
      hv = att ? tok * 1280 + ((cp ^ ((2 * lw + ((r8 & 3) >> 1)) & 7)) << 4) : 0x70000000;
    }
    const u64 hmask = lw == 0 ? 0xffffff00ull : 0xffffffffull;
    auto ldh = [&](int T, int slot) { dma1(rx, hmask, slot * SLOT + XOFF + lw * 512, hv, T * 128); };
    ldh(kt0, 0); ldw(kt0, 1, 0);
    const u32x4 rw = mkr(kq == 0 ? wo : w2, has2 ? (kq == 0 ? D_ * 1280 : F_ * 1280) : 0);
    const int cbyte = n_ * 128 + ((cp ^ ((lane >> 4) << 1)) << 4);
    const int v1 = r8 * 1280 + cbyte, v2 = kq == 0 ? (20 * (r8 >> 2) + (r8 & 3)) * 1280 + cbyte : v1;
    auto geom = [&](int s, int& rb, int& sa, int& sb, int& v) {
      const int P = s / 5, kp = s - 5 * P;
      v = kp == 2 ? v2 : v1;
      rb = 1280 * (kq == 0 ? (kp == 2 ? 160 * P + 16 : 160 * P + 40 * (kp > 2 ? kp - 1 : kp)) : 32 * (18 * (kq - 1) + s));
      sa = 1280 * (kq == 0 && kp == 2 ? 80 : 8);
      sb = 1280 * (kq == 0 ? (kp == 2 ? 40 : 20) : 16);
    };
    u32x4 pre[9][4];
    const int attf = att || !INLOOP;
    auto ldr = [&](auto R, auto LATE) {
      constexpr int r = decltype(R)::value;
      int rb, sa, sb, v; geom(9 + r, rb, sa, sb, v);
      bload4_if<decltype(LATE)::value>(pre[r], rw, v, rb, rb + sa, rb + sb, rb + sa + sb, attf);
    };
    auto ldl = [&](auto S) {
      constexpr int s = decltype(S)::value;
      int rb, sa, sb, v; geom(s, rb, sa, sb, v);
      dma4<1024>(rw, ~0ull, s < 9 ? s * 16384 + kq * 4096 : 147456 + (s - 18) * 4096, v, v, rb, rb + sa, rb + sb, rb + sa + sb);
    };
    const u32x4 rst = mkr(ws + WS_STAT, S_ * 16);
    const int spos = 36 * lw + (lane < 36 ? lane : 35), sv = row_token(p_, spos) * 16;
    u32x4 sg;
    sfor<9>([&](auto TT) {
      constexpr int T = decltype(TT)::value + 1, slot = T % 3;
      if (T == 9) bload_sc1(sg, rst, sv, 0);
      const int kt = kt0 + T >= 10 ? kt0 + T - 10 : kt0 + T;
      ldx(kt, slot); ldw(kt, 0, slot); ldh(kt, slot); ldw(kt, 1, slot);
      ldr(IC(T - 1), IC(0));
      bar_vm_sel<(T == 1 || T == 9 ? 17 : 21), 13>(attf);
    });
    STAMP(1);
    const unsigned tag = __builtin_amdgcn_readfirstlane(*(LAS unsigned*)(shm + LDS_CNT + 4));
    asm volatile("" : "+v"(sg));
#pragma nounroll
    for (unsigned spins = 0; !__all(sg[1] == tag && sg[3] == tag) && spins < SPIN_CAP; spins++) {
      __builtin_amdgcn_s_sleep(2);
      bload_sc1(sg, rst, sv, 0);
      asm volatile("s_waitcnt vmcnt(0)" : "+v"(sg) :: "memory");
    }
    *(LAS u32x2*)(shm + LDS_ST + spos * 8) = u32x2{sg[0], sg[2]};
    ldr(IC(0), IC(1));
    wait_vm<4>();
    STAMP(2);
    bar_lgkm();
    STAMP(3);
    if (!has2) { FINISH(); return; }
    sfor<8>([&](auto R) { ldr(IC(decltype(R)::value + 1), IC(1)); });
    ldl(IC(3)); ldl(IC(4));
    STAMP(4);
    await<1, 8>(2);
    ldl(IC(0)); wait_vm<THR>(); ldl(IC(1)); wait_vm<THR>(); ldl(IC(2)); wait_vm<THR>();
    if (att) await<1, 8>(4);
    STAMP(5);
    ldl(IC(5)); wait_vm<THR>(); ldl(IC(6)); wait_vm<THR>(); ldl(IC(7)); wait_vm<THR>(); ldl(IC(8));
    if (kq == 0) { ldl(IC(18)); ldl(IC(19)); }
    STAMP(6);
    bar();
    bar_vm<0>();
    const int wa = kq * 4096 + lane * 16;
#define NAP __builtin_amdgcn_s_sleep(PSL);
#define PUT(r, off) if (!(ABL & 32)) { dsw128<off>(wa + (r / 3) * 49152, pre[r][0]); dsw128<off + 1024>(wa + (r / 3) * 49152, pre[r][1]); dsw128<off + 2048>(wa + (r / 3) * 49152, pre[r][2]); dsw128<off + 3072>(wa + (r / 3) * 49152, pre[r][3]); } else asm volatile("" :: "v"(pre[r][0]), "v"(pre[r][1]), "v"(pre[r][2]), "v"(pre[r][3]));
    PUT(0, 0) NAP PUT(1, 16384) NAP PUT(2, 32768)
    bar_lgkm();
    PUT(3, 0) NAP PUT(4, 16384) NAP PUT(5, 32768)
    bar_lgkm();
    PUT(6, 0) NAP PUT(7, 16384) NAP PUT(8, 32768)
    bar_lgkm();
    bar();
  } else {
    const int rid = (att ? 0 : 8) + j_;
    const int srow = p_ * 128 + 8 * rid + w;
    unsigned u[5];
#pragma unroll
    for (int k = 0; k < 5; k++) u[k] = ((const GAS unsigned*)x)[srow * 320 + lane + 64 * k];
    const unsigned ep = __hip_atomic_load((GAS unsigned*)ws, __ATOMIC_RELAXED, __HIP_MEMORY_SCOPE_AGENT);
    const int bn = j_ * 224 + tid;
    const unsigned short bv = ((const GAS unsigned short*)b1)[bn < F_ ? bn : F_ - 1];
    if (tid < 24) *(LAS u32x4*)(shm + (tid >> 3) * SLOT + XOFF + (tid & 7) * 16) = u32x4{0x3f803f80u, 0x3f803f80u, 0x3f803f80u, 0x3f803f80u};
    if (w == 5 && att) att_table(shm, lane);
    unsigned tag;
    auto pre = [&]() {
      tag = __builtin_amdgcn_readfirstlane(ep) + 1u;
      float s = 0;
#pragma unroll
      for (int k = 0; k < 5; k++) s += blo(u[k]) + bhi(u[k]);
      const float m = wave_sum(s) * (1.f / D_);
      float q = 0;
#pragma unroll
      for (int k = 0; k < 5; k++) { float a = blo(u[k]) - m, b = bhi(u[k]) - m; q += a * a + b * b; }
      const float rs = __builtin_amdgcn_rsqf(wave_sum(q) * (1.f / D_) + 1e-5f);
      if (lane == 0) st_wt(mkr(ws + WS_STAT, S_ * 16), 0, srow * 16, u32x4{asu(rs), tag, asu(-m * rs), tag});
      if (tid < 256) *(LAS float*)(shm + LDS_B1 + tid * 4) = blo(bv);
      if (tid >= 448 && tid < 464) *(LAS unsigned*)(shm + LDS_CNT + 4 * (tid - 448)) = tid == 449 ? tag : 0u;
      if (!att) {
        const int wv = ((blockIdx.x >> 7) * 64 + (blockIdx.x & 63)) * 8 + w, id = wv * 64 + lane;
        if (wv < 240) touch(mkr((const char*)wqkv + 128 * 3840, 512 * 3840), id * 128);
        else if (wv < 456) touch(mkr((const char*)w1 + 128 * 3456, 512 * 3456), (id - 240 * 64) * 128);
        else if (wv < 712) touch(mkr(x, S_ * 1280), ((id - 456 * 64) >> 3) * 1280 + 256 + (id & 7) * 128);
        else if (PF_MALL > 1) {
          if (wv < 982) touch(mkr(w2, F_ * 1280), (id - 712 * 64) * 128);
          else for (int z = 0; z < 3; z++) touch(mkr(wo, D_ * 1280), (id - 982 * 64 + z * 42 * 64) * 128);
        }
      }
    };
    const bool light = att ? (w >= 6) : (w >= 4);
    if (light) s1_compute<3>(shm, att, w, lane, p_, j_, ws, pre PR_PASS);
    else s1_compute<4>(shm, att, w, lane, p_, j_, ws, pre PR_PASS);
    STAMP(3);
    if (att) s1_attention(shm, w, p_, j_, ws PR_PASS);
    STAMP(4);
    wait_vm<0>();
    const int lane = lane_id();
    GAS u64* flags = (GAS u64*)(ws + WS_FLAG);
    const u64 fv = flagval(tag);
    {
      unsigned old = 0;
      if (lane == 0) old = __hip_atomic_fetch_add((LAS unsigned*)(shm + LDS_CNT), 1u, __ATOMIC_RELAXED, __HIP_MEMORY_SCOPE_WORKGROUP);
      if (__builtin_amdgcn_readfirstlane(old) == 7) {
        const int dm = (lane * 26) >> 8, cm = (8 * p_) / 6 + dm;
        GAS u64* dst = lane == 30 ? flags + p_ * 16 + rid : (GAS u64*)(ws + WS_BOX) + (cm * 10 + lane - 10 * dm) * 32 + (p_ - ((6 * cm) >> 3)) * 16 + rid;
        if (lane == 30 || (lane < 30 && 6 * cm < 8 * p_ + 8)) __hip_atomic_store(dst, fv, __ATOMIC_RELAXED, __HIP_MEMORY_SCOPE_AGENT);
      }
    }
    STAMP(5);
    if (!has2) {
      if (blockIdx.x == 255 && w == 0) {
#pragma nounroll
        for (unsigned spins = 0; spins < SPIN_CAP; spins++) {
          bool ok = true;
#pragma unroll
          for (int k = 0; k < 4; k++) ok &= __hip_atomic_load(flags + lane + 64 * k, __ATOMIC_RELAXED, __HIP_MEMORY_SCOPE_AGENT) == fv;
          if (__all(ok)) break;
          __builtin_amdgcn_s_sleep(8);
        }
        if (lane == 0) __hip_atomic_store((GAS unsigned*)ws, tag, __ATOMIC_RELAXED, __HIP_MEMORY_SCOPE_AGENT);
      }
      FINISH();
      return;
    }
    const int mh = w >> 2, g = lane >> 4, q = (lane >> 2) & 3, p = lane & 3;
    const int ks0 = kq == 0 ? 54 : 18 * (kq - 1);
    const u32x4 ra = mkr(ws + WS_A2, A2_BYTES);
    int ao[3];
    const int av = lane * 16;
#pragma unroll
    for (int m = 0; m < 3; m++) { int G = G0 + 3 * mh + m; G = G < 128 ? G : 127; if (ABL & 16) G = 3 * mh + m; ao[m] = (G * KS_T + ks0) * 1024; if (ABL & 2) ao[m] = 0x70000000; }
    int ba[4], bs[4];
#pragma unroll
    for (int c = 0; c < 4; c++) ba[c] = kq * 4096 + (4 * g + q) * 128 + ((c ^ ((2 * g + (q >> 1)) & 3)) << 5) + 8 * p;
    f32x4 acc[3][4];
#pragma unroll
    for (int m = 0; m < 3; m++)
#pragma unroll
      for (int c = 0; c < 4; c++) { acc[m][c] = f32x4{0, 0, 0, 0}; asm volatile("" : "+v"(acc[m][c])); }
    if (w == 0) {
      const int Gb = G0 + 5 < 128 ? G0 + 5 : 127;
      const int nfl = (Gb >> 3) != (G0 >> 3) ? 31 : 15;
      GAS u64* fp = (GAS u64*)(ws + WS_BOX) + (mt * 10 + n_) * 32 + (lane & nfl);
#pragma nounroll
      for (unsigned spins = 0; spins < SPIN_CAP; spins++) {
        if (__all(__hip_atomic_load(fp, __ATOMIC_RELAXED, __HIP_MEMORY_SCOPE_AGENT) == fv)) break;
        __builtin_amdgcn_s_sleep(1);
      }
    }
    STAMP(6);
    bar();
    STAMP(7);
    u32x4 a[3][3];
    int ro[6];
#pragma unroll
    for (int u = 0; u < 6; u++) ro[u] = (u + rot >= 6 ? u + rot - 6 : u + rot) * 1024;
#pragma unroll
    for (int m = 0; m < 3; m++) bload_sc1(a[0][m], ra, av, ao[m] + ro[0]);
    u32x2 b0[4][2], b1f[4][2];
#pragma unroll
    for (int c = 0; c < 4; c++) { dstr<0>(b0[c][0], ba[c]); dstr<2048>(b0[c][1], ba[c]); }
#define LG0 asm volatile("s_waitcnt lgkmcnt(0)" ::: "memory")
    if (ABL & 1) { for (int c = 0; c < 4; c++) { dstr<0>(b1f[c][0], ba[c]); dstr<2048>(b1f[c][1], ba[c]); } }
    LG0;
#pragma unroll
    for (int m = 0; m < 3; m++) bload_sc1(a[1][m], ra, av, ao[m] + ro[1]);
    auto step = [&](auto U, auto VV, u32x2 (&bc)[4][2], u32x2 (&bn)[4][2], int sl, int so) {
      constexpr int u = decltype(U)::value % 3, V = decltype(VV)::value;
      sfor<3>([&](auto M) {
        constexpr int m = decltype(M)::value;
        if (V < 0) bload_sc1(a[(u + 2) % 3][m], ra, av, ao[m] + so);
        if (V < 10) wait_vm<(V < 0 ? 6 : V < 10 ? V + 2 - m : 0)>();
        else
          asm volatile("s_cmp_lg_u32 %4, 0\n\ts_cbranch_scc1 .Lt%=\n\tbuffer_load_dwordx4 %0, %1, %2, %3 offen sc1\n\ts_waitcnt vmcnt(6)\n\ts_branch .Le%=\n"
                       ".Lt%=:\n\ts_waitcnt vmcnt(%5)\n.Le%=:"
                       : "=v"(a[(u + 2) % 3][m]) : "v"(av), "s"(ra), "s"(ao[m] + so), "s"(kq), "i"(V - 10 + 2 - m) : "memory", "scc");
#pragma unroll
        for (int c = 0; c < 4; c++) {
          if (!(ABL & 8)) mfma_a(acc[m][c], cat(bc[c][0], bc[c][1]), a[u][m]); else asm volatile("" :: "v"(bc[c][0]), "v"(bc[c][1]), "v"(a[u][m]));
          if (m < 2 && !(ABL & 1)) { if (c & 1) dstr<2048>(bn[2 * m + (c >> 1)][1], bs[2 * m + (c >> 1)]); else { vadd(bs[2 * m + (c >> 1)], ba[2 * m + (c >> 1)], sl); dstr<0>(bn[2 * m + (c >> 1)][0], bs[2 * m + (c >> 1)]); } }
        }
      });
    };
    STAMP(8);
#pragma nounroll
    for (int it = 0; it < 2; it++) {
      const int sb = it * 6 * 16384, sw = it * -3 * 16384, so = it * 6144;
      step(IC(0), IC(-1), b0, b1f, sb + 16384, so + ro[2]); LG0;
      step(IC(1), IC(-1), b1f, b0, sb + 2 * 16384, so + ro[3]); LG0;
      step(IC(2), IC(-1), b0, b1f, sw + 3 * 16384, so + ro[4]); bar_lgkm();
      if (it == 0) { STAMPB(12); } else { STAMPB(14); }
      step(IC(3), IC(-1), b1f, b0, sw + 4 * 16384, so + ro[5]); LG0;
      step(IC(4), IC(-1), b0, b1f, sw + 5 * 16384, so + 6144 + ro[0]); LG0;
      step(IC(5), IC(-1), b1f, b0, sw + 6 * 16384, so + 6144 + ro[1]); bar_lgkm();
      if (it == 0) { STAMPB(13); } else { STAMPB(15); }
    }
    step(IC(0), IC(-1), b0, b1f, 4 * 16384, 12288 + ro[2]); LG0;
    step(IC(1), IC(-1), b1f, b0, 5 * 16384, 12288 + ro[3]); LG0;
    step(IC(2), IC(-1), b0, b1f, 6 * 16384, 12288 + ro[4]); bar_lgkm();
    step(IC(3), IC(-1), b1f, b0, 7 * 16384, 12288 + ro[5]); LG0;
    if (kq == 0) __builtin_amdgcn_s_setprio(3);
    step(IC(4), IC(13), b0, b1f, 8 * 16384, 18 * 1024); LG0;
    step(IC(5), IC(10), b1f, b0, 147456, 19 * 1024); LG0;
    if (kq == 0) {
      step(IC(0), IC(3), b0, b1f, 151552, 0); LG0;
      step(IC(1), IC(0), b1f, b0, 151552, 0); LG0;
    }
    asm volatile("s_nop 7" ::: "memory");
    STAMP(9);
    const int l2 = lane_id(), i2 = l2 & 15, g2 = l2 >> 4;
#pragma unroll
    for (int m = 0; m < 3; m++)
#pragma unroll
      for (int c = 0; c < 4; c++) {
        asm volatile("" : "+v"(acc[m][c]));
        *(LAS f32x4*)(shm + (3 * mh + m) * 16384 + kq * 4096 + i2 * 256 + (((4 * c + g2) ^ (i2 & 7)) << 4)) = acc[m][c];
      }
  }
  {
    const int lane = lane_id();
    const int frow = 8 * w + (lane >> 3), fG = G0 + (frow >> 4);
    const bool fvalid = fG < 128;
    const int ftok = 32 * ((fvalid ? fG : 0) >> 1) + 2 * sig(frow & 15) + (fG & 1);
    const int foff = ftok * 1280 + n_ * 128 + (lane & 7) * 16;
    const u32x4 xv = *(const u32x4*)((const char*)x + foff);
    const u32x4 bv = *(const u32x4*)(b2 + n_ * 128 + (lane & 7) * 16);
    bar_lgkm();
    STAMP(10);
    f32x4 s[2], y[2];
#pragma unroll
    for (int e = 0; e < 2; e++) {
      lptr pr = shm + (frow >> 4) * 16384 + (frow & 15) * 256 + (((2 * (lane & 7) + e) ^ (frow & 7)) << 4);
      s[e] = *(LAS f32x4*)pr;
      y[e] = *(LAS f32x4*)(pr + 4096) + *(LAS f32x4*)(pr + 8192) + *(LAS f32x4*)(pr + 12288);
    }
    u32x4 o;
#pragma unroll
    for (int k = 0; k < 4; k++) {
      f32x2 yy = f32x2{y[k >> 1][2 * (k & 1)], y[k >> 1][2 * (k & 1) + 1]} + f32x2{blo(bv[k]), bhi(bv[k])};
      f32x2 t = yy * -1.4426950408889634f;
      f32x2 e = f32x2{__builtin_amdgcn_exp2f(t[0]), __builtin_amdgcn_exp2f(t[1])} + 1.f;
      f32x2 gt = yy * f32x2{__builtin_amdgcn_rcpf(e[0]), __builtin_amdgcn_rcpf(e[1])};
      f32x2 r = f32x2{s[k >> 1][2 * (k & 1)], s[k >> 1][2 * (k & 1) + 1]} * gt + f32x2{blo(xv[k]), bhi(xv[k])};
      o[k] = pk2(r);
    }
    if (fvalid) *(u32x4*)(out + foff) = o;
  }
  STAMP(11);
  FINISH();
}

extern "C" void kernel_launch(void* const* d_in, const int* in_sizes, int n_in, void* d_out, int out_size, void* d_ws,
                              size_t ws_size, hipStream_t stream) {
  (void)hipFuncSetAttribute((const void*)fused_block, hipFuncAttributeMaxDynamicSharedMemorySize, LDS_TOTAL);
  fused_block<<<256, 768, LDS_TOTAL, stream>>>(d_in[0], d_in[1], d_in[2], d_in[4], (const unsigned short*)d_in[5], d_in[6],
                                               (const char*)d_in[7], (char*)d_out, (char*)d_ws);
}
```

```cpp
#include <hip/hip_runtime.h>
#include <hip/hip_bf16.h>
#include <type_traits>
#include <utility>

typedef __attribute__((ext_vector_type(4))) short s16x4;
typedef __attribute__((ext_vector_type(8))) __bf16 bf16x8;
typedef __attribute__((ext_vector_type(2))) __bf16 bf16x2;
typedef __attribute__((ext_vector_type(4))) float f32x4;
typedef __attribute__((ext_vector_type(2))) float f32x2;
typedef __attribute__((ext_vector_type(4))) unsigned u32x4;
typedef __attribute__((ext_vector_type(2))) unsigned u32x2;
#define LAS __attribute__((address_space(3)))
#define GAS __attribute__((address_space(1)))
#define DEVI __device__ __forceinline__
typedef LAS char* lptr;
typedef unsigned long long u64;
#define IC(n) std::integral_constant<int, n>{}

constexpr int S_ = 2048, D_ = 640, F_ = 1728;
constexpr int KS_T = 74;
constexpr int WSLAB = 15360;
constexpr int XOFF = 2 * WSLAB;
constexpr int SLOT = 49152;
constexpr int LDS_TB = 86016;
constexpr int LDS_ST = 155648;
constexpr int LDS_CS = LDS_ST + 1152;
constexpr int LDS_B1 = LDS_CS + 1024;
constexpr int LDS_CNT = LDS_B1 + 1024;
constexpr int LDS_ZERO = LDS_CNT + 24;
constexpr int LDS_TAB = LDS_CNT + 64;
constexpr int LDS_TOTAL = 163840;
constexpr int WS_FLAG = 4096, WS_STAT = 8192, WS_BOX = 65536, WS_A2 = 1 << 20;
constexpr int A2_BYTES = 128 * KS_T * 1024;
#define PF_CODE 1
#define PF_MALL 0
#define MAP2 0
#define S1MODE 0
#define S2MODE 0
#define THR 4
#define PSL 4
#define PF_TLB 0
#define INLOOP 0
#define PFS 128
#define PFK0 2
#define PFALL 0
#define PFW2 0
#define KHF (idx & 1)
#define KTX (((idx >> 1) ^ sub) & 1)
#define KT0 0
#define THR2 4
#define ABL 0
#define ROT1 0
#define ROT2 ((mt + nl) & 7)
constexpr int CODE_BYTES = 30080;
constexpr unsigned SPIN_CAP = 1u << 20;

#define STAMP(idx)
#define FINISH()
#define STAMPA(k) STAMP(k)
#define STAMPB(k)
#define PR_ARGS
#define PR_PASS

DEVI void bar_lgkm() { asm volatile("s_waitcnt lgkmcnt(0)\n\ts_barrier" ::: "memory"); }
DEVI void bar() { asm volatile("s_barrier" ::: "memory"); }
template <int N> DEVI void bar_vm() { asm volatile("s_waitcnt vmcnt(%0)\n\ts_barrier" ::"i"(N) : "memory"); }
template <int N> DEVI void wait_vm() { asm volatile("s_waitcnt vmcnt(%0)" ::"i"(N) : "memory"); }
DEVI unsigned pk2(float a, float b) { f32x2 v = {a, b}; return __builtin_bit_cast(unsigned, __builtin_convertvector(v, bf16x2)); }
DEVI unsigned pk2(f32x2 v) { return __builtin_bit_cast(unsigned, __builtin_convertvector(v, bf16x2)); }
DEVI float asf(unsigned u) { return __builtin_bit_cast(float, u); }
DEVI unsigned asu(float f) { return __builtin_bit_cast(unsigned, f); }
DEVI float blo(unsigned u) { return __builtin_bit_cast(float, u << 16); }
DEVI float bhi(unsigned u) { return __builtin_bit_cast(float, u & 0xffff0000u); }
DEVI u32x2 tr_read(lptr p) { return __builtin_bit_cast(u32x2, __builtin_amdgcn_ds_read_tr16_b64_v4i16((LAS s16x4*)p)); }
DEVI f32x4 mfma(u32x4 a, u32x4 b, f32x4 c) {
  return __builtin_amdgcn_mfma_f32_16x16x32_bf16(__builtin_bit_cast(bf16x8, a), __builtin_bit_cast(bf16x8, b), c, 0, 0, 0);
}
DEVI u32x4 cat(u32x2 a, u32x2 b) { return __builtin_shufflevector(a, b, 0, 1, 2, 3); }
DEVI void mfma_a(f32x4& c, u32x4 a, u32x4 b) { asm volatile("v_mfma_f32_16x16x32_bf16 %0, %1, %2, %0" : "+v"(c) : "v"(a), "v"(b)); }
template <int OFF> DEVI void ds128(u32x4& d, int addr) { asm volatile("ds_read_b128 %0, %1 offset:%2" : "=v"(d) : "v"(addr), "i"(OFF)); }
template <int OFF> DEVI void dstr(u32x2& d, int addr) { asm volatile("ds_read_b64_tr_b16 %0, %1 offset:%2" : "=v"(d) : "v"(addr), "i"(OFF)); }
template <int OFF> DEVI void dsw128(int addr, u32x4 d) { asm volatile("ds_write_b128 %0, %1 offset:%2" ::"v"(addr), "v"(d), "i"(OFF) : "memory"); }
DEVI u32x4 mkr(const void* p, unsigned bytes) {
  u64 a = (u64)p;
  return u32x4{(unsigned)a, (unsigned)(a >> 32) & 0xffffu, bytes, 0x00020000u};
}
DEVI void st_wt(u32x4 r, int voff, int soff, u32x4 v) {
  asm volatile("buffer_store_dwordx4 %0, %1, %2, %3 offen sc1\n\ts_nop 1" ::"v"(v), "v"(voff), "s"(r), "s"(soff) : "memory");
}
template <int LSTEP>
DEVI void dma4(u32x4 r, u64 mask, int l, int va, int vb, int s0, int s1, int s2, int s3) {
  asm volatile(
      "s_mov_b64 exec, %0\n\t"
      "s_mov_b32 m0, %1\n\ts_nop 0\n\tbuffer_load_dwordx4 %2, %4, %5 offen lds\n\t"
      "s_add_u32 m0, %1, %9\n\ts_nop 0\n\tbuffer_load_dwordx4 %3, %4, %6 offen lds\n\t"
      "s_add_u32 m0, %1, %10\n\ts_nop 0\n\tbuffer_load_dwordx4 %2, %4, %7 offen lds\n\t"
      "s_add_u32 m0, %1, %11\n\ts_nop 0\n\tbuffer_load_dwordx4 %3, %4, %8 offen lds\n\t"
      "s_mov_b64 exec, -1"
      :: "s"(mask), "s"(l), "v"(va), "v"(vb), "s"(r), "s"(s0), "s"(s1), "s"(s2), "s"(s3), "i"(LSTEP), "i"(2 * LSTEP), "i"(3 * LSTEP)
      : "memory", "scc");
}
DEVI void dma1(u32x4 r, u64 mask, int l, int v, int s0) {
  asm volatile("s_mov_b64 exec, %0\n\ts_mov_b32 m0, %1\n\ts_nop 0\n\tbuffer_load_dwordx4 %2, %3, %4 offen lds\n\ts_mov_b64 exec, -1"
               :: "s"(mask), "s"(l), "v"(v), "s"(r), "s"(s0) : "memory");
}
DEVI void setup_loads(u32x2 (&xr)[5], unsigned& ep, unsigned& bv, u32x4 rx, int vx, u32x4 re, u32x4 rb, int vb) {
  asm volatile("buffer_load_dwordx2 %0, %7, %8, 0 offen\n\tbuffer_load_dwordx2 %1, %7, %8, 0 offen offset:256\n\tbuffer_load_dwordx2 %2, %7, %8, 0 offen offset:512\n\t"
               "buffer_load_dwordx2 %3, %7, %8, 0 offen offset:768\n\tbuffer_load_dwordx2 %4, %7, %8, 0 offen offset:1024\n\t"
               "buffer_load_dword %5, off, %9, 0 sc1\n\tbuffer_load_ushort %6, %11, %10, 0 offen"
               : "=&v"(xr[0]), "=&v"(xr[1]), "=&v"(xr[2]), "=&v"(xr[3]), "=&v"(xr[4]), "=&v"(ep), "=&v"(bv)
               : "v"(vx), "s"(rx), "s"(re), "s"(rb), "v"(vb) : "memory");
}
template <class T> DEVI void pin(T& v) { asm volatile("" : "+v"(v)); }
DEVI void dsw_col0(int a0, f32x4 d0, int a1, f32x4 d1, int a2, f32x4 d2) {
  asm volatile("s_mov_b64 exec, %6\n\tds_write_b128 %0, %1\n\tds_write_b128 %2, %3\n\tds_write_b128 %4, %5\n\ts_mov_b64 exec, -1"
               ::"v"(a0), "v"(d0), "v"(a1), "v"(d1), "v"(a2), "v"(d2), "s"(0x0001000100010001ull) : "memory");
}
DEVI void vadd(int& d, int a, int s) { asm volatile("v_add_u32 %0, %2, %1" : "=v"(d) : "v"(a), "s"(s)); }
DEVI void touchg(const void* p) {
  asm volatile("s_mov_b32 m0, %1\n\ts_nop 0\n\tglobal_load_lds_dword %0, off" ::"v"(p), "i"(LDS_TOTAL - 256) : "memory");
}
DEVI void touch(u32x4 r, int v) {
  asm volatile("s_mov_b32 m0, %2\n\ts_nop 0\n\tbuffer_load_dword %0, %1, 0 offen lds" ::"v"(v), "s"(r), "i"(LDS_TOTAL - 256) : "memory");
}
DEVI void bload(u32x4& d, u32x4 r, int v, int s0) { asm volatile("buffer_load_dwordx4 %0, %1, %2, %3 offen" : "=v"(d) : "v"(v), "s"(r), "s"(s0)); }
DEVI void bload_sc1(u32x4& d, u32x4 r, int v, int s0) { if (ABL & 4) { bload(d, r, v, s0); return; } asm volatile("buffer_load_dwordx4 %0, %1, %2, %3 offen sc1" : "=v"(d) : "v"(v), "s"(r), "s"(s0) : "memory"); }
DEVI void arrive(int k) {
  asm volatile("s_mov_b64 exec, 1\n\tds_add_u32 %0, %1\n\ts_mov_b64 exec, -1" ::"v"(LDS_CNT + 4 * k), "v"(1) : "memory");
}
template <int SLEEP, unsigned N>
DEVI void await(int k) {
  unsigned v;
  for (;;) {
    asm volatile("ds_read_b32 %0, %1\n\ts_waitcnt lgkmcnt(0)" : "=v"(v) : "v"(LDS_CNT + 4 * k) : "memory");
    if (__builtin_amdgcn_readfirstlane(v) >= N) break;
    if (SLEEP) __builtin_amdgcn_s_sleep(SLEEP);
  }
}
DEVI int lane_id() { int l; asm volatile("v_mbcnt_lo_u32_b32 %0, -1, 0\n\tv_mbcnt_hi_u32_b32 %0, -1, %0" : "=v"(l)); return l; }
DEVI int sig(int i) { return 4 * (i & 3) + (i >> 2); }
DEVI u64 flagval(unsigned tag) { return ((u64)(tag ^ 0x5bd1e995u) << 32) | tag; }

DEVI float xmax16(float v) { auto r = __builtin_amdgcn_permlane16_swap(asu(v), asu(v), false, false); return fmaxf(asf(r[0]), asf(r[1])); }
DEVI float xmax32(float v) { auto r = __builtin_amdgcn_permlane32_swap(asu(v), asu(v), false, false); return fmaxf(asf(r[0]), asf(r[1])); }
DEVI float xadd16(float v) { auto r = __builtin_amdgcn_permlane16_swap(asu(v), asu(v), false, false); return asf(r[0]) + asf(r[1]); }
DEVI float xadd32(float v) { auto r = __builtin_amdgcn_permlane32_swap(asu(v), asu(v), false, false); return asf(r[0]) + asf(r[1]); }
template <int CTRL> DEVI float dpp(float v) { return asf(__builtin_amdgcn_mov_dpp(asu(v), CTRL, 0xf, 0xf, true)); }
DEVI float half_sum(float v) {
  v += dpp<0xb1>(v); v += dpp<0x4e>(v); v += dpp<0x141>(v); v += dpp<0x140>(v);
  return xadd16(v);
}

DEVI int row_token(int p_, int pos) {
  const int tau = pos >> 4, i = pos & 15;
  int tok = tau == 0 ? p_ * 128 - 16 + 8 * (i & 1) + 2 * (i >> 2) + ((i & 3) < 2) : p_ * 128 + 32 * ((tau - 1) >> 1) + 2 * sig(i) + ((tau - 1) & 1);
  return tok < 0 ? 0 : tok;
}

struct ColGeo { int A, B, C, ct; };
DEVI ColGeo colgeo(bool att, int nw, int mw, int c, bool heavy) {
  const int cc = heavy ? (c ^ (2 * mw)) : c;
  const int ct = 4 * nw + cc, sec = (ct * 13) >> 6, hh = ct - 5 * sec;
  const bool q4 = hh == 4, sp = !heavy && c == 2;
  ColGeo r;
  r.A = att ? sec * 160 + (q4 ? 32 : 40 * hh) : heavy ? 128 * nw + 32 * cc : (sp ? 352 + 16 * nw : 128 + 64 * nw + 32 * c);
  r.B = att && q4 ? 40 : 8;
  r.C = att ? (q4 ? 80 : 16) : (sp ? 32 : 16);
  r.ct = ct;
  return r;
}

template <int NC>
struct Frags { u32x4 x[5]; u32x2 w[NC][2]; };

template <class F, int... I> DEVI void sfor_(F f, std::integer_sequence<int, I...>) { (f(std::integral_constant<int, I>{}), ...); }
template <int N, class F> DEVI void sfor(F f) { sfor_(f, std::make_integer_sequence<int, N>{}); }
template <int N> DEVI void wait_lg() { asm volatile("s_waitcnt lgkmcnt(%0)" ::"i"(N) : "memory"); }

template <int NC, int K>
DEVI void s1_rd(Frags<NC>& n, int xo, int xh, const int (&wa)[NC]) {
  if constexpr (K == 0) ds128<0>(n.x[0], xo);
  else if constexpr (K <= 2 * NC) { if ((K - 1) & 1) dstr<7680>(n.w[(K - 1) >> 1][1], wa[(K - 1) >> 1]); else dstr<0>(n.w[(K - 1) >> 1][0], wa[(K - 1) >> 1]); }
  else if constexpr (K < 2 * NC + 4) ds128<(K - 2 * NC) * 2048>(n.x[K - 2 * NC], xo);
  else if constexpr (K == 2 * NC + 4) ds128<0>(n.x[4], xh);
}
template <int NC, int NH, int K>
DEVI void s1_mm(const Frags<NC>& c, f32x4 (&acc)[4][NC], f32x4 (&hacc)[NH]) {
  if constexpr (K < 4 * NC) mfma_a(acc[K / NC][K % NC], cat(c.w[K % NC][0], c.w[K % NC][1]), c.x[K / NC]);
  else mfma_a(hacc[K - 4 * NC], cat(c.w[K - 4 * NC][0], c.w[K - 4 * NC][1]), c.x[4]);
}
template <int NC, int NH, bool BAR>
DEVI void s1_step(const Frags<NC>& c, Frags<NC>& n, int xo, int xh, const int (&wa)[NC], f32x4 (&acc)[4][NC], f32x4 (&hacc)[NH]) {
  constexpr int NM = 4 * NC + NH, NR = 2 * NC + 5, D = BAR ? NM - NR : 0;
  sfor<NM>([&](auto KK) {
    constexpr int k = decltype(KK)::value;
    constexpr int need = k < NC ? 2 * k + 2 : (k % NC == 0 && k <= 4 * NC) ? 2 * NC + k / NC : -1;
    if constexpr (BAR && k == D) bar_lgkm();
    if constexpr (need >= 0 && (!BAR || k < D)) wait_lg<NR - 1 - need + (BAR ? 0 : (k < NR ? k : NR))>();
    s1_mm<NC, NH, k>(c, acc, hacc);
    if constexpr (k >= D) s1_rd<NC, k - D>(n, xo, xh, wa);
  });
}

template <int OFF> DEVI void dsw64(int addr, u32x2 d) { asm volatile("ds_write_b64 %0, %1 offset:%2" ::"v"(addr), "v"(d), "i"(OFF) : "memory"); }
template <int OFF> DEVI void ds64(u32x2& d, int addr) { asm volatile("ds_read_b64 %0, %1 offset:%2" : "=v"(d) : "v"(addr), "i"(OFF)); }

template <int NC>
DEVI void s1_compute(lptr shm, bool att, int w, int lane, int p_, int j_, int hf, char* ws PR_ARGS) {
  constexpr int NH = NC == 4 ? 2 : 3;
  const int mw = w & 1, nw = w >> 1, i = lane & 15, g = lane >> 4;
  f32x4 acc[4][NC], hacc[NH];
#pragma unroll
  for (int m = 0; m < 4; m++)
#pragma unroll
    for (int c = 0; c < NC; c++) { acc[m][c] = f32x4{0, 0, 0, 0}; asm volatile("" : "+v"(acc[m][c])); }
#pragma unroll
  for (int c = 0; c < NH; c++) { hacc[c] = f32x4{0, 0, 0, 0}; asm volatile("" : "+v"(hacc[c])); }
  int wb[NC], cb[NC], ct[NC];
#pragma unroll
  for (int c = 0; c < NC; c++) {
    const ColGeo cg = colgeo(att, nw, mw, c, NC == 4);
    wb[c] = (lane >> 2) * 480 + cg.A + cg.B * (lane & 1) + cg.C * ((lane >> 1) & 1);
    cb[c] = LDS_CS + 2 * (cg.A + cg.B * (g & 1) + cg.C * (g >> 1));
    ct[c] = cg.ct;
    asm volatile("" : "+v"(cb[c]), "+v"(wb[c]));
  }
  int xb = XOFF + i * 128 + ((g ^ (i >> 1) ^ (4 * hf)) << 4);
  int sta = LDS_ST + i * 8;
  asm volatile("" : "+v"(xb), "+v"(sta));
  const int own = (1 + 4 * mw) * 2048;
  Frags<NC> fa, fb;
  int wa[NC];
  STAMPB(12);
  bar_lgkm();
  STAMP(1);
  sfor<2 * NC + 5>([&](auto K) { s1_rd<NC, decltype(K)::value>(fa, xb + own, xb, wb); });
#pragma unroll
  for (int c = 0; c < NC; c++) wa[c] = wb[c] + WSLAB;
  s1_step<NC, NH, true>(fa, fb, (xb ^ 64) + own, xb ^ 64, wa, acc, hacc);
  int sl = 0;
#pragma nounroll
  for (int T = 1; T < 10; T++) {
    sl = sl == 2 * SLOT ? 0 : sl + SLOT;
#pragma unroll
    for (int c = 0; c < NC; c++) wa[c] = wb[c] + sl;
    s1_step<NC, NH, true>(fb, fa, xb + sl + own, xb + sl, wa, acc, hacc);
#pragma unroll
    for (int c = 0; c < NC; c++) wa[c] = wb[c] + sl + WSLAB;
    s1_step<NC, NH, false>(fa, fb, (xb ^ 64) + sl + own, (xb ^ 64) + sl, wa, acc, hacc);
  }
  wait_lg<0>();
  arrive(2);
  sfor<NH>([&](auto K) { s1_mm<NC, NH, 4 * NC + decltype(K)::value>(fb, acc, hacc); });
  u32x2 stu[5];
  u32x4 b1u[NC], csu[NC];
  sfor<4 * NC>([&](auto KK) {
    constexpr int k = decltype(KK)::value;
    s1_mm<NC, NH, k>(fb, acc, hacc);
    if constexpr (k < 4) ds64<(1 + k) * 128>(stu[k], sta + mw * 512);
    if constexpr (k == 4) ds64<0>(stu[4], sta);
    if constexpr (k == 5) {
      dsw_col0(cb[0], hacc[0], cb[1], hacc[1], cb[NH - 1], hacc[NH - 1]);
      if (NC == 4) arrive(8 + nw);
    }
    if constexpr (k >= 6 && k < 6 + NC) ds128<LDS_B1 - LDS_CS>(b1u[k - 6], cb[k - 6]);
  });
  STAMP(2);
  if (NC == 4) await<0, 2>(8 + nw);
#pragma unroll
  for (int c = 0; c < NC; c++) ds128<0>(csu[c], cb[c]);
  asm volatile("s_waitcnt lgkmcnt(0)\n\ts_nop 7" ::: "memory");
  f32x2 st[5];
  f32x4 cs[NC], b1[NC];
#pragma unroll
  for (int m = 0; m < 5; m++) { asm volatile("" : "+v"(stu[m])); st[m] = __builtin_bit_cast(f32x2, stu[m]); }
#pragma unroll
  for (int c = 0; c < NC; c++) { asm volatile("" : "+v"(csu[c]), "+v"(b1u[c])); cs[c] = __builtin_bit_cast(f32x4, csu[c]); b1[c] = __builtin_bit_cast(f32x4, b1u[c]); }
#pragma unroll
  for (int m = 0; m < 4; m++)
#pragma unroll
    for (int c = 0; c < NC; c++) pin(acc[m][c]);
#pragma unroll
  for (int c = 0; c < NH; c++) pin(hacc[c]);
  STAMPA(12);
#define SB __builtin_amdgcn_sched_barrier(0)
  const int l2 = lane_id();
  if (att) {
    const int g2 = l2 >> 4, piece = LDS_TB + ((l2 & 48) + ((l2 & 15) ^ (8 * (g2 >> 1)))) * 8;
    sfor<NC>([&](auto C) {
      constexpr int c = decltype(C)::value;
      const int ah = piece + ct[c] * 4608, ao = ah + (1 + 4 * mw) * 512;
      u32x2 o[5];
      SB;
#pragma unroll
      for (int m = 0; m < (c < NH ? 5 : 4); m++) {
        f32x4 a = m < 4 ? acc[m < 4 ? m : 0][c] : hacc[c < NH ? c : 0];
        f32x4 v = a * st[m][0] + cs[c] * st[m][1];
        o[m] = u32x2{pk2(v[0], v[1]), pk2(v[2], v[3])};
      }
      dsw64<0>(ao, o[0]); dsw64<512>(ao, o[1]); dsw64<1024>(ao, o[2]); dsw64<1536>(ao, o[3]);
      if (c < NH) dsw64<0>(ah, o[4]);
    });
  } else {
    const u32x4 ra = mkr(ws + WS_A2, A2_BYTES);
    const int base = ((8 * p_ + 4 * mw) * KS_T + 7 * j_) * 1024;
    const int o0 = base + (NC == 4 ? (2 * nw + mw) * 1024 : (j_ < 7 || nw == 2) ? (2 + nw) * 1024 : 0x70000000);
    const int o1 = base + (NC == 4 ? (2 * nw + (mw ^ 1)) * 1024 : j_ < 7 ? 6 * 1024 + (nw - 2) * 512 : 0x70000000);
    const float c1 = -2.f * 0.7978845608028654f * 1.4426950408889634f, c2 = c1 * 0.044715f;
    unsigned sp[2];
#pragma unroll
    for (int m = 0; m < 4; m++) {
      f32x4 v[NC], t[NC];
      SB;
#pragma unroll
      for (int c = 0; c < NC; c++) t[c] = cs[c] * st[m][1] + b1[c];
      SB;
#pragma unroll
      for (int c = 0; c < NC; c++) v[c] = acc[m][c] * st[m][0] + t[c];
      SB;
#pragma unroll
      for (int c = 0; c < NC; c++) t[c] = v[c] * v[c];
      SB;
#pragma unroll
      for (int c = 0; c < NC; c++) t[c] = t[c] * c2 + c1;
      SB;
#pragma unroll
      for (int c = 0; c < NC; c++) t[c] = t[c] * v[c];
      SB;
#pragma unroll
      for (int c = 0; c < NC; c++)
#pragma unroll
        for (int e = 0; e < 4; e++) t[c][e] = __builtin_amdgcn_exp2f(t[c][e]);
      SB;
#pragma unroll
      for (int c = 0; c < NC; c++) t[c] = t[c] + 1.f;
      SB;
#pragma unroll
      for (int c = 0; c < NC; c++)
#pragma unroll
        for (int e = 0; e < 4; e++) t[c][e] = __builtin_amdgcn_rcpf(t[c][e]);
      SB;
#pragma unroll
      for (int c = 0; c < NC; c++) v[c] = v[c] * t[c];
      SB;
      unsigned o[NC][2];
#pragma unroll
      for (int c = 0; c < NC; c++) { o[c][0] = pk2(v[c][0], v[c][1]); o[c][1] = pk2(v[c][2], v[c][3]); }
      st_wt(ra, l2 * 16, o0 + m * KS_T * 1024, u32x4{o[0][0], o[0][1], o[1][0], o[1][1]});
      if (NC == 4) st_wt(ra, l2 * 16, o1 + m * KS_T * 1024, u32x4{o[2][0], o[2][1], o[NC - 1][0], o[NC - 1][1]});
      else if (m & 1) {
        auto s0 = __builtin_amdgcn_permlane32_swap(sp[0], o[2][0], false, false);
        auto s1 = __builtin_amdgcn_permlane32_swap(sp[1], o[2][1], false, false);
        st_wt(ra, (l2 >> 5) * (KS_T * 1024) + (l2 & 31) * 16, o1 + (m - 1) * KS_T * 1024, u32x4{s0[0], s1[0], s0[1], s1[1]});
      } else { sp[0] = o[2][0]; sp[1] = o[2][1]; }
    }
  }
}

template <bool MAX> DEVI float red4(const float (&v)[4]) {
  auto op = [](unsigned a, unsigned b) { float r; if (MAX) asm("v_max_f32 %0, %1, %2" : "=v"(r) : "v"(a), "v"(b)); else r = asf(a) + asf(b); return r; };
  auto a = __builtin_amdgcn_permlane32_swap(asu(v[0]), asu(v[1]), false, false);
  auto b = __builtin_amdgcn_permlane32_swap(asu(v[2]), asu(v[3]), false, false);
  auto c = __builtin_amdgcn_permlane16_swap(asu(op(a[0], a[1])), asu(op(b[0], b[1])), false, false);
  return op(c[0], c[1]);
}
DEVI void bcast4(float r, float (&v)[4]) {
  auto e = __builtin_amdgcn_permlane16_swap(asu(r), asu(r), false, false);
  unsigned e0 = e[0], e1 = e[1];
  auto a = __builtin_amdgcn_permlane32_swap(e0, e0, false, false);
  auto b = __builtin_amdgcn_permlane32_swap(e1, e1, false, false);
  v[0] = asf(a[0]); v[1] = asf(a[1]); v[2] = asf(b[0]); v[3] = asf(b[1]);
}

DEVI void att_table(lptr shm, int lane) {
  const int i = lane & 15, g = lane >> 4, p = lane & 3;
  const int d = sig(i) - g;
  const unsigned NEG = 0xf149f2cau;
  bool vo[4], vp[4];
#pragma unroll
  for (int r = 0; r < 4; r++) { vo[r] = (unsigned)(d - 4 * r) <= 7u; vp[r] = d + 16 - 4 * r <= 7; }
  auto pm = [](bool lo, bool hi) { return (lo ? 0xffffu : 0u) | (hi ? 0xffff0000u : 0u); };
  LAS u32x4* t = (LAS u32x4*)(shm + LDS_TAB) + lane;
  t[0] = u32x4{vo[0] ? 0u : NEG, vo[1] ? 0u : NEG, vo[2] ? 0u : NEG, vo[3] ? 0u : NEG};
  t[64] = u32x4{pm(vp[2], vp[3]), pm(vo[0], vo[1]), vp[2] ? 0u : NEG, vp[3] ? 0u : NEG};
  t[128] = u32x4{pm(vo[2], vo[3]), (unsigned)(LDS_TB + (g * 16 + (i ^ (8 * (g >> 1)))) * 8), (unsigned)(LDS_TB + (p * 16 + ((lane >> 2) ^ (8 * (p >> 1)))) * 8), 0u};
}
DEVI int sel(u64 mask, int a, int b) { int r; asm("v_cndmask_b32_e64 %0, %2, %1, %3" : "=v"(r) : "v"(a), "v"(b), "s"(mask)); return r; }

DEVI void s1_attention(lptr shm, int w, int p_, int j_, char* ws PR_ARGS) {
  arrive(3);
  const int lane = lane_id();
  const int tau = (w + 1) * 512, taup = w >= 2 ? tau - 1024 : 0, flip = w == 1 ? 16 : 0;
  const LAS u32x4* t = (const LAS u32x4*)(shm + LDS_TAB) + lane;
  const u32x4 t0 = t[0], t2 = t[128];
  u32x4 t1 = t[64];
  if (p_ == 0 && w < 2) { t1[0] = 0u; t1[2] = 0xf149f2cau; t1[3] = 0xf149f2cau; }
  const f32x4 mO = __builtin_bit_cast(f32x4, t0), mP = __builtin_bit_cast(f32x4, t1);
  const unsigned M1 = t1[0], M2 = t1[1], M3 = t2[0];
  const int po = t2[1] + tau, pp = (t2[1] ^ flip) + taup, to = t2[2] + tau, tp = (t2[2] ^ flip) + taup;
  int aq[4], avo[4], avp[4];
#pragma unroll
  for (int h = 0; h < 4; h++) {
    aq[h] = sel(0xffffull << (16 * h), po, LDS_ZERO - 4 * 4608);
    avo[h] = sel(0x1111111111111111ull << h, to, LDS_ZERO - 14 * 4608);
    avp[h] = sel(0x1111111111111111ull << h, tp, LDS_ZERO - 14 * 4608);
    asm volatile("" : "+v"(aq[h]), "+v"(avo[h]), "+v"(avp[h]));
  }
  STAMPA(14);
  await<0, 8>(3);
  STAMPA(15);
  u32x2 qa[4][2], ko[4][2], kp[4][2], va[4][2], vb[4][2];
  f32x4 sp[4], so[4];
  auto rd = [&](auto H) {
    constexpr int h = decltype(H)::value;
    ds64<(5 + h) * 4608>(ko[h][0], po); ds64<9 * 4608>(ko[h][1], po); ds64<h * 4608>(qa[h][0], po); ds64<4 * 4608>(qa[h][1], aq[h]);
    ds64<(5 + h) * 4608>(kp[h][0], pp); ds64<9 * 4608>(kp[h][1], pp);
  };
  auto rv = [&](auto H) {
    constexpr int h = decltype(H)::value;
    dstr<(10 + h) * 4608>(va[h][0], tp); dstr<(10 + h) * 4608>(va[h][1], to); dstr<14 * 4608>(vb[h][0], avp[h]); dstr<14 * 4608>(vb[h][1], avo[h]);
  };
  auto qk = [&](auto H, auto N) {
    constexpr int h = decltype(H)::value;
    asm volatile("s_waitcnt lgkmcnt(%6)" : "+v"(ko[h][0]), "+v"(ko[h][1]), "+v"(qa[h][0]), "+v"(qa[h][1]), "+v"(kp[h][0]), "+v"(kp[h][1]) : "i"(decltype(N)::value));
    so[h] = mfma(cat(ko[h][0], ko[h][1]), cat(qa[h][0], qa[h][1]), mO);
    sp[h] = mfma(cat(kp[h][0], kp[h][1]), cat(qa[h][0], qa[h][1]), mP);
  };
  rd(IC(0)); rd(IC(1));
  qk(IC(0), IC(6)); rd(IC(2));
  qk(IC(1), IC(6)); rd(IC(3));
  qk(IC(2), IC(6)); rv(IC(0)); rv(IC(1));
  qk(IC(3), IC(8)); rv(IC(2));
  asm volatile("s_waitcnt lgkmcnt(8)");
  rv(IC(3));
  const float qs = 0.22360679774997896f * 1.4426950408889634f;
  float ee[4], eo[4], mx[4], l[4];
#pragma unroll
  for (int h = 0; h < 4; h++) { ee[h] = fmaxf(fmaxf(sp[h][2], so[h][0]), so[h][2]); eo[h] = fmaxf(fmaxf(sp[h][3], so[h][1]), so[h][3]); mx[h] = fmaxf(ee[h], eo[h]); }
  bcast4(red4<true>(mx) * -qs, mx);
#pragma unroll
  for (int h = 0; h < 4; h++) {
    f32x2 t = f32x2{ee[h], eo[h]} * qs + mx[h];
    ee[h] = __builtin_amdgcn_exp2f(t[0]); eo[h] = __builtin_amdgcn_exp2f(t[1]); l[h] = ee[h] + eo[h];
  }
  bcast4(__builtin_amdgcn_rcpf(red4<false>(l)), l);
  asm volatile("s_waitcnt lgkmcnt(0)" : "+v"(va[0][0]), "+v"(va[0][1]), "+v"(va[1][0]), "+v"(va[1][1]), "+v"(va[2][0]), "+v"(va[2][1]), "+v"(va[3][0]), "+v"(va[3][1]),
               "+v"(vb[0][0]), "+v"(vb[0][1]), "+v"(vb[1][0]), "+v"(vb[1][1]), "+v"(vb[2][0]), "+v"(vb[2][1]), "+v"(vb[3][0]), "+v"(vb[3][1]));
  f32x4 o4 = {0, 0, 0, 0}, oh[4];
#pragma unroll
  for (int h = 0; h < 4; h++) {
    const unsigned pk = pk2(f32x2{ee[h], eo[h]} * l[h]);
    u32x4 pf = {0u, pk & M1, pk & M2, pk & M3};
    o4 = mfma(cat(vb[h][0], vb[h][1]), pf, o4);
    oh[h] = mfma(cat(va[h][0], va[h][1]), pf, f32x4{0, 0, 0, 0});
  }
  const u32x4 ra = mkr(ws + WS_A2, A2_BYTES);
  const int base = ((8 * p_ + w) * KS_T + 54 + 5 * (j_ >> 1)) * 1024;
#pragma unroll
  for (int hp = 0; hp < 2; hp++)
    st_wt(ra, lane * 16, base + ((j_ & 1) * 3 + hp) * 1024,
          u32x4{pk2(oh[2 * hp][0], oh[2 * hp][1]), pk2(oh[2 * hp][2], oh[2 * hp][3]), pk2(oh[2 * hp + 1][0], oh[2 * hp + 1][1]), pk2(oh[2 * hp + 1][2], oh[2 * hp + 1][3])});
  unsigned a0 = pk2(o4[0], o4[1]), a1 = pk2(o4[2], o4[3]);
  auto s0 = __builtin_amdgcn_permlane32_swap(a0, a0, false, false);
  auto s1 = __builtin_amdgcn_permlane32_swap(a1, a1, false, false);
  st_wt(ra, lane < 32 ? lane * 16 : 0x70000000, base + 2 * 1024 + (j_ & 1) * 512, u32x4{s0[0], s1[0], s0[1], s1[1]});
  arrive(4);
}

__global__ __launch_bounds__(768, 3) void fused_block(const void* __restrict__ x, const void* __restrict__ wqkv, const void* __restrict__ wo,
                                                      const void* __restrict__ w1, const unsigned short* __restrict__ b1,
                                                      const void* __restrict__ w2, const char* __restrict__ b2, char* __restrict__ out,
                                                      char* ws) {
  extern __shared__ __attribute__((aligned(16))) char shm_[];
  lptr shm = (lptr)shm_;
  const int tid = threadIdx.x, lane = tid & 63, w = __builtin_amdgcn_readfirstlane(tid >> 6);
  const int xcd = blockIdx.x & 7, idx = blockIdx.x >> 3;
  const int p_ = (xcd & 1) * 8 + (idx & 7), sub = idx >> 3;
  const bool att = !(sub & 1);
  const int j_ = (xcd >> 1) * 2 + (sub >> 1);
  const int unit = (xcd >> 1) * 28 + idx;
  const bool has2 = idx < 28 && unit < 110;
  const int mt = unit / 5, nl = unit - 5 * mt, n_ = (xcd & 1) * 5 + nl;
  const int G0 = 6 * mt;
  const int kq = w < 8 ? (w + (w >> 2)) & 3 : w & 3;
  const int rot = (ROT2);
  auto kperm = [&](int s) { return s ^ (s < 16 ? rot : rot & 1); };
  STAMP(0);
  if (w == 7) {
    u64 pc; asm volatile("s_getpc_b64 %0" : "=s"(pc));
    touch(mkr((const void*)(pc & ~255ull), CODE_BYTES), (idx + 32 * lane) * 128);
  }
  asm volatile("" : "+s"(x), "+s"(wqkv), "+s"(wo), "+s"(w1), "+s"(b1), "+s"(w2), "+s"(b2), "+s"(out), "+s"(ws));
  const int hf = KHF, tx = KTX;
  const int rid = (att ? 0 : 8) + j_;
  if (__builtin_expect(w >= 8, 1)) {
    __builtin_amdgcn_s_setprio(3);
    const int lw = w - 8;
    const int kt0 = (KT0) % 10;
    auto ktile = [&](int T) { const int k = kt0 + (T ^ (T < 8 ? tx : tx & 1)); return k >= 10 ? k - 10 : k; };
    const int RS = att ? 3840 : 3456;
    const u32x4 rx = mkr(x, S_ * D_ * 2), rw1 = mkr(att ? wqkv : w1, D_ * RS);
    const int r8 = lane >> 3, cp = lane & 7, t0 = p_ * 128;
    int xv[2], hv, wv;
#pragma unroll
    for (int e = 0; e < 2; e++)
      xv[e] = (t0 + 32 * lw + 2 * (4 * (r8 & 3) + 2 * e + (r8 >> 2))) * 1280 + ((cp ^ (4 * e + (r8 >> 1))) << 4);
    auto ldx = [&](int T) { const int o = ktile(T) * 128; dma4<1024>(rx, ~0ull, (T % 3) * SLOT + XOFF + 2048 + 4 * lw * 1024, xv[0], xv[1], o, o, o + 1280, o + 1280); };
    STAMP(12);
    ldx(0);
    {
      const int hi = lane >= 30, ch = lane - 30 * hi, lim = j_ < 7 ? 27 : 19;
      wv = hi * RS + (att ? ((ch >= 10) + (ch >= 20)) * 1120 + j_ * 160 + ch * 16 : j_ * 448 + (ch < lim ? ch : lim) * 16);
    }
    const int wrow = (16 * (lw & 1) + 4 * (lw >> 1)) * RS;
    auto ldw = [&](int T, int h) {
      const int so = (64 * ktile(T) + 32 * (h ^ hf)) * RS + wrow;
      dma4<960>(rw1, 0x0fffffffffffffffull, (T % 3) * SLOT + h * WSLAB + 4 * lw * 960, wv, wv, so, so + 2 * RS, so + 8 * RS, so + 10 * RS);
    };
    ldw(0, 0);
    {
      int tok = t0 - 16 + 8 * (r8 & 1) + 2 * lw + ((r8 & 3) < 2);
      tok = tok < 0 ? 0 : tok;
      hv = att ? tok * 1280 + ((cp ^ ((2 * lw + ((r8 & 3) >> 1)) & 7)) << 4) : 0x70000000;
    }
    const u64 hmask = lw == 0 ? 0xffffff00ull : 0xffffffffull;
    auto ldh = [&](int T) { dma1(rx, hmask, (T % 3) * SLOT + XOFF + lw * 512, hv, ktile(T) * 128); };
    ldh(0); ldw(0, 1);
    STAMP(13);
    bar_vm<4>();
    STAMP(14);
    ldx(1); ldh(1);
    bar_vm<5>();
    ldw(1, 0); ldw(1, 1);
    ldx(2); ldh(2);
    bar_vm<5>();
    const int srow = p_ * 128 + 8 * rid + 2 * lw + (lane >> 5);
    u32x2 xr[5];
    unsigned ep, bv;
    setup_loads(xr, ep, bv, rx, srow * 1280 + (lane & 31) * 8, mkr(ws, 4), mkr(b1, F_ * 2), (j_ * 224 + 64 * lw + lane) * 2);
    ldw(2, 0); ldw(2, 1); ldx(3); ldh(3); ldw(3, 0);
    bar_vm<9>();
    pin(ep); pin(bv);
#pragma unroll
    for (int k = 0; k < 5; k++) pin(xr[k]);
    const unsigned tag = __builtin_amdgcn_readfirstlane(ep) + 1u;
    ldw(3, 1); ldx(4); ldw(4, 0); ldh(4); ldw(4, 1);
    {
      float s = 0;
#pragma unroll
      for (int k = 0; k < 5; k++) s += (blo(xr[k][0]) + bhi(xr[k][0])) + (blo(xr[k][1]) + bhi(xr[k][1]));
      const float m = half_sum(s) * (1.f / D_);
      float q = 0;
#pragma unroll
      for (int k = 0; k < 10; k++) { const unsigned u = xr[k >> 1][k & 1]; const float a = blo(u) - m, b = bhi(u) - m; q = __builtin_fmaf(a, a, q); q = __builtin_fmaf(b, b, q); }
      const float rs = __builtin_amdgcn_rsqf(half_sum(q) * (1.f / D_) + 1e-5f);
      st_wt(mkr(ws + WS_STAT, S_ * 16), (lane & 31) == 0 ? srow * 16 : 0x70000000, 0, u32x4{asu(rs), tag, asu(-m * rs), tag});
      *(LAS float*)(shm + LDS_B1 + (64 * lw + lane) * 4) = blo(bv);
      *(LAS unsigned*)(shm + LDS_CNT + 4) = tag;
    }
    bar_vm<14>();
    const u32x4 rw = mkr(kq == 0 ? wo : w2, has2 ? (kq == 0 ? D_ * 1280 : F_ * 1280) : 0);
    const int cbyte = n_ * 128 + ((cp ^ ((lane >> 4) << 1)) << 4);
    const int v1 = r8 * 1280 + cbyte, v2 = kq == 0 ? (20 * (r8 >> 2) + (r8 & 3)) * 1280 + cbyte : v1;
    auto geom = [&](int s, int& rb, int& sa, int& sb, int& v) {
      s = kperm(s);
      const int P = (s * 13) >> 6, kp = s - 5 * P;
      v = v1 + ((v2 - v1) & -(int)(kp == 2));
      const int r0 = 160 * P + (kp == 2 ? 16 : 40 * (kp - (kp > 2))), r1 = 32 * (18 * (kq - 1) + s);
      rb = 1280 * (kq == 0 ? r0 : r1);
      sa = 1280 * (kq == 0 && kp == 2 ? 80 : 8);
      sb = 1280 * (kq == 0 ? (kp == 2 ? 40 : 20) : 16);
    };
    u32x4 pre[9][4];
    auto ldr = [&](auto R) {
      constexpr int r = decltype(R)::value;
      int rb, sa, sb, v; geom(9 + r, rb, sa, sb, v);
      bload(pre[r][0], rw, v, rb); bload(pre[r][1], rw, v, rb + sa); bload(pre[r][2], rw, v, rb + sb); bload(pre[r][3], rw, v, rb + sa + sb);
    };
    auto ldl = [&](auto S) {
      constexpr int s = decltype(S)::value;
      int rb, sa, sb, v; geom(s, rb, sa, sb, v);
      dma4<1024>(rw, ~0ull, s < 9 ? s * 16384 + kq * 4096 : 147456 + (s - 18) * 4096, v, v, rb, rb + sa, rb + sb, rb + sa + sb);
    };
    const u32x4 rst = mkr(ws + WS_STAT, S_ * 16);
    const int spos = 36 * lw + (lane < 36 ? lane : 35), sv = row_token(p_, spos) * 16;
    u32x4 sg;
    sfor<5>([&](auto TT) {
      constexpr int T = decltype(TT)::value + 5;
      if (T == 9) bload_sc1(sg, rst, sv, 0);
      ldx(T); ldw(T, 0); ldh(T); ldw(T, 1);
      bar_vm<13>();
    });
    STAMP(1);
    asm volatile("" : "+v"(sg));
#pragma nounroll
    for (unsigned spins = 0; !__all(sg[1] == tag && sg[3] == tag) && spins < SPIN_CAP; spins++) {
      __builtin_amdgcn_s_sleep(2);
      bload_sc1(sg, rst, sv, 0);
      asm volatile("s_waitcnt vmcnt(0)" : "+v"(sg) :: "memory");
    }
    *(LAS u32x2*)(shm + LDS_ST + spos * 8) = u32x2{sg[0], sg[2]};
    ldr(IC(0));
    wait_vm<4>();
    STAMP(2);
    bar_lgkm();
    __builtin_amdgcn_s_setprio(0);
    STAMP(3);
    if (!has2) { FINISH(); return; }
    sfor<8>([&](auto R) { ldr(IC(decltype(R)::value + 1)); wait_vm<THR2>(); });
    ldl(IC(3)); ldl(IC(4));
    STAMP(4);
    await<1, 8>(2);
    ldl(IC(0)); wait_vm<THR>(); ldl(IC(1)); wait_vm<THR>(); ldl(IC(2)); wait_vm<THR>();
    if (att) await<1, 8>(4);
    STAMP(5);
    ldl(IC(5)); wait_vm<THR>(); ldl(IC(6)); wait_vm<THR>(); ldl(IC(7)); wait_vm<THR>(); ldl(IC(8));
    if (kq == 0) { ldl(IC(18)); ldl(IC(19)); }
    STAMP(6);
    bar();
    bar_vm<0>();
    const int wa = kq * 4096 + lane * 16;
#define NAP __builtin_amdgcn_s_sleep(PSL);
#define PUT(r, off) if (!(ABL & 32)) { dsw128<off>(wa + (r / 3) * 49152, pre[r][0]); dsw128<off + 1024>(wa + (r / 3) * 49152, pre[r][1]); dsw128<off + 2048>(wa + (r / 3) * 49152, pre[r][2]); dsw128<off + 3072>(wa + (r / 3) * 49152, pre[r][3]); } else asm volatile("" :: "v"(pre[r][0]), "v"(pre[r][1]), "v"(pre[r][2]), "v"(pre[r][3]));
    PUT(0, 0) NAP PUT(1, 16384) NAP PUT(2, 32768)
    bar_lgkm();
    PUT(3, 0) NAP PUT(4, 16384) NAP PUT(5, 32768)
    bar_lgkm();
    PUT(6, 0) NAP PUT(7, 16384) NAP PUT(8, 32768)
    bar_lgkm();
    bar();
  } else {
    if (tid < 24) *(LAS u32x4*)(shm + (tid >> 3) * SLOT + XOFF + (tid & 7) * 16) = u32x4{0x3f803f80u, 0x3f803f80u, 0x3f803f80u, 0x3f803f80u};
    if (tid >= 448 && tid < 464) *(LAS unsigned*)(shm + LDS_CNT + 4 * (tid - 448)) = 0u;
    if (w == 5 && att) att_table(shm, lane);
    const bool light = att ? (w >= 6) : (w >= 4);
    if (light) s1_compute<3>(shm, att, w, lane, p_, j_, hf, ws PR_PASS);
    else s1_compute<4>(shm, att, w, lane, p_, j_, hf, ws PR_PASS);
    STAMP(3);
    if (att) s1_attention(shm, w, p_, j_, ws PR_PASS);
    STAMP(4);
    const unsigned tag = __builtin_amdgcn_readfirstlane(*(LAS unsigned*)(shm + LDS_CNT + 4));
    wait_vm<0>();
    const int lane = lane_id();
    GAS u64* flags = (GAS u64*)(ws + WS_FLAG);
    const u64 fv = flagval(tag);
    {
      unsigned old = 0;
      if (lane == 0) old = __hip_atomic_fetch_add((LAS unsigned*)(shm + LDS_CNT), 1u, __ATOMIC_RELAXED, __HIP_MEMORY_SCOPE_WORKGROUP);
      if (__builtin_amdgcn_readfirstlane(old) == 7) {
        const int dm = (lane * 26) >> 8, cm = (8 * p_) / 6 + dm;
        GAS u64* dst = lane == 30 ? flags + p_ * 16 + rid : (GAS u64*)(ws + WS_BOX) + (cm * 10 + lane - 10 * dm) * 32 + (p_ - ((6 * cm) >> 3)) * 16 + rid;
        if (lane == 30 || (lane < 30 && 6 * cm < 8 * p_ + 8)) __hip_atomic_store(dst, fv, __ATOMIC_RELAXED, __HIP_MEMORY_SCOPE_AGENT);
      }
    }
    STAMP(5);
    if (!has2) {
      if (blockIdx.x == 255 && w == 0) {
#pragma nounroll
        for (unsigned spins = 0; spins < SPIN_CAP; spins++) {
          bool ok = true;
#pragma unroll
          for (int k = 0; k < 4; k++) ok &= __hip_atomic_load(flags + lane + 64 * k, __ATOMIC_RELAXED, __HIP_MEMORY_SCOPE_AGENT) == fv;
          if (__all(ok)) break;
          __builtin_amdgcn_s_sleep(8);
        }
        if (lane == 0) __hip_atomic_store((GAS unsigned*)ws, tag, __ATOMIC_RELAXED, __HIP_MEMORY_SCOPE_AGENT);
      }
      FINISH();
      return;
    }
    const int mh = w >> 2, g = lane >> 4, q = (lane >> 2) & 3, p = lane & 3;
    const int ks0 = kq == 0 ? 54 : 18 * (kq - 1);
    const u32x4 ra = mkr(ws + WS_A2, A2_BYTES);
    int ao[3];
    const int av = lane * 16;
#pragma unroll
    for (int m = 0; m < 3; m++) { int G = G0 + 3 * mh + m; G = G < 128 ? G : 127; if (ABL & 16) G = 3 * mh + m; ao[m] = (G * KS_T + ks0) * 1024; if (ABL & 2) ao[m] = 0x70000000; }
    int ba[4], bs[4];
#pragma unroll
    for (int c = 0; c < 4; c++) ba[c] = kq * 4096 + (4 * g + q) * 128 + ((c ^ ((2 * g + (q >> 1)) & 3)) << 5) + 8 * p;
    f32x4 acc[3][4];
#pragma unroll
    for (int m = 0; m < 3; m++)
#pragma unroll
      for (int c = 0; c < 4; c++) { acc[m][c] = f32x4{0, 0, 0, 0}; asm volatile("" : "+v"(acc[m][c])); }
    if (w == 0) {
      const int Gb = G0 + 5 < 128 ? G0 + 5 : 127;
      const int nfl = (Gb >> 3) != (G0 >> 3) ? 31 : 15;
      GAS u64* fp = (GAS u64*)(ws + WS_BOX) + (mt * 10 + n_) * 32 + (lane & nfl);
#pragma nounroll
      for (unsigned spins = 0; spins < SPIN_CAP; spins++) {
        if (__all(__hip_atomic_load(fp, __ATOMIC_RELAXED, __HIP_MEMORY_SCOPE_AGENT) == fv)) break;
        __builtin_amdgcn_s_sleep(1);
      }
    }
    STAMP(6);
    bar();
    STAMP(7);
    u32x4 a[3][3];
#pragma unroll
    for (int m = 0; m < 3; m++) bload_sc1(a[0][m], ra, av, ao[m] + kperm(0) * 1024);
    u32x2 b0[4][2], b1f[4][2];
#pragma unroll
    for (int c = 0; c < 4; c++) { dstr<0>(b0[c][0], ba[c]); dstr<2048>(b0[c][1], ba[c]); }
#define LG0 asm volatile("s_waitcnt lgkmcnt(0)" ::: "memory")
    if (ABL & 1) { for (int c = 0; c < 4; c++) { dstr<0>(b1f[c][0], ba[c]); dstr<2048>(b1f[c][1], ba[c]); } }
    LG0;
#pragma unroll
    for (int m = 0; m < 3; m++) bload_sc1(a[1][m], ra, av, ao[m] + kperm(1) * 1024);
    auto step = [&](auto U, auto VV, u32x2 (&bc)[4][2], u32x2 (&bn)[4][2], int sl, int so) {
      constexpr int u = decltype(U)::value % 3, V = decltype(VV)::value;
      sfor<3>([&](auto M) {
        constexpr int m = decltype(M)::value;
        if (V < 0) bload_sc1(a[(u + 2) % 3][m], ra, av, ao[m] + so);
        if (V < 10) wait_vm<(V < 0 ? 6 : V < 10 ? V + 2 - m : 0)>();
        else
          asm volatile("s_cmp_lg_u32 %4, 0\n\ts_cbranch_scc1 .Lt%=\n\tbuffer_load_dwordx4 %0, %1, %2, %3 offen sc1\n\ts_waitcnt vmcnt(6)\n\ts_branch .Le%=\n"
                       ".Lt%=:\n\ts_waitcnt vmcnt(%5)\n.Le%=:"
                       : "=v"(a[(u + 2) % 3][m]) : "v"(av), "s"(ra), "s"(ao[m] + so), "s"(kq), "i"(V - 10 + 2 - m) : "memory", "scc");
#pragma unroll
        for (int c = 0; c < 4; c++) {
          if (!(ABL & 8)) mfma_a(acc[m][c], cat(bc[c][0], bc[c][1]), a[u][m]); else asm volatile("" :: "v"(bc[c][0]), "v"(bc[c][1]), "v"(a[u][m]));
          if (m < 2 && !(ABL & 1)) { if (c & 1) dstr<2048>(bn[2 * m + (c >> 1)][1], bs[2 * m + (c >> 1)]); else { vadd(bs[2 * m + (c >> 1)], ba[2 * m + (c >> 1)], sl); dstr<0>(bn[2 * m + (c >> 1)][0], bs[2 * m + (c >> 1)]); } }
        }
      });
    };
    STAMP(8);
#pragma nounroll
    for (int it = 0; it < 2; it++) {
      const int sb = it * 6 * 16384, sw = it * -3 * 16384, s0 = it * 6;
      step(IC(0), IC(-1), b0, b1f, sb + 16384, ((s0 + 2) ^ rot) * 1024); LG0;
      step(IC(1), IC(-1), b1f, b0, sb + 2 * 16384, ((s0 + 3) ^ rot) * 1024); LG0;
      step(IC(2), IC(-1), b0, b1f, sw + 3 * 16384, ((s0 + 4) ^ rot) * 1024); bar_lgkm();

      step(IC(3), IC(-1), b1f, b0, sw + 4 * 16384, ((s0 + 5) ^ rot) * 1024); LG0;
      step(IC(4), IC(-1), b0, b1f, sw + 5 * 16384, ((s0 + 6) ^ rot) * 1024); LG0;
      step(IC(5), IC(-1), b1f, b0, sw + 6 * 16384, ((s0 + 7) ^ rot) * 1024); bar_lgkm();

    }
    step(IC(0), IC(-1), b0, b1f, 4 * 16384, kperm(14) * 1024); LG0;
    step(IC(1), IC(-1), b1f, b0, 5 * 16384, kperm(15) * 1024); LG0;
    step(IC(2), IC(-1), b0, b1f, 6 * 16384, kperm(16) * 1024); bar_lgkm();
    step(IC(3), IC(-1), b1f, b0, 7 * 16384, kperm(17) * 1024); LG0;
    if (kq == 0) __builtin_amdgcn_s_setprio(3);
    step(IC(4), IC(13), b0, b1f, 8 * 16384, kperm(18) * 1024); LG0;
    step(IC(5), IC(10), b1f, b0, 147456, kperm(19) * 1024); LG0;
    if (kq == 0) {
      step(IC(0), IC(3), b0, b1f, 151552, 0); LG0;
      step(IC(1), IC(0), b1f, b0, 151552, 0); LG0;
    }
    asm volatile("s_nop 7" ::: "memory");
    STAMP(9);
    const int l2 = lane_id(), i2 = l2 & 15, g2 = l2 >> 4;
#pragma unroll
    for (int m = 0; m < 3; m++)
#pragma unroll
      for (int c = 0; c < 4; c++) {
        asm volatile("" : "+v"(acc[m][c]));
        *(LAS f32x4*)(shm + (3 * mh + m) * 16384 + kq * 4096 + i2 * 256 + (((4 * c + g2) ^ (i2 & 7)) << 4)) = acc[m][c];
      }
  }
  {
    const int lane = lane_id();
    const int frow = 8 * w + (lane >> 3), fG = G0 + (frow >> 4);
    const bool fvalid = fG < 128;
    const int ftok = 32 * ((fvalid ? fG : 0) >> 1) + 2 * sig(frow & 15) + (fG & 1);
    const int foff = ftok * 1280 + n_ * 128 + (lane & 7) * 16;
    const u32x4 xv = *(const u32x4*)((const char*)x + foff);
    const u32x4 bv = *(const u32x4*)(b2 + n_ * 128 + (lane & 7) * 16);
    bar_lgkm();
    STAMP(10);
    f32x4 s[2], y[2];
#pragma unroll
    for (int e = 0; e < 2; e++) {
      lptr pr = shm + (frow >> 4) * 16384 + (frow & 15) * 256 + (((2 * (lane & 7) + e) ^ (frow & 7)) << 4);
      s[e] = *(LAS f32x4*)pr;
      y[e] = *(LAS f32x4*)(pr + 4096) + *(LAS f32x4*)(pr + 8192) + *(LAS f32x4*)(pr + 12288);
    }
    u32x4 o;
#pragma unroll
    for (int k = 0; k < 4; k++) {
      f32x2 yy = f32x2{y[k >> 1][2 * (k & 1)], y[k >> 1][2 * (k & 1) + 1]} + f32x2{blo(bv[k]), bhi(bv[k])};
      f32x2 t = yy * -1.4426950408889634f;
      f32x2 e = f32x2{__builtin_amdgcn_exp2f(t[0]), __builtin_amdgcn_exp2f(t[1])} + 1.f;
      f32x2 gt = yy * f32x2{__builtin_amdgcn_rcpf(e[0]), __builtin_amdgcn_rcpf(e[1])};
      f32x2 r = f32x2{s[k >> 1][2 * (k & 1)], s[k >> 1][2 * (k & 1) + 1]} * gt + f32x2{blo(xv[k]), bhi(xv[k])};
      o[k] = pk2(r);
    }
    if (fvalid) *(u32x4*)(out + foff) = o;
  }
  STAMP(11);
  FINISH();
}

extern "C" void kernel_launch(void* const* d_in, const int* in_sizes, int n_in, void* d_out, int out_size, void* d_ws,
                              size_t ws_size, hipStream_t stream) {
  (void)hipFuncSetAttribute((const void*)fused_block, hipFuncAttributeMaxDynamicSharedMemorySize, LDS_TOTAL);
  fused_block<<<256, 768, LDS_TOTAL, stream>>>(d_in[0], d_in[1], d_in[2], d_in[4], (const unsigned short*)d_in[5], d_in[6],
                                               (const char*)d_in[7], (char*)d_out, (char*)d_ws);
}
```

```cpp
#define OUTWT 2
#include <hip/hip_runtime.h>
#include <hip/hip_bf16.h>
#include <type_traits>
#include <utility>

typedef __attribute__((ext_vector_type(4))) short s16x4;
typedef __attribute__((ext_vector_type(8))) __bf16 bf16x8;
typedef __attribute__((ext_vector_type(2))) __bf16 bf16x2;
typedef __attribute__((ext_vector_type(4))) float f32x4;
typedef __attribute__((ext_vector_type(2))) float f32x2;
typedef __attribute__((ext_vector_type(4))) unsigned u32x4;
typedef __attribute__((ext_vector_type(2))) unsigned u32x2;
#define LAS __attribute__((address_space(3)))
#define GAS __attribute__((address_space(1)))
#define DEVI __device__ __forceinline__
typedef LAS char* lptr;
typedef unsigned long long u64;
#define IC(n) std::integral_constant<int, n>{}

constexpr int S_ = 2048, D_ = 640, F_ = 1728;
constexpr int KS_T = 74;
constexpr int WSLAB = 15360;
constexpr int XOFF = 2 * WSLAB;
constexpr int SLOT = 49152;
constexpr int LDS_TB = 86016;
constexpr int LDS_ST = 155648;
constexpr int LDS_CS = LDS_ST + 1152;
constexpr int LDS_B1 = LDS_CS + 1024;
constexpr int LDS_CNT = LDS_B1 + 1024;
constexpr int LDS_ZERO = LDS_CNT + 24;
constexpr int LDS_CONS = LDS_CNT + 64;
constexpr int LDS_PROD = LDS_CONS + 32;
constexpr int LDS_TAB = LDS_CNT + 128;
constexpr int LDS_TOTAL = 163840;
constexpr int WS_FLAG = 4096, WS_STAT = 8192, WS_BOX = 65536, WS_A2 = 1 << 20;
constexpr int A2_BYTES = 128 * KS_T * 1024;
#define PF_CODE 1
#define PF_MALL 0
#define MAP2 0
#define S1MODE 0
#define S2MODE 0
#define THR 4
#define PSL 0
#define PF_TLB 0
#define INLOOP 0
#define PFS 128
#define PFK0 2
#define PFALL 0
#define PFW2 0
#define KHF (idx & 1)
#define KTX (((idx >> 1) ^ sub) & 1)
#define KT0 0
#define THR2 4
#define SPS 0
#define LPRIO 0
#define SPRIO 0
#define AD 3
#define ASL 3
#define GSL 1
#define FSL 1
#define FSL2 0
#define STW 0
#define WSOFF 0
#define LP2 0
#define ABL 0
#define ROT1 0
#define ROT2 (((mt + nl) & 7) | ((mt & 1) << 3))
constexpr int CODE_BYTES = 37760;
constexpr unsigned SPIN_CAP = 1u << 20;

#define STAMP(idx)
#define FINISH()
#define STAMPC(k)
#define STAMPA(k) STAMP(k)
#define STAMPB(k)
#define PR_ARGS
#define PR_PASS

DEVI void bar_lgkm() { asm volatile("s_waitcnt lgkmcnt(0)\n\ts_barrier" ::: "memory"); }
DEVI void bar() { asm volatile("s_barrier" ::: "memory"); }
template <int N> DEVI void bar_vm() { asm volatile("s_waitcnt vmcnt(%0)\n\ts_barrier" ::"i"(N) : "memory"); }
template <int N> DEVI void wait_vm() { asm volatile("s_waitcnt vmcnt(%0)" ::"i"(N) : "memory"); }
DEVI unsigned pk2(float a, float b) { f32x2 v = {a, b}; return __builtin_bit_cast(unsigned, __builtin_convertvector(v, bf16x2)); }
DEVI unsigned pk2(f32x2 v) { return __builtin_bit_cast(unsigned, __builtin_convertvector(v, bf16x2)); }
DEVI float asf(unsigned u) { return __builtin_bit_cast(float, u); }
DEVI unsigned asu(float f) { return __builtin_bit_cast(unsigned, f); }
DEVI float blo(unsigned u) { return __builtin_bit_cast(float, u << 16); }
DEVI float bhi(unsigned u) { return __builtin_bit_cast(float, u & 0xffff0000u); }
DEVI u32x2 tr_read(lptr p) { return __builtin_bit_cast(u32x2, __builtin_amdgcn_ds_read_tr16_b64_v4i16((LAS s16x4*)p)); }
DEVI f32x4 mfma(u32x4 a, u32x4 b, f32x4 c) {
  return __builtin_amdgcn_mfma_f32_16x16x32_bf16(__builtin_bit_cast(bf16x8, a), __builtin_bit_cast(bf16x8, b), c, 0, 0, 0);
}
DEVI u32x4 cat(u32x2 a, u32x2 b) { return __builtin_shufflevector(a, b, 0, 1, 2, 3); }
DEVI void mfma_a(f32x4& c, u32x4 a, u32x4 b) { asm volatile("v_mfma_f32_16x16x32_bf16 %0, %1, %2, %0" : "+v"(c) : "v"(a), "v"(b)); }
template <int OFF> DEVI void ds128(u32x4& d, int addr) { asm volatile("ds_read_b128 %0, %1 offset:%2" : "=v"(d) : "v"(addr), "i"(OFF)); }
template <int OFF> DEVI void dstr(u32x2& d, int addr) { asm volatile("ds_read_b64_tr_b16 %0, %1 offset:%2" : "=v"(d) : "v"(addr), "i"(OFF)); }
template <int OFF> DEVI void dsw128(int addr, u32x4 d) { asm volatile("ds_write_b128 %0, %1 offset:%2" ::"v"(addr), "v"(d), "i"(OFF) : "memory"); }
DEVI u32x4 mkr(const void* p, unsigned bytes) {
  u64 a = (u64)p;
  return u32x4{(unsigned)a, (unsigned)(a >> 32) & 0xffffu, bytes, 0x00020000u};
}
DEVI void st_wt(u32x4 r, int voff, int soff, u32x4 v) {
  asm volatile("buffer_store_dwordx4 %0, %1, %2, %3 offen sc1\n\ts_nop 1" ::"v"(v), "v"(voff), "s"(r), "s"(soff) : "memory");
}
template <int LSTEP>
DEVI void dma4(u32x4 r, u64 mask, int l, int va, int vb, int s0, int s1, int s2, int s3) {
  asm volatile(
      "s_mov_b64 exec, %0\n\t"
      "s_mov_b32 m0, %1\n\ts_nop 0\n\tbuffer_load_dwordx4 %2, %4, %5 offen lds\n\t"
      "s_add_u32 m0, %1, %9\n\ts_nop 0\n\tbuffer_load_dwordx4 %3, %4, %6 offen lds\n\t"
      "s_add_u32 m0, %1, %10\n\ts_nop 0\n\tbuffer_load_dwordx4 %2, %4, %7 offen lds\n\t"
      "s_add_u32 m0, %1, %11\n\ts_nop 0\n\tbuffer_load_dwordx4 %3, %4, %8 offen lds\n\t"
      "s_mov_b64 exec, -1"
      :: "s"(mask), "s"(l), "v"(va), "v"(vb), "s"(r), "s"(s0), "s"(s1), "s"(s2), "s"(s3), "i"(LSTEP), "i"(2 * LSTEP), "i"(3 * LSTEP)
      : "memory", "scc");
}
DEVI void dma1(u32x4 r, u64 mask, int l, int v, int s0) {
  asm volatile("s_mov_b64 exec, %0\n\ts_mov_b32 m0, %1\n\ts_nop 0\n\tbuffer_load_dwordx4 %2, %3, %4 offen lds\n\ts_mov_b64 exec, -1"
               :: "s"(mask), "s"(l), "v"(v), "s"(r), "s"(s0) : "memory");
}
DEVI void setup_loads(u32x2 (&xr)[5], unsigned& ep, unsigned& bv, u32x4 rx, int vx, u32x4 re, u32x4 rb, int vb) {
  asm volatile("buffer_load_dwordx2 %0, %7, %8, 0 offen\n\tbuffer_load_dwordx2 %1, %7, %8, 0 offen offset:256\n\tbuffer_load_dwordx2 %2, %7, %8, 0 offen offset:512\n\t"
               "buffer_load_dwordx2 %3, %7, %8, 0 offen offset:768\n\tbuffer_load_dwordx2 %4, %7, %8, 0 offen offset:1024\n\t"
               "buffer_load_dword %5, off, %9, 0 sc1\n\tbuffer_load_ushort %6, %11, %10, 0 offen"
               : "=&v"(xr[0]), "=&v"(xr[1]), "=&v"(xr[2]), "=&v"(xr[3]), "=&v"(xr[4]), "=&v"(ep), "=&v"(bv)
               : "v"(vx), "s"(rx), "s"(re), "s"(rb), "v"(vb) : "memory");
}
template <int WHEN>
DEVI void bload4_if(u32x4 (&d)[4], u32x4 r, int v, int s0, int s1, int s2, int s3, int flag) {
#define LDR_BODY "s_cmp_lg_u32 %10, %11\n\ts_cbranch_scc1 .Lr%=\n\tbuffer_load_dwordx4 %0, %4, %5, %6 offen\n\tbuffer_load_dwordx4 %1, %4, %5, %7 offen\n\t" \
                 "buffer_load_dwordx4 %2, %4, %5, %8 offen\n\tbuffer_load_dwordx4 %3, %4, %5, %9 offen\n.Lr%=:"
  if (WHEN) asm volatile(LDR_BODY : "+v"(d[0]), "+v"(d[1]), "+v"(d[2]), "+v"(d[3]) : "v"(v), "s"(r), "s"(s0), "s"(s1), "s"(s2), "s"(s3), "s"(flag), "i"(WHEN) : "scc");
  else asm volatile(LDR_BODY : "=&v"(d[0]), "=&v"(d[1]), "=&v"(d[2]), "=&v"(d[3]) : "v"(v), "s"(r), "s"(s0), "s"(s1), "s"(s2), "s"(s3), "s"(flag), "i"(WHEN) : "scc");
}
template <int N0, int N1>
DEVI void bar_vm_sel(int flag) {
  asm volatile("s_cmp_lg_u32 %0, 0\n\ts_cbranch_scc1 .La%=\n\ts_waitcnt vmcnt(%1)\n\ts_branch .Lb%=\n.La%=:\n\ts_waitcnt vmcnt(%2)\n.Lb%=:\n\ts_barrier"
               ::"s"(flag), "i"(N0), "i"(N1) : "memory", "scc");
}
template <class T> DEVI void pin(T& v) { asm volatile("" : "+v"(v)); }
DEVI void dsw_col0(int a0, f32x4 d0, int a1, f32x4 d1, int a2, f32x4 d2) {
  asm volatile("s_mov_b64 exec, %6\n\tds_write_b128 %0, %1\n\tds_write_b128 %2, %3\n\tds_write_b128 %4, %5\n\ts_mov_b64 exec, -1"
               ::"v"(a0), "v"(d0), "v"(a1), "v"(d1), "v"(a2), "v"(d2), "s"(0x0001000100010001ull) : "memory");
}
DEVI void vadd(int& d, int a, int s) { asm volatile("v_add_u32 %0, %2, %1" : "=v"(d) : "v"(a), "s"(s)); }
template <class F, int... I> DEVI void sfor_(F f, std::integer_sequence<int, I...>) { (f(std::integral_constant<int, I>{}), ...); }
template <int N, class F> DEVI void sfor(F f) { sfor_(f, std::make_integer_sequence<int, N>{}); }
template <int OFF> DEVI void stouch1(unsigned& sink, u32x4 r, int off) { asm volatile("s_buffer_load_dword %0, %1, %2 offset:%3" : "+s"(sink) : "s"(r), "s"(off), "i"(OFF)); }
template <int N>
DEVI void stouch(unsigned& sink, u32x4 r, int off) {
  sfor<N>([&](auto K) { stouch1<decltype(K)::value * 128>(sink, r, off); });
}
DEVI void touchg(const void* p) {
  asm volatile("s_mov_b32 m0, %1\n\ts_nop 0\n\tglobal_load_lds_dword %0, off" ::"v"(p), "i"(LDS_TOTAL - 256) : "memory");
}
DEVI void touch(u32x4 r, int v) {
  asm volatile("s_mov_b32 m0, %2\n\ts_nop 0\n\tbuffer_load_dword %0, %1, 0 offen lds" ::"v"(v), "s"(r), "i"(LDS_TOTAL - 256) : "memory");
}
DEVI void bload(u32x4& d, u32x4 r, int v, int s0) { asm volatile("buffer_load_dwordx4 %0, %1, %2, %3 offen" : "=v"(d) : "v"(v), "s"(r), "s"(s0)); }
DEVI void bload_sc1(u32x4& d, u32x4 r, int v, int s0) { if (ABL & 4) { bload(d, r, v, s0); return; } asm volatile("buffer_load_dwordx4 %0, %1, %2, %3 offen sc1" : "=v"(d) : "v"(v), "s"(r), "s"(s0) : "memory"); }
DEVI void arrive(int k) {
  asm volatile("s_mov_b64 exec, 1\n\tds_add_u32 %0, %1\n\ts_mov_b64 exec, -1" ::"v"(LDS_CNT + 4 * k), "v"(1) : "memory");
}
template <int SLEEP, unsigned N>
DEVI void await(int k) {
  unsigned v;
  for (;;) {
    asm volatile("ds_read_b32 %0, %1\n\ts_waitcnt lgkmcnt(0)" : "=v"(v) : "v"(LDS_CNT + 4 * k) : "memory");
    if (__builtin_amdgcn_readfirstlane(v) >= N) break;
    if (SLEEP) __builtin_amdgcn_s_sleep(SLEEP);
  }
}
DEVI void post(int addr, int val) { asm volatile("ds_write_b32 %0, %1" ::"v"(addr), "v"(val) : "memory"); }
DEVI void peek(unsigned& v, int addr) { asm volatile("ds_read_b32 %0, %1" : "=v"(v) : "v"(addr) : "memory"); }
DEVI void require(unsigned v, int addr, unsigned n) {
  unsigned t;
  asm volatile("v_readfirstlane_b32 %1, %0\n\ts_cmp_ge_u32 %1, %3\n\ts_cbranch_scc1 .Lq%=\n"
               ".Lw%=:\n\tds_read_b32 %0, %2\n\ts_waitcnt lgkmcnt(0)\n\tv_readfirstlane_b32 %1, %0\n\ts_cmp_ge_u32 %1, %3\n\ts_cbranch_scc0 .Lw%=\n.Lq%=:"
               : "+&v"(v), "=&s"(t) : "v"(addr), "s"(n) : "memory", "scc");
}
DEVI void await_pair(int addr, unsigned n) {
  for (;;) {
    u32x2 v;
    asm volatile("ds_read_b64 %0, %1\n\ts_waitcnt lgkmcnt(0)" : "=v"(v) : "v"(addr) : "memory");
    const unsigned a = v[0], b = v[1];
    if (__builtin_amdgcn_readfirstlane(a < b ? a : b) >= n) break;
    __builtin_amdgcn_s_sleep(1);
  }
}
DEVI int lane_id() { int l; asm volatile("v_mbcnt_lo_u32_b32 %0, -1, 0\n\tv_mbcnt_hi_u32_b32 %0, -1, %0" : "=v"(l)); return l; }
DEVI int sig(int i) { return 4 * (i & 3) + (i >> 2); }
DEVI u64 flagval(unsigned tag) { return ((u64)(tag ^ 0x5bd1e995u) << 32) | tag; }

DEVI float xmax16(float v) { auto r = __builtin_amdgcn_permlane16_swap(asu(v), asu(v), false, false); return fmaxf(asf(r[0]), asf(r[1])); }
DEVI float xmax32(float v) { auto r = __builtin_amdgcn_permlane32_swap(asu(v), asu(v), false, false); return fmaxf(asf(r[0]), asf(r[1])); }
DEVI float xadd16(float v) { auto r = __builtin_amdgcn_permlane16_swap(asu(v), asu(v), false, false); return asf(r[0]) + asf(r[1]); }
DEVI float xadd32(float v) { auto r = __builtin_amdgcn_permlane32_swap(asu(v), asu(v), false, false); return asf(r[0]) + asf(r[1]); }
template <int CTRL> DEVI float dpp(float v) { return asf(__builtin_amdgcn_mov_dpp(asu(v), CTRL, 0xf, 0xf, true)); }
DEVI float half_sum(float v) {
  v += dpp<0xb1>(v); v += dpp<0x4e>(v); v += dpp<0x141>(v); v += dpp<0x140>(v);
  return xadd16(v);
}

DEVI int row_token(int p_, int pos) {
  const int tau = pos >> 4, i = pos & 15;
  int tok = tau == 0 ? p_ * 128 - 16 + 8 * (i & 1) + 2 * (i >> 2) + ((i & 3) < 2) : p_ * 128 + 32 * ((tau - 1) >> 1) + 2 * sig(i) + ((tau - 1) & 1);
  return tok < 0 ? 0 : tok;
}

struct ColGeo { int A, B, C, ct; };
DEVI ColGeo colgeo(bool att, int nw, int mw, int c, bool heavy) {
  const int cc = heavy ? (c ^ (2 * mw)) : c;
  const int ct = 4 * nw + cc, sec = (ct * 13) >> 6, hh = ct - 5 * sec;
  const bool q4 = hh == 4, sp = !heavy && c == 2;
  ColGeo r;
  r.A = att ? sec * 160 + (q4 ? 32 : 40 * hh) : heavy ? 128 * nw + 32 * cc : (sp ? 352 + 16 * nw : 128 + 64 * nw + 32 * c);
  r.B = att && q4 ? 40 : 8;
  r.C = att ? (q4 ? 80 : 16) : (sp ? 32 : 16);
  r.ct = ct;
  return r;
}

template <int NC>
struct Frags { u32x4 x[5]; u32x2 w[NC][2]; };

template <int N> DEVI void wait_lg() { asm volatile("s_waitcnt lgkmcnt(%0)" ::"i"(N) : "memory"); }

template <int NC, int K>
DEVI void s1_rd(Frags<NC>& n, int xo, int xh, const int (&wa)[NC]) {
  if constexpr (K == 0) ds128<0>(n.x[0], xo);
  else if constexpr (K <= 2 * NC) { if ((K - 1) & 1) dstr<7680>(n.w[(K - 1) >> 1][1], wa[(K - 1) >> 1]); else dstr<0>(n.w[(K - 1) >> 1][0], wa[(K - 1) >> 1]); }
  else if constexpr (K < 2 * NC + 4) ds128<(K - 2 * NC) * 2048>(n.x[K - 2 * NC], xo);
  else if constexpr (K == 2 * NC + 4) ds128<0>(n.x[4], xh);
}
template <int NC, int NH, int K>
DEVI void s1_mm(const Frags<NC>& c, f32x4 (&acc)[4][NC], f32x4 (&hacc)[NH]) {
  if constexpr (K < 4 * NC) mfma_a(acc[K / NC][K % NC], cat(c.w[K % NC][0], c.w[K % NC][1]), c.x[K / NC]);
  else mfma_a(hacc[K - 4 * NC], cat(c.w[K - 4 * NC][0], c.w[K - 4 * NC][1]), c.x[4]);
}
template <int NC, int NH, bool BAR>
DEVI void s1_step(const Frags<NC>& c, Frags<NC>& n, int xo, int xh, const int (&wa)[NC], f32x4 (&acc)[4][NC], f32x4 (&hacc)[NH]) {
  constexpr int NM = 4 * NC + NH, NR = 2 * NC + 5, D = BAR ? NM - NR : 0;
  sfor<NM>([&](auto KK) {
    constexpr int k = decltype(KK)::value;
    constexpr int need = k < NC ? 2 * k + 2 : (k % NC == 0 && k <= 4 * NC) ? 2 * NC + k / NC : -1;
    if constexpr (BAR && k == D) bar_lgkm();
    if constexpr (need >= 0 && (!BAR || k < D)) wait_lg<NR - 1 - need + (BAR ? 0 : (k < NR ? k : NR))>();
    s1_mm<NC, NH, k>(c, acc, hacc);
    if constexpr (k >= D) s1_rd<NC, k - D>(n, xo, xh, wa);
  });
}

template <int OFF> DEVI void dsw64(int addr, u32x2 d) { asm volatile("ds_write_b64 %0, %1 offset:%2" ::"v"(addr), "v"(d), "i"(OFF) : "memory"); }
template <int OFF> DEVI void ds64(u32x2& d, int addr) { asm volatile("ds_read_b64 %0, %1 offset:%2" : "=v"(d) : "v"(addr), "i"(OFF)); }

template <int NC>
DEVI void s1_compute(lptr shm, bool att, int w, int lane, int p_, int j_, int hf, char* ws PR_ARGS) {
  constexpr int NH = NC == 4 ? 2 : 3;
  const int mw = w & 1, nw = w >> 1, i = lane & 15, g = lane >> 4;
  f32x4 acc[4][NC], hacc[NH];
#pragma unroll
  for (int m = 0; m < 4; m++)
#pragma unroll
    for (int c = 0; c < NC; c++) { acc[m][c] = f32x4{0, 0, 0, 0}; asm volatile("" : "+v"(acc[m][c])); }
#pragma unroll
  for (int c = 0; c < NH; c++) { hacc[c] = f32x4{0, 0, 0, 0}; asm volatile("" : "+v"(hacc[c])); }
  int wb[NC], cb[NC], ct[NC];
#pragma unroll
  for (int c = 0; c < NC; c++) {
    const ColGeo cg = colgeo(att, nw, mw, c, NC == 4);
    wb[c] = (lane >> 2) * 480 + cg.A + cg.B * (lane & 1) + cg.C * ((lane >> 1) & 1);
    cb[c] = LDS_CS + 2 * (cg.A + cg.B * (g & 1) + cg.C * (g >> 1));
    ct[c] = cg.ct;
    asm volatile("" : "+v"(cb[c]), "+v"(wb[c]));
  }
  int xb = XOFF + i * 128 + ((g ^ (i >> 1) ^ (4 * hf)) << 4);
  int sta = LDS_ST + i * 8;
  asm volatile("" : "+v"(xb), "+v"(sta));
  const int own = (1 + 4 * mw) * 2048;
  Frags<NC> fa, fb;
  int wa[NC];
  STAMPB(12);
  bar_lgkm();
  STAMP(1);
  sfor<2 * NC + 5>([&](auto K) { s1_rd<NC, decltype(K)::value>(fa, xb + own, xb, wb); });
#pragma unroll
  for (int c = 0; c < NC; c++) wa[c] = wb[c] + WSLAB;
  s1_step<NC, NH, true>(fa, fb, (xb ^ 64) + own, xb ^ 64, wa, acc, hacc);
  int sl = 0;
#pragma nounroll
  for (int T = 1; T < 10; T++) {
    sl = sl == 2 * SLOT ? 0 : sl + SLOT;
#pragma unroll
    for (int c = 0; c < NC; c++) wa[c] = wb[c] + sl;
    s1_step<NC, NH, true>(fb, fa, xb + sl + own, xb + sl, wa, acc, hacc);
#pragma unroll
    for (int c = 0; c < NC; c++) wa[c] = wb[c] + sl + WSLAB;
    s1_step<NC, NH, false>(fa, fb, (xb ^ 64) + sl + own, (xb ^ 64) + sl, wa, acc, hacc);
  }
  wait_lg<0>();
  arrive(2);
  sfor<NH>([&](auto K) { s1_mm<NC, NH, 4 * NC + decltype(K)::value>(fb, acc, hacc); });
  u32x2 stu[5];
  u32x4 b1u[NC], csu[NC];
  sfor<4 * NC>([&](auto KK) {
    constexpr int k = decltype(KK)::value;
    s1_mm<NC, NH, k>(fb, acc, hacc);
    if constexpr (k < 4) ds64<(1 + k) * 128>(stu[k], sta + mw * 512);
    if constexpr (k == 4) ds64<0>(stu[4], sta);
    if constexpr (k == 5) {
      dsw_col0(cb[0], hacc[0], cb[1], hacc[1], cb[NH - 1], hacc[NH - 1]);
      if (NC == 4) arrive(8 + nw);
    }
    if constexpr (k >= 6 && k < 6 + NC) ds128<LDS_B1 - LDS_CS>(b1u[k - 6], cb[k - 6]);
  });
  STAMP(2);
  if (NC == 4) await<0, 2>(8 + nw);
  if (ABL & 16384) { arrive(5); await<1, 8>(5); }
#pragma unroll
  for (int c = 0; c < NC; c++) ds128<0>(csu[c], cb[c]);
  asm volatile("s_waitcnt lgkmcnt(0)\n\ts_nop 7" ::: "memory");
  f32x2 st[5];
  f32x4 cs[NC], b1[NC];
#pragma unroll
  for (int m = 0; m < 5; m++) { asm volatile("" : "+v"(stu[m])); st[m] = __builtin_bit_cast(f32x2, stu[m]); }
#pragma unroll
  for (int c = 0; c < NC; c++) { asm volatile("" : "+v"(csu[c]), "+v"(b1u[c])); cs[c] = __builtin_bit_cast(f32x4, csu[c]); b1[c] = __builtin_bit_cast(f32x4, b1u[c]); }
#pragma unroll
  for (int m = 0; m < 4; m++)
#pragma unroll
    for (int c = 0; c < NC; c++) pin(acc[m][c]);
#pragma unroll
  for (int c = 0; c < NH; c++) pin(hacc[c]);
  STAMPA(12);
#define SB __builtin_amdgcn_sched_barrier(0)
  const int l2 = lane_id();
  if (att) {
    const int g2 = l2 >> 4, piece = LDS_TB + ((l2 & 48) + ((l2 & 15) ^ (8 * (g2 >> 1)))) * 8;
    sfor<NC>([&](auto C) {
      constexpr int c = decltype(C)::value;
      const int ah = piece + ct[c] * 4608, ao = ah + (1 + 4 * mw) * 512;
      u32x2 o[5];
      SB;
#pragma unroll
      for (int m = 0; m < (c < NH ? 5 : 4); m++) {
        f32x4 a = m < 4 ? acc[m < 4 ? m : 0][c] : hacc[c < NH ? c : 0];
        f32x4 v = a * st[m][0] + cs[c] * st[m][1];
        o[m] = u32x2{pk2(v[0], v[1]), pk2(v[2], v[3])};
      }
      if (ABL & 4096) { pin(o[0]); pin(o[1]); pin(o[2]); pin(o[3]); if (c < NH) pin(o[4]); }
      else if ((ABL & 8192) && (c & 1)) { pin(o[0]); pin(o[1]); pin(o[2]); pin(o[3]); if (c < NH) pin(o[4]); }
      else {
      dsw64<0>(ao, o[0]); dsw64<512>(ao, o[1]); dsw64<1024>(ao, o[2]); dsw64<1536>(ao, o[3]);
      if (c < NH) dsw64<0>(ah, o[4]);
      }
    });
  } else {
    const u32x4 ra = mkr(ws + WS_A2, A2_BYTES);
    const int base = ((8 * p_ + 4 * mw) * KS_T + 7 * j_) * 1024;
    const int o0 = base + (NC == 4 ? (2 * nw + mw) * 1024 : (j_ < 7 || nw == 2) ? (2 + nw) * 1024 : 0x70000000);
    const int o1 = base + (NC == 4 ? (2 * nw + (mw ^ 1)) * 1024 : j_ < 7 ? 6 * 1024 + (nw - 2) * 512 : 0x70000000);
    const float c1 = -2.f * 0.7978845608028654f * 1.4426950408889634f, c2 = c1 * 0.044715f;
    unsigned sp[2];
#pragma unroll
    for (int m = 0; m < 4; m++) {
      f32x4 v[NC], t[NC];
      SB;
#pragma unroll
      for (int c = 0; c < NC; c++) t[c] = cs[c] * st[m][1] + b1[c];
      SB;
#pragma unroll
      for (int c = 0; c < NC; c++) v[c] = acc[m][c] * st[m][0] + t[c];
      SB;
#pragma unroll
      for (int c = 0; c < NC; c++) t[c] = v[c] * v[c];
      SB;
#pragma unroll
      for (int c = 0; c < NC; c++) t[c] = t[c] * c2 + c1;
      SB;
#pragma unroll
      for (int c = 0; c < NC; c++) t[c] = t[c] * v[c];
      SB;
#pragma unroll
      for (int c = 0; c < NC; c++)
#pragma unroll
        for (int e = 0; e < 4; e++) t[c][e] = __builtin_amdgcn_exp2f(t[c][e]);
      SB;
#pragma unroll
      for (int c = 0; c < NC; c++) t[c] = t[c] + 1.f;
      SB;
#pragma unroll
      for (int c = 0; c < NC; c++)
#pragma unroll
        for (int e = 0; e < 4; e++) t[c][e] = __builtin_amdgcn_rcpf(t[c][e]);
      SB;
#pragma unroll
      for (int c = 0; c < NC; c++) v[c] = v[c] * t[c];
      SB;
      unsigned o[NC][2];
#pragma unroll
      for (int c = 0; c < NC; c++) { o[c][0] = pk2(v[c][0], v[c][1]); o[c][1] = pk2(v[c][2], v[c][3]); }
      st_wt(ra, l2 * 16, o0 + m * KS_T * 1024, u32x4{o[0][0], o[0][1], o[1][0], o[1][1]});
      if (NC == 4) st_wt(ra, l2 * 16, o1 + m * KS_T * 1024, u32x4{o[2][0], o[2][1], o[NC - 1][0], o[NC - 1][1]});
      else if (m & 1) {
        auto s0 = __builtin_amdgcn_permlane32_swap(sp[0], o[2][0], false, false);
        auto s1 = __builtin_amdgcn_permlane32_swap(sp[1], o[2][1], false, false);
        st_wt(ra, (l2 >> 5) * (KS_T * 1024) + (l2 & 31) * 16, o1 + (m - 1) * KS_T * 1024, u32x4{s0[0], s1[0], s0[1], s1[1]});
      } else { sp[0] = o[2][0]; sp[1] = o[2][1]; }
    }
  }
}

template <bool MAX> DEVI float red4(const float (&v)[4]) {
  auto op = [](unsigned a, unsigned b) { float r; if (MAX) asm("v_max_f32 %0, %1, %2" : "=v"(r) : "v"(a), "v"(b)); else r = asf(a) + asf(b); return r; };
  auto a = __builtin_amdgcn_permlane32_swap(asu(v[0]), asu(v[1]), false, false);
  auto b = __builtin_amdgcn_permlane32_swap(asu(v[2]), asu(v[3]), false, false);
  auto c = __builtin_amdgcn_permlane16_swap(asu(op(a[0], a[1])), asu(op(b[0], b[1])), false, false);
  return op(c[0], c[1]);
}
DEVI void bcast4(float r, float (&v)[4]) {
  auto e = __builtin_amdgcn_permlane16_swap(asu(r), asu(r), false, false);
  unsigned e0 = e[0], e1 = e[1];
  auto a = __builtin_amdgcn_permlane32_swap(e0, e0, false, false);
  auto b = __builtin_amdgcn_permlane32_swap(e1, e1, false, false);
  v[0] = asf(a[0]); v[1] = asf(a[1]); v[2] = asf(b[0]); v[3] = asf(b[1]);
}

DEVI void att_table(lptr shm, int lane) {
  const int i = lane & 15, g = lane >> 4, p = lane & 3;
  const int d = sig(i) - g;
  const unsigned NEG = 0xf149f2cau;
  bool vo[4], vp[4];
#pragma unroll
  for (int r = 0; r < 4; r++) { vo[r] = (unsigned)(d - 4 * r) <= 7u; vp[r] = d + 16 - 4 * r <= 7; }
  auto pm = [](bool lo, bool hi) { return (lo ? 0xffffu : 0u) | (hi ? 0xffff0000u : 0u); };
  LAS u32x4* t = (LAS u32x4*)(shm + LDS_TAB) + lane;
  t[0] = u32x4{vo[0] ? 0u : NEG, vo[1] ? 0u : NEG, vo[2] ? 0u : NEG, vo[3] ? 0u : NEG};
  t[64] = u32x4{pm(vp[2], vp[3]), pm(vo[0], vo[1]), vp[2] ? 0u : NEG, vp[3] ? 0u : NEG};
  t[128] = u32x4{pm(vo[2], vo[3]), (unsigned)(LDS_TB + (g * 16 + (i ^ (8 * (g >> 1)))) * 8), (unsigned)(LDS_TB + (p * 16 + ((lane >> 2) ^ (8 * (p >> 1)))) * 8), 0u};
}
DEVI int sel(u64 mask, int a, int b) { int r; asm("v_cndmask_b32_e64 %0, %2, %1, %3" : "=v"(r) : "v"(a), "v"(b), "s"(mask)); return r; }

DEVI void s1_attention(lptr shm, int w, int p_, int j_, char* ws PR_ARGS) {
  arrive(3);
  const int lane = lane_id();
  const int tau = (w + 1) * 512, taup = w >= 2 ? tau - 1024 : 0, flip = w == 1 ? 16 : 0;
  const LAS u32x4* t = (const LAS u32x4*)(shm + LDS_TAB) + lane;
  const u32x4 t0 = t[0], t2 = t[128];
  u32x4 t1 = t[64];
  if (p_ == 0 && w < 2) { t1[0] = 0u; t1[2] = 0xf149f2cau; t1[3] = 0xf149f2cau; }
  const f32x4 mO = __builtin_bit_cast(f32x4, t0), mP = __builtin_bit_cast(f32x4, t1);
  const unsigned M1 = t1[0], M2 = t1[1], M3 = t2[0];
  const int po = t2[1] + tau, pp = (t2[1] ^ flip) + taup, to = t2[2] + tau, tp = (t2[2] ^ flip) + taup;
  int aq[4], avo[4], avp[4];
#pragma unroll
  for (int h = 0; h < 4; h++) {
    aq[h] = sel(0xffffull << (16 * h), po, LDS_ZERO - 4 * 4608);
    avo[h] = sel(0x1111111111111111ull << h, to, LDS_ZERO - 14 * 4608);
    avp[h] = sel(0x1111111111111111ull << h, tp, LDS_ZERO - 14 * 4608);
    asm volatile("" : "+v"(aq[h]), "+v"(avo[h]), "+v"(avp[h]));
  }
  STAMPA(14);
  await<SPS, 8>(3);
  STAMPA(15);
  u32x2 qa[4][2], ko[4][2], kp[4][2], va[4][2], vb[4][2];
  f32x4 sp[4], so[4];
  auto rd = [&](auto H) {
    constexpr int h = decltype(H)::value;
    ds64<(5 + h) * 4608>(ko[h][0], po); ds64<9 * 4608>(ko[h][1], po); ds64<h * 4608>(qa[h][0], po); ds64<4 * 4608>(qa[h][1], aq[h]);
    ds64<(5 + h) * 4608>(kp[h][0], pp); ds64<9 * 4608>(kp[h][1], pp);
  };
  auto rv = [&](auto H) {
    constexpr int h = decltype(H)::value;
    dstr<(10 + h) * 4608>(va[h][0], tp); dstr<(10 + h) * 4608>(va[h][1], to); dstr<14 * 4608>(vb[h][0], avp[h]); dstr<14 * 4608>(vb[h][1], avo[h]);
  };
  auto qk = [&](auto H, auto N) {
    constexpr int h = decltype(H)::value;
    asm volatile("s_waitcnt lgkmcnt(%6)" : "+v"(ko[h][0]), "+v"(ko[h][1]), "+v"(qa[h][0]), "+v"(qa[h][1]), "+v"(kp[h][0]), "+v"(kp[h][1]) : "i"(decltype(N)::value));
    so[h] = mfma(cat(ko[h][0], ko[h][1]), cat(qa[h][0], qa[h][1]), mO);
    sp[h] = mfma(cat(kp[h][0], kp[h][1]), cat(qa[h][0], qa[h][1]), mP);
  };
  rd(IC(0)); rd(IC(1));
  qk(IC(0), IC(6)); rd(IC(2));
  qk(IC(1), IC(6)); rd(IC(3));
  qk(IC(2), IC(6)); rv(IC(0)); rv(IC(1));
  qk(IC(3), IC(8)); rv(IC(2));
  asm volatile("s_waitcnt lgkmcnt(8)");
  rv(IC(3));
  const float qs = 0.22360679774997896f * 1.4426950408889634f;
  float ee[4], eo[4], mx[4], l[4];
#pragma unroll
  for (int h = 0; h < 4; h++) { ee[h] = fmaxf(fmaxf(sp[h][2], so[h][0]), so[h][2]); eo[h] = fmaxf(fmaxf(sp[h][3], so[h][1]), so[h][3]); mx[h] = fmaxf(ee[h], eo[h]); }
  bcast4(red4<true>(mx) * -qs, mx);
#pragma unroll
  for (int h = 0; h < 4; h++) {
    f32x2 t = f32x2{ee[h], eo[h]} * qs + mx[h];
    ee[h] = __builtin_amdgcn_exp2f(t[0]); eo[h] = __builtin_amdgcn_exp2f(t[1]); l[h] = ee[h] + eo[h];
  }
  bcast4(__builtin_amdgcn_rcpf(red4<false>(l)), l);
  asm volatile("s_waitcnt lgkmcnt(0)" : "+v"(va[0][0]), "+v"(va[0][1]), "+v"(va[1][0]), "+v"(va[1][1]), "+v"(va[2][0]), "+v"(va[2][1]), "+v"(va[3][0]), "+v"(va[3][1]),
               "+v"(vb[0][0]), "+v"(vb[0][1]), "+v"(vb[1][0]), "+v"(vb[1][1]), "+v"(vb[2][0]), "+v"(vb[2][1]), "+v"(vb[3][0]), "+v"(vb[3][1]));
  f32x4 o4 = {0, 0, 0, 0}, oh[4];
#pragma unroll
  for (int h = 0; h < 4; h++) {
    const unsigned pk = pk2(f32x2{ee[h], eo[h]} * l[h]);
    u32x4 pf = {0u, pk & M1, pk & M2, pk & M3};
    o4 = mfma(cat(vb[h][0], vb[h][1]), pf, o4);
    oh[h] = mfma(cat(va[h][0], va[h][1]), pf, f32x4{0, 0, 0, 0});
  }
  const u32x4 ra = mkr(ws + WS_A2, A2_BYTES);
  const int base = ((8 * p_ + w) * KS_T + 54 + 5 * (j_ >> 1)) * 1024;
#pragma unroll
  for (int hp = 0; hp < 2; hp++)
    st_wt(ra, lane * 16, base + ((j_ & 1) * 3 + hp) * 1024,
          u32x4{pk2(oh[2 * hp][0], oh[2 * hp][1]), pk2(oh[2 * hp][2], oh[2 * hp][3]), pk2(oh[2 * hp + 1][0], oh[2 * hp + 1][1]), pk2(oh[2 * hp + 1][2], oh[2 * hp + 1][3])});
  unsigned a0 = pk2(o4[0], o4[1]), a1 = pk2(o4[2], o4[3]);
  auto s0 = __builtin_amdgcn_permlane32_swap(a0, a0, false, false);
  auto s1 = __builtin_amdgcn_permlane32_swap(a1, a1, false, false);
  st_wt(ra, lane < 32 ? lane * 16 : 0x70000000, base + 2 * 1024 + (j_ & 1) * 512, u32x4{s0[0], s1[0], s0[1], s1[1]});
  arrive(4);
}

constexpr int s2_slot(int r) { return r < 18 ? (r % 9) * 16384 : 147456 + (r - 18) * 4096; }
template <int NS, class GO>
DEVI void s2_compute(int w, int kq, int G0, int rot, char* ws, GO go PR_ARGS) {
  const int mh = w >> 2, lane = lane_id();
  const u32x4 ra = mkr(ws + WS_A2, A2_BYTES);
  const int av = lane * 16;
  int ao[3], bs[4];
#pragma unroll
  for (int m = 0; m < 3; m++) { int G = G0 + 3 * mh + m; G = G < 128 ? G : 127; ao[m] = (G * KS_T + (NS == 20 ? 54 : 18 * (kq - 1))) * 1024; }
#pragma unroll
  for (int c = 0; c < 4; c++) {
    bs[c] = kq * 4096 + (lane >> 2) * 128 + ((c ^ ((lane >> 3) & 3)) << 5) + 8 * (lane & 3);
  }
  f32x4 acc[3][4];
#pragma unroll
  for (int m = 0; m < 3; m++)
#pragma unroll
    for (int c = 0; c < 4; c++) { acc[m][c] = f32x4{0, 0, 0, 0}; pin(acc[m][c]); }
  u32x4 a[AD][3];
  u32x2 b[3][4][2];
  int ca = LDS_CONS + kq * 8 + mh * 4, pa = LDS_PROD + kq * 4;
  pin(ca); pin(pa);
  unsigned pv;
  auto rdb = [&](auto R, auto K) {
    constexpr int r = decltype(R)::value, k = decltype(K)::value;
    constexpr int off = s2_slot(r), nxt = s2_slot(r + 1) / 65536 - off / 65536;
    dstr<off % 65536 + (k & 1) * 2048>(b[r % 3][k >> 1][k & 1], bs[k >> 1]);
    if constexpr (nxt != 0 && (k & 1)) vadd(bs[k >> 1], bs[k >> 1], nxt * 65536);
  };
  auto lda = [&](auto S, int m) {
    constexpr int s = decltype(S)::value;
    bload_sc1(a[s % AD][m], ra, av, ao[m] + ((s ^ (s < 16 ? rot : rot & 1)) << 10));
  };
  peek(pv, pa); wait_lg<0>(); require(pv, pa, 1);
  sfor<3>([&](auto R) { sfor<8>([&](auto K) { rdb(R, K); }); wait_lg<0>(); });
  post(ca, 1);
  STAMP(6);
  go();
  STAMP(7);
  sfor<AD - 1>([&](auto S) {
#pragma unroll
    for (int m = 0; m < 3; m++) lda(S, m);
    if (decltype(S)::value < AD - 2) __builtin_amdgcn_s_sleep(ASL);
  });
  if (NS == 20) __builtin_amdgcn_s_setprio(SPRIO);
  sfor<NS>([&](auto S) {
    constexpr int s = decltype(S)::value;
    constexpr bool LA = s + AD - 1 < NS, RB = s >= 1 && s + 2 < NS;
    constexpr int later = NS - 1 - s < AD - 2 ? NS - 1 - s : AD - 2;
    if constexpr (s == 3) require(pv, pa, 2);
    if constexpr (s == 10) require(pv, pa, 3);
    if constexpr (s == 13) require(pv, pa, 4);
    if constexpr (s == 2 || s == 9 || s == 12) peek(pv, pa);
    if constexpr (s == NS - 2) peek(pv, ca ^ 4);
    sfor<3>([&](auto M) {
      constexpr int m = decltype(M)::value;
      if constexpr (LA) lda(IC(s + AD - 1), m);
      wait_vm<3 * later + 2 - m + (LA ? m + 1 : 0)>();
      if constexpr (m == 1 && s >= 2 && s + 1 < NS) wait_lg<(RB ? 4 : 0)>();
      sfor<4>([&](auto C) {
        constexpr int c = decltype(C)::value;
        mfma_a(acc[m][c], cat(b[s % 3][c][0], b[s % 3][c][1]), a[s % AD][m]);
        if constexpr (RB && m < 2) rdb(IC(s + 2), IC(4 * m + c));
      });
    });
    if constexpr (s == 3 || s == 6 || s == 9 || s == 12) post(ca, s);
    if constexpr (s == 2) { STAMPC(12); }
    if constexpr (s == 11) { STAMPC(13); }
  });
  int da[4];
#pragma unroll
  for (int c = 0; c < 4; c++) da[c] = mh * 49152 + kq * 4096 + (av & 0xf0) * 16 + ((((4 * c) << 4) + (av >> 8 << 4)) ^ (av & 0x70));
  require(pv, ca ^ 4, mh ? 12 : 9);
  STAMP(9);
  sfor<3>([&](auto M) {
    constexpr int m = decltype(M)::value;
    if (m == 2) asm volatile("s_nop 3");
#pragma unroll
    for (int c = 0; c < 4; c++) dsw128<m * 16384>(da[c], __builtin_bit_cast(u32x4, acc[m][c]));
  });
  arrive((NS == 20 ? 28 : 12) + mh);
}

__global__ __launch_bounds__(768, 3) void fused_block(const void* __restrict__ x, const void* __restrict__ wqkv, const void* __restrict__ wo,
                                                      const void* __restrict__ w1, const unsigned short* __restrict__ b1,
                                                      const void* __restrict__ w2, const char* __restrict__ b2, char* __restrict__ out,
                                                      char* ws) {
  extern __shared__ __attribute__((aligned(16))) char shm_[];
  lptr shm = (lptr)shm_;
  const int tid = threadIdx.x, lane = tid & 63, w = __builtin_amdgcn_readfirstlane(tid >> 6);
  const int xcd = blockIdx.x & 7, idx = blockIdx.x >> 3;
  const int p_ = (xcd & 1) * 8 + (idx & 7), sub = idx >> 3;
  const bool att = !(sub & 1);
  const int j_ = (xcd >> 1) * 2 + (sub >> 1);
  const int uidx = (att ? 16 : 0) + (idx & 7) + 8 * (idx >> 4);
  const int unit = (xcd >> 1) * 28 + uidx;
  const bool has2 = uidx < 28 && unit < 110;
  const int mt = unit / 5, nl = unit - 5 * mt, n_ = (xcd & 1) * 5 + nl;
  const int G0 = 6 * mt;
  const int kq = w < 8 ? (w + (w >> 2)) & 3 : w & 3;
  const int rot = (ROT2);
  auto kperm = [&](int s) { return s ^ (s < 16 ? rot : rot & 1); };
  STAMP(0);
  if (w == 7) {
    u64 pc; asm volatile("s_getpc_b64 %0" : "=s"(pc));
    touch(mkr((const void*)(pc & ~255ull), CODE_BYTES), (idx + 32 * lane) * 128);
  }
  asm volatile("" : "+s"(x), "+s"(wqkv), "+s"(wo), "+s"(w1), "+s"(b1), "+s"(w2), "+s"(b2), "+s"(out), "+s"(ws));
  const int hf = KHF, tx = KTX;
  const int rid = (att ? 0 : 8) + j_;
  int fu;
  if (__builtin_expect(w >= 8, 1)) {
    __builtin_amdgcn_s_setprio(3);
    const int lw = w - 8;
    const int kt0 = (KT0) % 10;
    auto ktile = [&](int T) { const int k = kt0 + (T ^ (T < 8 ? tx : tx & 1)); return k >= 10 ? k - 10 : k; };
    const int RS = att ? 3840 : 3456;
    const u32x4 rx = mkr(x, S_ * D_ * 2), rw1 = mkr(att ? wqkv : w1, D_ * RS);
    const int r8 = lane >> 3, cp = lane & 7, t0 = p_ * 128;
    int xv[2], hv, wv;
#pragma unroll
    for (int e = 0; e < 2; e++)
      xv[e] = (t0 + 32 * lw + 2 * (4 * (r8 & 3) + 2 * e + (r8 >> 2))) * 1280 + ((cp ^ (4 * e + (r8 >> 1))) << 4);
    auto ldx = [&](int T) { const int o = ktile(T) * 128; dma4<1024>(rx, ~0ull, (T % 3) * SLOT + XOFF + 2048 + 4 * lw * 1024, xv[0], xv[1], o, o, o + 1280, o + 1280); };
    STAMP(12);
    ldx(0);
    {
      const int hi = lane >= 30, ch = lane - 30 * hi, lim = j_ < 7 ? 27 : 19;
      wv = hi * RS + (att ? ((ch >= 10) + (ch >= 20)) * 1120 + j_ * 160 + ch * 16 : j_ * 448 + (ch < lim ? ch : lim) * 16);
    }
    const int wrow = (16 * (lw & 1) + 4 * (lw >> 1)) * RS;
    auto ldw = [&](int T, int h) {
      const int so = (64 * ktile(T) + 32 * (h ^ hf)) * RS + wrow;
      dma4<960>(rw1, 0x0fffffffffffffffull, (T % 3) * SLOT + h * WSLAB + 4 * lw * 960, wv, wv, so, so + 2 * RS, so + 8 * RS, so + 10 * RS);
    };
    ldw(0, 0);
    {
      int tok = t0 - 16 + 8 * (r8 & 1) + 2 * lw + ((r8 & 3) < 2);
      tok = tok < 0 ? 0 : tok;
      hv = att ? tok * 1280 + ((cp ^ ((2 * lw + ((r8 & 3) >> 1)) & 7)) << 4) : 0x70000000;
    }
    const u64 hmask = lw == 0 ? 0xffffff00ull : 0xffffffffull;
    auto ldh = [&](int T) { dma1(rx, hmask, (T % 3) * SLOT + XOFF + lw * 512, hv, ktile(T) * 128); };
    ldh(0); ldw(0, 1);
    STAMP(13);
    bar_vm<4>();
    STAMP(14);
    ldx(1); ldh(1);
    bar_vm<5>();
    ldw(1, 0); ldw(1, 1);
    unsigned sink = 0;
    const int wv2 = blockIdx.x * 4 + lw;
    const u32x4 rs2 = mkr(wv2 < 720 ? w2 : wo, wv2 < 720 ? F_ * 1280 : D_ * 1280);
    const int so2 = wv2 < 720 ? wv2 * 3072 : min((wv2 - 720) * 3072, D_ * 1280 - 3072);
    ldx(2); ldh(2);
    if (STW) stouch<8>(sink, rs2, so2);
    bar_vm<5>();
    const int srow = p_ * 128 + 8 * rid + 2 * lw + (lane >> 5);
    u32x2 xr[5];
    unsigned ep, bv;
    setup_loads(xr, ep, bv, rx, srow * 1280 + (lane & 31) * 8, mkr(ws, 4), mkr(b1, F_ * 2), (j_ * 224 + 64 * lw + lane) * 2);
    ldw(2, 0); ldw(2, 1); ldx(3); ldh(3); ldw(3, 0);
    if (STW) stouch<8>(sink, rs2, so2 + 1024);
    bar_vm<9>();
    pin(ep); pin(bv);
#pragma unroll
    for (int k = 0; k < 5; k++) pin(xr[k]);
    const unsigned tag = __builtin_amdgcn_readfirstlane(ep) + 1u;
    ldw(3, 1); ldx(4); ldw(4, 0); ldh(4); ldw(4, 1);
    {
      float s = 0;
#pragma unroll
      for (int k = 0; k < 5; k++) s += (blo(xr[k][0]) + bhi(xr[k][0])) + (blo(xr[k][1]) + bhi(xr[k][1]));
      const float m = half_sum(s) * (1.f / D_);
      float q = 0;
#pragma unroll
      for (int k = 0; k < 10; k++) { const unsigned u = xr[k >> 1][k & 1]; const float a = blo(u) - m, b = bhi(u) - m; q = __builtin_fmaf(a, a, q); q = __builtin_fmaf(b, b, q); }
      const float rs = __builtin_amdgcn_rsqf(half_sum(q) * (1.f / D_) + 1e-5f);
      st_wt(mkr(ws + WS_STAT, S_ * 16), (lane & 31) == 0 ? srow * 16 : 0x70000000, 0, u32x4{asu(rs), tag, asu(-m * rs), tag});
      *(LAS float*)(shm + LDS_B1 + (64 * lw + lane) * 4) = blo(bv);
      *(LAS unsigned*)(shm + LDS_CNT + 4) = tag;
    }
    if (STW) stouch<8>(sink, rs2, so2 + 2048);
    bar_vm<14>();
    const u32x4 rw = mkr(kq == 0 ? wo : w2, has2 ? (kq == 0 ? D_ * 1280 : F_ * 1280) : 0);
    const int cbyte = n_ * 128 + ((cp ^ ((lane >> 4) << 1)) << 4);
    const int v1 = r8 * 1280 + cbyte, v2 = kq == 0 ? (20 * (r8 >> 2) + (r8 & 3)) * 1280 + cbyte : v1;
    auto geom = [&](int s, int& rb, int& sa, int& sb, int& v) {
      s = kperm(s);
      const int P = (s * 13) >> 6, kp = s - 5 * P;
      v = v1 + ((v2 - v1) & -(int)(kp == 2));
      const int r0 = 160 * P + (kp == 2 ? 16 : 40 * (kp - (kp > 2))), r1 = 32 * (18 * (kq - 1) + s);
      rb = 1280 * (kq == 0 ? r0 : r1);
      sa = 1280 * (kq == 0 && kp == 2 ? 80 : 8);
      sb = 1280 * (kq == 0 ? (kp == 2 ? 40 : 20) : 16);
    };
    u32x4 pre[9][4];
    const int attf = att || !INLOOP;
    auto ldr = [&](auto R, auto LATE) {
      constexpr int r = decltype(R)::value;
      int rb, sa, sb, v; geom(9 + r, rb, sa, sb, v);
      bload4_if<decltype(LATE)::value>(pre[r], rw, v, rb, rb + sa, rb + sb, rb + sa + sb, attf);
    };
    auto ldl = [&](auto S) {
      constexpr int s = decltype(S)::value;
      int rb, sa, sb, v; geom(s, rb, sa, sb, v);
      dma4<1024>(rw, ~0ull, s < 9 ? s * 16384 + kq * 4096 : 147456 + (s - 18) * 4096, v, v, rb, rb + sa, rb + sb, rb + sa + sb);
    };
    const u32x4 rst = mkr(ws + WS_STAT, S_ * 16);
    const int spos = 36 * lw + (lane < 36 ? lane : 35), sv = row_token(p_, spos) * 16;
    u32x4 sg;
    sfor<5>([&](auto TT) {
      constexpr int T = decltype(TT)::value + 5;
      if (T == 9) bload_sc1(sg, rst, sv, 0);
      if (T == 6) { STAMP(15); }
      if (!(ABL & 1024)) { if (!(ABL & 2048)) ldx(T); ldw(T, 0); ldh(T); ldw(T, 1); }
      ldr(IC(2 * (T - 5)), IC(0));
      if constexpr (T < 9) ldr(IC(2 * (T - 5) + 1), IC(0));
      bar_vm_sel<(T == 5 ? 21 : T == 9 ? 17 : 29), 13>(attf);
    });
    STAMP(1);
    asm volatile("" : "+v"(sg));
#pragma nounroll
    for (unsigned spins = 0; !__all(sg[1] == tag && sg[3] == tag) && spins < SPIN_CAP; spins++) {
      __builtin_amdgcn_s_sleep(2);
      bload_sc1(sg, rst, sv, 0);
      asm volatile("s_waitcnt vmcnt(0)" : "+v"(sg) :: "memory");
    }
    *(LAS u32x2*)(shm + LDS_ST + spos * 8) = u32x2{sg[0], sg[2]};
    ldr(IC(0), IC(1));
    wait_vm<4>();
    STAMP(2);
    bar_lgkm();
    asm volatile("" : "+s"(sink));
    __builtin_amdgcn_s_setprio(LP2);
    STAMP(3);
    if (!has2) return;
    sfor<8>([&](auto R) { ldr(IC(decltype(R)::value + 1), IC(1)); wait_vm<THR2>(); });
    ldl(IC(3)); ldl(IC(4));
    STAMP(4);
    await<1, 8>(2);
    ldl(IC(0)); wait_vm<THR>(); ldl(IC(1)); wait_vm<THR>(); ldl(IC(2));
    const int pa = LDS_PROD + kq * 4, wa = kq * 4096 + lane * 16;
    if (att) { wait_vm<0>(); post(pa, 1); await<1, 8>(4); }
    STAMP(5);
    ldl(IC(5)); wait_vm<4>(); post(pa, 1);
    ldl(IC(6)); if (!att) wait_vm<THR>(); ldl(IC(7)); if (!att) wait_vm<THR>(); ldl(IC(8));
    if (kq == 0) { ldl(IC(18)); ldl(IC(19)); }
#define NAP __builtin_amdgcn_s_sleep(PSL);
#define PUT(r, off) dsw128<off>(wa + (r / 3) * 49152, pre[r][0]); NAP dsw128<off + 1024>(wa + (r / 3) * 49152, pre[r][1]); NAP dsw128<off + 2048>(wa + (r / 3) * 49152, pre[r][2]); NAP dsw128<off + 3072>(wa + (r / 3) * 49152, pre[r][3]); NAP
    __builtin_amdgcn_s_setprio(0);
    await_pair(LDS_CONS + kq * 8, 1);
    PUT(0, 0) PUT(1, 16384) PUT(2, 32768)
    wait_vm<0>();
    post(pa, 2);
    STAMP(6);
    await_pair(LDS_CONS + kq * 8, 3);
    PUT(3, 0) PUT(4, 16384) PUT(5, 32768)
    post(pa, 3);
    await_pair(LDS_CONS + kq * 8, 6);
    PUT(6, 0) PUT(7, 16384) PUT(8, 32768)
    post(pa, 4);
    fu = lw < 2 ? 4 + lw : 8 + lw;
  } else {
    if (tid < 24) *(LAS u32x4*)(shm + (tid >> 3) * SLOT + XOFF + (tid & 7) * 16) = u32x4{0x3f803f80u, 0x3f803f80u, 0x3f803f80u, 0x3f803f80u};
    if (tid >= 448 && tid < 480) *(LAS unsigned*)(shm + LDS_CNT + 4 * (tid - 448)) = 0u;
    if (w == 5 && att) att_table(shm, lane);
    const bool light = att ? (w >= 6) : (w >= 4);
    if (light) s1_compute<3>(shm, att, w, lane, p_, j_, hf, ws PR_PASS);
    else s1_compute<4>(shm, att, w, lane, p_, j_, hf, ws PR_PASS);
    STAMP(3);
    if (att) s1_attention(shm, w, p_, j_, ws PR_PASS);
    STAMP(4);
    const unsigned tag = __builtin_amdgcn_readfirstlane(*(LAS unsigned*)(shm + LDS_CNT + 4));
    wait_vm<0>();
    const int lane = lane_id();
    GAS u64* flags = (GAS u64*)(ws + WS_FLAG);
    const u64 fv = flagval(tag);
    {
      unsigned old = 0;
      if (lane == 0) old = __hip_atomic_fetch_add((LAS unsigned*)(shm + LDS_CNT), 1u, __ATOMIC_RELAXED, __HIP_MEMORY_SCOPE_WORKGROUP);
      if (__builtin_amdgcn_readfirstlane(old) == 7) {
        const int dm = (lane * 26) >> 8, cm = (8 * p_) / 6 + dm;
        GAS u64* dst = lane == 30 ? flags + p_ * 16 + rid : (GAS u64*)(ws + WS_BOX) + (cm * 10 + lane - 10 * dm) * 32 + (p_ - ((6 * cm) >> 3)) * 16 + rid;
        if (lane == 30 || (lane < 30 && 6 * cm < 8 * p_ + 8)) __hip_atomic_store(dst, fv, __ATOMIC_RELAXED, __HIP_MEMORY_SCOPE_AGENT);
      }
    }
    STAMP(5);
    if (!has2) {
      if (blockIdx.x == 191 && w == 0) {
#pragma nounroll
        for (unsigned spins = 0; spins < SPIN_CAP; spins++) {
          bool ok = true;
#pragma unroll
          for (int k = 0; k < 4; k++) ok &= __hip_atomic_load(flags + lane + 64 * k, __ATOMIC_RELAXED, __HIP_MEMORY_SCOPE_AGENT) == fv;
          if (__all(ok)) break;
          __builtin_amdgcn_s_sleep(8);
        }
        if (lane == 0) __hip_atomic_store((GAS unsigned*)ws, tag, __ATOMIC_RELAXED, __HIP_MEMORY_SCOPE_AGENT);
      }
      FINISH();
      return;
    }
    auto go = [&]() {
      if (w < 2) {
        const int Gb = G0 + 5 < 128 ? G0 + 5 : 127, lane = lane_id();
        GAS u64* fp = (GAS u64*)(ws + WS_BOX) + (mt * 10 + n_) * 32 + 8 * w + (lane & 7) + ((Gb >> 3) != (G0 >> 3) ? 2 * (lane & 8) : 0);
#pragma nounroll
        for (unsigned spins = 0; spins < SPIN_CAP; spins++) {
          if (__all(__hip_atomic_load(fp, __ATOMIC_RELAXED, __HIP_MEMORY_SCOPE_AGENT) == fv)) break;
          __builtin_amdgcn_s_sleep(1);
        }
        post(LDS_CNT + 4 * (14 + w), 1);
      } else await<GSL, 1>(kq == 0 ? 14 : 15);
    };
    if (kq == 0) s2_compute<20>(w, kq, G0, rot, ws, go PR_PASS);
    else s2_compute<18>(w, kq, G0, rot, ws, go PR_PASS);
    if (w >= 4) { STAMP(11); FINISH(); return; }
    fu = w;
  }
#pragma nounroll
  for (;; fu += 6) {
    const int lane = lane_id();
    const int frow = 8 * fu + (lane >> 3), fG = G0 + (frow >> 4);
    const bool fvalid = fG < 128;
    const int ftok = 32 * ((fvalid ? fG : 0) >> 1) + 2 * sig(frow & 15) + (fG & 1);
    const int foff = ftok * 1280 + n_ * 128 + (lane & 7) * 16;
    const u32x4 xv = *(const GAS u32x4*)((const char*)x + foff);
    const u32x4 bv = *(const GAS u32x4*)(b2 + n_ * 128 + (lane & 7) * 16);
    int pr0 = (frow >> 4) * 16384 + (frow & 15) * 256 + (((2 * (lane & 7)) ^ (frow & 7)) << 4);
    pin(pr0);
    await<FSL, 3>(fu < 6 ? 12 : 13);
    STAMP(10);
    f32x4 y[2];
#pragma unroll
    for (int e = 0; e < 2; e++) {
      lptr pr = shm + (pr0 ^ (16 * e));
      y[e] = *(LAS f32x4*)(pr + 4096) + *(LAS f32x4*)(pr + 8192) + *(LAS f32x4*)(pr + 12288);
    }
    f32x2 gt[4], xf[4];
#pragma unroll
    for (int k = 0; k < 4; k++) {
      f32x2 yy = f32x2{y[k >> 1][2 * (k & 1)], y[k >> 1][2 * (k & 1) + 1]} + f32x2{blo(bv[k]), bhi(bv[k])};
      f32x2 t = yy * -1.4426950408889634f;
      f32x2 e = f32x2{__builtin_amdgcn_exp2f(t[0]), __builtin_amdgcn_exp2f(t[1])} + 1.f;
      gt[k] = yy * f32x2{__builtin_amdgcn_rcpf(e[0]), __builtin_amdgcn_rcpf(e[1])};
      xf[k] = f32x2{blo(xv[k]), bhi(xv[k])};
      pin(gt[k]); pin(xf[k]);
    }
    await<FSL2, 1>(fu < 6 ? 28 : 29);
    u32x4 o;
#pragma unroll
    for (int e = 0; e < 2; e++) {
      const f32x4 s = *(LAS f32x4*)(shm + (pr0 ^ (16 * e)));
      o[2 * e] = pk2(f32x2{s[0], s[1]} * gt[2 * e] + xf[2 * e]);
      o[2 * e + 1] = pk2(f32x2{s[2], s[3]} * gt[2 * e + 1] + xf[2 * e + 1]);
    }
    if (fvalid) __builtin_nontemporal_store(o, (GAS u32x4*)(out + foff));
    if (fu >= 4) break;
  }
  STAMP(11);
  FINISH();
}

extern "C" void kernel_launch(void* const* d_in, const int* in_sizes, int n_in, void* d_out, int out_size, void* d_ws,
                              size_t ws_size, hipStream_t stream) {
  (void)hipFuncSetAttribute((const void*)fused_block, hipFuncAttributeMaxDynamicSharedMemorySize, LDS_TOTAL);
  fused_block<<<256, 768, LDS_TOTAL, stream>>>(d_in[0], d_in[1], d_in[2], d_in[4], (const unsigned short*)d_in[5], d_in[6],
                                               (const char*)d_in[7], (char*)d_out, (char*)d_ws + WSOFF);
}
```

```cpp
#include <hip/hip_runtime.h>
#include <hip/hip_bf16.h>
#include <type_traits>
#include <utility>

typedef __attribute__((ext_vector_type(4))) short s16x4;
typedef __attribute__((ext_vector_type(8))) __bf16 bf16x8;
typedef __attribute__((ext_vector_type(2))) __bf16 bf16x2;
typedef __attribute__((ext_vector_type(4))) float f32x4;
typedef __attribute__((ext_vector_type(2))) float f32x2;
typedef __attribute__((ext_vector_type(4))) unsigned u32x4;
typedef __attribute__((ext_vector_type(2))) unsigned u32x2;
#define LAS __attribute__((address_space(3)))
#define GAS __attribute__((address_space(1)))
#define DEVI __device__ __forceinline__
typedef LAS char* lptr;
typedef unsigned long long u64;
#define IC(n) std::integral_constant<int, n>{}

constexpr int S_ = 2048, D_ = 640, F_ = 1728;
constexpr int KS_T = 74;
constexpr int WSLAB = 15360;
constexpr int XOFF = 2 * WSLAB;
constexpr int SLOT = 49152;
constexpr int LDS_TB = 86016;
constexpr int LDS_ST = 155648;
constexpr int LDS_CS = LDS_ST + 1152;
constexpr int LDS_B1 = LDS_CS + 1024;
constexpr int LDS_CNT = LDS_B1 + 1024;
constexpr int LDS_ZERO = LDS_CNT + 24;
constexpr int LDS_CONS = LDS_CNT + 64;
constexpr int LDS_PROD = LDS_CONS + 32;
constexpr int LDS_TAB = LDS_CNT + 128;
constexpr int LDS_TOTAL = 163840;
constexpr int WS_FLAG = 4096, WS_STAT = 8192, WS_BOX = 65536, WS_A2 = 1 << 20;
constexpr int A2_BYTES = 128 * KS_T * 1024;
#define PF_CODE 1
#define PF_MALL 0
#define MAP2 0
#define S1MODE 0
#define S2MODE 0
#define THR 32
#define PSL 0
#define PF_TLB 0
#define INLOOP 0
#define PFS 128
#define PFK0 2
#define PFALL 0
#define PFW2 0
#define KHF (idx & 1)
#define KTX (((idx >> 1) ^ sub) & 1)
#define KT0 0
#define THR2 32
#define SPS 0
#define LPRIO 0
#define SPRIO 0
#define ADY 3
#define ADS 4
#define ASL 3
#define GSL 0
#define FSL 1
#define OUTWT 2
#define FSL2 0
#define STW 0
#define WSOFF 0
#define LP2 0
#define WARM2 0
#define WARM3 WARM2
#define LP1 0
#define IDLE_ATT 1
#define FW 2
#define LATE_ALL 3
#define BOXW 1
#define NOEARLY 0
#define ABL 0
#define ROT1 0
#define ROT2 ((mt + nl) & 7)
constexpr int CODE_BYTES = 38400;
constexpr unsigned SPIN_CAP = 1u << 20;

#define STAMP(idx)
#define FINISH()
#define STAMPC(k)
#define STAMPA(k) STAMP(k)
#define STAMPB(k)
#define PR_ARGS
#define PR_PASS

DEVI void bar_lgkm() { asm volatile("s_waitcnt lgkmcnt(0)\n\ts_barrier" ::: "memory"); }
DEVI void bar() { asm volatile("s_barrier" ::: "memory"); }
template <int N> DEVI void bar_vm() { asm volatile("s_waitcnt vmcnt(%0)\n\ts_barrier" ::"i"(N) : "memory"); }
template <int N> DEVI void wait_vm() { asm volatile("s_waitcnt vmcnt(%0)" ::"i"(N) : "memory"); }
DEVI unsigned pk2(float a, float b) { f32x2 v = {a, b}; return __builtin_bit_cast(unsigned, __builtin_convertvector(v, bf16x2)); }
DEVI unsigned pk2(f32x2 v) { return __builtin_bit_cast(unsigned, __builtin_convertvector(v, bf16x2)); }
DEVI float asf(unsigned u) { return __builtin_bit_cast(float, u); }
DEVI unsigned asu(float f) { return __builtin_bit_cast(unsigned, f); }
DEVI float blo(unsigned u) { return __builtin_bit_cast(float, u << 16); }
DEVI float bhi(unsigned u) { return __builtin_bit_cast(float, u & 0xffff0000u); }
DEVI u32x2 tr_read(lptr p) { return __builtin_bit_cast(u32x2, __builtin_amdgcn_ds_read_tr16_b64_v4i16((LAS s16x4*)p)); }
DEVI f32x4 mfma(u32x4 a, u32x4 b, f32x4 c) {
  return __builtin_amdgcn_mfma_f32_16x16x32_bf16(__builtin_bit_cast(bf16x8, a), __builtin_bit_cast(bf16x8, b), c, 0, 0, 0);
}
DEVI u32x4 cat(u32x2 a, u32x2 b) { return __builtin_shufflevector(a, b, 0, 1, 2, 3); }
DEVI void mfma_a(f32x4& c, u32x4 a, u32x4 b) { asm volatile("v_mfma_f32_16x16x32_bf16 %0, %1, %2, %0" : "+v"(c) : "v"(a), "v"(b)); }
template <int OFF> DEVI void ds128(u32x4& d, int addr) { asm volatile("ds_read_b128 %0, %1 offset:%2" : "=v"(d) : "v"(addr), "i"(OFF)); }
template <int OFF> DEVI void dstr(u32x2& d, int addr) { asm volatile("ds_read_b64_tr_b16 %0, %1 offset:%2" : "=v"(d) : "v"(addr), "i"(OFF)); }
template <int OFF> DEVI void dsw128(int addr, u32x4 d) { asm volatile("ds_write_b128 %0, %1 offset:%2" ::"v"(addr), "v"(d), "i"(OFF) : "memory"); }
DEVI u32x4 mkr(const void* p, unsigned bytes) {
  u64 a = (u64)p;
  return u32x4{(unsigned)a, (unsigned)(a >> 32) & 0xffffu, bytes, 0x00020000u};
}
DEVI void st_wt(u32x4 r, int voff, int soff, u32x4 v) {
  asm volatile("buffer_store_dwordx4 %0, %1, %2, %3 offen sc1\n\ts_nop 1" ::"v"(v), "v"(voff), "s"(r), "s"(soff) : "memory");
}
template <int LSTEP>
DEVI void dma4(u32x4 r, u64 mask, int l, int va, int vb, int s0, int s1, int s2, int s3) {
  asm volatile(
      "s_mov_b64 exec, %0\n\t"
      "s_mov_b32 m0, %1\n\ts_nop 0\n\tbuffer_load_dwordx4 %2, %4, %5 offen lds\n\t"
      "s_add_u32 m0, %1, %9\n\ts_nop 0\n\tbuffer_load_dwordx4 %3, %4, %6 offen lds\n\t"
      "s_add_u32 m0, %1, %10\n\ts_nop 0\n\tbuffer_load_dwordx4 %2, %4, %7 offen lds\n\t"
      "s_add_u32 m0, %1, %11\n\ts_nop 0\n\tbuffer_load_dwordx4 %3, %4, %8 offen lds\n\t"
      "s_mov_b64 exec, -1"
      :: "s"(mask), "s"(l), "v"(va), "v"(vb), "s"(r), "s"(s0), "s"(s1), "s"(s2), "s"(s3), "i"(LSTEP), "i"(2 * LSTEP), "i"(3 * LSTEP)
      : "memory", "scc");
}
DEVI void dma1(u32x4 r, u64 mask, int l, int v, int s0) {
  asm volatile("s_mov_b64 exec, %0\n\ts_mov_b32 m0, %1\n\ts_nop 0\n\tbuffer_load_dwordx4 %2, %3, %4 offen lds\n\ts_mov_b64 exec, -1"
               :: "s"(mask), "s"(l), "v"(v), "s"(r), "s"(s0) : "memory");
}
DEVI void setup_loads(u32x2 (&xr)[5], unsigned& ep, unsigned& bv, u32x4 rx, int vx, u32x4 re, u32x4 rb, int vb) {
  asm volatile("buffer_load_dwordx2 %0, %7, %8, 0 offen\n\tbuffer_load_dwordx2 %1, %7, %8, 0 offen offset:256\n\tbuffer_load_dwordx2 %2, %7, %8, 0 offen offset:512\n\t"
               "buffer_load_dwordx2 %3, %7, %8, 0 offen offset:768\n\tbuffer_load_dwordx2 %4, %7, %8, 0 offen offset:1024\n\t"
               "buffer_load_dword %5, off, %9, 0 sc1\n\tbuffer_load_ushort %6, %11, %10, 0 offen"
               : "=&v"(xr[0]), "=&v"(xr[1]), "=&v"(xr[2]), "=&v"(xr[3]), "=&v"(xr[4]), "=&v"(ep), "=&v"(bv)
               : "v"(vx), "s"(rx), "s"(re), "s"(rb), "v"(vb) : "memory");
}
template <int WHEN>
DEVI void bload4_if(u32x4 (&d)[4], u32x4 r, int v, int s0, int s1, int s2, int s3, int flag) {
#define LDR_BODY "s_cmp_lg_u32 %10, %11\n\ts_cbranch_scc1 .Lr%=\n\tbuffer_load_dwordx4 %0, %4, %5, %6 offen\n\tbuffer_load_dwordx4 %1, %4, %5, %7 offen\n\t" \
                 "buffer_load_dwordx4 %2, %4, %5, %8 offen\n\tbuffer_load_dwordx4 %3, %4, %5, %9 offen\n.Lr%=:"
  if (WHEN) asm volatile(LDR_BODY : "+v"(d[0]), "+v"(d[1]), "+v"(d[2]), "+v"(d[3]) : "v"(v), "s"(r), "s"(s0), "s"(s1), "s"(s2), "s"(s3), "s"(flag), "i"(WHEN) : "scc");
  else asm volatile(LDR_BODY : "=&v"(d[0]), "=&v"(d[1]), "=&v"(d[2]), "=&v"(d[3]) : "v"(v), "s"(r), "s"(s0), "s"(s1), "s"(s2), "s"(s3), "s"(flag), "i"(WHEN) : "scc");
}
template <int N0, int N1>
DEVI void bar_vm_sel(int flag) {
  asm volatile("s_cmp_lg_u32 %0, 0\n\ts_cbranch_scc1 .La%=\n\ts_waitcnt vmcnt(%1)\n\ts_branch .Lb%=\n.La%=:\n\ts_waitcnt vmcnt(%2)\n.Lb%=:\n\ts_barrier"
               ::"s"(flag), "i"(N0), "i"(N1) : "memory", "scc");
}
template <bool WHEN>
DEVI void bload3_if(u32x4 (&d)[3], u32x4 r, int v, int s0, int s1, int s2, int flag) {
#define L3_BODY(BR) "s_cmp_lg_u32 %8, 0\n\t" BR " .Lc%=\n\tbuffer_load_dwordx4 %0, %3, %4, %5 offen sc1\n\tbuffer_load_dwordx4 %1, %3, %4, %6 offen sc1\n\t" \
                    "buffer_load_dwordx4 %2, %3, %4, %7 offen sc1\n.Lc%=:"
  if (WHEN) asm volatile(L3_BODY("s_cbranch_scc0") : "=&v"(d[0]), "=&v"(d[1]), "=&v"(d[2]) : "v"(v), "s"(r), "s"(s0), "s"(s1), "s"(s2), "s"(flag) : "memory", "scc");
  else asm volatile(L3_BODY("s_cbranch_scc1") : "+v"(d[0]), "+v"(d[1]), "+v"(d[2]) : "v"(v), "s"(r), "s"(s0), "s"(s1), "s"(s2), "s"(flag) : "memory", "scc");
}
template <class T> DEVI void pin(T& v) { asm volatile("" : "+v"(v)); }
DEVI void dsw_col0(int a0, f32x4 d0, int a1, f32x4 d1, int a2, f32x4 d2) {
  asm volatile("s_mov_b64 exec, %6\n\tds_write_b128 %0, %1\n\tds_write_b128 %2, %3\n\tds_write_b128 %4, %5\n\ts_mov_b64 exec, -1"
               ::"v"(a0), "v"(d0), "v"(a1), "v"(d1), "v"(a2), "v"(d2), "s"(0x0001000100010001ull) : "memory");
}
DEVI void reload_sc1(u32x4& d, u32x4 r, int v, int s0) { asm volatile("buffer_load_dwordx4 %0, %1, %2, %3 offen sc1" : "+v"(d) : "v"(v), "s"(r), "s"(s0) : "memory"); }
template <int OFF> DEVI void redstr(u32x2& d, int addr) { asm volatile("ds_read_b64_tr_b16 %0, %1 offset:%2" : "+v"(d) : "v"(addr), "i"(OFF)); }
DEVI void vinc(int& d, int s) { asm volatile("v_add_u32 %0, %1, %0" : "+v"(d) : "s"(s)); }
DEVI void vadd(int& d, int a, int s) { asm volatile("v_add_u32 %0, %2, %1" : "=v"(d) : "v"(a), "s"(s)); }
template <class F, int... I> DEVI void sfor_(F f, std::integer_sequence<int, I...>) { (f(std::integral_constant<int, I>{}), ...); }
template <int N, class F> DEVI void sfor(F f) { sfor_(f, std::make_integer_sequence<int, N>{}); }
template <int OFF> DEVI void stouch1(unsigned& sink, u32x4 r, int off) { asm volatile("s_buffer_load_dword %0, %1, %2 offset:%3" : "+s"(sink) : "s"(r), "s"(off), "i"(OFF)); }
template <int N>
DEVI void stouch(unsigned& sink, u32x4 r, int off) {
  sfor<N>([&](auto K) { stouch1<decltype(K)::value * 128>(sink, r, off); });
}
DEVI void touchg(const void* p) {
  asm volatile("s_mov_b32 m0, %1\n\ts_nop 0\n\tglobal_load_lds_dword %0, off" ::"v"(p), "i"(LDS_TOTAL - 256) : "memory");
}
DEVI void touch_sc1(u32x4 r, int v) {
  asm volatile("s_mov_b32 m0, %2\n\ts_nop 0\n\tbuffer_load_dword %0, %1, 0 offen sc1 lds" ::"v"(v), "s"(r), "i"(LDS_TOTAL - 256) : "memory");
}
DEVI void touch(u32x4 r, int v) {
  asm volatile("s_mov_b32 m0, %2\n\ts_nop 0\n\tbuffer_load_dword %0, %1, 0 offen lds" ::"v"(v), "s"(r), "i"(LDS_TOTAL - 256) : "memory");
}
DEVI void bload(u32x4& d, u32x4 r, int v, int s0) { asm volatile("buffer_load_dwordx4 %0, %1, %2, %3 offen" : "=v"(d) : "v"(v), "s"(r), "s"(s0)); }
DEVI void bload_sc1(u32x4& d, u32x4 r, int v, int s0) { if (ABL & 4) { bload(d, r, v, s0); return; } asm volatile("buffer_load_dwordx4 %0, %1, %2, %3 offen sc1" : "=v"(d) : "v"(v), "s"(r), "s"(s0) : "memory"); }
DEVI void arrive(int k) {
  asm volatile("s_mov_b64 exec, 1\n\tds_add_u32 %0, %1\n\ts_mov_b64 exec, -1" ::"v"(LDS_CNT + 4 * k), "v"(1) : "memory");
}
template <int SLEEP, unsigned N>
DEVI void await(int k) {
  unsigned v;
  for (;;) {
    asm volatile("ds_read_b32 %0, %1\n\ts_waitcnt lgkmcnt(0)" : "=v"(v) : "v"(LDS_CNT + 4 * k) : "memory");
    if (__builtin_amdgcn_readfirstlane(v) >= N) break;
    if (SLEEP) __builtin_amdgcn_s_sleep(SLEEP);
  }
}
DEVI void post(int addr, int val) { asm volatile("ds_write_b32 %0, %1" ::"v"(addr), "v"(val) : "memory"); }
DEVI void peek(unsigned& v, int addr) { asm volatile("ds_read_b32 %0, %1" : "=v"(v) : "v"(addr) : "memory"); }
DEVI void require(unsigned v, int addr, unsigned n) {
  unsigned t;
  asm volatile("v_readfirstlane_b32 %1, %0\n\ts_cmp_ge_u32 %1, %3\n\ts_cbranch_scc1 .Lq%=\n"
               ".Lw%=:\n\tds_read_b32 %0, %2\n\ts_waitcnt lgkmcnt(0)\n\tv_readfirstlane_b32 %1, %0\n\ts_cmp_ge_u32 %1, %3\n\ts_cbranch_scc0 .Lw%=\n.Lq%=:"
               : "+&v"(v), "=&s"(t) : "v"(addr), "s"(n) : "memory", "scc");
}
DEVI void await_pair(int addr, unsigned n) {
  for (;;) {
    u32x2 v;
    asm volatile("ds_read_b64 %0, %1\n\ts_waitcnt lgkmcnt(0)" : "=v"(v) : "v"(addr) : "memory");
    const unsigned a = v[0], b = v[1];
    if (__builtin_amdgcn_readfirstlane(a < b ? a : b) >= n) break;
    __builtin_amdgcn_s_sleep(1);
  }
}
DEVI int lane_id() { int l; asm volatile("v_mbcnt_lo_u32_b32 %0, -1, 0\n\tv_mbcnt_hi_u32_b32 %0, -1, %0" : "=v"(l)); return l; }
DEVI int sig(int i) { return 4 * (i & 3) + (i >> 2); }
DEVI u64 flagval(unsigned tag) { return ((u64)(tag ^ 0x5bd1e995u) << 32) | tag; }

DEVI float xmax16(float v) { auto r = __builtin_amdgcn_permlane16_swap(asu(v), asu(v), false, false); return fmaxf(asf(r[0]), asf(r[1])); }
DEVI float xmax32(float v) { auto r = __builtin_amdgcn_permlane32_swap(asu(v), asu(v), false, false); return fmaxf(asf(r[0]), asf(r[1])); }
DEVI float xadd16(float v) { auto r = __builtin_amdgcn_permlane16_swap(asu(v), asu(v), false, false); return asf(r[0]) + asf(r[1]); }
DEVI float xadd32(float v) { auto r = __builtin_amdgcn_permlane32_swap(asu(v), asu(v), false, false); return asf(r[0]) + asf(r[1]); }
template <int CTRL> DEVI float dpp(float v) { return asf(__builtin_amdgcn_mov_dpp(asu(v), CTRL, 0xf, 0xf, true)); }
DEVI float half_sum(float v) {
  v += dpp<0xb1>(v); v += dpp<0x4e>(v); v += dpp<0x141>(v); v += dpp<0x140>(v);
  return xadd16(v);
}

DEVI int h_slot(int j, int l) { const int t = l < 4 ? 4 * j + l : 28 + 3 * j + l, d = (t * 43) >> 7; return 18 * (t - 3 * d) + d; }

DEVI int row_token(int p_, int pos) {
  const int tau = pos >> 4, i = pos & 15;
  int tok = tau == 0 ? p_ * 128 - 16 + 8 * (i & 1) + 2 * (i >> 2) + ((i & 3) < 2) : p_ * 128 + 32 * ((tau - 1) >> 1) + 2 * sig(i) + ((tau - 1) & 1);
  return tok < 0 ? 0 : tok;
}

struct ColGeo { int A, B, C, ct; };
DEVI ColGeo colgeo(bool att, int nw, int mw, int c, bool heavy) {
  const int cc = heavy ? (c ^ (2 * mw)) : c;
  const int ct = 4 * nw + cc, sec = (ct * 13) >> 6, hh = ct - 5 * sec;
  const bool q4 = hh == 4, sp = !heavy && c == 2;
  ColGeo r;
  r.A = att ? sec * 160 + (q4 ? 32 : 40 * hh) : heavy ? 128 * nw + 32 * cc : (sp ? 352 + 16 * nw : 128 + 64 * nw + 32 * c);
  r.B = att && q4 ? 40 : 8;
  r.C = att ? (q4 ? 80 : 16) : (sp ? 32 : 16);
  r.ct = ct;
  return r;
}

template <int NC>
struct Frags { u32x4 x[5]; u32x2 w[NC][2]; };

template <int N> DEVI void wait_lg() { asm volatile("s_waitcnt lgkmcnt(%0)" ::"i"(N) : "memory"); }

template <int NC, int K>
DEVI void s1_rd(Frags<NC>& n, int xo, int xh, const int (&wa)[NC]) {
  if constexpr (K == 0) ds128<0>(n.x[0], xo);
  else if constexpr (K <= 2 * NC) { if ((K - 1) & 1) dstr<7680>(n.w[(K - 1) >> 1][1], wa[(K - 1) >> 1]); else dstr<0>(n.w[(K - 1) >> 1][0], wa[(K - 1) >> 1]); }
  else if constexpr (K < 2 * NC + 4) ds128<(K - 2 * NC) * 2048>(n.x[K - 2 * NC], xo);
  else if constexpr (K == 2 * NC + 4) ds128<0>(n.x[4], xh);
}
template <int NC, int NH, int K>
DEVI void s1_mm(const Frags<NC>& c, f32x4 (&acc)[4][NC], f32x4 (&hacc)[NH]) {
  if constexpr (K < 4 * NC) mfma_a(acc[K / NC][K % NC], cat(c.w[K % NC][0], c.w[K % NC][1]), c.x[K / NC]);
  else mfma_a(hacc[K - 4 * NC], cat(c.w[K - 4 * NC][0], c.w[K - 4 * NC][1]), c.x[4]);
}
template <int NC, int NH, bool BAR>
DEVI void s1_step(const Frags<NC>& c, Frags<NC>& n, int xo, int xh, const int (&wa)[NC], f32x4 (&acc)[4][NC], f32x4 (&hacc)[NH]) {
  constexpr int NM = 4 * NC + NH, NR = 2 * NC + 5, D = BAR ? NM - NR : 0;
  sfor<NM>([&](auto KK) {
    constexpr int k = decltype(KK)::value;
    constexpr int need = k < NC ? 2 * k + 2 : (k % NC == 0 && k <= 4 * NC) ? 2 * NC + k / NC : -1;
    if constexpr (BAR && k == D) bar_lgkm();
    if constexpr (need >= 0 && (!BAR || k < D)) wait_lg<NR - 1 - need + (BAR ? 0 : (k < NR ? k : NR))>();
    s1_mm<NC, NH, k>(c, acc, hacc);
    if constexpr (k >= D) s1_rd<NC, k - D>(n, xo, xh, wa);
  });
}

template <int OFF> DEVI void dsw64(int addr, u32x2 d) { asm volatile("ds_write_b64 %0, %1 offset:%2" ::"v"(addr), "v"(d), "i"(OFF) : "memory"); }
template <int OFF> DEVI void ds64(u32x2& d, int addr) { asm volatile("ds_read_b64 %0, %1 offset:%2" : "=v"(d) : "v"(addr), "i"(OFF)); }

template <int NC>
DEVI void s1_compute(lptr shm, bool att, int w, int lane, int p_, int j_, int hf, char* ws PR_ARGS) {
  constexpr int NH = NC == 4 ? 2 : 3;
  const int mw = w & 1, nw = w >> 1, i = lane & 15, g = lane >> 4;
  f32x4 acc[4][NC], hacc[NH];
#pragma unroll
  for (int m = 0; m < 4; m++)
#pragma unroll
    for (int c = 0; c < NC; c++) { acc[m][c] = f32x4{0, 0, 0, 0}; asm volatile("" : "+v"(acc[m][c])); }
#pragma unroll
  for (int c = 0; c < NH; c++) { hacc[c] = f32x4{0, 0, 0, 0}; asm volatile("" : "+v"(hacc[c])); }
  int wb[NC], cb[NC], ct[NC];
#pragma unroll
  for (int c = 0; c < NC; c++) {
    const ColGeo cg = colgeo(att, nw, mw, c, NC == 4);
    wb[c] = (lane >> 2) * 480 + cg.A + cg.B * (lane & 1) + cg.C * ((lane >> 1) & 1);
    cb[c] = LDS_CS + 2 * (cg.A + cg.B * (g & 1) + cg.C * (g >> 1));
    ct[c] = cg.ct;
    asm volatile("" : "+v"(cb[c]), "+v"(wb[c]));
  }
  int xb = XOFF + i * 128 + ((g ^ (i >> 1) ^ (4 * hf)) << 4);
  int sta = LDS_ST + i * 8;
  asm volatile("" : "+v"(xb), "+v"(sta));
  const int own = (1 + 4 * mw) * 2048;
  Frags<NC> fa, fb;
  int wa[NC];
  STAMPB(12);
  bar_lgkm();
  STAMP(1);
  sfor<2 * NC + 5>([&](auto K) { s1_rd<NC, decltype(K)::value>(fa, xb + own, xb, wb); });
#pragma unroll
  for (int c = 0; c < NC; c++) wa[c] = wb[c] + WSLAB;
  s1_step<NC, NH, true>(fa, fb, (xb ^ 64) + own, xb ^ 64, wa, acc, hacc);
  int sl = 0;
#pragma nounroll
  for (int T = 1; T < 10; T++) {
    sl = sl == 2 * SLOT ? 0 : sl + SLOT;
#pragma unroll
    for (int c = 0; c < NC; c++) wa[c] = wb[c] + sl;
    s1_step<NC, NH, true>(fb, fa, xb + sl + own, xb + sl, wa, acc, hacc);
#pragma unroll
    for (int c = 0; c < NC; c++) wa[c] = wb[c] + sl + WSLAB;
    s1_step<NC, NH, false>(fa, fb, (xb ^ 64) + sl + own, (xb ^ 64) + sl, wa, acc, hacc);
  }
  wait_lg<0>();
  arrive(2);
  sfor<NH>([&](auto K) { s1_mm<NC, NH, 4 * NC + decltype(K)::value>(fb, acc, hacc); });
  u32x2 stu[5];
  u32x4 b1u[NC], csu[NC];
  sfor<4 * NC>([&](auto KK) {
    constexpr int k = decltype(KK)::value;
    s1_mm<NC, NH, k>(fb, acc, hacc);
    if constexpr (k < 4) ds64<(1 + k) * 128>(stu[k], sta + mw * 512);
    if constexpr (k == 4) ds64<0>(stu[4], sta);
    if constexpr (k == 5) {
      dsw_col0(cb[0], hacc[0], cb[1], hacc[1], cb[NH - 1], hacc[NH - 1]);
      if (NC == 4) arrive(8 + nw);
    }
    if constexpr (k >= 6 && k < 6 + NC) ds128<LDS_B1 - LDS_CS>(b1u[k - 6], cb[k - 6]);
  });
  STAMP(2);
  if (NC == 4) await<0, 2>(8 + nw);
#pragma unroll
  for (int c = 0; c < NC; c++) ds128<0>(csu[c], cb[c]);
  asm volatile("s_waitcnt lgkmcnt(0)\n\ts_nop 7" ::: "memory");
  f32x2 st[5];
  f32x4 cs[NC], b1[NC];
#pragma unroll
  for (int m = 0; m < 5; m++) { asm volatile("" : "+v"(stu[m])); st[m] = __builtin_bit_cast(f32x2, stu[m]); }
#pragma unroll
  for (int c = 0; c < NC; c++) { asm volatile("" : "+v"(csu[c]), "+v"(b1u[c])); cs[c] = __builtin_bit_cast(f32x4, csu[c]); b1[c] = __builtin_bit_cast(f32x4, b1u[c]); }
#pragma unroll
  for (int m = 0; m < 4; m++)
#pragma unroll
    for (int c = 0; c < NC; c++) pin(acc[m][c]);
#pragma unroll
  for (int c = 0; c < NH; c++) pin(hacc[c]);
  STAMPA(12);
#define SB __builtin_amdgcn_sched_barrier(0)
  const int l2 = lane_id();
  if (att) {
    const int g2 = l2 >> 4, piece = LDS_TB + ((l2 & 48) + ((l2 & 15) ^ (8 * (g2 >> 1)))) * 8;
    sfor<NC>([&](auto C) {
      constexpr int c = decltype(C)::value;
      const int ah = piece + ct[c] * 4608, ao = ah + (1 + 4 * mw) * 512;
      u32x2 o[5];
      SB;
#pragma unroll
      for (int m = 0; m < (c < NH ? 5 : 4); m++) {
        f32x4 a = m < 4 ? acc[m < 4 ? m : 0][c] : hacc[c < NH ? c : 0];
        f32x4 v = a * st[m][0] + cs[c] * st[m][1];
        o[m] = u32x2{pk2(v[0], v[1]), pk2(v[2], v[3])};
      }
      if (ABL & 4096) { pin(o[0]); pin(o[1]); pin(o[2]); pin(o[3]); if (c < NH) pin(o[4]); }
      else if ((ABL & 8192) && (c & 1)) { pin(o[0]); pin(o[1]); pin(o[2]); pin(o[3]); if (c < NH) pin(o[4]); }
      else {
      dsw64<0>(ao, o[0]); dsw64<512>(ao, o[1]); dsw64<1024>(ao, o[2]); dsw64<1536>(ao, o[3]);
      if (c < NH) dsw64<0>(ah, o[4]);
      }
    });
  } else {
    const u32x4 ra = mkr(ws + WS_A2, (ABL & 8) ? 0 : A2_BYTES);
    const int base = (8 * p_ + 4 * mw) * KS_T * 1024;
    const int o0 = base + (NC == 4 || j_ < 7 || nw == 2 ? h_slot(j_, NC == 4 ? 2 * nw + mw : 2 + nw) * 1024 : 0x70000000);
    const int o1 = base + (NC == 4 ? h_slot(j_, 2 * nw + (mw ^ 1)) * 1024 : j_ < 7 ? h_slot(j_, 6) * 1024 + (nw - 2) * 512 : 0x70000000);
    const float c1 = -2.f * 0.7978845608028654f * 1.4426950408889634f, c2 = c1 * 0.044715f;
    unsigned sp[2];
#pragma unroll
    for (int m = 0; m < 4; m++) {
      f32x4 v[NC], t[NC];
      SB;
#pragma unroll
      for (int c = 0; c < NC; c++) t[c] = cs[c] * st[m][1] + b1[c];
      SB;
#pragma unroll
      for (int c = 0; c < NC; c++) v[c] = acc[m][c] * st[m][0] + t[c];
      SB;
#pragma unroll
      for (int c = 0; c < NC; c++) t[c] = v[c] * v[c];
      SB;
#pragma unroll
      for (int c = 0; c < NC; c++) t[c] = t[c] * c2 + c1;
      SB;
#pragma unroll
      for (int c = 0; c < NC; c++) t[c] = t[c] * v[c];
      SB;
#pragma unroll
      for (int c = 0; c < NC; c++)
#pragma unroll
        for (int e = 0; e < 4; e++) t[c][e] = __builtin_amdgcn_exp2f(t[c][e]);
      SB;
#pragma unroll
      for (int c = 0; c < NC; c++) t[c] = t[c] + 1.f;
      SB;
#pragma unroll
      for (int c = 0; c < NC; c++)
#pragma unroll
        for (int e = 0; e < 4; e++) t[c][e] = __builtin_amdgcn_rcpf(t[c][e]);
      SB;
#pragma unroll
      for (int c = 0; c < NC; c++) v[c] = v[c] * t[c];
      SB;
      unsigned o[NC][2];
#pragma unroll
      for (int c = 0; c < NC; c++) { o[c][0] = pk2(v[c][0], v[c][1]); o[c][1] = pk2(v[c][2], v[c][3]); }
      st_wt(ra, l2 * 16, o0 + m * KS_T * 1024, u32x4{o[0][0], o[0][1], o[1][0], o[1][1]});
      if (NC == 4) st_wt(ra, l2 * 16, o1 + m * KS_T * 1024, u32x4{o[2][0], o[2][1], o[NC - 1][0], o[NC - 1][1]});
      else if (m & 1) {
        auto s0 = __builtin_amdgcn_permlane32_swap(sp[0], o[2][0], false, false);
        auto s1 = __builtin_amdgcn_permlane32_swap(sp[1], o[2][1], false, false);
        st_wt(ra, (l2 >> 5) * (KS_T * 1024) + (l2 & 31) * 16, o1 + (m - 1) * KS_T * 1024, u32x4{s0[0], s1[0], s0[1], s1[1]});
      } else { sp[0] = o[2][0]; sp[1] = o[2][1]; }
    }
  }
}

template <bool MAX> DEVI float red4(const float (&v)[4]) {
  auto op = [](unsigned a, unsigned b) { float r; if (MAX) asm("v_max_f32 %0, %1, %2" : "=v"(r) : "v"(a), "v"(b)); else r = asf(a) + asf(b); return r; };
  auto a = __builtin_amdgcn_permlane32_swap(asu(v[0]), asu(v[1]), false, false);
  auto b = __builtin_amdgcn_permlane32_swap(asu(v[2]), asu(v[3]), false, false);
  auto c = __builtin_amdgcn_permlane16_swap(asu(op(a[0], a[1])), asu(op(b[0], b[1])), false, false);
  return op(c[0], c[1]);
}
DEVI void bcast4(float r, float (&v)[4]) {
  auto e = __builtin_amdgcn_permlane16_swap(asu(r), asu(r), false, false);
  unsigned e0 = e[0], e1 = e[1];
  auto a = __builtin_amdgcn_permlane32_swap(e0, e0, false, false);
  auto b = __builtin_amdgcn_permlane32_swap(e1, e1, false, false);
  v[0] = asf(a[0]); v[1] = asf(a[1]); v[2] = asf(b[0]); v[3] = asf(b[1]);
}

DEVI void att_table(lptr shm, int lane) {
  const int i = lane & 15, g = lane >> 4, p = lane & 3;
  const int d = sig(i) - g;
  const unsigned NEG = 0xf149f2cau;
  bool vo[4], vp[4];
#pragma unroll
  for (int r = 0; r < 4; r++) { vo[r] = (unsigned)(d - 4 * r) <= 7u; vp[r] = d + 16 - 4 * r <= 7; }
  auto pm = [](bool lo, bool hi) { return (lo ? 0xffffu : 0u) | (hi ? 0xffff0000u : 0u); };
  LAS u32x4* t = (LAS u32x4*)(shm + LDS_TAB) + lane;
  t[0] = u32x4{vo[0] ? 0u : NEG, vo[1] ? 0u : NEG, vo[2] ? 0u : NEG, vo[3] ? 0u : NEG};
  t[64] = u32x4{pm(vp[2], vp[3]), pm(vo[0], vo[1]), vp[2] ? 0u : NEG, vp[3] ? 0u : NEG};
  t[128] = u32x4{pm(vo[2], vo[3]), (unsigned)(LDS_TB + (g * 16 + (i ^ (8 * (g >> 1)))) * 8), (unsigned)(LDS_TB + (p * 16 + ((lane >> 2) ^ (8 * (p >> 1)))) * 8), 0u};
}
DEVI int sel(u64 mask, int a, int b) { int r; asm("v_cndmask_b32_e64 %0, %2, %1, %3" : "=v"(r) : "v"(a), "v"(b), "s"(mask)); return r; }

DEVI void s1_attention(lptr shm, int w, int p_, int j_, char* ws PR_ARGS) {
  arrive(3);
  const int lane = lane_id();
  const int tau = (w + 1) * 512, taup = w >= 2 ? tau - 1024 : 0, flip = w == 1 ? 16 : 0;
  const LAS u32x4* t = (const LAS u32x4*)(shm + LDS_TAB) + lane;
  const u32x4 t0 = t[0], t2 = t[128];
  u32x4 t1 = t[64];
  if (p_ == 0 && w < 2) { t1[0] = 0u; t1[2] = 0xf149f2cau; t1[3] = 0xf149f2cau; }
  const f32x4 mO = __builtin_bit_cast(f32x4, t0), mP = __builtin_bit_cast(f32x4, t1);
  const unsigned M1 = t1[0], M2 = t1[1], M3 = t2[0];
  const int po = t2[1] + tau, pp = (t2[1] ^ flip) + taup, to = t2[2] + tau, tp = (t2[2] ^ flip) + taup;
  int aq[4], avo[4], avp[4];
#pragma unroll
  for (int h = 0; h < 4; h++) {
    aq[h] = sel(0xffffull << (16 * h), po, LDS_ZERO - 4 * 4608);
    avo[h] = sel(0x1111111111111111ull << h, to, LDS_ZERO - 14 * 4608);
    avp[h] = sel(0x1111111111111111ull << h, tp, LDS_ZERO - 14 * 4608);
    asm volatile("" : "+v"(aq[h]), "+v"(avo[h]), "+v"(avp[h]));
  }
  STAMPA(14);
  await<SPS, 8>(3);
  STAMPA(15);
  u32x2 qa[4][2], ko[4][2], kp[4][2], va[4][2], vb[4][2];
  f32x4 sp[4], so[4];
  auto rd = [&](auto H) {
    constexpr int h = decltype(H)::value;
    ds64<(5 + h) * 4608>(ko[h][0], po); ds64<9 * 4608>(ko[h][1], po); ds64<h * 4608>(qa[h][0], po); ds64<4 * 4608>(qa[h][1], aq[h]);
    ds64<(5 + h) * 4608>(kp[h][0], pp); ds64<9 * 4608>(kp[h][1], pp);
  };
  auto rv = [&](auto H) {
    constexpr int h = decltype(H)::value;
    dstr<(10 + h) * 4608>(va[h][0], tp); dstr<(10 + h) * 4608>(va[h][1], to); dstr<14 * 4608>(vb[h][0], avp[h]); dstr<14 * 4608>(vb[h][1], avo[h]);
  };
  auto qk = [&](auto H, auto N) {
    constexpr int h = decltype(H)::value;
    asm volatile("s_waitcnt lgkmcnt(%6)" : "+v"(ko[h][0]), "+v"(ko[h][1]), "+v"(qa[h][0]), "+v"(qa[h][1]), "+v"(kp[h][0]), "+v"(kp[h][1]) : "i"(decltype(N)::value));
    so[h] = mfma(cat(ko[h][0], ko[h][1]), cat(qa[h][0], qa[h][1]), mO);
    sp[h] = mfma(cat(kp[h][0], kp[h][1]), cat(qa[h][0], qa[h][1]), mP);
  };
  rd(IC(0)); rd(IC(1));
  qk(IC(0), IC(6)); rd(IC(2));
  qk(IC(1), IC(6)); rd(IC(3));
  qk(IC(2), IC(6)); rv(IC(0)); rv(IC(1));
  qk(IC(3), IC(8)); rv(IC(2));
  asm volatile("s_waitcnt lgkmcnt(8)");
  rv(IC(3));
  const float qs = 0.22360679774997896f * 1.4426950408889634f;
  float ee[4], eo[4], mx[4], l[4];
#pragma unroll
  for (int h = 0; h < 4; h++) { ee[h] = fmaxf(fmaxf(sp[h][2], so[h][0]), so[h][2]); eo[h] = fmaxf(fmaxf(sp[h][3], so[h][1]), so[h][3]); mx[h] = fmaxf(ee[h], eo[h]); }
  bcast4(red4<true>(mx) * -qs, mx);
#pragma unroll
  for (int h = 0; h < 4; h++) {
    f32x2 t = f32x2{ee[h], eo[h]} * qs + mx[h];
    ee[h] = __builtin_amdgcn_exp2f(t[0]); eo[h] = __builtin_amdgcn_exp2f(t[1]); l[h] = ee[h] + eo[h];
  }
  bcast4(__builtin_amdgcn_rcpf(red4<false>(l)), l);
  asm volatile("s_waitcnt lgkmcnt(0)" : "+v"(va[0][0]), "+v"(va[0][1]), "+v"(va[1][0]), "+v"(va[1][1]), "+v"(va[2][0]), "+v"(va[2][1]), "+v"(va[3][0]), "+v"(va[3][1]),
               "+v"(vb[0][0]), "+v"(vb[0][1]), "+v"(vb[1][0]), "+v"(vb[1][1]), "+v"(vb[2][0]), "+v"(vb[2][1]), "+v"(vb[3][0]), "+v"(vb[3][1]));
  f32x4 o4 = {0, 0, 0, 0}, oh[4];
#pragma unroll
  for (int h = 0; h < 4; h++) {
    const unsigned pk = pk2(f32x2{ee[h], eo[h]} * l[h]);
    u32x4 pf = {0u, pk & M1, pk & M2, pk & M3};
    o4 = mfma(cat(vb[h][0], vb[h][1]), pf, o4);
    oh[h] = mfma(cat(va[h][0], va[h][1]), pf, f32x4{0, 0, 0, 0});
  }
  const u32x4 ra = mkr(ws + WS_A2, A2_BYTES);
  const int base = ((8 * p_ + w) * KS_T + 54 + 5 * (j_ >> 1)) * 1024;
#pragma unroll
  for (int hp = 0; hp < 2; hp++)
    st_wt(ra, lane * 16, base + ((j_ & 1) * 3 + hp) * 1024,
          u32x4{pk2(oh[2 * hp][0], oh[2 * hp][1]), pk2(oh[2 * hp][2], oh[2 * hp][3]), pk2(oh[2 * hp + 1][0], oh[2 * hp + 1][1]), pk2(oh[2 * hp + 1][2], oh[2 * hp + 1][3])});
  unsigned a0 = pk2(o4[0], o4[1]), a1 = pk2(o4[2], o4[3]);
  auto s0 = __builtin_amdgcn_permlane32_swap(a0, a0, false, false);
  auto s1 = __builtin_amdgcn_permlane32_swap(a1, a1, false, false);
  st_wt(ra, lane < 32 ? lane * 16 : 0x70000000, base + 2 * 1024 + (j_ & 1) * 512, u32x4{s0[0], s1[0], s0[1], s1[1]});
  arrive(4);
}

DEVI int s2_perm(int s, bool o, int rot) { return o ? s ^ (s < 16 ? rot : rot & 1) : s < 8 ? s ^ rot : s < 10 ? s ^ (rot & 1) : ((s - 10) ^ rot) + 10; }
DEVI int fin_off(int G0, int n_, int r0, int lane) {
  const int frow = r0 + (lane >> 3), fG = G0 + (frow >> 4);
  return (32 * (fG >> 1) + 2 * sig(frow & 15) + (fG & 1)) * 1280 + n_ * 128 + (lane & 7) * 16;
}
constexpr int s2_slot(int r) { return r < 18 ? (r % 9) * 16384 : 147456 + (r - 18) * 4096; }
template <int NS, class GO, class PUB>
DEVI void s2_compute(int w, int kq, int G0, int rot, bool late, char* ws, GO go, PUB pub PR_ARGS) {
  constexpr int AD = NS == 20 ? ADS : ADY;
  const int mh = w >> 2, lane = lane_id();
  if (ABL & 16) G0 = 0;
  const u32x4 ra = mkr(ws + WS_A2, (ABL & 2) ? 0 : A2_BYTES);
  int av = lane * 16;
  pin(av);
  int ao[3], bs[4];
#pragma unroll
  for (int m = 0; m < 3; m++) { int G = G0 + 3 * mh + m; G = G < 128 ? G : 127; ao[m] = (G * KS_T + (NS == 20 ? 54 : 18 * (kq - 1))) * 1024; }
#pragma unroll
  for (int c = 0; c < 4; c++) {
    bs[c] = kq * 4096 + (lane >> 2) * 128 + ((c ^ ((lane >> 3) & 3)) << 5) + 8 * (lane & 3);
  }
  f32x4 acc[3][4];
#pragma unroll
  for (int m = 0; m < 3; m++)
#pragma unroll
    for (int c = 0; c < 4; c++) { acc[m][c] = f32x4{0, 0, 0, 0}; pin(acc[m][c]); }
  u32x4 a[AD][3];
  u32x2 b[3][4][2];
  int ca = LDS_CONS + kq * 8 + mh * 4, pa = LDS_PROD + kq * 4;
  pin(ca); pin(pa);
  unsigned pv, lv;
  auto rdb = [&](auto R, auto K) {
    constexpr int r = decltype(R)::value, k = decltype(K)::value;
    constexpr int off = s2_slot(r), nxt = s2_slot(r + 1) / 65536 - off / 65536;
    if constexpr (r < 3) dstr<off % 65536 + (k & 1) * 2048>(b[r % 3][k >> 1][k & 1], bs[k >> 1]);
    else redstr<off % 65536 + (k & 1) * 2048>(b[r % 3][k >> 1][k & 1], bs[k >> 1]);
    if constexpr (nxt != 0 && (k & 1)) vinc(bs[k >> 1], nxt * 65536);
  };
  auto lda = [&](auto S, int m) {
    constexpr int s = decltype(S)::value;
    if constexpr (s < AD) bload_sc1(a[s % AD][m], ra, av, ao[m] + (s2_perm(s, NS == 20, rot) << 10));
    else reload_sc1(a[s % AD][m], ra, av, ao[m] + (s2_perm(s, NS == 20, rot) << 10));
  };
  auto ldb = [&]() {
    peek(pv, pa); wait_lg<0>(); require(pv, pa, 1);
    sfor<3>([&](auto R) { sfor<8>([&](auto K) { rdb(R, K); }); wait_lg<(decltype(R)::value < 2 ? 4 : 0)>(); });
    post(ca, 1);
  };
  if constexpr (NS == 20) {
    go(0);
    ldb();
    STAMP(6);
    go(1);
    STAMP(7);
    sfor<AD - 1>([&](auto S) {
#pragma unroll
      for (int m = 0; m < 3; m++) lda(S, m);
    });
  } else {
    peek(lv, LDS_CNT + 4 * 15); wait_lg<0>(); pin(lv);
    const int ready = (NOEARLY && late) ? 0 : __builtin_amdgcn_readfirstlane(lv);
    sfor<AD - 1>([&](auto S) { const int o = s2_perm(decltype(S)::value, false, rot) << 10; bload3_if<true>(a[decltype(S)::value], ra, av, ao[0] + o, ao[1] + o, ao[2] + o, ready); });
    ldb();
    STAMP(6);
    if (late) {
      wait_vm<0>();
      pub();
      STAMP(5);
    }
    go(1);
    STAMP(7);
    sfor<AD - 1>([&](auto S) { const int o = s2_perm(decltype(S)::value, false, rot) << 10; bload3_if<false>(a[decltype(S)::value], ra, av, ao[0] + o, ao[1] + o, ao[2] + o, ready); });
  }
  if (NS == 20) __builtin_amdgcn_s_setprio(SPRIO);
  sfor<NS - 3>([&](auto S) {
    constexpr int s = decltype(S)::value;
    constexpr bool LA = s + AD - 1 < NS, RB = s >= 1 && s + 2 < NS;
    constexpr int later = NS - 1 - s < AD - 2 ? NS - 1 - s : AD - 2;
    if constexpr (s == 3) require(pv, pa, 2);
    if constexpr (s == 7) require(pv, pa, 3);
    if constexpr (s == 10) require(pv, pa, 4);
    if constexpr (s == 13) require(pv, pa, 5);
    if constexpr (s == 2 || s == 6 || s == 9 || s == 12) peek(pv, pa);
    if constexpr (NS == 18 && s == 11 - AD) require(lv, LDS_CNT + 4 * 5, 1);
    if constexpr (NS == 18 && s == 10 - AD) peek(lv, LDS_CNT + 4 * 5);
    if constexpr (s == NS - 4) peek(lv, ca ^ 4);
    sfor<3>([&](auto M) {
      constexpr int m = decltype(M)::value;
      if constexpr (LA) lda(IC(s + AD - 1), m);
      wait_vm<3 * later + 2 - m + (LA ? m + 1 : 0)>();
      if constexpr (m == 1 && s >= 2 && s + 1 < NS) wait_lg<(RB ? 4 : 0)>();
      sfor<4>([&](auto C) {
        constexpr int c = decltype(C)::value;
        mfma_a(acc[m][c], cat(b[s % 3][c][0], b[s % 3][c][1]), a[s % AD][m]);
        if constexpr (RB && m < 2) rdb(IC(s + 2), IC(4 * m + c));
      });
    });
    if constexpr (s == 3 || s == 6 || s == 9 || s == 12) post(ca, s);
    if constexpr (s == 2) { STAMPC(12); }
    if constexpr (s == 11) { STAMPC(13); }
  });
  wait_lg<4>();
  sfor<8>([&](auto K) { rdb(IC(NS - 1), K); });
  if constexpr (AD == 3) {
#pragma unroll
    for (int m = 0; m < 3; m++) lda(IC(NS - 1), m);
  }
  int da[4];
#pragma unroll
  for (int c = 0; c < 4; c++) da[c] = mh * 49152 + kq * 4096 + (av & 0xf0) * 16 + ((((4 * c) << 4) + (av >> 8 << 4)) ^ (av & 0x70));
  require(lv, ca ^ 4, mh ? 12 : 9);
  sfor<3>([&](auto M) {
    constexpr int m = decltype(M)::value;
    sfor<3>([&](auto U) {
      constexpr int u = decltype(U)::value, s = NS - 3 + u;
      if constexpr (m == 0) wait_vm<3 * (2 - u) + 2>();
      if constexpr (m > 0 && u == 2) wait_vm<2 - m>();
      if constexpr (m == 0 && u == 1) wait_lg<8>();
      if constexpr (m == 0 && u == 2) wait_lg<0>();
      sfor<4>([&](auto C) {
        constexpr int c = decltype(C)::value;
        mfma_a(acc[m][c], cat(b[s % 3][c][0], b[s % 3][c][1]), a[s % AD][m]);
        if constexpr (m > 0 && u > 0 && (c & 1)) dsw128<(m - 1) * 16384>(da[2 * (u - 1) + (c >> 1)], __builtin_bit_cast(u32x4, acc[m - 1][2 * (u - 1) + (c >> 1)]));
      });
    });
  });
  STAMP(9);
  asm volatile("s_nop 4");
#pragma unroll
  for (int c = 0; c < 4; c++) dsw128<2 * 16384>(da[c], __builtin_bit_cast(u32x4, acc[2][c]));
  arrive((NS == 20 ? 28 : 12) + mh);
}

__global__ __launch_bounds__(768, 3) void fused_block(const void* __restrict__ x, const void* __restrict__ wqkv, const void* __restrict__ wo,
                                                      const void* __restrict__ w1, const unsigned short* __restrict__ b1,
                                                      const void* __restrict__ w2, const char* __restrict__ b2, char* __restrict__ out,
                                                      char* ws) {
  extern __shared__ __attribute__((aligned(16))) char shm_[];
  lptr shm = (lptr)shm_;
  const int tid = threadIdx.x, lane = tid & 63, w = __builtin_amdgcn_readfirstlane(tid >> 6);
  const int xcd = blockIdx.x & 7, idx = blockIdx.x >> 3;
  const int p_ = (xcd & 1) * 8 + (idx & 7), sub = idx >> 3;
  const bool att = !(sub & 1);
  const int j_ = (xcd >> 1) * 2 + (sub >> 1);
  const int uidx = (att ? 16 : 0) + (idx & 7) + 8 * (idx >> 4);
  const int unit = (xcd >> 1) * 28 + uidx;
  const bool has2 = uidx < 28 && unit < 110;
  const int mt = unit / 5, nl = unit - 5 * mt, n_ = (xcd & 1) * 5 + nl;
  const int G0 = 6 * mt;
  const int kq = w < 8 ? (w + (w >> 2)) & 3 : w & 3;
  const int rot = (ROT2);
  STAMP(0);
  if (w == 7) {
    u64 pc; asm volatile("s_getpc_b64 %0" : "=s"(pc));
    touch(mkr((const void*)(pc & ~255ull), CODE_BYTES), (idx + 32 * lane) * 128);
  }
  asm volatile("" : "+s"(x), "+s"(wqkv), "+s"(wo), "+s"(w1), "+s"(b1), "+s"(w2), "+s"(b2), "+s"(out), "+s"(ws));
  const int hf = KHF, tx = KTX;
  const int rid = (att ? 0 : 8) + j_;
  auto watch = [&](int kind, int word, u64 fv) {
    const int Gb = G0 + 5 < 128 ? G0 + 5 : 127, lane = lane_id();
    GAS u64* fp = (GAS u64*)(ws + WS_BOX) + (mt * 10 + n_) * 64 + 8 * kind + (lane & 7) + ((Gb >> 3) != (G0 >> 3) ? 4 * (lane & 8) : 0);
#pragma nounroll
    for (unsigned spins = 0; spins < SPIN_CAP; spins++) {
      if (((ABL & 32) && kind == 1) || ((ABL & 64) && kind == 0) || ((ABL & 128) && kind == 2)) break;
      if (__all(__hip_atomic_load(fp, __ATOMIC_RELAXED, __HIP_MEMORY_SCOPE_AGENT) == fv)) break;
      __builtin_amdgcn_s_sleep(1);
    }
    post(LDS_CNT + 4 * word, 1);
  };
  int fu;
  if (__builtin_expect(w >= 8, 1)) {
    __builtin_amdgcn_s_setprio(3);
    const int lw = w - 8;
    const int kt0 = (KT0) % 10;
    auto ktile = [&](int T) { const int k = kt0 + (T ^ (T < 8 ? tx : tx & 1)); return k >= 10 ? k - 10 : k; };
    const int RS = att ? 3840 : 3456;
    const u32x4 rx = mkr(x, S_ * D_ * 2), rw1 = mkr(att ? wqkv : w1, D_ * RS);
    const int r8 = lane >> 3, cp = lane & 7, t0 = p_ * 128;
    int xo[2], hv, wv;
#pragma unroll
    for (int e = 0; e < 2; e++)
      xo[e] = (t0 + 32 * lw + 2 * (4 * (r8 & 3) + 2 * e + (r8 >> 2))) * 1280 + ((cp ^ (4 * e + (r8 >> 1))) << 4);
    auto ldx = [&](int T) { const int o = ktile(T) * 128; dma4<1024>(rx, ~0ull, (T % 3) * SLOT + XOFF + 2048 + 4 * lw * 1024, xo[0], xo[1], o, o, o + 1280, o + 1280); };
    STAMP(12);
    ldx(0);
    {
      const int hi = lane >= 30, ch = lane - 30 * hi, lim = j_ < 7 ? 27 : 19;
      wv = hi * RS + (att ? ((ch >= 10) + (ch >= 20)) * 1120 + j_ * 160 + ch * 16 : j_ * 448 + (ch < lim ? ch : lim) * 16);
    }
    const int wrow = (16 * (lw & 1) + 4 * (lw >> 1)) * RS;
    auto ldw = [&](int T, int h) {
      const int so = (64 * ktile(T) + 32 * (h ^ hf)) * RS + wrow;
      dma4<960>(rw1, 0x0fffffffffffffffull, (T % 3) * SLOT + h * WSLAB + 4 * lw * 960, wv, wv, so, so + 2 * RS, so + 8 * RS, so + 10 * RS);
    };
    ldw(0, 0);
    {
      int tok = t0 - 16 + 8 * (r8 & 1) + 2 * lw + ((r8 & 3) < 2);
      tok = tok < 0 ? 0 : tok;
      hv = att ? tok * 1280 + ((cp ^ ((2 * lw + ((r8 & 3) >> 1)) & 7)) << 4) : 0x70000000;
    }
    const u64 hmask = lw == 0 ? 0xffffff00ull : 0xffffffffull;
    auto ldh = [&](int T) { dma1(rx, hmask, (T % 3) * SLOT + XOFF + lw * 512, hv, ktile(T) * 128); };
    ldh(0); ldw(0, 1);
    STAMP(13);
    bar_vm<4>();
    STAMP(14);
    ldx(1); ldh(1);
    bar_vm<5>();
    ldw(1, 0); ldw(1, 1);
    unsigned sink = 0;
    const int wv2 = blockIdx.x * 4 + lw;
    const u32x4 rs2 = mkr(wv2 < 720 ? w2 : wo, wv2 < 720 ? F_ * 1280 : D_ * 1280);
    const int so2 = wv2 < 720 ? wv2 * 3072 : min((wv2 - 720) * 3072, D_ * 1280 - 3072);
    ldx(2); ldh(2);
    if (STW) stouch<8>(sink, rs2, so2);
    bar_vm<5>();
    const int srow = p_ * 128 + 8 * rid + 2 * lw + (lane >> 5);
    u32x2 xr[5];
    unsigned ep, b1v;
    setup_loads(xr, ep, b1v, rx, srow * 1280 + (lane & 31) * 8, mkr(ws, 4), mkr(b1, F_ * 2), (j_ * 224 + 64 * lw + lane) * 2);
    if (BOXW) touch_sc1(mkr(ws + WS_BOX, has2 && lw == 0 ? 220 * 512 : 0), (mt * 10 + n_) * 512 + (lane & 3) * 128);
    ldw(2, 0); ldw(2, 1); ldx(3); ldh(3); ldw(3, 0);
    if (STW) stouch<8>(sink, rs2, so2 + 1024);
    bar_vm<9>();
    pin(ep); pin(b1v);
#pragma unroll
    for (int k = 0; k < 5; k++) pin(xr[k]);
    const unsigned tag = __builtin_amdgcn_readfirstlane(ep) + 1u;
    auto warm2 = [&](int ev) {
      const int id = min(WARM2 == 2 ? (ev * 64 + uidx * 4 + lw) * 62 + min(lane, 61) : (ev * 128 + idx * 4 + lw) * 31 + min(lane, 30), 11839), row = (id * 52429) >> 18;
      if (WARM2 == 2 && att) touch(mkr(w2, 0), 0); else if (WARM2) touchg((row < F_ ? (const char*)w2 : (const char*)wo - F_ * 1280) + row * 1280 + (xcd & 1) * 640 + (id - 5 * row) * 128);
    };
    ldw(3, 1); ldx(4); ldw(4, 0); warm2(0); ldh(4); ldw(4, 1);
    {
      float s = 0;
#pragma unroll
      for (int k = 0; k < 5; k++) s += (blo(xr[k][0]) + bhi(xr[k][0])) + (blo(xr[k][1]) + bhi(xr[k][1]));
      const float m = half_sum(s) * (1.f / D_);
      float q = 0;
#pragma unroll
      for (int k = 0; k < 10; k++) { const unsigned u = xr[k >> 1][k & 1]; const float a = blo(u) - m, b = bhi(u) - m; q = __builtin_fmaf(a, a, q); q = __builtin_fmaf(b, b, q); }
      const float rs = __builtin_amdgcn_rsqf(half_sum(q) * (1.f / D_) + 1e-5f);
      st_wt(mkr(ws + WS_STAT, S_ * 16), (lane & 31) == 0 ? srow * 16 : 0x70000000, 0, u32x4{asu(rs), tag, asu(-m * rs), tag});
      *(LAS float*)(shm + LDS_B1 + (64 * lw + lane) * 4) = blo(b1v);
      *(LAS unsigned*)(shm + LDS_CNT + 4) = tag;
    }
    if (STW) stouch<8>(sink, rs2, so2 + 2048);
    bar_vm<14 + (WARM2 != 0)>();
    const u32x4 rw = mkr(kq == 0 ? wo : w2, has2 && !(ABL & 1) ? (kq == 0 ? D_ * 1280 : F_ * 1280) : 0);
    const int cbyte = n_ * 128 + ((cp ^ ((lane >> 4) << 1)) << 4);
    const int v1 = r8 * 1280 + cbyte, v2 = kq == 0 ? (20 * (r8 >> 2) + (r8 & 3)) * 1280 + cbyte : v1;
    int gtab;
    {
      const int so = s2_perm(lane, true, rot), P = (so * 13) >> 6, kp = so - 5 * P;
      const int r0 = 160 * P + (kp == 2 ? 16 : 40 * (kp - (kp > 2)));
      const int t = 3 * s2_perm(lane, false, rot) + kq - 1, u = t - 32;
      const int r1 = 32 * (t < 32 ? 7 * (t >> 2) + (t & 3) : 4 * ((u * 43) >> 7) + 4 + u);
      gtab = kq == 0 ? r0 * 1280 + (kp == 2) : r1 * 1280;
      pin(gtab);
    }
    const int sbn = 1280 * (kq == 0 ? 20 : 16);
    auto geom = [&](int s, int& rb, int& sa, int& sb, int& v) {
      const int e = __builtin_amdgcn_readlane(gtab, s), f = e & 1;
      v = v1 + ((v2 - v1) & -f);
      rb = e - f;
      sa = f ? 1280 * 80 : 1280 * 8;
      sb = f ? 1280 * 40 : sbn;
    };
    u32x4 pre[9][4];
    auto ldr = [&](auto R) {
      constexpr int r = decltype(R)::value;
      int rb, sa, sb, v; geom(9 + r, rb, sa, sb, v);
      bload(pre[r][0], rw, v, rb); bload(pre[r][1], rw, v, rb + sa); bload(pre[r][2], rw, v, rb + sb); bload(pre[r][3], rw, v, rb + sa + sb);
    };
    auto ldl = [&](auto S) {
      constexpr int s = decltype(S)::value;
      int rb, sa, sb, v; geom(s, rb, sa, sb, v);
      dma4<1024>(rw, ~0ull, s < 9 ? s * 16384 + kq * 4096 : 147456 + (s - 18) * 4096, v, v, rb, rb + sa, rb + sb, rb + sa + sb);
    };
    const u32x4 rst = mkr(ws + WS_STAT, S_ * 16);
    const int spos = 36 * lw + (lane < 36 ? lane : 35), sv = row_token(p_, spos) * 16;
    u32x4 sg;
    sfor<5>([&](auto TT) {
      constexpr int T = decltype(TT)::value + 5;
      if (T == 9) bload_sc1(sg, rst, sv, 0);
      if (T == 6) { STAMP(15); }
      ldx(T); ldw(T, 0);
      if (!(T & 1)) warm2(T / 2 - 2);
      ldh(T); ldw(T, 1);
      bar_vm<13 + (WARM2 && !(T & 1))>();
    });
    STAMP(1);
    asm volatile("" : "+v"(sg));
#pragma nounroll
    for (unsigned spins = 0; !__all(sg[1] == tag && sg[3] == tag) && spins < SPIN_CAP; spins++) {
      __builtin_amdgcn_s_sleep(2);
      bload_sc1(sg, rst, sv, 0);
      asm volatile("s_waitcnt vmcnt(0)" : "+v"(sg) :: "memory");
    }
    *(LAS u32x2*)(shm + LDS_ST + spos * 8) = u32x2{sg[0], sg[2]};
    ldl(IC(3));
    wait_vm<4>();
    STAMP(2);
    bar_lgkm();
    asm volatile("" : "+s"(sink));
    __builtin_amdgcn_s_setprio(LP1);
    STAMP(3);
    if (!has2) return;
    const int pa = LDS_PROD + kq * 4, wa = kq * 4096 + lane * 16;
    ldl(IC(4));
    if (!att) { ldl(IC(5)); ldl(IC(6)); }
    ldr(IC(0)); ldr(IC(1));
    STAMP(4);
    await<1, 8>(2);
    ldl(IC(0)); wait_vm<THR>(); ldl(IC(1)); wait_vm<THR>(); ldl(IC(2)); wait_vm<THR>();
    if (!att) { ldl(IC(7)); wait_vm<THR>(); ldl(IC(8)); wait_vm<THR>(); if (kq == 0) { ldl(IC(18)); wait_vm<THR>(); ldl(IC(19)); } }
    __builtin_amdgcn_s_setprio(LP2);
    ldr(IC(2)); wait_vm<4>();
    post(pa, att ? 1 : 2);
    STAMP(5);
    sfor<6>([&](auto R) { ldr(IC(decltype(R)::value + 3)); wait_vm<THR2>(); });
    if (att) {
      await<1, 8>(4);
      ldl(IC(5)); ldl(IC(6)); ldl(IC(7)); ldl(IC(8));
      if (kq == 0) { ldl(IC(18)); ldl(IC(19)); }
    }
    if (lw == 3) touch(mkr(b2, D_ * 2), n_ * 128);
    wait_vm<0>();
    post(pa, 2);
#define NAP __builtin_amdgcn_s_sleep(PSL);
#define PUT(r, off) dsw128<off>(wa + (r / 3) * 49152, pre[r][0]); NAP dsw128<off + 1024>(wa + (r / 3) * 49152, pre[r][1]); NAP dsw128<off + 2048>(wa + (r / 3) * 49152, pre[r][2]); NAP dsw128<off + 3072>(wa + (r / 3) * 49152, pre[r][3]); NAP
    __builtin_amdgcn_s_setprio(0);
    await_pair(LDS_CONS + kq * 8, 1);
    PUT(0, 0) PUT(1, 16384) PUT(2, 32768)
    post(pa, 3);
    STAMP(6);
    await_pair(LDS_CONS + kq * 8, 3);
    PUT(3, 0) PUT(4, 16384) PUT(5, 32768)
    post(pa, 4);
    await_pair(LDS_CONS + kq * 8, 6);
    PUT(6, 0) PUT(7, 16384) PUT(8, 32768)
    post(pa, 5);
    fu = 2 + lw;
  } else {
    if (tid < 24) *(LAS u32x4*)(shm + (tid >> 3) * SLOT + XOFF + (tid & 7) * 16) = u32x4{0x3f803f80u, 0x3f803f80u, 0x3f803f80u, 0x3f803f80u};
    if (tid >= 448 && tid < 480) *(LAS unsigned*)(shm + LDS_CNT + 4 * (tid - 448)) = 0u;
    if (w == 5 && att) att_table(shm, lane);
    const bool light = att ? (w >= 6) : (w >= 4);
    if (light) s1_compute<3>(shm, att, w, lane, p_, j_, hf, ws PR_PASS);
    else s1_compute<4>(shm, att, w, lane, p_, j_, hf, ws PR_PASS);
    STAMP(3);
    if (att) s1_attention(shm, w, p_, j_, ws PR_PASS);
    STAMP(4);
    const unsigned tag = __builtin_amdgcn_readfirstlane(*(LAS unsigned*)(shm + LDS_CNT + 4));
    GAS u64* flags = (GAS u64*)(ws + WS_FLAG);
    const u64 fv = flagval(tag);
    auto pub = [&]() {
      const int lane = lane_id(), kind = att ? 0 : 1 + (w >> 2);
      unsigned old = 0;
      if (lane == 0) old = __hip_atomic_fetch_add((LAS unsigned*)(shm + LDS_CNT + (kind == 1 ? 120 : 0)), 1u, __ATOMIC_RELAXED, __HIP_MEMORY_SCOPE_WORKGROUP);
      if (__builtin_amdgcn_readfirstlane(old) == (att ? 7u : 3u)) {
        const int dm = (lane * 26) >> 8, cm = (8 * p_) / 6 + dm;
        GAS u64* dst = lane == 30 ? flags + p_ * 16 + rid : (GAS u64*)(ws + WS_BOX) + (cm * 10 + lane - 10 * dm) * 64 + (p_ - ((6 * cm) >> 3)) * 32 + 8 * kind + j_;
        if (lane == 30 || (lane < 30 && 6 * cm < 8 * p_ + 8)) __hip_atomic_store(dst, fv, __ATOMIC_RELAXED, __HIP_MEMORY_SCOPE_AGENT);
      }
    };
    const bool late = has2 && kq != 0 && (LATE_ALL == 1 ? (att || w >= 4) : LATE_ALL == 2 ? (!att && w >= 4) : LATE_ALL == 3 ? false : att);
    if (!late) {
      wait_vm<0>();
      pub();
      STAMP(5);
    }
    if (!has2) {
      if (blockIdx.x == 191 && w == 0) {
        const int lane = lane_id();
#pragma nounroll
        for (unsigned spins = 0; spins < SPIN_CAP; spins++) {
          bool ok = true;
#pragma unroll
          for (int k = 0; k < 4; k++) ok &= __hip_atomic_load(flags + lane + 64 * k, __ATOMIC_RELAXED, __HIP_MEMORY_SCOPE_AGENT) == fv;
          if (__all(ok)) break;
          __builtin_amdgcn_s_sleep(8);
        }
        if (lane == 0) __hip_atomic_store((GAS unsigned*)ws, tag, __ATOMIC_RELAXED, __HIP_MEMORY_SCOPE_AGENT);
      }
      FINISH();
      return;
    }
    if (kq == 0) {
      auto go = [&](int ph) { if (ph) { watch(w == 0 ? 0 : 2, w == 0 ? 14 : 5, fv); await<GSL, 1>(14); } else if (w == 0) watch(1, 15, fv); };
      s2_compute<20>(w, kq, G0, rot, false, ws, go, pub PR_PASS);
    } else {
      auto go = [&](int ph) { if (ph) await<GSL, 1>(15); };
      s2_compute<18>(w, kq, G0, rot, late, ws, go, pub PR_PASS);
    }
    if (w == 0 || w > FW) { STAMP(11); FINISH(); return; }
    fu = w - 1;
  }
  {
    const int lane = lane_id();
    const u32x4 bv = *(const GAS u32x4*)(b2 + n_ * 128 + (lane & 7) * 16);
    int foff[2], pr0[2];
    u32x4 xv[2];
    f32x2 gt[2][4];
#pragma unroll
    for (int hh = 0; hh < 2; hh++) {
      const int frow = 48 * hh + 8 * fu + (lane >> 3);
      foff[hh] = fin_off(G0, n_, 48 * hh + 8 * fu, lane);
      xv[hh] = *(const GAS u32x4*)((const char*)x + (foff[hh] < S_ * D_ * 2 ? foff[hh] : 0));
      pr0[hh] = (frow >> 4) * 16384 + (frow & 15) * 256 + (((2 * (lane & 7)) ^ (frow & 7)) << 4);
      pin(pr0[hh]);
    }
#pragma unroll
    for (int hh = 0; hh < 2; hh++) {
      await<FSL, 3>(12 + hh);
      f32x4 y[2];
#pragma unroll
      for (int e = 0; e < 2; e++) {
        lptr pr = shm + (pr0[hh] ^ (16 * e));
        y[e] = *(LAS f32x4*)(pr + 4096) + *(LAS f32x4*)(pr + 8192) + *(LAS f32x4*)(pr + 12288);
      }
#pragma unroll
      for (int k = 0; k < 4; k++) {
        f32x2 yy = f32x2{y[k >> 1][2 * (k & 1)], y[k >> 1][2 * (k & 1) + 1]} + f32x2{blo(bv[k]), bhi(bv[k])};
        f32x2 t = yy * -1.4426950408889634f;
        f32x2 e = f32x2{__builtin_amdgcn_exp2f(t[0]), __builtin_amdgcn_exp2f(t[1])} + 1.f;
        gt[hh][k] = yy * f32x2{__builtin_amdgcn_rcpf(e[0]), __builtin_amdgcn_rcpf(e[1])};
        pin(gt[hh][k]);
      }
    }
    STAMP(10);
#pragma unroll
    for (int hh = 0; hh < 2; hh++) {
      await<FSL2, 1>(28 + hh);
      u32x4 o;
#pragma unroll
      for (int e = 0; e < 2; e++) {
        const f32x4 s = *(LAS f32x4*)(shm + (pr0[hh] ^ (16 * e)));
        o[2 * e] = pk2(f32x2{s[0], s[1]} * gt[hh][2 * e] + f32x2{blo(xv[hh][2 * e]), bhi(xv[hh][2 * e])});
        o[2 * e + 1] = pk2(f32x2{s[2], s[3]} * gt[hh][2 * e + 1] + f32x2{blo(xv[hh][2 * e + 1]), bhi(xv[hh][2 * e + 1])});
      }
      if (foff[hh] < S_ * D_ * 2) __builtin_nontemporal_store(o, (GAS u32x4*)(out + foff[hh]));
    }
  }
  STAMP(11);
  FINISH();
}

extern "C" void kernel_launch(void* const* d_in, const int* in_sizes, int n_in, void* d_out, int out_size, void* d_ws,
                              size_t ws_size, hipStream_t stream) {
  (void)hipFuncSetAttribute((const void*)fused_block, hipFuncAttributeMaxDynamicSharedMemorySize, LDS_TOTAL);
  fused_block<<<256, 768, LDS_TOTAL, stream>>>(d_in[0], d_in[1], d_in[2], d_in[4], (const unsigned short*)d_in[5], d_in[6],
                                               (const char*)d_in[7], (char*)d_out, (char*)d_ws + WSOFF);
}
```

```cpp
#define PROBE 1
#define PROBE_SEL w<8
#include <hip/hip_runtime.h>
#include <hip/hip_bf16.h>
#include <type_traits>
#include <utility>

typedef __attribute__((ext_vector_type(4))) short s16x4;
typedef __attribute__((ext_vector_type(8))) __bf16 bf16x8;
typedef __attribute__((ext_vector_type(2))) __bf16 bf16x2;
typedef __attribute__((ext_vector_type(4))) float f32x4;
typedef __attribute__((ext_vector_type(2))) float f32x2;
typedef __attribute__((ext_vector_type(4))) unsigned u32x4;
typedef __attribute__((ext_vector_type(2))) unsigned u32x2;
#define LAS __attribute__((address_space(3)))
#define GAS __attribute__((address_space(1)))
#define DEVI __device__ __forceinline__
typedef LAS char* lptr;
typedef unsigned long long u64;
#define IC(n) std::integral_constant<int, n>{}

constexpr int S_ = 2048, D_ = 640, F_ = 1728;
constexpr int KS_T = 74;
constexpr int WSLAB = 15360;
constexpr int XOFF = 2 * WSLAB;
constexpr int SLOT = 49152;
constexpr int LDS_TB = 86016;
constexpr int LDS_ST = 155648;
constexpr int LDS_CS = LDS_ST + 1152;
constexpr int LDS_B1 = LDS_CS + 1024;
constexpr int LDS_CNT = LDS_B1 + 1024;
constexpr int LDS_ZERO = LDS_CNT + 24;
constexpr int LDS_CONS = LDS_CNT + 64;
constexpr int LDS_PROD = LDS_CONS + 32;
constexpr int LDS_TAB = LDS_CNT + 128;
constexpr int LDS_TOTAL = 163840;
constexpr int WS_FLAG = 4096, WS_STAT = 8192, WS_BOX = 65536, WS_A2 = 1 << 20;
constexpr int A2_BYTES = 128 * KS_T * 1024;
#define PF_CODE 1
#define PF_MALL 0
#define MAP2 0
#define S1MODE 0
#define S2MODE 0
#define THR 32
#define PSL 0
#define PF_TLB 0
#define INLOOP 0
#define PFS 128
#define PFK0 2
#define PFALL 0
#define PFW2 0
#define KHF (idx & 1)
#define KTX (((idx >> 1) ^ sub) & 1)
#define KT0 0
#define THR2 32
#define SPS 0
#define LPRIO 0
#define SPRIO 0
#define ADY 3
#define ADS 4
#define ASL 3
#define GSL 0
#define FSL 1
#define OUTWT 2
#define FSL2 0
#define STW 0
#define WSOFF 0
#define LP2 0
#define WARM2 0
#define WARM3 WARM2
#define LP1 0
#define IDLE_ATT 1
#define FW 2
#define LATE_ALL 3
#define BOXW 1
#define NOEARLY 0
#define ABL 0
#define ROT1 0
#define ROT2 ((mt + nl) & 7)
constexpr int CODE_BYTES = 38400;
constexpr unsigned SPIN_CAP = 1u << 20;

#define STAMP(idx) do{ if((idx)==0) pr_t0=__builtin_amdgcn_s_memrealtime(); if((idx)==PROBE && (PROBE_SEL)) pr_tau=__builtin_amdgcn_s_memrealtime()-pr_t0; }while(0)
#define FINISH() do{ while(__builtin_amdgcn_s_memrealtime()-pr_t0 < 6000+10*pr_tau) __builtin_amdgcn_s_sleep(8); }while(0)
#define PR_ARGS , u64& pr_t0, u64& pr_tau
#define PR_PASS , pr_t0, pr_tau
#define STAMPC(k)
#define STAMPA(k) STAMP(k)
#define STAMPB(k)

DEVI void bar_lgkm() { asm volatile("s_waitcnt lgkmcnt(0)\n\ts_barrier" ::: "memory"); }
DEVI void bar() { asm volatile("s_barrier" ::: "memory"); }
template <int N> DEVI void bar_vm() { asm volatile("s_waitcnt vmcnt(%0)\n\ts_barrier" ::"i"(N) : "memory"); }
template <int N> DEVI void wait_vm() { asm volatile("s_waitcnt vmcnt(%0)" ::"i"(N) : "memory"); }
DEVI unsigned pk2(float a, float b) { f32x2 v = {a, b}; return __builtin_bit_cast(unsigned, __builtin_convertvector(v, bf16x2)); }
DEVI unsigned pk2(f32x2 v) { return __builtin_bit_cast(unsigned, __builtin_convertvector(v, bf16x2)); }
DEVI float asf(unsigned u) { return __builtin_bit_cast(float, u); }
DEVI unsigned asu(float f) { return __builtin_bit_cast(unsigned, f); }
DEVI float blo(unsigned u) { return __builtin_bit_cast(float, u << 16); }
DEVI float bhi(unsigned u) { return __builtin_bit_cast(float, u & 0xffff0000u); }
DEVI u32x2 tr_read(lptr p) { return __builtin_bit_cast(u32x2, __builtin_amdgcn_ds_read_tr16_b64_v4i16((LAS s16x4*)p)); }
DEVI f32x4 mfma(u32x4 a, u32x4 b, f32x4 c) {
  return __builtin_amdgcn_mfma_f32_16x16x32_bf16(__builtin_bit_cast(bf16x8, a), __builtin_bit_cast(bf16x8, b), c, 0, 0, 0);
}
DEVI u32x4 cat(u32x2 a, u32x2 b) { return __builtin_shufflevector(a, b, 0, 1, 2, 3); }
DEVI void mfma_a(f32x4& c, u32x4 a, u32x4 b) { asm volatile("v_mfma_f32_16x16x32_bf16 %0, %1, %2, %0" : "+v"(c) : "v"(a), "v"(b)); }
template <int OFF> DEVI void ds128(u32x4& d, int addr) { asm volatile("ds_read_b128 %0, %1 offset:%2" : "=v"(d) : "v"(addr), "i"(OFF)); }
template <int OFF> DEVI void dstr(u32x2& d, int addr) { asm volatile("ds_read_b64_tr_b16 %0, %1 offset:%2" : "=v"(d) : "v"(addr), "i"(OFF)); }
template <int OFF> DEVI void dsw128(int addr, u32x4 d) { asm volatile("ds_write_b128 %0, %1 offset:%2" ::"v"(addr), "v"(d), "i"(OFF) : "memory"); }
DEVI u32x4 mkr(const void* p, unsigned bytes) {
  u64 a = (u64)p;
  return u32x4{(unsigned)a, (unsigned)(a >> 32) & 0xffffu, bytes, 0x00020000u};
}
DEVI void st_wt(u32x4 r, int voff, int soff, u32x4 v) {
  asm volatile("buffer_store_dwordx4 %0, %1, %2, %3 offen sc1\n\ts_nop 1" ::"v"(v), "v"(voff), "s"(r), "s"(soff) : "memory");
}
template <int LSTEP>
DEVI void dma4(u32x4 r, u64 mask, int l, int va, int vb, int s0, int s1, int s2, int s3) {
  asm volatile(
      "s_mov_b64 exec, %0\n\t"
      "s_mov_b32 m0, %1\n\ts_nop 0\n\tbuffer_load_dwordx4 %2, %4, %5 offen lds\n\t"
      "s_add_u32 m0, %1, %9\n\ts_nop 0\n\tbuffer_load_dwordx4 %3, %4, %6 offen lds\n\t"
      "s_add_u32 m0, %1, %10\n\ts_nop 0\n\tbuffer_load_dwordx4 %2, %4, %7 offen lds\n\t"
      "s_add_u32 m0, %1, %11\n\ts_nop 0\n\tbuffer_load_dwordx4 %3, %4, %8 offen lds\n\t"
      "s_mov_b64 exec, -1"
      :: "s"(mask), "s"(l), "v"(va), "v"(vb), "s"(r), "s"(s0), "s"(s1), "s"(s2), "s"(s3), "i"(LSTEP), "i"(2 * LSTEP), "i"(3 * LSTEP)
      : "memory", "scc");
}
DEVI void dma1(u32x4 r, u64 mask, int l, int v, int s0) {
  asm volatile("s_mov_b64 exec, %0\n\ts_mov_b32 m0, %1\n\ts_nop 0\n\tbuffer_load_dwordx4 %2, %3, %4 offen lds\n\ts_mov_b64 exec, -1"
               :: "s"(mask), "s"(l), "v"(v), "s"(r), "s"(s0) : "memory");
}
DEVI void setup_loads(u32x2 (&xr)[5], unsigned& ep, unsigned& bv, u32x4 rx, int vx, u32x4 re, u32x4 rb, int vb) {
  asm volatile("buffer_load_dwordx2 %0, %7, %8, 0 offen\n\tbuffer_load_dwordx2 %1, %7, %8, 0 offen offset:256\n\tbuffer_load_dwordx2 %2, %7, %8, 0 offen offset:512\n\t"
               "buffer_load_dwordx2 %3, %7, %8, 0 offen offset:768\n\tbuffer_load_dwordx2 %4, %7, %8, 0 offen offset:1024\n\t"
               "buffer_load_dword %5, off, %9, 0 sc1\n\tbuffer_load_ushort %6, %11, %10, 0 offen"
               : "=&v"(xr[0]), "=&v"(xr[1]), "=&v"(xr[2]), "=&v"(xr[3]), "=&v"(xr[4]), "=&v"(ep), "=&v"(bv)
               : "v"(vx), "s"(rx), "s"(re), "s"(rb), "v"(vb) : "memory");
}
template <int WHEN>
DEVI void bload4_if(u32x4 (&d)[4], u32x4 r, int v, int s0, int s1, int s2, int s3, int flag) {
#define LDR_BODY "s_cmp_lg_u32 %10, %11\n\ts_cbranch_scc1 .Lr%=\n\tbuffer_load_dwordx4 %0, %4, %5, %6 offen\n\tbuffer_load_dwordx4 %1, %4, %5, %7 offen\n\t" \
                 "buffer_load_dwordx4 %2, %4, %5, %8 offen\n\tbuffer_load_dwordx4 %3, %4, %5, %9 offen\n.Lr%=:"
  if (WHEN) asm volatile(LDR_BODY : "+v"(d[0]), "+v"(d[1]), "+v"(d[2]), "+v"(d[3]) : "v"(v), "s"(r), "s"(s0), "s"(s1), "s"(s2), "s"(s3), "s"(flag), "i"(WHEN) : "scc");
  else asm volatile(LDR_BODY : "=&v"(d[0]), "=&v"(d[1]), "=&v"(d[2]), "=&v"(d[3]) : "v"(v), "s"(r), "s"(s0), "s"(s1), "s"(s2), "s"(s3), "s"(flag), "i"(WHEN) : "scc");
}
template <int N0, int N1>
DEVI void bar_vm_sel(int flag) {
  asm volatile("s_cmp_lg_u32 %0, 0\n\ts_cbranch_scc1 .La%=\n\ts_waitcnt vmcnt(%1)\n\ts_branch .Lb%=\n.La%=:\n\ts_waitcnt vmcnt(%2)\n.Lb%=:\n\ts_barrier"
               ::"s"(flag), "i"(N0), "i"(N1) : "memory", "scc");
}
template <bool WHEN>
DEVI void bload3_if(u32x4 (&d)[3], u32x4 r, int v, int s0, int s1, int s2, int flag) {
#define L3_BODY(BR) "s_cmp_lg_u32 %8, 0\n\t" BR " .Lc%=\n\tbuffer_load_dwordx4 %0, %3, %4, %5 offen sc1\n\tbuffer_load_dwordx4 %1, %3, %4, %6 offen sc1\n\t" \
                    "buffer_load_dwordx4 %2, %3, %4, %7 offen sc1\n.Lc%=:"
  if (WHEN) asm volatile(L3_BODY("s_cbranch_scc0") : "=&v"(d[0]), "=&v"(d[1]), "=&v"(d[2]) : "v"(v), "s"(r), "s"(s0), "s"(s1), "s"(s2), "s"(flag) : "memory", "scc");
  else asm volatile(L3_BODY("s_cbranch_scc1") : "+v"(d[0]), "+v"(d[1]), "+v"(d[2]) : "v"(v), "s"(r), "s"(s0), "s"(s1), "s"(s2), "s"(flag) : "memory", "scc");
}
template <class T> DEVI void pin(T& v) { asm volatile("" : "+v"(v)); }
DEVI void dsw_col0(int a0, f32x4 d0, int a1, f32x4 d1, int a2, f32x4 d2) {
  asm volatile("s_mov_b64 exec, %6\n\tds_write_b128 %0, %1\n\tds_write_b128 %2, %3\n\tds_write_b128 %4, %5\n\ts_mov_b64 exec, -1"
               ::"v"(a0), "v"(d0), "v"(a1), "v"(d1), "v"(a2), "v"(d2), "s"(0x0001000100010001ull) : "memory");
}
DEVI void reload_sc1(u32x4& d, u32x4 r, int v, int s0) { asm volatile("buffer_load_dwordx4 %0, %1, %2, %3 offen sc1" : "+v"(d) : "v"(v), "s"(r), "s"(s0) : "memory"); }
template <int OFF> DEVI void redstr(u32x2& d, int addr) { asm volatile("ds_read_b64_tr_b16 %0, %1 offset:%2" : "+v"(d) : "v"(addr), "i"(OFF)); }
DEVI void vinc(int& d, int s) { asm volatile("v_add_u32 %0, %1, %0" : "+v"(d) : "s"(s)); }
DEVI void vadd(int& d, int a, int s) { asm volatile("v_add_u32 %0, %2, %1" : "=v"(d) : "v"(a), "s"(s)); }
template <class F, int... I> DEVI void sfor_(F f, std::integer_sequence<int, I...>) { (f(std::integral_constant<int, I>{}), ...); }
template <int N, class F> DEVI void sfor(F f) { sfor_(f, std::make_integer_sequence<int, N>{}); }
template <int OFF> DEVI void stouch1(unsigned& sink, u32x4 r, int off) { asm volatile("s_buffer_load_dword %0, %1, %2 offset:%3" : "+s"(sink) : "s"(r), "s"(off), "i"(OFF)); }
template <int N>
DEVI void stouch(unsigned& sink, u32x4 r, int off) {
  sfor<N>([&](auto K) { stouch1<decltype(K)::value * 128>(sink, r, off); });
}
DEVI void touchg(const void* p) {
  asm volatile("s_mov_b32 m0, %1\n\ts_nop 0\n\tglobal_load_lds_dword %0, off" ::"v"(p), "i"(LDS_TOTAL - 256) : "memory");
}
DEVI void touch_sc1(u32x4 r, int v) {
  asm volatile("s_mov_b32 m0, %2\n\ts_nop 0\n\tbuffer_load_dword %0, %1, 0 offen sc1 lds" ::"v"(v), "s"(r), "i"(LDS_TOTAL - 256) : "memory");
}
DEVI void touch(u32x4 r, int v) {
  asm volatile("s_mov_b32 m0, %2\n\ts_nop 0\n\tbuffer_load_dword %0, %1, 0 offen lds" ::"v"(v), "s"(r), "i"(LDS_TOTAL - 256) : "memory");
}
DEVI void bload(u32x4& d, u32x4 r, int v, int s0) { asm volatile("buffer_load_dwordx4 %0, %1, %2, %3 offen" : "=v"(d) : "v"(v), "s"(r), "s"(s0)); }
DEVI void bload_sc1(u32x4& d, u32x4 r, int v, int s0) { if (ABL & 4) { bload(d, r, v, s0); return; } asm volatile("buffer_load_dwordx4 %0, %1, %2, %3 offen sc1" : "=v"(d) : "v"(v), "s"(r), "s"(s0) : "memory"); }
DEVI void arrive(int k) {
  asm volatile("s_mov_b64 exec, 1\n\tds_add_u32 %0, %1\n\ts_mov_b64 exec, -1" ::"v"(LDS_CNT + 4 * k), "v"(1) : "memory");
}
template <int SLEEP, unsigned N>
DEVI void await(int k) {
  unsigned v;
  for (;;) {
    asm volatile("ds_read_b32 %0, %1\n\ts_waitcnt lgkmcnt(0)" : "=v"(v) : "v"(LDS_CNT + 4 * k) : "memory");
    if (__builtin_amdgcn_readfirstlane(v) >= N) break;
    if (SLEEP) __builtin_amdgcn_s_sleep(SLEEP);
  }
}
DEVI void post(int addr, int val) { asm volatile("ds_write_b32 %0, %1" ::"v"(addr), "v"(val) : "memory"); }
DEVI void peek(unsigned& v, int addr) { asm volatile("ds_read_b32 %0, %1" : "=v"(v) : "v"(addr) : "memory"); }
DEVI void require(unsigned v, int addr, unsigned n) {
  unsigned t;
  asm volatile("v_readfirstlane_b32 %1, %0\n\ts_cmp_ge_u32 %1, %3\n\ts_cbranch_scc1 .Lq%=\n"
               ".Lw%=:\n\tds_read_b32 %0, %2\n\ts_waitcnt lgkmcnt(0)\n\tv_readfirstlane_b32 %1, %0\n\ts_cmp_ge_u32 %1, %3\n\ts_cbranch_scc0 .Lw%=\n.Lq%=:"
               : "+&v"(v), "=&s"(t) : "v"(addr), "s"(n) : "memory", "scc");
}
DEVI void await_pair(int addr, unsigned n) {
  for (;;) {
    u32x2 v;
    asm volatile("ds_read_b64 %0, %1\n\ts_waitcnt lgkmcnt(0)" : "=v"(v) : "v"(addr) : "memory");
    const unsigned a = v[0], b = v[1];
    if (__builtin_amdgcn_readfirstlane(a < b ? a : b) >= n) break;
    __builtin_amdgcn_s_sleep(1);
  }
}
DEVI int lane_id() { int l; asm volatile("v_mbcnt_lo_u32_b32 %0, -1, 0\n\tv_mbcnt_hi_u32_b32 %0, -1, %0" : "=v"(l)); return l; }
DEVI int sig(int i) { return 4 * (i & 3) + (i >> 2); }
DEVI u64 flagval(unsigned tag) { return ((u64)(tag ^ 0x5bd1e995u) << 32) | tag; }

DEVI float xmax16(float v) { auto r = __builtin_amdgcn_permlane16_swap(asu(v), asu(v), false, false); return fmaxf(asf(r[0]), asf(r[1])); }
DEVI float xmax32(float v) { auto r = __builtin_amdgcn_permlane32_swap(asu(v), asu(v), false, false); return fmaxf(asf(r[0]), asf(r[1])); }
DEVI float xadd16(float v) { auto r = __builtin_amdgcn_permlane16_swap(asu(v), asu(v), false, false); return asf(r[0]) + asf(r[1]); }
DEVI float xadd32(float v) { auto r = __builtin_amdgcn_permlane32_swap(asu(v), asu(v), false, false); return asf(r[0]) + asf(r[1]); }
template <int CTRL> DEVI float dpp(float v) { return asf(__builtin_amdgcn_mov_dpp(asu(v), CTRL, 0xf, 0xf, true)); }
DEVI float half_sum(float v) {
  v += dpp<0xb1>(v); v += dpp<0x4e>(v); v += dpp<0x141>(v); v += dpp<0x140>(v);
  return xadd16(v);
}

DEVI int h_slot(int j, int l) { const int t = l < 4 ? 4 * j + l : 28 + 3 * j + l, d = (t * 43) >> 7; return 18 * (t - 3 * d) + d; }

DEVI int row_token(int p_, int pos) {
  const int tau = pos >> 4, i = pos & 15;
  int tok = tau == 0 ? p_ * 128 - 16 + 8 * (i & 1) + 2 * (i >> 2) + ((i & 3) < 2) : p_ * 128 + 32 * ((tau - 1) >> 1) + 2 * sig(i) + ((tau - 1) & 1);
  return tok < 0 ? 0 : tok;
}

struct ColGeo { int A, B, C, ct; };
DEVI ColGeo colgeo(bool att, int nw, int mw, int c, bool heavy) {
  const int cc = heavy ? (c ^ (2 * mw)) : c;
  const int ct = 4 * nw + cc, sec = (ct * 13) >> 6, hh = ct - 5 * sec;
  const bool q4 = hh == 4, sp = !heavy && c == 2;
  ColGeo r;
  r.A = att ? sec * 160 + (q4 ? 32 : 40 * hh) : heavy ? 128 * nw + 32 * cc : (sp ? 352 + 16 * nw : 128 + 64 * nw + 32 * c);
  r.B = att && q4 ? 40 : 8;
  r.C = att ? (q4 ? 80 : 16) : (sp ? 32 : 16);
  r.ct = ct;
  return r;
}

template <int NC>
struct Frags { u32x4 x[5]; u32x2 w[NC][2]; };

template <int N> DEVI void wait_lg() { asm volatile("s_waitcnt lgkmcnt(%0)" ::"i"(N) : "memory"); }

template <int NC, int K>
DEVI void s1_rd(Frags<NC>& n, int xo, int xh, const int (&wa)[NC]) {
  if constexpr (K == 0) ds128<0>(n.x[0], xo);
  else if constexpr (K <= 2 * NC) { if ((K - 1) & 1) dstr<7680>(n.w[(K - 1) >> 1][1], wa[(K - 1) >> 1]); else dstr<0>(n.w[(K - 1) >> 1][0], wa[(K - 1) >> 1]); }
  else if constexpr (K < 2 * NC + 4) ds128<(K - 2 * NC) * 2048>(n.x[K - 2 * NC], xo);
  else if constexpr (K == 2 * NC + 4) ds128<0>(n.x[4], xh);
}
template <int NC, int NH, int K>
DEVI void s1_mm(const Frags<NC>& c, f32x4 (&acc)[4][NC], f32x4 (&hacc)[NH]) {
  if constexpr (K < 4 * NC) mfma_a(acc[K / NC][K % NC], cat(c.w[K % NC][0], c.w[K % NC][1]), c.x[K / NC]);
  else mfma_a(hacc[K - 4 * NC], cat(c.w[K - 4 * NC][0], c.w[K - 4 * NC][1]), c.x[4]);
}
template <int NC, int NH, bool BAR>
DEVI void s1_step(const Frags<NC>& c, Frags<NC>& n, int xo, int xh, const int (&wa)[NC], f32x4 (&acc)[4][NC], f32x4 (&hacc)[NH]) {
  constexpr int NM = 4 * NC + NH, NR = 2 * NC + 5, D = BAR ? NM - NR : 0;
  sfor<NM>([&](auto KK) {
    constexpr int k = decltype(KK)::value;
    constexpr int need = k < NC ? 2 * k + 2 : (k % NC == 0 && k <= 4 * NC) ? 2 * NC + k / NC : -1;
    if constexpr (BAR && k == D) bar_lgkm();
    if constexpr (need >= 0 && (!BAR || k < D)) wait_lg<NR - 1 - need + (BAR ? 0 : (k < NR ? k : NR))>();
    s1_mm<NC, NH, k>(c, acc, hacc);
    if constexpr (k >= D) s1_rd<NC, k - D>(n, xo, xh, wa);
  });
}

template <int OFF> DEVI void dsw64(int addr, u32x2 d) { asm volatile("ds_write_b64 %0, %1 offset:%2" ::"v"(addr), "v"(d), "i"(OFF) : "memory"); }
template <int OFF> DEVI void ds64(u32x2& d, int addr) { asm volatile("ds_read_b64 %0, %1 offset:%2" : "=v"(d) : "v"(addr), "i"(OFF)); }

template <int NC>
DEVI void s1_compute(lptr shm, bool att, int w, int lane, int p_, int j_, int hf, char* ws PR_ARGS) {
  constexpr int NH = NC == 4 ? 2 : 3;
  const int mw = w & 1, nw = w >> 1, i = lane & 15, g = lane >> 4;
  f32x4 acc[4][NC], hacc[NH];
#pragma unroll
  for (int m = 0; m < 4; m++)
#pragma unroll
    for (int c = 0; c < NC; c++) { acc[m][c] = f32x4{0, 0, 0, 0}; asm volatile("" : "+v"(acc[m][c])); }
#pragma unroll
  for (int c = 0; c < NH; c++) { hacc[c] = f32x4{0, 0, 0, 0}; asm volatile("" : "+v"(hacc[c])); }
  int wb[NC], cb[NC], ct[NC];
#pragma unroll
  for (int c = 0; c < NC; c++) {
    const ColGeo cg = colgeo(att, nw, mw, c, NC == 4);
    wb[c] = (lane >> 2) * 480 + cg.A + cg.B * (lane & 1) + cg.C * ((lane >> 1) & 1);
    cb[c] = LDS_CS + 2 * (cg.A + cg.B * (g & 1) + cg.C * (g >> 1));
    ct[c] = cg.ct;
    asm volatile("" : "+v"(cb[c]), "+v"(wb[c]));
  }
  int xb = XOFF + i * 128 + ((g ^ (i >> 1) ^ (4 * hf)) << 4);
  int sta = LDS_ST + i * 8;
  asm volatile("" : "+v"(xb), "+v"(sta));
  const int own = (1 + 4 * mw) * 2048;
  Frags<NC> fa, fb;
  int wa[NC];
  STAMPB(12);
  bar_lgkm();
  STAMP(1);
  sfor<2 * NC + 5>([&](auto K) { s1_rd<NC, decltype(K)::value>(fa, xb + own, xb, wb); });
#pragma unroll
  for (int c = 0; c < NC; c++) wa[c] = wb[c] + WSLAB;
  s1_step<NC, NH, true>(fa, fb, (xb ^ 64) + own, xb ^ 64, wa, acc, hacc);
  int sl = 0;
#pragma nounroll
  for (int T = 1; T < 10; T++) {
    sl = sl == 2 * SLOT ? 0 : sl + SLOT;
#pragma unroll
    for (int c = 0; c < NC; c++) wa[c] = wb[c] + sl;
    s1_step<NC, NH, true>(fb, fa, xb + sl + own, xb + sl, wa, acc, hacc);
#pragma unroll
    for (int c = 0; c < NC; c++) wa[c] = wb[c] + sl + WSLAB;
    s1_step<NC, NH, false>(fa, fb, (xb ^ 64) + sl + own, (xb ^ 64) + sl, wa, acc, hacc);
  }
  wait_lg<0>();
  arrive(2);
  sfor<NH>([&](auto K) { s1_mm<NC, NH, 4 * NC + decltype(K)::value>(fb, acc, hacc); });
  u32x2 stu[5];
  u32x4 b1u[NC], csu[NC];
  sfor<4 * NC>([&](auto KK) {
    constexpr int k = decltype(KK)::value;
    s1_mm<NC, NH, k>(fb, acc, hacc);
    if constexpr (k < 4) ds64<(1 + k) * 128>(stu[k], sta + mw * 512);
    if constexpr (k == 4) ds64<0>(stu[4], sta);
    if constexpr (k == 5) {
      dsw_col0(cb[0], hacc[0], cb[1], hacc[1], cb[NH - 1], hacc[NH - 1]);
      if (NC == 4) arrive(8 + nw);
    }
    if constexpr (k >= 6 && k < 6 + NC) ds128<LDS_B1 - LDS_CS>(b1u[k - 6], cb[k - 6]);
  });
  STAMP(2);
  if (NC == 4) await<0, 2>(8 + nw);
#pragma unroll
  for (int c = 0; c < NC; c++) ds128<0>(csu[c], cb[c]);
  asm volatile("s_waitcnt lgkmcnt(0)\n\ts_nop 7" ::: "memory");
  f32x2 st[5];
  f32x4 cs[NC], b1[NC];
#pragma unroll
  for (int m = 0; m < 5; m++) { asm volatile("" : "+v"(stu[m])); st[m] = __builtin_bit_cast(f32x2, stu[m]); }
#pragma unroll
  for (int c = 0; c < NC; c++) { asm volatile("" : "+v"(csu[c]), "+v"(b1u[c])); cs[c] = __builtin_bit_cast(f32x4, csu[c]); b1[c] = __builtin_bit_cast(f32x4, b1u[c]); }
#pragma unroll
  for (int m = 0; m < 4; m++)
#pragma unroll
    for (int c = 0; c < NC; c++) pin(acc[m][c]);
#pragma unroll
  for (int c = 0; c < NH; c++) pin(hacc[c]);
  STAMPA(12);
#define SB __builtin_amdgcn_sched_barrier(0)
  const int l2 = lane_id();
  if (att) {
    const int g2 = l2 >> 4, piece = LDS_TB + ((l2 & 48) + ((l2 & 15) ^ (8 * (g2 >> 1)))) * 8;
    sfor<NC>([&](auto C) {
      constexpr int c = decltype(C)::value;
      const int ah = piece + ct[c] * 4608, ao = ah + (1 + 4 * mw) * 512;
      u32x2 o[5];
      SB;
#pragma unroll
      for (int m = 0; m < (c < NH ? 5 : 4); m++) {
        f32x4 a = m < 4 ? acc[m < 4 ? m : 0][c] : hacc[c < NH ? c : 0];
        f32x4 v = a * st[m][0] + cs[c] * st[m][1];
        o[m] = u32x2{pk2(v[0], v[1]), pk2(v[2], v[3])};
      }
      if (ABL & 4096) { pin(o[0]); pin(o[1]); pin(o[2]); pin(o[3]); if (c < NH) pin(o[4]); }
      else if ((ABL & 8192) && (c & 1)) { pin(o[0]); pin(o[1]); pin(o[2]); pin(o[3]); if (c < NH) pin(o[4]); }
      else {
      dsw64<0>(ao, o[0]); dsw64<512>(ao, o[1]); dsw64<1024>(ao, o[2]); dsw64<1536>(ao, o[3]);
      if (c < NH) dsw64<0>(ah, o[4]);
      }
    });
  } else {
    const u32x4 ra = mkr(ws + WS_A2, (ABL & 8) ? 0 : A2_BYTES);
    const int base = (8 * p_ + 4 * mw) * KS_T * 1024;
    const int o0 = base + (NC == 4 || j_ < 7 || nw == 2 ? h_slot(j_, NC == 4 ? 2 * nw + mw : 2 + nw) * 1024 : 0x70000000);
    const int o1 = base + (NC == 4 ? h_slot(j_, 2 * nw + (mw ^ 1)) * 1024 : j_ < 7 ? h_slot(j_, 6) * 1024 + (nw - 2) * 512 : 0x70000000);
    const float c1 = -2.f * 0.7978845608028654f * 1.4426950408889634f, c2 = c1 * 0.044715f;
    unsigned sp[2];
#pragma unroll
    for (int m = 0; m < 4; m++) {
      f32x4 v[NC], t[NC];
      SB;
#pragma unroll
      for (int c = 0; c < NC; c++) t[c] = cs[c] * st[m][1] + b1[c];
      SB;
#pragma unroll
      for (int c = 0; c < NC; c++) v[c] = acc[m][c] * st[m][0] + t[c];
      SB;
#pragma unroll
      for (int c = 0; c < NC; c++) t[c] = v[c] * v[c];
      SB;
#pragma unroll
      for (int c = 0; c < NC; c++) t[c] = t[c] * c2 + c1;
      SB;
#pragma unroll
      for (int c = 0; c < NC; c++) t[c] = t[c] * v[c];
      SB;
#pragma unroll
      for (int c = 0; c < NC; c++)
#pragma unroll
        for (int e = 0; e < 4; e++) t[c][e] = __builtin_amdgcn_exp2f(t[c][e]);
      SB;
#pragma unroll
      for (int c = 0; c < NC; c++) t[c] = t[c] + 1.f;
      SB;
#pragma unroll
      for (int c = 0; c < NC; c++)
#pragma unroll
        for (int e = 0; e < 4; e++) t[c][e] = __builtin_amdgcn_rcpf(t[c][e]);
      SB;
#pragma unroll
      for (int c = 0; c < NC; c++) v[c] = v[c] * t[c];
      SB;
      unsigned o[NC][2];
#pragma unroll
      for (int c = 0; c < NC; c++) { o[c][0] = pk2(v[c][0], v[c][1]); o[c][1] = pk2(v[c][2], v[c][3]); }
      st_wt(ra, l2 * 16, o0 + m * KS_T * 1024, u32x4{o[0][0], o[0][1], o[1][0], o[1][1]});
      if (NC == 4) st_wt(ra, l2 * 16, o1 + m * KS_T * 1024, u32x4{o[2][0], o[2][1], o[NC - 1][0], o[NC - 1][1]});
      else if (m & 1) {
        auto s0 = __builtin_amdgcn_permlane32_swap(sp[0], o[2][0], false, false);
        auto s1 = __builtin_amdgcn_permlane32_swap(sp[1], o[2][1], false, false);
        st_wt(ra, (l2 >> 5) * (KS_T * 1024) + (l2 & 31) * 16, o1 + (m - 1) * KS_T * 1024, u32x4{s0[0], s1[0], s0[1], s1[1]});
      } else { sp[0] = o[2][0]; sp[1] = o[2][1]; }
    }
  }
}

template <bool MAX> DEVI float red4(const float (&v)[4]) {
  auto op = [](unsigned a, unsigned b) { float r; if (MAX) asm("v_max_f32 %0, %1, %2" : "=v"(r) : "v"(a), "v"(b)); else r = asf(a) + asf(b); return r; };
  auto a = __builtin_amdgcn_permlane32_swap(asu(v[0]), asu(v[1]), false, false);
  auto b = __builtin_amdgcn_permlane32_swap(asu(v[2]), asu(v[3]), false, false);
  auto c = __builtin_amdgcn_permlane16_swap(asu(op(a[0], a[1])), asu(op(b[0], b[1])), false, false);
  return op(c[0], c[1]);
}
DEVI void bcast4(float r, float (&v)[4]) {
  auto e = __builtin_amdgcn_permlane16_swap(asu(r), asu(r), false, false);
  unsigned e0 = e[0], e1 = e[1];
  auto a = __builtin_amdgcn_permlane32_swap(e0, e0, false, false);
  auto b = __builtin_amdgcn_permlane32_swap(e1, e1, false, false);
  v[0] = asf(a[0]); v[1] = asf(a[1]); v[2] = asf(b[0]); v[3] = asf(b[1]);
}

DEVI void att_table(lptr shm, int lane) {
  const int i = lane & 15, g = lane >> 4, p = lane & 3;
  const int d = sig(i) - g;
  const unsigned NEG = 0xf149f2cau;
  bool vo[4], vp[4];
#pragma unroll
  for (int r = 0; r < 4; r++) { vo[r] = (unsigned)(d - 4 * r) <= 7u; vp[r] = d + 16 - 4 * r <= 7; }
  auto pm = [](bool lo, bool hi) { return (lo ? 0xffffu : 0u) | (hi ? 0xffff0000u : 0u); };
  LAS u32x4* t = (LAS u32x4*)(shm + LDS_TAB) + lane;
  t[0] = u32x4{vo[0] ? 0u : NEG, vo[1] ? 0u : NEG, vo[2] ? 0u : NEG, vo[3] ? 0u : NEG};
  t[64] = u32x4{pm(vp[2], vp[3]), pm(vo[0], vo[1]), vp[2] ? 0u : NEG, vp[3] ? 0u : NEG};
  t[128] = u32x4{pm(vo[2], vo[3]), (unsigned)(LDS_TB + (g * 16 + (i ^ (8 * (g >> 1)))) * 8), (unsigned)(LDS_TB + (p * 16 + ((lane >> 2) ^ (8 * (p >> 1)))) * 8), 0u};
}
DEVI int sel(u64 mask, int a, int b) { int r; asm("v_cndmask_b32_e64 %0, %2, %1, %3" : "=v"(r) : "v"(a), "v"(b), "s"(mask)); return r; }

DEVI void s1_attention(lptr shm, int w, int p_, int j_, char* ws PR_ARGS) {
  arrive(3);
  const int lane = lane_id();
  const int tau = (w + 1) * 512, taup = w >= 2 ? tau - 1024 : 0, flip = w == 1 ? 16 : 0;
  const LAS u32x4* t = (const LAS u32x4*)(shm + LDS_TAB) + lane;
  const u32x4 t0 = t[0], t2 = t[128];
  u32x4 t1 = t[64];
  if (p_ == 0 && w < 2) { t1[0] = 0u; t1[2] = 0xf149f2cau; t1[3] = 0xf149f2cau; }
  const f32x4 mO = __builtin_bit_cast(f32x4, t0), mP = __builtin_bit_cast(f32x4, t1);
  const unsigned M1 = t1[0], M2 = t1[1], M3 = t2[0];
  const int po = t2[1] + tau, pp = (t2[1] ^ flip) + taup, to = t2[2] + tau, tp = (t2[2] ^ flip) + taup;
  int aq[4], avo[4], avp[4];
#pragma unroll
  for (int h = 0; h < 4; h++) {
    aq[h] = sel(0xffffull << (16 * h), po, LDS_ZERO - 4 * 4608);
    avo[h] = sel(0x1111111111111111ull << h, to, LDS_ZERO - 14 * 4608);
    avp[h] = sel(0x1111111111111111ull << h, tp, LDS_ZERO - 14 * 4608);
    asm volatile("" : "+v"(aq[h]), "+v"(avo[h]), "+v"(avp[h]));
  }
  STAMPA(14);
  await<SPS, 8>(3);
  STAMPA(15);
  u32x2 qa[4][2], ko[4][2], kp[4][2], va[4][2], vb[4][2];
  f32x4 sp[4], so[4];
  auto rd = [&](auto H) {
    constexpr int h = decltype(H)::value;
    ds64<(5 + h) * 4608>(ko[h][0], po); ds64<9 * 4608>(ko[h][1], po); ds64<h * 4608>(qa[h][0], po); ds64<4 * 4608>(qa[h][1], aq[h]);
    ds64<(5 + h) * 4608>(kp[h][0], pp); ds64<9 * 4608>(kp[h][1], pp);
  };
  auto rv = [&](auto H) {
    constexpr int h = decltype(H)::value;
    dstr<(10 + h) * 4608>(va[h][0], tp); dstr<(10 + h) * 4608>(va[h][1], to); dstr<14 * 4608>(vb[h][0], avp[h]); dstr<14 * 4608>(vb[h][1], avo[h]);
  };
  auto qk = [&](auto H, auto N) {
    constexpr int h = decltype(H)::value;
    asm volatile("s_waitcnt lgkmcnt(%6)" : "+v"(ko[h][0]), "+v"(ko[h][1]), "+v"(qa[h][0]), "+v"(qa[h][1]), "+v"(kp[h][0]), "+v"(kp[h][1]) : "i"(decltype(N)::value));
    so[h] = mfma(cat(ko[h][0], ko[h][1]), cat(qa[h][0], qa[h][1]), mO);
    sp[h] = mfma(cat(kp[h][0], kp[h][1]), cat(qa[h][0], qa[h][1]), mP);
  };
  rd(IC(0)); rd(IC(1));
  qk(IC(0), IC(6)); rd(IC(2));
  qk(IC(1), IC(6)); rd(IC(3));
  qk(IC(2), IC(6)); rv(IC(0)); rv(IC(1));
  qk(IC(3), IC(8)); rv(IC(2));
  asm volatile("s_waitcnt lgkmcnt(8)");
  rv(IC(3));
  const float qs = 0.22360679774997896f * 1.4426950408889634f;
  float ee[4], eo[4], mx[4], l[4];
#pragma unroll
  for (int h = 0; h < 4; h++) { ee[h] = fmaxf(fmaxf(sp[h][2], so[h][0]), so[h][2]); eo[h] = fmaxf(fmaxf(sp[h][3], so[h][1]), so[h][3]); mx[h] = fmaxf(ee[h], eo[h]); }
  bcast4(red4<true>(mx) * -qs, mx);
#pragma unroll
  for (int h = 0; h < 4; h++) {
    f32x2 t = f32x2{ee[h], eo[h]} * qs + mx[h];
    ee[h] = __builtin_amdgcn_exp2f(t[0]); eo[h] = __builtin_amdgcn_exp2f(t[1]); l[h] = ee[h] + eo[h];
  }
  bcast4(__builtin_amdgcn_rcpf(red4<false>(l)), l);
  asm volatile("s_waitcnt lgkmcnt(0)" : "+v"(va[0][0]), "+v"(va[0][1]), "+v"(va[1][0]), "+v"(va[1][1]), "+v"(va[2][0]), "+v"(va[2][1]), "+v"(va[3][0]), "+v"(va[3][1]),
               "+v"(vb[0][0]), "+v"(vb[0][1]), "+v"(vb[1][0]), "+v"(vb[1][1]), "+v"(vb[2][0]), "+v"(vb[2][1]), "+v"(vb[3][0]), "+v"(vb[3][1]));
  f32x4 o4 = {0, 0, 0, 0}, oh[4];
#pragma unroll
  for (int h = 0; h < 4; h++) {
    const unsigned pk = pk2(f32x2{ee[h], eo[h]} * l[h]);
    u32x4 pf = {0u, pk & M1, pk & M2, pk & M3};
    o4 = mfma(cat(vb[h][0], vb[h][1]), pf, o4);
    oh[h] = mfma(cat(va[h][0], va[h][1]), pf, f32x4{0, 0, 0, 0});
  }
  const u32x4 ra = mkr(ws + WS_A2, A2_BYTES);
  const int base = ((8 * p_ + w) * KS_T + 54 + 5 * (j_ >> 1)) * 1024;
#pragma unroll
  for (int hp = 0; hp < 2; hp++)
    st_wt(ra, lane * 16, base + ((j_ & 1) * 3 + hp) * 1024,
          u32x4{pk2(oh[2 * hp][0], oh[2 * hp][1]), pk2(oh[2 * hp][2], oh[2 * hp][3]), pk2(oh[2 * hp + 1][0], oh[2 * hp + 1][1]), pk2(oh[2 * hp + 1][2], oh[2 * hp + 1][3])});
  unsigned a0 = pk2(o4[0], o4[1]), a1 = pk2(o4[2], o4[3]);
  auto s0 = __builtin_amdgcn_permlane32_swap(a0, a0, false, false);
  auto s1 = __builtin_amdgcn_permlane32_swap(a1, a1, false, false);
  st_wt(ra, lane < 32 ? lane * 16 : 0x70000000, base + 2 * 1024 + (j_ & 1) * 512, u32x4{s0[0], s1[0], s0[1], s1[1]});
  arrive(4);
}

DEVI int s2_perm(int s, bool o, int rot) { return o ? s ^ (s < 16 ? rot : rot & 1) : s < 8 ? s ^ rot : s < 10 ? s ^ (rot & 1) : ((s - 10) ^ rot) + 10; }
DEVI int fin_off(int G0, int n_, int r0, int lane) {
  const int frow = r0 + (lane >> 3), fG = G0 + (frow >> 4);
  return (32 * (fG >> 1) + 2 * sig(frow & 15) + (fG & 1)) * 1280 + n_ * 128 + (lane & 7) * 16;
}
constexpr int s2_slot(int r) { return r < 18 ? (r % 9) * 16384 : 147456 + (r - 18) * 4096; }
template <int NS, class GO, class PUB>
DEVI void s2_compute(int w, int kq, int G0, int rot, bool late, char* ws, GO go, PUB pub PR_ARGS) {
  constexpr int AD = NS == 20 ? ADS : ADY;
  const int mh = w >> 2, lane = lane_id();
  if (ABL & 16) G0 = 0;
  const u32x4 ra = mkr(ws + WS_A2, (ABL & 2) ? 0 : A2_BYTES);
  int av = lane * 16;
  pin(av);
  int ao[3], bs[4];
#pragma unroll
  for (int m = 0; m < 3; m++) { int G = G0 + 3 * mh + m; G = G < 128 ? G : 127; ao[m] = (G * KS_T + (NS == 20 ? 54 : 18 * (kq - 1))) * 1024; }
#pragma unroll
  for (int c = 0; c < 4; c++) {
    bs[c] = kq * 4096 + (lane >> 2) * 128 + ((c ^ ((lane >> 3) & 3)) << 5) + 8 * (lane & 3);
  }
  f32x4 acc[3][4];
#pragma unroll
  for (int m = 0; m < 3; m++)
#pragma unroll
    for (int c = 0; c < 4; c++) { acc[m][c] = f32x4{0, 0, 0, 0}; pin(acc[m][c]); }
  u32x4 a[AD][3];
  u32x2 b[3][4][2];
  int ca = LDS_CONS + kq * 8 + mh * 4, pa = LDS_PROD + kq * 4;
  pin(ca); pin(pa);
  unsigned pv, lv;
  auto rdb = [&](auto R, auto K) {
    constexpr int r = decltype(R)::value, k = decltype(K)::value;
    constexpr int off = s2_slot(r), nxt = s2_slot(r + 1) / 65536 - off / 65536;
    if constexpr (r < 3) dstr<off % 65536 + (k & 1) * 2048>(b[r % 3][k >> 1][k & 1], bs[k >> 1]);
    else redstr<off % 65536 + (k & 1) * 2048>(b[r % 3][k >> 1][k & 1], bs[k >> 1]);
    if constexpr (nxt != 0 && (k & 1)) vinc(bs[k >> 1], nxt * 65536);
  };
  auto lda = [&](auto S, int m) {
    constexpr int s = decltype(S)::value;
    if constexpr (s < AD) bload_sc1(a[s % AD][m], ra, av, ao[m] + (s2_perm(s, NS == 20, rot) << 10));
    else reload_sc1(a[s % AD][m], ra, av, ao[m] + (s2_perm(s, NS == 20, rot) << 10));
  };
  auto ldb = [&]() {
    peek(pv, pa); wait_lg<0>(); require(pv, pa, 1);
    sfor<3>([&](auto R) { sfor<8>([&](auto K) { rdb(R, K); }); wait_lg<(decltype(R)::value < 2 ? 4 : 0)>(); });
    post(ca, 1);
  };
  if constexpr (NS == 20) {
    go(0);
    ldb();
    STAMP(6);
    go(1);
    STAMP(7);
    sfor<AD - 1>([&](auto S) {
#pragma unroll
      for (int m = 0; m < 3; m++) lda(S, m);
    });
  } else {
    peek(lv, LDS_CNT + 4 * 15); wait_lg<0>(); pin(lv);
    const int ready = (NOEARLY && late) ? 0 : __builtin_amdgcn_readfirstlane(lv);
    sfor<AD - 1>([&](auto S) { const int o = s2_perm(decltype(S)::value, false, rot) << 10; bload3_if<true>(a[decltype(S)::value], ra, av, ao[0] + o, ao[1] + o, ao[2] + o, ready); });
    ldb();
    STAMP(6);
    if (late) {
      wait_vm<0>();
      pub();
      STAMP(5);
    }
    go(1);
    STAMP(7);
    sfor<AD - 1>([&](auto S) { const int o = s2_perm(decltype(S)::value, false, rot) << 10; bload3_if<false>(a[decltype(S)::value], ra, av, ao[0] + o, ao[1] + o, ao[2] + o, ready); });
  }
  if (NS == 20) __builtin_amdgcn_s_setprio(SPRIO);
  sfor<NS - 3>([&](auto S) {
    constexpr int s = decltype(S)::value;
    constexpr bool LA = s + AD - 1 < NS, RB = s >= 1 && s + 2 < NS;
    constexpr int later = NS - 1 - s < AD - 2 ? NS - 1 - s : AD - 2;
    if constexpr (s == 3) require(pv, pa, 2);
    if constexpr (s == 7) require(pv, pa, 3);
    if constexpr (s == 10) require(pv, pa, 4);
    if constexpr (s == 13) require(pv, pa, 5);
    if constexpr (s == 2 || s == 6 || s == 9 || s == 12) peek(pv, pa);
    if constexpr (NS == 18 && s == 11 - AD) require(lv, LDS_CNT + 4 * 5, 1);
    if constexpr (NS == 18 && s == 10 - AD) peek(lv, LDS_CNT + 4 * 5);
    if constexpr (s == NS - 4) peek(lv, ca ^ 4);
    sfor<3>([&](auto M) {
      constexpr int m = decltype(M)::value;
      if constexpr (LA) lda(IC(s + AD - 1), m);
      wait_vm<3 * later + 2 - m + (LA ? m + 1 : 0)>();
      if constexpr (m == 1 && s >= 2 && s + 1 < NS) wait_lg<(RB ? 4 : 0)>();
      sfor<4>([&](auto C) {
        constexpr int c = decltype(C)::value;
        mfma_a(acc[m][c], cat(b[s % 3][c][0], b[s % 3][c][1]), a[s % AD][m]);
        if constexpr (RB && m < 2) rdb(IC(s + 2), IC(4 * m + c));
      });
    });
    if constexpr (s == 3 || s == 6 || s == 9 || s == 12) post(ca, s);
    if constexpr (s == 2) { STAMPC(12); }
    if constexpr (s == 11) { STAMPC(13); }
  });
  wait_lg<4>();
  sfor<8>([&](auto K) { rdb(IC(NS - 1), K); });
  if constexpr (AD == 3) {
#pragma unroll
    for (int m = 0; m < 3; m++) lda(IC(NS - 1), m);
  }
  int da[4];
#pragma unroll
  for (int c = 0; c < 4; c++) da[c] = mh * 49152 + kq * 4096 + (av & 0xf0) * 16 + ((((4 * c) << 4) + (av >> 8 << 4)) ^ (av & 0x70));
  require(lv, ca ^ 4, mh ? 12 : 9);
  sfor<3>([&](auto M) {
    constexpr int m = decltype(M)::value;
    sfor<3>([&](auto U) {
      constexpr int u = decltype(U)::value, s = NS - 3 + u;
      if constexpr (m == 0) wait_vm<3 * (2 - u) + 2>();
      if constexpr (m > 0 && u == 2) wait_vm<2 - m>();
      if constexpr (m == 0 && u == 1) wait_lg<8>();
      if constexpr (m == 0 && u == 2) wait_lg<0>();
      sfor<4>([&](auto C) {
        constexpr int c = decltype(C)::value;
        mfma_a(acc[m][c], cat(b[s % 3][c][0], b[s % 3][c][1]), a[s % AD][m]);
        if constexpr (m > 0 && u > 0 && (c & 1)) dsw128<(m - 1) * 16384>(da[2 * (u - 1) + (c >> 1)], __builtin_bit_cast(u32x4, acc[m - 1][2 * (u - 1) + (c >> 1)]));
      });
    });
  });
  STAMP(9);
  asm volatile("s_nop 4");
#pragma unroll
  for (int c = 0; c < 4; c++) dsw128<2 * 16384>(da[c], __builtin_bit_cast(u32x4, acc[2][c]));
  arrive((NS == 20 ? 28 : 12) + mh);
}

__global__ __launch_bounds__(768, 3) void fused_block(const void* __restrict__ x, const void* __restrict__ wqkv, const void* __restrict__ wo,
                                                      const void* __restrict__ w1, const unsigned short* __restrict__ b1,
                                                      const void* __restrict__ w2, const char* __restrict__ b2, char* __restrict__ out,
                                                      char* ws) {
  extern __shared__ __attribute__((aligned(16))) char shm_[];
  lptr shm = (lptr)shm_;
  const int tid = threadIdx.x, lane = tid & 63, w = __builtin_amdgcn_readfirstlane(tid >> 6);
  const int xcd = blockIdx.x & 7, idx = blockIdx.x >> 3;
  const int p_ = (xcd & 1) * 8 + (idx & 7), sub = idx >> 3;
  const bool att = !(sub & 1);
  const int j_ = (xcd >> 1) * 2 + (sub >> 1);
  const int uidx = (att ? 16 : 0) + (idx & 7) + 8 * (idx >> 4);
  const int unit = (xcd >> 1) * 28 + uidx;
  const bool has2 = uidx < 28 && unit < 110;
  const int mt = unit / 5, nl = unit - 5 * mt, n_ = (xcd & 1) * 5 + nl;
  const int G0 = 6 * mt;
  const int kq = w < 8 ? (w + (w >> 2)) & 3 : w & 3;
  const int rot = (ROT2);
  u64 pr_t0 = 0, pr_tau = 0;
  STAMP(0);
  if (w == 7) {
    u64 pc; asm volatile("s_getpc_b64 %0" : "=s"(pc));
    touch(mkr((const void*)(pc & ~255ull), CODE_BYTES), (idx + 32 * lane) * 128);
  }
  asm volatile("" : "+s"(x), "+s"(wqkv), "+s"(wo), "+s"(w1), "+s"(b1), "+s"(w2), "+s"(b2), "+s"(out), "+s"(ws));
  const int hf = KHF, tx = KTX;
  const int rid = (att ? 0 : 8) + j_;
  auto watch = [&](int kind, int word, u64 fv) {
    const int Gb = G0 + 5 < 128 ? G0 + 5 : 127, lane = lane_id();
    GAS u64* fp = (GAS u64*)(ws + WS_BOX) + (mt * 10 + n_) * 64 + 8 * kind + (lane & 7) + ((Gb >> 3) != (G0 >> 3) ? 4 * (lane & 8) : 0);
#pragma nounroll
    for (unsigned spins = 0; spins < SPIN_CAP; spins++) {
      if (((ABL & 32) && kind == 1) || ((ABL & 64) && kind == 0) || ((ABL & 128) && kind == 2)) break;
      if (__all(__hip_atomic_load(fp, __ATOMIC_RELAXED, __HIP_MEMORY_SCOPE_AGENT) == fv)) break;
      __builtin_amdgcn_s_sleep(1);
    }
    post(LDS_CNT + 4 * word, 1);
  };
  int fu;
  if (__builtin_expect(w >= 8, 1)) {
    __builtin_amdgcn_s_setprio(3);
    const int lw = w - 8;
    const int kt0 = (KT0) % 10;
    auto ktile = [&](int T) { const int k = kt0 + (T ^ (T < 8 ? tx : tx & 1)); return k >= 10 ? k - 10 : k; };
    const int RS = att ? 3840 : 3456;
    const u32x4 rx = mkr(x, S_ * D_ * 2), rw1 = mkr(att ? wqkv : w1, D_ * RS);
    const int r8 = lane >> 3, cp = lane & 7, t0 = p_ * 128;
    int xo[2], hv, wv;
#pragma unroll
    for (int e = 0; e < 2; e++)
      xo[e] = (t0 + 32 * lw + 2 * (4 * (r8 & 3) + 2 * e + (r8 >> 2))) * 1280 + ((cp ^ (4 * e + (r8 >> 1))) << 4);
    auto ldx = [&](int T) { const int o = ktile(T) * 128; dma4<1024>(rx, ~0ull, (T % 3) * SLOT + XOFF + 2048 + 4 * lw * 1024, xo[0], xo[1], o, o, o + 1280, o + 1280); };
    STAMP(12);
    ldx(0);
    {
      const int hi = lane >= 30, ch = lane - 30 * hi, lim = j_ < 7 ? 27 : 19;
      wv = hi * RS + (att ? ((ch >= 10) + (ch >= 20)) * 1120 + j_ * 160 + ch * 16 : j_ * 448 + (ch < lim ? ch : lim) * 16);
    }
    const int wrow = (16 * (lw & 1) + 4 * (lw >> 1)) * RS;
    auto ldw = [&](int T, int h) {
      const int so = (64 * ktile(T) + 32 * (h ^ hf)) * RS + wrow;
      dma4<960>(rw1, 0x0fffffffffffffffull, (T % 3) * SLOT + h * WSLAB + 4 * lw * 960, wv, wv, so, so + 2 * RS, so + 8 * RS, so + 10 * RS);
    };
    ldw(0, 0);
    {
      int tok = t0 - 16 + 8 * (r8 & 1) + 2 * lw + ((r8 & 3) < 2);
      tok = tok < 0 ? 0 : tok;
      hv = att ? tok * 1280 + ((cp ^ ((2 * lw + ((r8 & 3) >> 1)) & 7)) << 4) : 0x70000000;
    }
    const u64 hmask = lw == 0 ? 0xffffff00ull : 0xffffffffull;
    auto ldh = [&](int T) { dma1(rx, hmask, (T % 3) * SLOT + XOFF + lw * 512, hv, ktile(T) * 128); };
    ldh(0); ldw(0, 1);
    STAMP(13);
    bar_vm<4>();
    STAMP(14);
    ldx(1); ldh(1);
    bar_vm<5>();
    ldw(1, 0); ldw(1, 1);
    unsigned sink = 0;
    const int wv2 = blockIdx.x * 4 + lw;
    const u32x4 rs2 = mkr(wv2 < 720 ? w2 : wo, wv2 < 720 ? F_ * 1280 : D_ * 1280);
    const int so2 = wv2 < 720 ? wv2 * 3072 : min((wv2 - 720) * 3072, D_ * 1280 - 3072);
    ldx(2); ldh(2);
    if (STW) stouch<8>(sink, rs2, so2);
    bar_vm<5>();
    const int srow = p_ * 128 + 8 * rid + 2 * lw + (lane >> 5);
    u32x2 xr[5];
    unsigned ep, b1v;
    setup_loads(xr, ep, b1v, rx, srow * 1280 + (lane & 31) * 8, mkr(ws, 4), mkr(b1, F_ * 2), (j_ * 224 + 64 * lw + lane) * 2);
    if (BOXW) touch_sc1(mkr(ws + WS_BOX, has2 && lw == 0 ? 220 * 512 : 0), (mt * 10 + n_) * 512 + (lane & 3) * 128);
    ldw(2, 0); ldw(2, 1); ldx(3); ldh(3); ldw(3, 0);
    if (STW) stouch<8>(sink, rs2, so2 + 1024);
    bar_vm<9>();
    pin(ep); pin(b1v);
#pragma unroll
    for (int k = 0; k < 5; k++) pin(xr[k]);
    const unsigned tag = __builtin_amdgcn_readfirstlane(ep) + 1u;
    auto warm2 = [&](int ev) {
      const int id = min(WARM2 == 2 ? (ev * 64 + uidx * 4 + lw) * 62 + min(lane, 61) : (ev * 128 + idx * 4 + lw) * 31 + min(lane, 30), 11839), row = (id * 52429) >> 18;
      if (WARM2 == 2 && att) touch(mkr(w2, 0), 0); else if (WARM2) touchg((row < F_ ? (const char*)w2 : (const char*)wo - F_ * 1280) + row * 1280 + (xcd & 1) * 640 + (id - 5 * row) * 128);
    };
    ldw(3, 1); ldx(4); ldw(4, 0); warm2(0); ldh(4); ldw(4, 1);
    {
      float s = 0;
#pragma unroll
      for (int k = 0; k < 5; k++) s += (blo(xr[k][0]) + bhi(xr[k][0])) + (blo(xr[k][1]) + bhi(xr[k][1]));
      const float m = half_sum(s) * (1.f / D_);
      float q = 0;
#pragma unroll
      for (int k = 0; k < 10; k++) { const unsigned u = xr[k >> 1][k & 1]; const float a = blo(u) - m, b = bhi(u) - m; q = __builtin_fmaf(a, a, q); q = __builtin_fmaf(b, b, q); }
      const float rs = __builtin_amdgcn_rsqf(half_sum(q) * (1.f / D_) + 1e-5f);
      st_wt(mkr(ws + WS_STAT, S_ * 16), (lane & 31) == 0 ? srow * 16 : 0x70000000, 0, u32x4{asu(rs), tag, asu(-m * rs), tag});
      *(LAS float*)(shm + LDS_B1 + (64 * lw + lane) * 4) = blo(b1v);
      *(LAS unsigned*)(shm + LDS_CNT + 4) = tag;
    }
    if (STW) stouch<8>(sink, rs2, so2 + 2048);
    bar_vm<14 + (WARM2 != 0)>();
    const u32x4 rw = mkr(kq == 0 ? wo : w2, has2 && !(ABL & 1) ? (kq == 0 ? D_ * 1280 : F_ * 1280) : 0);
    const int cbyte = n_ * 128 + ((cp ^ ((lane >> 4) << 1)) << 4);
    const int v1 = r8 * 1280 + cbyte, v2 = kq == 0 ? (20 * (r8 >> 2) + (r8 & 3)) * 1280 + cbyte : v1;
    int gtab;
    {
      const int so = s2_perm(lane, true, rot), P = (so * 13) >> 6, kp = so - 5 * P;
      const int r0 = 160 * P + (kp == 2 ? 16 : 40 * (kp - (kp > 2)));
      const int t = 3 * s2_perm(lane, false, rot) + kq - 1, u = t - 32;
      const int r1 = 32 * (t < 32 ? 7 * (t >> 2) + (t & 3) : 4 * ((u * 43) >> 7) + 4 + u);
      gtab = kq == 0 ? r0 * 1280 + (kp == 2) : r1 * 1280;
      pin(gtab);
    }
    const int sbn = 1280 * (kq == 0 ? 20 : 16);
    auto geom = [&](int s, int& rb, int& sa, int& sb, int& v) {
      const int e = __builtin_amdgcn_readlane(gtab, s), f = e & 1;
      v = v1 + ((v2 - v1) & -f);
      rb = e - f;
      sa = f ? 1280 * 80 : 1280 * 8;
      sb = f ? 1280 * 40 : sbn;
    };
    u32x4 pre[9][4];
    auto ldr = [&](auto R) {
      constexpr int r = decltype(R)::value;
      int rb, sa, sb, v; geom(9 + r, rb, sa, sb, v);
      bload(pre[r][0], rw, v, rb); bload(pre[r][1], rw, v, rb + sa); bload(pre[r][2], rw, v, rb + sb); bload(pre[r][3], rw, v, rb + sa + sb);
    };
    auto ldl = [&](auto S) {
      constexpr int s = decltype(S)::value;
      int rb, sa, sb, v; geom(s, rb, sa, sb, v);
      dma4<1024>(rw, ~0ull, s < 9 ? s * 16384 + kq * 4096 : 147456 + (s - 18) * 4096, v, v, rb, rb + sa, rb + sb, rb + sa + sb);
    };
    const u32x4 rst = mkr(ws + WS_STAT, S_ * 16);
    const int spos = 36 * lw + (lane < 36 ? lane : 35), sv = row_token(p_, spos) * 16;
    u32x4 sg;
    sfor<5>([&](auto TT) {
      constexpr int T = decltype(TT)::value + 5;
      if (T == 9) bload_sc1(sg, rst, sv, 0);
      if (T == 6) { STAMP(15); }
      ldx(T); ldw(T, 0);
      if (!(T & 1)) warm2(T / 2 - 2);
      ldh(T); ldw(T, 1);
      bar_vm<13 + (WARM2 && !(T & 1))>();
    });
    STAMP(1);
    asm volatile("" : "+v"(sg));
#pragma nounroll
    for (unsigned spins = 0; !__all(sg[1] == tag && sg[3] == tag) && spins < SPIN_CAP; spins++) {
      __builtin_amdgcn_s_sleep(2);
      bload_sc1(sg, rst, sv, 0);
      asm volatile("s_waitcnt vmcnt(0)" : "+v"(sg) :: "memory");
    }
    *(LAS u32x2*)(shm + LDS_ST + spos * 8) = u32x2{sg[0], sg[2]};
    ldl(IC(3));
    wait_vm<4>();
    STAMP(2);
    bar_lgkm();
    asm volatile("" : "+s"(sink));
    __builtin_amdgcn_s_setprio(LP1);
    STAMP(3);
    if (!has2) return;
    const int pa = LDS_PROD + kq * 4, wa = kq * 4096 + lane * 16;
    ldl(IC(4));
    if (!att) { ldl(IC(5)); ldl(IC(6)); }
    ldr(IC(0)); ldr(IC(1));
    STAMP(4);
    await<1, 8>(2);
    ldl(IC(0)); wait_vm<THR>(); ldl(IC(1)); wait_vm<THR>(); ldl(IC(2)); wait_vm<THR>();
    if (!att) { ldl(IC(7)); wait_vm<THR>(); ldl(IC(8)); wait_vm<THR>(); if (kq == 0) { ldl(IC(18)); wait_vm<THR>(); ldl(IC(19)); } }
    __builtin_amdgcn_s_setprio(LP2);
    ldr(IC(2)); wait_vm<4>();
    post(pa, att ? 1 : 2);
    STAMP(5);
    sfor<6>([&](auto R) { ldr(IC(decltype(R)::value + 3)); wait_vm<THR2>(); });
    if (att) {
      await<1, 8>(4);
      ldl(IC(5)); ldl(IC(6)); ldl(IC(7)); ldl(IC(8));
      if (kq == 0) { ldl(IC(18)); ldl(IC(19)); }
    }
    if (lw == 3) touch(mkr(b2, D_ * 2), n_ * 128);
    wait_vm<0>();
    post(pa, 2);
#define NAP __builtin_amdgcn_s_sleep(PSL);
#define PUT(r, off) dsw128<off>(wa + (r / 3) * 49152, pre[r][0]); NAP dsw128<off + 1024>(wa + (r / 3) * 49152, pre[r][1]); NAP dsw128<off + 2048>(wa + (r / 3) * 49152, pre[r][2]); NAP dsw128<off + 3072>(wa + (r / 3) * 49152, pre[r][3]); NAP
    __builtin_amdgcn_s_setprio(0);
    await_pair(LDS_CONS + kq * 8, 1);
    PUT(0, 0) PUT(1, 16384) PUT(2, 32768)
    post(pa, 3);
    STAMP(6);
    await_pair(LDS_CONS + kq * 8, 3);
    PUT(3, 0) PUT(4, 16384) PUT(5, 32768)
    post(pa, 4);
    await_pair(LDS_CONS + kq * 8, 6);
    PUT(6, 0) PUT(7, 16384) PUT(8, 32768)
    post(pa, 5);
    fu = 2 + lw;
  } else {
    if (tid < 24) *(LAS u32x4*)(shm + (tid >> 3) * SLOT + XOFF + (tid & 7) * 16) = u32x4{0x3f803f80u, 0x3f803f80u, 0x3f803f80u, 0x3f803f80u};
    if (tid >= 448 && tid < 480) *(LAS unsigned*)(shm + LDS_CNT + 4 * (tid - 448)) = 0u;
    if (w == 5 && att) att_table(shm, lane);
    const bool light = att ? (w >= 6) : (w >= 4);
    if (light) s1_compute<3>(shm, att, w, lane, p_, j_, hf, ws PR_PASS);
    else s1_compute<4>(shm, att, w, lane, p_, j_, hf, ws PR_PASS);
    STAMP(3);
    if (att) s1_attention(shm, w, p_, j_, ws PR_PASS);
    STAMP(4);
    const unsigned tag = __builtin_amdgcn_readfirstlane(*(LAS unsigned*)(shm + LDS_CNT + 4));
    GAS u64* flags = (GAS u64*)(ws + WS_FLAG);
    const u64 fv = flagval(tag);
    auto pub = [&]() {
      const int lane = lane_id(), kind = att ? 0 : 1 + (w >> 2);
      unsigned old = 0;
      if (lane == 0) old = __hip_atomic_fetch_add((LAS unsigned*)(shm + LDS_CNT + (kind == 1 ? 120 : 0)), 1u, __ATOMIC_RELAXED, __HIP_MEMORY_SCOPE_WORKGROUP);
      if (__builtin_amdgcn_readfirstlane(old) == (att ? 7u : 3u)) {
        const int dm = (lane * 26) >> 8, cm = (8 * p_) / 6 + dm;
        GAS u64* dst = lane == 30 ? flags + p_ * 16 + rid : (GAS u64*)(ws + WS_BOX) + (cm * 10 + lane - 10 * dm) * 64 + (p_ - ((6 * cm) >> 3)) * 32 + 8 * kind + j_;
        if (lane == 30 || (lane < 30 && 6 * cm < 8 * p_ + 8)) __hip_atomic_store(dst, fv, __ATOMIC_RELAXED, __HIP_MEMORY_SCOPE_AGENT);
      }
    };
    const bool late = has2 && kq != 0 && (LATE_ALL == 1 ? (att || w >= 4) : LATE_ALL == 2 ? (!att && w >= 4) : LATE_ALL == 3 ? false : att);
    if (!late) {
      wait_vm<0>();
      pub();
      STAMP(5);
    }
    if (!has2) {
      if (blockIdx.x == 191 && w == 0) {
        const int lane = lane_id();
#pragma nounroll
        for (unsigned spins = 0; spins < SPIN_CAP; spins++) {
          bool ok = true;
#pragma unroll
          for (int k = 0; k < 4; k++) ok &= __hip_atomic_load(flags + lane + 64 * k, __ATOMIC_RELAXED, __HIP_MEMORY_SCOPE_AGENT) == fv;
          if (__all(ok)) break;
          __builtin_amdgcn_s_sleep(8);
        }
        if (lane == 0) __hip_atomic_store((GAS unsigned*)ws, tag, __ATOMIC_RELAXED, __HIP_MEMORY_SCOPE_AGENT);
      }
      FINISH();
      return;
    }
    if (kq == 0) {
      auto go = [&](int ph) { if (ph) { watch(w == 0 ? 0 : 2, w == 0 ? 14 : 5, fv); await<GSL, 1>(14); } else if (w == 0) watch(1, 15, fv); };
      s2_compute<20>(w, kq, G0, rot, false, ws, go, pub PR_PASS);
    } else {
      auto go = [&](int ph) { if (ph) await<GSL, 1>(15); };
      s2_compute<18>(w, kq, G0, rot, late, ws, go, pub PR_PASS);
    }
    if (w == 0 || w > FW) { STAMP(11); FINISH(); return; }
    fu = w - 1;
  }
  {
    const int lane = lane_id();
    const u32x4 bv = *(const GAS u32x4*)(b2 + n_ * 128 + (lane & 7) * 16);
    int foff[2], pr0[2];
    u32x4 xv[2];
    f32x2 gt[2][4];
#pragma unroll
    for (int hh = 0; hh < 2; hh++) {
      const int frow = 48 * hh + 8 * fu + (lane >> 3);
      foff[hh] = fin_off(G0, n_, 48 * hh + 8 * fu, lane);
      xv[hh] = *(const GAS u32x4*)((const char*)x + (foff[hh] < S_ * D_ * 2 ? foff[hh] : 0));
      pr0[hh] = (frow >> 4) * 16384 + (frow & 15) * 256 + (((2 * (lane & 7)) ^ (frow & 7)) << 4);
      pin(pr0[hh]);
    }
#pragma unroll
    for (int hh = 0; hh < 2; hh++) {
      await<FSL, 3>(12 + hh);
      f32x4 y[2];
#pragma unroll
      for (int e = 0; e < 2; e++) {
        lptr pr = shm + (pr0[hh] ^ (16 * e));
        y[e] = *(LAS f32x4*)(pr + 4096) + *(LAS f32x4*)(pr + 8192) + *(LAS f32x4*)(pr + 12288);
      }
#pragma unroll
      for (int k = 0; k < 4; k++) {
        f32x2 yy = f32x2{y[k >> 1][2 * (k & 1)], y[k >> 1][2 * (k & 1) + 1]} + f32x2{blo(bv[k]), bhi(bv[k])};
        f32x2 t = yy * -1.4426950408889634f;
        f32x2 e = f32x2{__builtin_amdgcn_exp2f(t[0]), __builtin_amdgcn_exp2f(t[1])} + 1.f;
        gt[hh][k] = yy * f32x2{__builtin_amdgcn_rcpf(e[0]), __builtin_amdgcn_rcpf(e[1])};
        pin(gt[hh][k]);
      }
    }
    STAMP(10);
#pragma unroll
    for (int hh = 0; hh < 2; hh++) {
      await<FSL2, 1>(28 + hh);
      u32x4 o;
#pragma unroll
      for (int e = 0; e < 2; e++) {
        const f32x4 s = *(LAS f32x4*)(shm + (pr0[hh] ^ (16 * e)));
        o[2 * e] = pk2(f32x2{s[0], s[1]} * gt[hh][2 * e] + f32x2{blo(xv[hh][2 * e]), bhi(xv[hh][2 * e])});
        o[2 * e + 1] = pk2(f32x2{s[2], s[3]} * gt[hh][2 * e + 1] + f32x2{blo(xv[hh][2 * e + 1]), bhi(xv[hh][2 * e + 1])});
      }
      if (foff[hh] < S_ * D_ * 2) __builtin_nontemporal_store(o, (GAS u32x4*)(out + foff[hh]));
    }
  }
  STAMP(11);
  FINISH();
}

extern "C" void kernel_launch(void* const* d_in, const int* in_sizes, int n_in, void* d_out, int out_size, void* d_ws,
                              size_t ws_size, hipStream_t stream) {
  (void)hipFuncSetAttribute((const void*)fused_block, hipFuncAttributeMaxDynamicSharedMemorySize, LDS_TOTAL);
  fused_block<<<256, 768, LDS_TOTAL, stream>>>(d_in[0], d_in[1], d_in[2], d_in[4], (const unsigned short*)d_in[5], d_in[6],
                                               (const char*)d_in[7], (char*)d_out, (char*)d_ws + WSOFF);
}
```

```cpp
#define LPM 3
#define LPA 3
#include <hip/hip_runtime.h>
#include <hip/hip_bf16.h>
#include <type_traits>
#include <utility>

typedef __attribute__((ext_vector_type(4))) short s16x4;
typedef __attribute__((ext_vector_type(8))) __bf16 bf16x8;
typedef __attribute__((ext_vector_type(2))) __bf16 bf16x2;
typedef __attribute__((ext_vector_type(4))) float f32x4;
typedef __attribute__((ext_vector_type(2))) float f32x2;
typedef __attribute__((ext_vector_type(4))) unsigned u32x4;
typedef __attribute__((ext_vector_type(2))) unsigned u32x2;
#define LAS __attribute__((address_space(3)))
#define GAS __attribute__((address_space(1)))
#define DEVI __device__ __forceinline__
typedef LAS char* lptr;
typedef unsigned long long u64;
#define IC(n) std::integral_constant<int, n>{}

constexpr int S_ = 2048, D_ = 640, F_ = 1728;
constexpr int KS_T = 74;
constexpr int WSLAB = 15360;
constexpr int XOFF = 2 * WSLAB;
constexpr int SLOT = 49152;
constexpr int LDS_TB = 86016;
constexpr int LDS_ST = 155648;
constexpr int LDS_CS = LDS_ST + 1152;
constexpr int LDS_B1 = LDS_CS + 1024;
constexpr int LDS_CNT = LDS_B1 + 1024;
constexpr int LDS_ZERO = LDS_CNT + 24;
constexpr int LDS_CONS = LDS_CNT + 64;
constexpr int LDS_PROD = LDS_CONS + 32;
constexpr int LDS_TAB = LDS_CNT + 128;
constexpr int LDS_TOTAL = 163840;
constexpr int WS_FLAG = 4096, WS_STAT = 8192, WS_BOX = 65536, WS_A2 = 1 << 20;
constexpr int A2_BYTES = 128 * KS_T * 1024;
#define AD3Y 3
#define AD3S 4
#define AD2Y 4
#define AD2S 5
#define LPP 0
#define SPRIO 0
#define THR 32
#define KTX (((idx >> 1) ^ sub) & 1)
#define ROT2 (uidx & 7)
constexpr int CODE_BYTES = 46464;
constexpr unsigned SPIN_CAP = 1u << 20;

#define STAMP(idx)
#define FINISH()
#define STAMPC(k)
#define STAMPA(k) STAMP(k)
#define STAMPB(k)
#define PR_ARGS
#define PR_PASS

DEVI void bar_lgkm() { asm volatile("s_waitcnt lgkmcnt(0)\n\ts_barrier" ::: "memory"); }
DEVI void bar() { asm volatile("s_barrier" ::: "memory"); }
template <int N> DEVI void bar_vm() { asm volatile("s_waitcnt vmcnt(%0)\n\ts_barrier" ::"i"(N) : "memory"); }
template <int N> DEVI void wait_vm() { asm volatile("s_waitcnt vmcnt(%0)" ::"i"(N) : "memory"); }
DEVI unsigned pk2(float a, float b) { f32x2 v = {a, b}; return __builtin_bit_cast(unsigned, __builtin_convertvector(v, bf16x2)); }
DEVI unsigned pk2(f32x2 v) { return __builtin_bit_cast(unsigned, __builtin_convertvector(v, bf16x2)); }
DEVI float asf(unsigned u) { return __builtin_bit_cast(float, u); }
DEVI unsigned asu(float f) { return __builtin_bit_cast(unsigned, f); }
DEVI float blo(unsigned u) { return __builtin_bit_cast(float, u << 16); }
DEVI float bhi(unsigned u) { return __builtin_bit_cast(float, u & 0xffff0000u); }
DEVI u32x2 tr_read(lptr p) { return __builtin_bit_cast(u32x2, __builtin_amdgcn_ds_read_tr16_b64_v4i16((LAS s16x4*)p)); }
DEVI f32x4 mfma(u32x4 a, u32x4 b, f32x4 c) {
  return __builtin_amdgcn_mfma_f32_16x16x32_bf16(__builtin_bit_cast(bf16x8, a), __builtin_bit_cast(bf16x8, b), c, 0, 0, 0);
}
DEVI u32x4 cat(u32x2 a, u32x2 b) { return __builtin_shufflevector(a, b, 0, 1, 2, 3); }
DEVI void mfma_a(f32x4& c, u32x4 a, u32x4 b) { asm volatile("v_mfma_f32_16x16x32_bf16 %0, %1, %2, %0" : "+v"(c) : "v"(a), "v"(b)); }
template <int OFF> DEVI void ds128(u32x4& d, int addr) { asm volatile("ds_read_b128 %0, %1 offset:%2" : "=v"(d) : "v"(addr), "i"(OFF)); }
template <int OFF> DEVI void dstr(u32x2& d, int addr) { asm volatile("ds_read_b64_tr_b16 %0, %1 offset:%2" : "=v"(d) : "v"(addr), "i"(OFF)); }
template <int OFF> DEVI void dsw128(int addr, u32x4 d) { asm volatile("ds_write_b128 %0, %1 offset:%2" ::"v"(addr), "v"(d), "i"(OFF) : "memory"); }
DEVI u32x4 mkr(const void* p, unsigned bytes) {
  u64 a = (u64)p;
  return u32x4{(unsigned)a, (unsigned)(a >> 32) & 0xffffu, bytes, 0x00020000u};
}
DEVI void st_wt(u32x4 r, int voff, int soff, u32x4 v) {
  asm volatile("buffer_store_dwordx4 %0, %1, %2, %3 offen sc1\n\ts_nop 1" ::"v"(v), "v"(voff), "s"(r), "s"(soff) : "memory");
}
template <int LSTEP>
DEVI void dma4(u32x4 r, u64 mask, int l, int va, int vb, int s0, int s1, int s2, int s3) {
  asm volatile(
      "s_mov_b64 exec, %0\n\t"
      "s_mov_b32 m0, %1\n\ts_nop 0\n\tbuffer_load_dwordx4 %2, %4, %5 offen lds\n\t"
      "s_add_u32 m0, %1, %9\n\ts_nop 0\n\tbuffer_load_dwordx4 %3, %4, %6 offen lds\n\t"
      "s_add_u32 m0, %1, %10\n\ts_nop 0\n\tbuffer_load_dwordx4 %2, %4, %7 offen lds\n\t"
      "s_add_u32 m0, %1, %11\n\ts_nop 0\n\tbuffer_load_dwordx4 %3, %4, %8 offen lds\n\t"
      "s_mov_b64 exec, -1"
      :: "s"(mask), "s"(l), "v"(va), "v"(vb), "s"(r), "s"(s0), "s"(s1), "s"(s2), "s"(s3), "i"(LSTEP), "i"(2 * LSTEP), "i"(3 * LSTEP)
      : "memory", "scc");
}
DEVI void dma1(u32x4 r, u64 mask, int l, int v, int s0) {
  asm volatile("s_mov_b64 exec, %0\n\ts_mov_b32 m0, %1\n\ts_nop 0\n\tbuffer_load_dwordx4 %2, %3, %4 offen lds\n\ts_mov_b64 exec, -1"
               :: "s"(mask), "s"(l), "v"(v), "s"(r), "s"(s0) : "memory");
}
DEVI void setup_loads(u32x2 (&xr)[5], unsigned& ep, unsigned& bv, u32x4 rx, int vx, u32x4 re, u32x4 rb, int vb) {
  asm volatile("buffer_load_dwordx2 %0, %7, %8, 0 offen\n\tbuffer_load_dwordx2 %1, %7, %8, 0 offen offset:256\n\tbuffer_load_dwordx2 %2, %7, %8, 0 offen offset:512\n\t"
               "buffer_load_dwordx2 %3, %7, %8, 0 offen offset:768\n\tbuffer_load_dwordx2 %4, %7, %8, 0 offen offset:1024\n\t"
               "buffer_load_dword %5, off, %9, 0 sc1\n\tbuffer_load_ushort %6, %11, %10, 0 offen"
               : "=&v"(xr[0]), "=&v"(xr[1]), "=&v"(xr[2]), "=&v"(xr[3]), "=&v"(xr[4]), "=&v"(ep), "=&v"(bv)
               : "v"(vx), "s"(rx), "s"(re), "s"(rb), "v"(vb) : "memory");
}
template <class T> DEVI void pin(T& v) { asm volatile("" : "+v"(v)); }
DEVI void dsw_col0(int a0, f32x4 d0, int a1, f32x4 d1, int a2, f32x4 d2) {
  asm volatile("s_mov_b64 exec, %6\n\tds_write_b128 %0, %1\n\tds_write_b128 %2, %3\n\tds_write_b128 %4, %5\n\ts_mov_b64 exec, -1"
               ::"v"(a0), "v"(d0), "v"(a1), "v"(d1), "v"(a2), "v"(d2), "s"(0x0001000100010001ull) : "memory");
}
DEVI void reload_sc1(u32x4& d, u32x4 r, int v, int s0) { asm volatile("buffer_load_dwordx4 %0, %1, %2, %3 offen sc1" : "+v"(d) : "v"(v), "s"(r), "s"(s0) : "memory"); }
template <int OFF> DEVI void redstr(u32x2& d, int addr) { asm volatile("ds_read_b64_tr_b16 %0, %1 offset:%2" : "+v"(d) : "v"(addr), "i"(OFF)); }
DEVI void vinc(int& d, int s) { asm volatile("v_add_u32 %0, %1, %0" : "+v"(d) : "s"(s)); }
DEVI void vadd(int& d, int a, int s) { asm volatile("v_add_u32 %0, %2, %1" : "=v"(d) : "v"(a), "s"(s)); }
template <class F, int... I> DEVI void sfor_(F f, std::integer_sequence<int, I...>) { (f(std::integral_constant<int, I>{}), ...); }
template <int N, class F> DEVI void sfor(F f) { sfor_(f, std::make_integer_sequence<int, N>{}); }
DEVI void touch_sc1(u32x4 r, int v) {
  asm volatile("s_mov_b32 m0, %2\n\ts_nop 0\n\tbuffer_load_dword %0, %1, 0 offen sc1 lds" ::"v"(v), "s"(r), "i"(LDS_TOTAL - 256) : "memory");
}
DEVI void touch(u32x4 r, int v) {
  asm volatile("s_mov_b32 m0, %2\n\ts_nop 0\n\tbuffer_load_dword %0, %1, 0 offen lds" ::"v"(v), "s"(r), "i"(LDS_TOTAL - 256) : "memory");
}
DEVI void bload(u32x4& d, u32x4 r, int v, int s0) { asm volatile("buffer_load_dwordx4 %0, %1, %2, %3 offen" : "=v"(d) : "v"(v), "s"(r), "s"(s0)); }
DEVI void bload_sc1(u32x4& d, u32x4 r, int v, int s0) { asm volatile("buffer_load_dwordx4 %0, %1, %2, %3 offen sc1" : "=v"(d) : "v"(v), "s"(r), "s"(s0) : "memory"); }
DEVI void arrive(int k) {
  asm volatile("s_mov_b64 exec, 1\n\tds_add_u32 %0, %1\n\ts_mov_b64 exec, -1" ::"v"(LDS_CNT + 4 * k), "v"(1) : "memory");
}
template <int SLEEP, unsigned N>
DEVI void await(int k) {
  unsigned v;
  for (;;) {
    asm volatile("ds_read_b32 %0, %1\n\ts_waitcnt lgkmcnt(0)" : "=v"(v) : "v"(LDS_CNT + 4 * k) : "memory");
    if (__builtin_amdgcn_readfirstlane(v) >= N) break;
    if (SLEEP) __builtin_amdgcn_s_sleep(SLEEP);
  }
}
DEVI void post(int addr, int val) { asm volatile("ds_write_b32 %0, %1" ::"v"(addr), "v"(val) : "memory"); }
DEVI void peek(unsigned& v, int addr) { asm volatile("ds_read_b32 %0, %1" : "=v"(v) : "v"(addr) : "memory"); }
DEVI void require(unsigned v, int addr, unsigned n) {
  unsigned t;
  asm volatile("v_readfirstlane_b32 %1, %0\n\ts_cmp_ge_u32 %1, %3\n\ts_cbranch_scc1 .Lq%=\n"
               ".Lw%=:\n\tds_read_b32 %0, %2\n\ts_waitcnt lgkmcnt(0)\n\tv_readfirstlane_b32 %1, %0\n\ts_cmp_ge_u32 %1, %3\n\ts_cbranch_scc0 .Lw%=\n.Lq%=:"
               : "+&v"(v), "=&s"(t) : "v"(addr), "s"(n) : "memory", "scc");
}
DEVI void await_pair(int addr, unsigned n) {
  for (;;) {
    u32x2 v;
    asm volatile("ds_read_b64 %0, %1\n\ts_waitcnt lgkmcnt(0)" : "=v"(v) : "v"(addr) : "memory");
    const unsigned a = v[0], b = v[1];
    if (__builtin_amdgcn_readfirstlane(a < b ? a : b) >= n) break;
    __builtin_amdgcn_s_sleep(1);
  }
}
DEVI int lane_id() { int l; asm volatile("v_mbcnt_lo_u32_b32 %0, -1, 0\n\tv_mbcnt_hi_u32_b32 %0, -1, %0" : "=v"(l)); return l; }
DEVI int sig(int i) { return 4 * (i & 3) + (i >> 2); }
DEVI u64 flagval(unsigned tag) { return ((u64)(tag ^ 0x5bd1e995u) << 32) | tag; }

DEVI float xmax16(float v) { auto r = __builtin_amdgcn_permlane16_swap(asu(v), asu(v), false, false); return fmaxf(asf(r[0]), asf(r[1])); }
DEVI float xmax32(float v) { auto r = __builtin_amdgcn_permlane32_swap(asu(v), asu(v), false, false); return fmaxf(asf(r[0]), asf(r[1])); }
DEVI float xadd16(float v) { auto r = __builtin_amdgcn_permlane16_swap(asu(v), asu(v), false, false); return asf(r[0]) + asf(r[1]); }
DEVI float xadd32(float v) { auto r = __builtin_amdgcn_permlane32_swap(asu(v), asu(v), false, false); return asf(r[0]) + asf(r[1]); }
template <int CTRL> DEVI float dpp(float v) { return asf(__builtin_amdgcn_mov_dpp(asu(v), CTRL, 0xf, 0xf, true)); }
DEVI float half_sum(float v) {
  v += dpp<0xb1>(v); v += dpp<0x4e>(v); v += dpp<0x141>(v); v += dpp<0x140>(v);
  return xadd16(v);
}

DEVI int h_slot(int j, int l) { const int t = l < 4 ? 4 * j + l : 28 + 3 * j + l, d = (t * 43) >> 7; return 18 * (t - 3 * d) + d; }

DEVI int row_token(int p_, int pos) {
  const int tau = pos >> 4, i = pos & 15;
  int tok = tau == 0 ? p_ * 128 - 16 + 8 * (i & 1) + 2 * (i >> 2) + ((i & 3) < 2) : p_ * 128 + 32 * ((tau - 1) >> 1) + 2 * sig(i) + ((tau - 1) & 1);
  return tok < 0 ? 0 : tok;
}

struct ColGeo { int A, B, C, ct; };
DEVI ColGeo colgeo(bool att, int nw, int mw, int c, bool heavy) {
  const int cc = heavy ? (c ^ (2 * mw)) : c;
  const int ct = 4 * nw + cc, sec = (ct * 13) >> 6, hh = ct - 5 * sec;
  const bool q4 = hh == 4, sp = !heavy && c == 2;
  ColGeo r;
  r.A = att ? sec * 160 + (q4 ? 32 : 40 * hh) : heavy ? 128 * nw + 32 * cc : (sp ? 352 + 16 * nw : 128 + 64 * nw + 32 * c);
  r.B = att && q4 ? 40 : 8;
  r.C = att ? (q4 ? 80 : 16) : (sp ? 32 : 16);
  r.ct = ct;
  return r;
}

template <int NC>
struct Frags { u32x4 x[5]; u32x2 w[NC][2]; };

template <int N> DEVI void wait_lg() { asm volatile("s_waitcnt lgkmcnt(%0)" ::"i"(N) : "memory"); }

template <int NC, int K>
DEVI void s1_rd(Frags<NC>& n, int xo, int xh, const int (&wa)[NC]) {
  if constexpr (K == 0) ds128<0>(n.x[0], xo);
  else if constexpr (K <= 2 * NC) { if ((K - 1) & 1) dstr<7680>(n.w[(K - 1) >> 1][1], wa[(K - 1) >> 1]); else dstr<0>(n.w[(K - 1) >> 1][0], wa[(K - 1) >> 1]); }
  else if constexpr (K < 2 * NC + 4) ds128<(K - 2 * NC) * 2048>(n.x[K - 2 * NC], xo);
  else if constexpr (K == 2 * NC + 4) ds128<0>(n.x[4], xh);
}
template <int NC, int NH, int K>
DEVI void s1_mm(const Frags<NC>& c, f32x4 (&acc)[4][NC], f32x4 (&hacc)[NH]) {
  if constexpr (K < 4 * NC) mfma_a(acc[K / NC][K % NC], cat(c.w[K % NC][0], c.w[K % NC][1]), c.x[K / NC]);
  else mfma_a(hacc[K - 4 * NC], cat(c.w[K - 4 * NC][0], c.w[K - 4 * NC][1]), c.x[4]);
}
template <int NC, int NH, bool BAR>
DEVI void s1_step(const Frags<NC>& c, Frags<NC>& n, int xo, int xh, const int (&wa)[NC], f32x4 (&acc)[4][NC], f32x4 (&hacc)[NH]) {
  constexpr int NM = 4 * NC + NH, NR = 2 * NC + 5, D = BAR ? NM - NR : 0;
  sfor<NM>([&](auto KK) {
    constexpr int k = decltype(KK)::value;
    constexpr int need = k < NC ? 2 * k + 2 : (k % NC == 0 && k <= 4 * NC) ? 2 * NC + k / NC : -1;
    if constexpr (BAR && k == D) bar_lgkm();
    if constexpr (need >= 0 && (!BAR || k < D)) wait_lg<NR - 1 - need + (BAR ? 0 : (k < NR ? k : NR))>();
    s1_mm<NC, NH, k>(c, acc, hacc);
    if constexpr (k >= D) s1_rd<NC, k - D>(n, xo, xh, wa);
  });
}

template <int OFF> DEVI void dsw64(int addr, u32x2 d) { asm volatile("ds_write_b64 %0, %1 offset:%2" ::"v"(addr), "v"(d), "i"(OFF) : "memory"); }
template <int OFF> DEVI void ds64(u32x2& d, int addr) { asm volatile("ds_read_b64 %0, %1 offset:%2" : "=v"(d) : "v"(addr), "i"(OFF)); }

template <int NC>
DEVI void s1_compute(lptr shm, bool att, int w, int lane, int p_, int j_, int hf, char* ws PR_ARGS) {
  constexpr int NH = NC == 4 ? 2 : 3;
  const int mw = w & 1, nw = w >> 1, i = lane & 15, g = lane >> 4;
  f32x4 acc[4][NC], hacc[NH];
#pragma unroll
  for (int m = 0; m < 4; m++)
#pragma unroll
    for (int c = 0; c < NC; c++) { acc[m][c] = f32x4{0, 0, 0, 0}; asm volatile("" : "+v"(acc[m][c])); }
#pragma unroll
  for (int c = 0; c < NH; c++) { hacc[c] = f32x4{0, 0, 0, 0}; asm volatile("" : "+v"(hacc[c])); }
  int wb[NC], cb[NC], ct[NC];
#pragma unroll
  for (int c = 0; c < NC; c++) {
    const ColGeo cg = colgeo(att, nw, mw, c, NC == 4);
    wb[c] = (lane >> 2) * 480 + cg.A + cg.B * (lane & 1) + cg.C * ((lane >> 1) & 1);
    cb[c] = LDS_CS + 2 * (cg.A + cg.B * (g & 1) + cg.C * (g >> 1));
    ct[c] = cg.ct;
    asm volatile("" : "+v"(cb[c]), "+v"(wb[c]));
  }
  int xb = XOFF + i * 128 + ((g ^ (i >> 1) ^ (4 * hf)) << 4);
  int sta = LDS_ST + i * 8;
  asm volatile("" : "+v"(xb), "+v"(sta));
  const int own = (1 + 4 * mw) * 2048;
  Frags<NC> fa, fb;
  int wa[NC];
  STAMPB(12);
  bar_lgkm();
  STAMP(1);
  sfor<2 * NC + 5>([&](auto K) { s1_rd<NC, decltype(K)::value>(fa, xb + own, xb, wb); });
#pragma unroll
  for (int c = 0; c < NC; c++) wa[c] = wb[c] + WSLAB;
  s1_step<NC, NH, true>(fa, fb, (xb ^ 64) + own, xb ^ 64, wa, acc, hacc);
  int sl = 0;
#pragma nounroll
  for (int T = 1; T < 10; T++) {
    sl = sl == 2 * SLOT ? 0 : sl + SLOT;
#pragma unroll
    for (int c = 0; c < NC; c++) wa[c] = wb[c] + sl;
    s1_step<NC, NH, true>(fb, fa, xb + sl + own, xb + sl, wa, acc, hacc);
#pragma unroll
    for (int c = 0; c < NC; c++) wa[c] = wb[c] + sl + WSLAB;
    s1_step<NC, NH, false>(fa, fb, (xb ^ 64) + sl + own, (xb ^ 64) + sl, wa, acc, hacc);
  }
  wait_lg<0>();
  arrive(2);
  sfor<NH>([&](auto K) { s1_mm<NC, NH, 4 * NC + decltype(K)::value>(fb, acc, hacc); });
  u32x2 stu[5];
  u32x4 b1u[NC], csu[NC];
  sfor<4 * NC>([&](auto KK) {
    constexpr int k = decltype(KK)::value;
    s1_mm<NC, NH, k>(fb, acc, hacc);
    if constexpr (k < 4) ds64<(1 + k) * 128>(stu[k], sta + mw * 512);
    if constexpr (k == 4) ds64<0>(stu[4], sta);
    if constexpr (k == 5) {
      dsw_col0(cb[0], hacc[0], cb[1], hacc[1], cb[NH - 1], hacc[NH - 1]);
      if (NC == 4) arrive(8 + nw);
    }
    if constexpr (k >= 6 && k < 6 + NC) ds128<LDS_B1 - LDS_CS>(b1u[k - 6], cb[k - 6]);
  });
  STAMP(2);
  if (NC == 4) await<0, 2>(8 + nw);
#pragma unroll
  for (int c = 0; c < NC; c++) ds128<0>(csu[c], cb[c]);
  asm volatile("s_waitcnt lgkmcnt(0)\n\ts_nop 7" ::: "memory");
  f32x2 st[5];
  f32x4 cs[NC], b1[NC];
#pragma unroll
  for (int m = 0; m < 5; m++) { asm volatile("" : "+v"(stu[m])); st[m] = __builtin_bit_cast(f32x2, stu[m]); }
#pragma unroll
  for (int c = 0; c < NC; c++) { asm volatile("" : "+v"(csu[c]), "+v"(b1u[c])); cs[c] = __builtin_bit_cast(f32x4, csu[c]); b1[c] = __builtin_bit_cast(f32x4, b1u[c]); }
#pragma unroll
  for (int m = 0; m < 4; m++)
#pragma unroll
    for (int c = 0; c < NC; c++) pin(acc[m][c]);
#pragma unroll
  for (int c = 0; c < NH; c++) pin(hacc[c]);
  STAMPA(12);
#define SB __builtin_amdgcn_sched_barrier(0)
  const int l2 = lane_id();
  if (att) {
    const int g2 = l2 >> 4, piece = LDS_TB + ((l2 & 48) + ((l2 & 15) ^ (8 * (g2 >> 1)))) * 8;
    sfor<NC>([&](auto C) {
      constexpr int c = decltype(C)::value;
      const int ah = piece + ct[c] * 4608, ao = ah + (1 + 4 * mw) * 512;
      u32x2 o[5];
      SB;
#pragma unroll
      for (int m = 0; m < (c < NH ? 5 : 4); m++) {
        f32x4 a = m < 4 ? acc[m < 4 ? m : 0][c] : hacc[c < NH ? c : 0];
        f32x4 v = a * st[m][0] + cs[c] * st[m][1];
        o[m] = u32x2{pk2(v[0], v[1]), pk2(v[2], v[3])};
      }
      dsw64<0>(ao, o[0]); dsw64<512>(ao, o[1]); dsw64<1024>(ao, o[2]); dsw64<1536>(ao, o[3]);
      if (c < NH) dsw64<0>(ah, o[4]);
    });
  } else {
    const u32x4 ra = mkr(ws + WS_A2, A2_BYTES);
    const int base = (8 * p_ + 4 * mw) * KS_T * 1024;
    const int o0 = base + (NC == 4 || j_ < 7 || nw == 2 ? h_slot(j_, NC == 4 ? 2 * nw + mw : 2 + nw) * 1024 : 0x70000000);
    const int o1 = base + (NC == 4 ? h_slot(j_, 2 * nw + (mw ^ 1)) * 1024 : j_ < 7 ? h_slot(j_, 6) * 1024 + (nw - 2) * 512 : 0x70000000);
    const float c1 = -2.f * 0.7978845608028654f * 1.4426950408889634f, c2 = c1 * 0.044715f;
    unsigned sp[2];
#pragma unroll
    for (int m = 0; m < 4; m++) {
      f32x4 v[NC], t[NC];
      SB;
#pragma unroll
      for (int c = 0; c < NC; c++) t[c] = cs[c] * st[m][1] + b1[c];
      SB;
#pragma unroll
      for (int c = 0; c < NC; c++) v[c] = acc[m][c] * st[m][0] + t[c];
      SB;
#pragma unroll
      for (int c = 0; c < NC; c++) t[c] = v[c] * v[c];
      SB;
#pragma unroll
      for (int c = 0; c < NC; c++) t[c] = t[c] * c2 + c1;
      SB;
#pragma unroll
      for (int c = 0; c < NC; c++) t[c] = t[c] * v[c];
      SB;
#pragma unroll
      for (int c = 0; c < NC; c++)
#pragma unroll
        for (int e = 0; e < 4; e++) t[c][e] = __builtin_amdgcn_exp2f(t[c][e]);
      SB;
#pragma unroll
      for (int c = 0; c < NC; c++) t[c] = t[c] + 1.f;
      SB;
#pragma unroll
      for (int c = 0; c < NC; c++)
#pragma unroll
        for (int e = 0; e < 4; e++) t[c][e] = __builtin_amdgcn_rcpf(t[c][e]);
      SB;
#pragma unroll
      for (int c = 0; c < NC; c++) v[c] = v[c] * t[c];
      SB;
      unsigned o[NC][2];
#pragma unroll
      for (int c = 0; c < NC; c++) { o[c][0] = pk2(v[c][0], v[c][1]); o[c][1] = pk2(v[c][2], v[c][3]); }
      st_wt(ra, l2 * 16, o0 + m * KS_T * 1024, u32x4{o[0][0], o[0][1], o[1][0], o[1][1]});
      if (NC == 4) st_wt(ra, l2 * 16, o1 + m * KS_T * 1024, u32x4{o[2][0], o[2][1], o[NC - 1][0], o[NC - 1][1]});
      else if (m & 1) {
        auto s0 = __builtin_amdgcn_permlane32_swap(sp[0], o[2][0], false, false);
        auto s1 = __builtin_amdgcn_permlane32_swap(sp[1], o[2][1], false, false);
        st_wt(ra, (l2 >> 5) * (KS_T * 1024) + (l2 & 31) * 16, o1 + (m - 1) * KS_T * 1024, u32x4{s0[0], s1[0], s0[1], s1[1]});
      } else { sp[0] = o[2][0]; sp[1] = o[2][1]; }
    }
  }
}

template <bool MAX> DEVI float red4(const float (&v)[4]) {
  auto op = [](unsigned a, unsigned b) { float r; if (MAX) asm("v_max_f32 %0, %1, %2" : "=v"(r) : "v"(a), "v"(b)); else r = asf(a) + asf(b); return r; };
  auto a = __builtin_amdgcn_permlane32_swap(asu(v[0]), asu(v[1]), false, false);
  auto b = __builtin_amdgcn_permlane32_swap(asu(v[2]), asu(v[3]), false, false);
  auto c = __builtin_amdgcn_permlane16_swap(asu(op(a[0], a[1])), asu(op(b[0], b[1])), false, false);
  return op(c[0], c[1]);
}
DEVI void bcast4(float r, float (&v)[4]) {
  auto e = __builtin_amdgcn_permlane16_swap(asu(r), asu(r), false, false);
  unsigned e0 = e[0], e1 = e[1];
  auto a = __builtin_amdgcn_permlane32_swap(e0, e0, false, false);
  auto b = __builtin_amdgcn_permlane32_swap(e1, e1, false, false);
  v[0] = asf(a[0]); v[1] = asf(a[1]); v[2] = asf(b[0]); v[3] = asf(b[1]);
}

DEVI void att_table(lptr shm, int lane) {
  const int i = lane & 15, g = lane >> 4, p = lane & 3;
  const int d = sig(i) - g;
  const unsigned NEG = 0xf149f2cau;
  bool vo[4], vp[4];
#pragma unroll
  for (int r = 0; r < 4; r++) { vo[r] = (unsigned)(d - 4 * r) <= 7u; vp[r] = d + 16 - 4 * r <= 7; }
  auto pm = [](bool lo, bool hi) { return (lo ? 0xffffu : 0u) | (hi ? 0xffff0000u : 0u); };
  LAS u32x4* t = (LAS u32x4*)(shm + LDS_TAB) + lane;
  t[0] = u32x4{vo[0] ? 0u : NEG, vo[1] ? 0u : NEG, vo[2] ? 0u : NEG, vo[3] ? 0u : NEG};
  t[64] = u32x4{pm(vp[2], vp[3]), pm(vo[0], vo[1]), vp[2] ? 0u : NEG, vp[3] ? 0u : NEG};
  t[128] = u32x4{pm(vo[2], vo[3]), (unsigned)(LDS_TB + (g * 16 + (i ^ (8 * (g >> 1)))) * 8), (unsigned)(LDS_TB + (p * 16 + ((lane >> 2) ^ (8 * (p >> 1)))) * 8), 0u};
}
DEVI int sel(u64 mask, int a, int b) { int r; asm("v_cndmask_b32_e64 %0, %2, %1, %3" : "=v"(r) : "v"(a), "v"(b), "s"(mask)); return r; }

DEVI void s1_attention(lptr shm, int w, int p_, int j_, char* ws PR_ARGS) {
  arrive(3);
  const int lane = lane_id();
  const int tau = (w + 1) * 512, taup = w >= 2 ? tau - 1024 : 0, flip = w == 1 ? 16 : 0;
  const LAS u32x4* t = (const LAS u32x4*)(shm + LDS_TAB) + lane;
  const u32x4 t0 = t[0], t2 = t[128];
  u32x4 t1 = t[64];
  if (p_ == 0 && w < 2) { t1[0] = 0u; t1[2] = 0xf149f2cau; t1[3] = 0xf149f2cau; }
  const f32x4 mO = __builtin_bit_cast(f32x4, t0), mP = __builtin_bit_cast(f32x4, t1);
  const unsigned M1 = t1[0], M2 = t1[1], M3 = t2[0];
  const int po = t2[1] + tau, pp = (t2[1] ^ flip) + taup, to = t2[2] + tau, tp = (t2[2] ^ flip) + taup;
  int aq[4], avo[4], avp[4];
#pragma unroll
  for (int h = 0; h < 4; h++) {
    aq[h] = sel(0xffffull << (16 * h), po, LDS_ZERO - 4 * 4608);
    avo[h] = sel(0x1111111111111111ull << h, to, LDS_ZERO - 14 * 4608);
    avp[h] = sel(0x1111111111111111ull << h, tp, LDS_ZERO - 14 * 4608);
    asm volatile("" : "+v"(aq[h]), "+v"(avo[h]), "+v"(avp[h]));
  }
  STAMPA(14);
  await<0, 8>(3);
  STAMPA(15);
  u32x2 qa[4][2], ko[4][2], kp[4][2], va[4][2], vb[4][2];
  f32x4 sp[4], so[4];
  auto rd = [&](auto H) {
    constexpr int h = decltype(H)::value;
    ds64<(5 + h) * 4608>(ko[h][0], po); ds64<9 * 4608>(ko[h][1], po); ds64<h * 4608>(qa[h][0], po); ds64<4 * 4608>(qa[h][1], aq[h]);
    ds64<(5 + h) * 4608>(kp[h][0], pp); ds64<9 * 4608>(kp[h][1], pp);
  };
  auto rv = [&](auto H) {
    constexpr int h = decltype(H)::value;
    dstr<(10 + h) * 4608>(va[h][0], tp); dstr<(10 + h) * 4608>(va[h][1], to); dstr<14 * 4608>(vb[h][0], avp[h]); dstr<14 * 4608>(vb[h][1], avo[h]);
  };
  auto qk = [&](auto H, auto N) {
    constexpr int h = decltype(H)::value;
    asm volatile("s_waitcnt lgkmcnt(%6)" : "+v"(ko[h][0]), "+v"(ko[h][1]), "+v"(qa[h][0]), "+v"(qa[h][1]), "+v"(kp[h][0]), "+v"(kp[h][1]) : "i"(decltype(N)::value));
    so[h] = mfma(cat(ko[h][0], ko[h][1]), cat(qa[h][0], qa[h][1]), mO);
    sp[h] = mfma(cat(kp[h][0], kp[h][1]), cat(qa[h][0], qa[h][1]), mP);
  };
  rd(IC(0)); rd(IC(1));
  qk(IC(0), IC(6)); rd(IC(2));
  qk(IC(1), IC(6)); rd(IC(3));
  qk(IC(2), IC(6)); rv(IC(0)); rv(IC(1));
  qk(IC(3), IC(8)); rv(IC(2));
  asm volatile("s_waitcnt lgkmcnt(8)");
  rv(IC(3));
  const float qs = 0.22360679774997896f * 1.4426950408889634f;
  float ee[4], eo[4], mx[4], l[4];
#pragma unroll
  for (int h = 0; h < 4; h++) { ee[h] = fmaxf(fmaxf(sp[h][2], so[h][0]), so[h][2]); eo[h] = fmaxf(fmaxf(sp[h][3], so[h][1]), so[h][3]); mx[h] = fmaxf(ee[h], eo[h]); }
  bcast4(red4<true>(mx) * -qs, mx);
#pragma unroll
  for (int h = 0; h < 4; h++) {
    f32x2 t = f32x2{ee[h], eo[h]} * qs + mx[h];
    ee[h] = __builtin_amdgcn_exp2f(t[0]); eo[h] = __builtin_amdgcn_exp2f(t[1]); l[h] = ee[h] + eo[h];
  }
  bcast4(__builtin_amdgcn_rcpf(red4<false>(l)), l);
  asm volatile("s_waitcnt lgkmcnt(0)" : "+v"(va[0][0]), "+v"(va[0][1]), "+v"(va[1][0]), "+v"(va[1][1]), "+v"(va[2][0]), "+v"(va[2][1]), "+v"(va[3][0]), "+v"(va[3][1]),
               "+v"(vb[0][0]), "+v"(vb[0][1]), "+v"(vb[1][0]), "+v"(vb[1][1]), "+v"(vb[2][0]), "+v"(vb[2][1]), "+v"(vb[3][0]), "+v"(vb[3][1]));
  f32x4 o4 = {0, 0, 0, 0}, oh[4];
#pragma unroll
  for (int h = 0; h < 4; h++) {
    const unsigned pk = pk2(f32x2{ee[h], eo[h]} * l[h]);
    u32x4 pf = {0u, pk & M1, pk & M2, pk & M3};
    o4 = mfma(cat(vb[h][0], vb[h][1]), pf, o4);
    oh[h] = mfma(cat(va[h][0], va[h][1]), pf, f32x4{0, 0, 0, 0});
  }
  const u32x4 ra = mkr(ws + WS_A2, A2_BYTES);
  const int base = ((8 * p_ + w) * KS_T + 54 + 5 * (j_ >> 1)) * 1024;
#pragma unroll
  for (int hp = 0; hp < 2; hp++)
    st_wt(ra, lane * 16, base + ((j_ & 1) * 3 + hp) * 1024,
          u32x4{pk2(oh[2 * hp][0], oh[2 * hp][1]), pk2(oh[2 * hp][2], oh[2 * hp][3]), pk2(oh[2 * hp + 1][0], oh[2 * hp + 1][1]), pk2(oh[2 * hp + 1][2], oh[2 * hp + 1][3])});
  unsigned a0 = pk2(o4[0], o4[1]), a1 = pk2(o4[2], o4[3]);
  auto s0 = __builtin_amdgcn_permlane32_swap(a0, a0, false, false);
  auto s1 = __builtin_amdgcn_permlane32_swap(a1, a1, false, false);
  st_wt(ra, lane < 32 ? lane * 16 : 0x70000000, base + 2 * 1024 + (j_ & 1) * 512, u32x4{s0[0], s1[0], s0[1], s1[1]});
  arrive(4);
}

DEVI int s2_perm(int s, bool o, int rot) { return o ? s ^ (s < 16 ? rot : rot & 1) : s < 8 ? s ^ rot : s < 10 ? s ^ (rot & 1) : ((s - 10) ^ rot) + 10; }
DEVI int fin_off(int G0, int n_, int r0, int lane) {
  const int frow = r0 + (lane >> 3), fG = G0 + (frow >> 4);
  return (32 * (fG >> 1) + 2 * sig(frow & 15) + (fG & 1)) * 1280 + n_ * 128 + (lane & 7) * 16;
}
constexpr int s2_slot(int r) { return r < 18 ? (r % 9) * 16384 : 147456 + (r - 18) * 4096; }
DEVI void watch(char* ws, int Ga, int Gb, int n_, int kind, int word, u64 fv) {
  const int lane = lane_id();
  GAS u64* fp = (GAS u64*)(ws + WS_BOX) + ((((lane & 8) ? Gb : Ga) >> 3) * 10 + n_) * 32 + 8 * kind + (lane & 7);
#pragma nounroll
  for (unsigned spins = 0; spins < SPIN_CAP; spins++) {
    if (__all(__hip_atomic_load(fp, __ATOMIC_RELAXED, __HIP_MEMORY_SCOPE_AGENT) == fv)) break;
    __builtin_amdgcn_s_sleep(1);
  }
  post(LDS_CNT + 4 * word, 1);
}

template <int NS, int MT>
DEVI void s2_compute(int w, int kq, int G0, int n_, int rot, char* ws, u64 fv PR_ARGS) {
  constexpr int AD = MT == 3 ? (NS == 20 ? AD3S : AD3Y) : (NS == 20 ? AD2S : AD2Y);
  const int mh = w >> 2, lane = lane_id();
  const u32x4 ra = mkr(ws + WS_A2, A2_BYTES);
  int av = lane * 16;
  pin(av);
  const int ao = ((G0 + MT * mh) * KS_T + (NS == 20 ? 54 : 18 * (kq - 1))) * 1024;
  int bs[4];
#pragma unroll
  for (int c = 0; c < 4; c++) bs[c] = kq * 4096 + (lane >> 2) * 128 + ((c ^ ((lane >> 3) & 3)) << 5) + 8 * (lane & 3);
  f32x4 acc[MT][4];
#pragma unroll
  for (int m = 0; m < MT; m++)
#pragma unroll
    for (int c = 0; c < 4; c++) { acc[m][c] = f32x4{0, 0, 0, 0}; pin(acc[m][c]); }
  u32x4 a[AD][MT];
  u32x2 b[3][4][2];
  int ca = LDS_CONS + kq * 8 + mh * 4, pa = LDS_PROD + kq * 4;
  pin(ca); pin(pa);
  unsigned pv, lv;
  auto rdb = [&](auto R, auto K) {
    constexpr int r = decltype(R)::value, k = decltype(K)::value;
    constexpr int off = s2_slot(r), nxt = s2_slot(r + 1) / 65536 - off / 65536;
    if constexpr (r < 3) dstr<off % 65536 + (k & 1) * 2048>(b[r % 3][k >> 1][k & 1], bs[k >> 1]);
    else redstr<off % 65536 + (k & 1) * 2048>(b[r % 3][k >> 1][k & 1], bs[k >> 1]);
    if constexpr (nxt != 0 && (k & 1)) vinc(bs[k >> 1], nxt * 65536);
  };
  auto lda = [&](auto S, int m) {
    constexpr int s = decltype(S)::value;
    const int o = ao + m * KS_T * 1024 + (s2_perm(s, NS == 20, rot) << 10);
    if constexpr (s < AD) bload_sc1(a[s % AD][m], ra, av, o); else reload_sc1(a[s % AD][m], ra, av, o);
  };
  auto ldb = [&]() {
    peek(pv, pa); wait_lg<0>(); require(pv, pa, 1);
    sfor<3>([&](auto R) { sfor<8>([&](auto K) { rdb(R, K); }); wait_lg<(decltype(R)::value < 2 ? 4 : 0)>(); });
    post(ca, 1);
  };
  constexpr bool BFIRST = NS == 20 || MT == 3;
  if (BFIRST) ldb();
  STAMP(6);
  if (NS == 20) watch(ws, G0, G0 + 2 * MT - 1, n_, w == 0 ? 0 : 2, w == 0 ? 14 : 5, fv);
  await<0, 1>(NS == 20 ? 14 : 15);
  STAMP(7);
  sfor<AD - 1>([&](auto S) {
#pragma unroll
    for (int m = 0; m < MT; m++) lda(S, m);
  });
  if (!BFIRST) ldb();
  if (NS == 20) __builtin_amdgcn_s_setprio(SPRIO);
  sfor<NS - 3>([&](auto S) {
    constexpr int s = decltype(S)::value;
    constexpr bool LA = s + AD - 1 < NS;
    constexpr int later = NS - 1 - s < AD - 2 ? NS - 1 - s : AD - 2;
    if constexpr (s == 3) require(pv, pa, 2);
    if constexpr (s == 7) require(pv, pa, 3);
    if constexpr (s == 10) require(pv, pa, 4);
    if constexpr (s == 13) require(pv, pa, 5);
    if constexpr (s == 2 || s == 6 || s == 9 || s == 12) peek(pv, pa);
    if constexpr (NS == 18 && s == 11 - AD) require(lv, LDS_CNT + 4 * 5, 1);
    if constexpr (NS == 18 && s == 10 - AD) peek(lv, LDS_CNT + 4 * 5);
    if constexpr (s == NS - 4) peek(lv, ca ^ 4);
    sfor<MT>([&](auto M) {
      constexpr int m = decltype(M)::value;
      if constexpr (LA) lda(IC(s + AD - 1), m);
      wait_vm<MT * later + MT - 1 - m + (LA ? m + 1 : 0)>();
      if constexpr (m == 1 && s >= 2) wait_lg<4>();
      sfor<4>([&](auto C) {
        constexpr int c = decltype(C)::value;
        mfma_a(acc[m][c], cat(b[s % 3][c][0], b[s % 3][c][1]), a[s % AD][m]);
        if constexpr (s >= 1 && m < 2) rdb(IC(s + 2), IC(4 * m + c));
      });
    });
    if constexpr (s == 3 || s == 6 || s == 9 || s == 12) post(ca, s);
  });
  wait_lg<4>();
  sfor<8>([&](auto K) { rdb(IC(NS - 1), K); });
  if constexpr (AD == 3) {
#pragma unroll
    for (int m = 0; m < MT; m++) lda(IC(NS - 1), m);
  }
  int da[4];
#pragma unroll
  for (int c = 0; c < 4; c++) da[c] = mh * 49152 + kq * 4096 + (av & 0xf0) * 16 + ((((4 * c) << 4) + (av >> 8 << 4)) ^ (av & 0x70));
  require(lv, ca ^ 4, mh ? 12 : 9);
  sfor<MT>([&](auto M) {
    constexpr int m = decltype(M)::value;
    sfor<3>([&](auto U) {
      constexpr int u = decltype(U)::value, s = NS - 3 + u;
      if constexpr (m == 0) wait_vm<MT * (2 - u) + MT - 1>();
      if constexpr (m > 0 && u == 2) wait_vm<MT - 1 - m>();
      if constexpr (m == 0 && u == 1) wait_lg<8>();
      if constexpr (m == 0 && u == 2) wait_lg<0>();
      sfor<4>([&](auto C) {
        constexpr int c = decltype(C)::value;
        mfma_a(acc[m][c], cat(b[s % 3][c][0], b[s % 3][c][1]), a[s % AD][m]);
        if constexpr (m > 0 && u > 0 && (c & 1)) dsw128<(m - 1) * 16384>(da[2 * (u - 1) + (c >> 1)], __builtin_bit_cast(u32x4, acc[m - 1][2 * (u - 1) + (c >> 1)]));
      });
    });
  });
  STAMP(9);
  asm volatile("s_nop 4");
#pragma unroll
  for (int c = 0; c < 4; c++) dsw128<(MT - 1) * 16384>(da[c], __builtin_bit_cast(u32x4, acc[MT - 1][c]));
  arrive((NS == 20 ? 28 : 12) + mh);
}

__global__ __launch_bounds__(768, 3) void fused_block(const void* __restrict__ x, const void* __restrict__ wqkv, const void* __restrict__ wo,
                                                      const void* __restrict__ w1, const unsigned short* __restrict__ b1,
                                                      const void* __restrict__ w2, const char* __restrict__ b2, char* __restrict__ out,
                                                      char* ws) {
  extern __shared__ __attribute__((aligned(16))) char shm_[];
  lptr shm = (lptr)shm_;
  const int tid = threadIdx.x, lane = tid & 63, w = __builtin_amdgcn_readfirstlane(tid >> 6);
  const int xcd = blockIdx.x & 7, idx = blockIdx.x >> 3;
  const int p_ = (xcd & 1) * 8 + (idx & 7), sub = idx >> 3;
  const bool att = !(sub & 1);
  const int j_ = (xcd >> 1) * 2 + (sub >> 1);
  const int uidx = (idx & 7) + 8 * (idx >> 4);
  const int u5 = uidx >= 11;
  const int nl = att ? (uidx < 6 ? uidx >> 1 : 3 + u5) : (uidx < 12 ? uidx >> 2 : 3 + ((uidx - 12) >> 1));
  const int G0 = 32 * (xcd >> 1) + (att ? (uidx < 6 ? 24 + 4 * (uidx & 1) : 12 + 4 * (uidx - 6 - 5 * u5)) : 6 * (uidx < 12 ? uidx & 3 : uidx & 1));
  const int n_ = (xcd & 1) * 5 + nl;
  const int kq = w < 8 ? (w + (w >> 2)) & 3 : w & 3;
  const int rot = (ROT2);
  STAMP(0);
  if (w == 7) {
    u64 pc; asm volatile("s_getpc_b64 %0" : "=s"(pc));
    touch(mkr((const void*)(pc & ~255ull), CODE_BYTES), (idx + 32 * lane) * 128);
  }
  asm volatile("" : "+s"(x), "+s"(wqkv), "+s"(wo), "+s"(w1), "+s"(b1), "+s"(w2), "+s"(b2), "+s"(out), "+s"(ws));
  const int hf = idx & 1, tx = KTX;
  const int rid = (att ? 0 : 8) + j_;
  int fu;
  if (__builtin_expect(w >= 8, 1)) {
    __builtin_amdgcn_s_setprio(3);
    const int lw = w - 8;
    auto ktile = [&](int T) { return T ^ (T < 8 ? tx : tx & 1); };
    const int RS = att ? 3840 : 3456;
    const u32x4 rx = mkr(x, S_ * D_ * 2), rw1 = mkr(att ? wqkv : w1, D_ * RS);
    const int r8 = lane >> 3, cp = lane & 7, t0 = p_ * 128;
    int xo[2], hv, wv;
#pragma unroll
    for (int e = 0; e < 2; e++)
      xo[e] = (t0 + 32 * lw + 2 * (4 * (r8 & 3) + 2 * e + (r8 >> 2))) * 1280 + ((cp ^ (4 * e + (r8 >> 1))) << 4);
    auto ldx = [&](int T) { const int o = ktile(T) * 128; dma4<1024>(rx, ~0ull, (T % 3) * SLOT + XOFF + 2048 + 4 * lw * 1024, xo[0], xo[1], o, o, o + 1280, o + 1280); };
    STAMP(12);
    ldx(0);
    {
      const int hi = lane >= 30, ch = lane - 30 * hi, lim = j_ < 7 ? 27 : 19;
      wv = hi * RS + (att ? ((ch >= 10) + (ch >= 20)) * 1120 + j_ * 160 + ch * 16 : j_ * 448 + (ch < lim ? ch : lim) * 16);
    }
    const int wrow = (16 * (lw & 1) + 4 * (lw >> 1)) * RS;
    auto ldw = [&](int T, int h) {
      const int so = (64 * ktile(T) + 32 * (h ^ hf)) * RS + wrow;
      dma4<960>(rw1, 0x0fffffffffffffffull, (T % 3) * SLOT + h * WSLAB + 4 * lw * 960, wv, wv, so, so + 2 * RS, so + 8 * RS, so + 10 * RS);
    };
    ldw(0, 0);
    {
      int tok = t0 - 16 + 8 * (r8 & 1) + 2 * lw + ((r8 & 3) < 2);
      tok = tok < 0 ? 0 : tok;
      hv = att ? tok * 1280 + ((cp ^ ((2 * lw + ((r8 & 3) >> 1)) & 7)) << 4) : 0x70000000;
    }
    const u64 hmask = lw == 0 ? 0xffffff00ull : 0xffffffffull;
    auto ldh = [&](int T) { dma1(rx, hmask, (T % 3) * SLOT + XOFF + lw * 512, hv, ktile(T) * 128); };
    ldh(0); ldw(0, 1);
    STAMP(13);
    bar_vm<4>();
    STAMP(14);
    ldx(1); ldh(1);
    bar_vm<5>();
    ldw(1, 0); ldw(1, 1);
    ldx(2); ldh(2);
    bar_vm<5>();
    const int srow = p_ * 128 + 8 * rid + 2 * lw + (lane >> 5);
    u32x2 xr[5];
    unsigned ep, b1v;
    setup_loads(xr, ep, b1v, rx, srow * 1280 + (lane & 31) * 8, mkr(ws, 4), mkr(b1, F_ * 2), (j_ * 224 + 64 * lw + lane) * 2);
    touch_sc1(mkr(ws + WS_BOX, lw == 0 ? 160 * 256 : 0), (((G0 >> 3) + (lane & 1)) * 10 + n_) * 256 + (lane & 2) * 64);
    ldw(2, 0); ldw(2, 1); ldx(3); ldh(3); ldw(3, 0);
    bar_vm<9>();
    pin(ep); pin(b1v);
#pragma unroll
    for (int k = 0; k < 5; k++) pin(xr[k]);
    const unsigned tag = __builtin_amdgcn_readfirstlane(ep) + 1u;
    ldw(3, 1); ldx(4); ldw(4, 0); ldh(4); ldw(4, 1);
    {
      float s = 0;
#pragma unroll
      for (int k = 0; k < 5; k++) s += (blo(xr[k][0]) + bhi(xr[k][0])) + (blo(xr[k][1]) + bhi(xr[k][1]));
      const float m = half_sum(s) * (1.f / D_);
      float q = 0;
#pragma unroll
      for (int k = 0; k < 10; k++) { const unsigned u = xr[k >> 1][k & 1]; const float a = blo(u) - m, b = bhi(u) - m; q = __builtin_fmaf(a, a, q); q = __builtin_fmaf(b, b, q); }
      const float rs = __builtin_amdgcn_rsqf(half_sum(q) * (1.f / D_) + 1e-5f);
      st_wt(mkr(ws + WS_STAT, S_ * 16), (lane & 31) == 0 ? srow * 16 : 0x70000000, 0, u32x4{asu(rs), tag, asu(-m * rs), tag});
      *(LAS float*)(shm + LDS_B1 + (64 * lw + lane) * 4) = blo(b1v);
      *(LAS unsigned*)(shm + LDS_CNT + 4) = tag;
    }
    bar_vm<14>();
    const u32x4 rw = mkr(kq == 0 ? wo : w2, kq == 0 ? D_ * 1280 : F_ * 1280);
    const int cbyte = n_ * 128 + ((cp ^ ((lane >> 4) << 1)) << 4);
    const int v1 = r8 * 1280 + cbyte, v2 = kq == 0 ? (20 * (r8 >> 2) + (r8 & 3)) * 1280 + cbyte : v1;
    int gtab;
    {
      const int so = s2_perm(lane, true, rot), P = (so * 13) >> 6, kp = so - 5 * P;
      const int r0 = 160 * P + (kp == 2 ? 16 : 40 * (kp - (kp > 2)));
      const int t = 3 * s2_perm(lane, false, rot) + kq - 1, u = t - 32;
      const int r1 = 32 * (t < 32 ? 7 * (t >> 2) + (t & 3) : 4 * ((u * 43) >> 7) + 4 + u);
      gtab = kq == 0 ? r0 * 1280 + (kp == 2) : r1 * 1280;
      pin(gtab);
    }
    const int sbn = 1280 * (kq == 0 ? 20 : 16);
    auto geom = [&](int s, int& rb, int& sa, int& sb, int& v) {
      const int e = __builtin_amdgcn_readlane(gtab, s), f = e & 1;
      v = v1 + ((v2 - v1) & -f);
      rb = e - f;
      sa = f ? 1280 * 80 : 1280 * 8;
      sb = f ? 1280 * 40 : sbn;
    };
    u32x4 pre[9][4];
    auto ldr = [&](auto R) {
      constexpr int r = decltype(R)::value;
      int rb, sa, sb, v; geom(9 + r, rb, sa, sb, v);
      bload(pre[r][0], rw, v, rb); bload(pre[r][1], rw, v, rb + sa); bload(pre[r][2], rw, v, rb + sb); bload(pre[r][3], rw, v, rb + sa + sb);
    };
    auto ldl = [&](auto S) {
      constexpr int s = decltype(S)::value;
      int rb, sa, sb, v; geom(s, rb, sa, sb, v);
      dma4<1024>(rw, ~0ull, s < 9 ? s * 16384 + kq * 4096 : 147456 + (s - 18) * 4096, v, v, rb, rb + sa, rb + sb, rb + sa + sb);
    };
    const u32x4 rst = mkr(ws + WS_STAT, S_ * 16);
    const int spos = 36 * lw + (lane < 36 ? lane : 35), sv = row_token(p_, spos) * 16;
    u32x4 sg;
    sfor<5>([&](auto TT) {
      constexpr int T = decltype(TT)::value + 5;
      if (T == 9) bload_sc1(sg, rst, sv, 0);
      if (T == 6) { STAMP(15); }
      ldx(T); ldw(T, 0); ldh(T); ldw(T, 1);
      bar_vm<13>();
    });
    STAMP(1);
    asm volatile("" : "+v"(sg));
#pragma nounroll
    for (unsigned spins = 0; !__all(sg[1] == tag && sg[3] == tag) && spins < SPIN_CAP; spins++) {
      __builtin_amdgcn_s_sleep(2);
      bload_sc1(sg, rst, sv, 0);
      asm volatile("s_waitcnt vmcnt(0)" : "+v"(sg) :: "memory");
    }
    *(LAS u32x2*)(shm + LDS_ST + spos * 8) = u32x2{sg[0], sg[2]};
    ldl(IC(3));
    wait_vm<4>();
    STAMP(2);
    bar_lgkm();
    __builtin_amdgcn_s_setprio(0);
    STAMP(3);
    const int pa = LDS_PROD + kq * 4, wa = kq * 4096 + lane * 16;
    ldl(IC(4));
    if (!att) { ldl(IC(5)); ldl(IC(6)); }
    ldr(IC(0)); ldr(IC(1));
    STAMP(4);
    await<1, 8>(2);
    ldl(IC(0)); wait_vm<THR>(); ldl(IC(1)); wait_vm<THR>(); ldl(IC(2)); wait_vm<THR>();
    if (!att) { ldl(IC(7)); wait_vm<THR>(); ldl(IC(8)); wait_vm<THR>(); if (kq == 0) { ldl(IC(18)); wait_vm<THR>(); ldl(IC(19)); } }
    ldr(IC(2)); wait_vm<4>();
    post(pa, att ? 1 : 2);
    STAMP(5);
    if (!att) __builtin_amdgcn_s_setprio(LPM);
    sfor<6>([&](auto R) { ldr(IC(decltype(R)::value + 3)); wait_vm<THR>(); });
    if (att) {
      await<1, 8>(4);
      __builtin_amdgcn_s_setprio(LPA);
      ldl(IC(5)); ldl(IC(6)); ldl(IC(7)); ldl(IC(8));
      if (kq == 0) { ldl(IC(18)); ldl(IC(19)); }
    }
    if (lw == 3) touch(mkr(b2, D_ * 2), n_ * 128);
    wait_vm<0>();
    post(pa, 2);
#define PUT(r, off) dsw128<off>(wa + (r / 3) * 49152, pre[r][0]); dsw128<off + 1024>(wa + (r / 3) * 49152, pre[r][1]); dsw128<off + 2048>(wa + (r / 3) * 49152, pre[r][2]); dsw128<off + 3072>(wa + (r / 3) * 49152, pre[r][3]);
    await_pair(LDS_CONS + kq * 8, 1);
    PUT(0, 0) PUT(1, 16384) PUT(2, 32768)
    post(pa, 3);
    __builtin_amdgcn_s_setprio(LPP);
    STAMP(6);
    await_pair(LDS_CONS + kq * 8, 3);
    PUT(3, 0) PUT(4, 16384) PUT(5, 32768)
    post(pa, 4);
    await_pair(LDS_CONS + kq * 8, 6);
    PUT(6, 0) PUT(7, 16384) PUT(8, 32768)
    post(pa, 5);
    fu = lw;
  } else {
    if (tid < 24) *(LAS u32x4*)(shm + (tid >> 3) * SLOT + XOFF + (tid & 7) * 16) = u32x4{0x3f803f80u, 0x3f803f80u, 0x3f803f80u, 0x3f803f80u};
    if (tid >= 448 && tid < 480) *(LAS unsigned*)(shm + LDS_CNT + 4 * (tid - 448)) = 0u;
    if (w == 5 && att) att_table(shm, lane);
    const bool light = att ? (w >= 6) : (w >= 4);
    if (light) s1_compute<3>(shm, att, w, lane, p_, j_, hf, ws PR_PASS);
    else s1_compute<4>(shm, att, w, lane, p_, j_, hf, ws PR_PASS);
    STAMP(3);
    if (att) s1_attention(shm, w, p_, j_, ws PR_PASS);
    STAMP(4);
    const unsigned tag = __builtin_amdgcn_readfirstlane(*(LAS unsigned*)(shm + LDS_CNT + 4));
    GAS u64* flags = (GAS u64*)(ws + WS_FLAG);
    const u64 fv = flagval(tag);
    wait_vm<0>();
    {
      const int lane = lane_id(), kind = att ? 0 : 1 + (w >> 2);
      unsigned old = 0;
      if (lane == 0) old = __hip_atomic_fetch_add((LAS unsigned*)(shm + LDS_CNT + (kind == 1 ? 120 : 0)), 1u, __ATOMIC_RELAXED, __HIP_MEMORY_SCOPE_WORKGROUP);
      if (__builtin_amdgcn_readfirstlane(old) == (att ? 7u : 3u)) {
        GAS u64* dst = lane == 10 ? flags + p_ * 16 + rid : (GAS u64*)(ws + WS_BOX) + (p_ * 10 + lane) * 32 + 8 * kind + j_;
        if (lane <= 10) __hip_atomic_store(dst, fv, __ATOMIC_RELAXED, __HIP_MEMORY_SCOPE_AGENT);
      }
    }
    STAMP(5);
    if (w == 0) watch(ws, G0, G0 + (att ? 3 : 5), n_, 1, 15, fv);
    if (kq == 0) {
      if (att) s2_compute<20, 2>(w, kq, G0, n_, rot, ws, fv PR_PASS);
      else s2_compute<20, 3>(w, kq, G0, n_, rot, ws, fv PR_PASS);
    } else {
      if (att) s2_compute<18, 2>(w, kq, G0, n_, rot, ws, fv PR_PASS);
      else s2_compute<18, 3>(w, kq, G0, n_, rot, ws, fv PR_PASS);
    }
    if (blockIdx.x == 0 && w == 3) {
      const int lane = lane_id();
#pragma nounroll
      for (unsigned spins = 0; spins < SPIN_CAP; spins++) {
        bool ok = true;
#pragma unroll
        for (int k = 0; k < 4; k++) ok &= __hip_atomic_load(flags + lane + 64 * k, __ATOMIC_RELAXED, __HIP_MEMORY_SCOPE_AGENT) == fv;
        if (__all(ok)) break;
        __builtin_amdgcn_s_sleep(8);
      }
      if (lane == 0) __hip_atomic_store((GAS unsigned*)ws, tag, __ATOMIC_RELAXED, __HIP_MEMORY_SCOPE_AGENT);
    }
    if (att || w == 0 || w > 2) { STAMP(11); FINISH(); return; }
    fu = w + 3;
  }
  {
    const int lane = lane_id();
    const u32x4 bv = *(const GAS u32x4*)(b2 + n_ * 128 + (lane & 7) * 16);
    int foff[2], pr0[2];
    u32x4 xv[2];
    f32x2 gt[2][4];
#pragma unroll
    for (int hh = 0; hh < 2; hh++) {
      const int frow = 48 * hh + 8 * fu + (lane >> 3);
      foff[hh] = fin_off(G0 + (att ? 2 : 3) * hh, n_, 8 * fu, lane);
      xv[hh] = *(const GAS u32x4*)((const char*)x + foff[hh]);
      pr0[hh] = (frow >> 4) * 16384 + (frow & 15) * 256 + (((2 * (lane & 7)) ^ (frow & 7)) << 4);
      pin(pr0[hh]);
    }
#pragma unroll
    for (int hh = 0; hh < 2; hh++) {
      await<1, 3>(12 + hh);
      f32x4 y[2];
#pragma unroll
      for (int e = 0; e < 2; e++) {
        lptr pr = shm + (pr0[hh] ^ (16 * e));
        y[e] = *(LAS f32x4*)(pr + 4096) + *(LAS f32x4*)(pr + 8192) + *(LAS f32x4*)(pr + 12288);
      }
#pragma unroll
      for (int k = 0; k < 4; k++) {
        f32x2 yy = f32x2{y[k >> 1][2 * (k & 1)], y[k >> 1][2 * (k & 1) + 1]} + f32x2{blo(bv[k]), bhi(bv[k])};
        f32x2 t = yy * -1.4426950408889634f;
        f32x2 e = f32x2{__builtin_amdgcn_exp2f(t[0]), __builtin_amdgcn_exp2f(t[1])} + 1.f;
        gt[hh][k] = yy * f32x2{__builtin_amdgcn_rcpf(e[0]), __builtin_amdgcn_rcpf(e[1])};
        pin(gt[hh][k]);
      }
    }
    STAMP(10);
#pragma unroll
    for (int hh = 0; hh < 2; hh++) {
      await<0, 1>(28 + hh);
      u32x4 o;
#pragma unroll
      for (int e = 0; e < 2; e++) {
        const f32x4 s = *(LAS f32x4*)(shm + (pr0[hh] ^ (16 * e)));
        o[2 * e] = pk2(f32x2{s[0], s[1]} * gt[hh][2 * e] + f32x2{blo(xv[hh][2 * e]), bhi(xv[hh][2 * e])});
        o[2 * e + 1] = pk2(f32x2{s[2], s[3]} * gt[hh][2 * e + 1] + f32x2{blo(xv[hh][2 * e + 1]), bhi(xv[hh][2 * e + 1])});
      }
      __builtin_nontemporal_store(o, (GAS u32x4*)(out + foff[hh]));
    }
  }
  STAMP(11);
  FINISH();
}

extern "C" void kernel_launch(void* const* d_in, const int* in_sizes, int n_in, void* d_out, int out_size, void* d_ws,
                              size_t ws_size, hipStream_t stream) {
  (void)hipFuncSetAttribute((const void*)fused_block, hipFuncAttributeMaxDynamicSharedMemorySize, LDS_TOTAL);
  fused_block<<<256, 768, LDS_TOTAL, stream>>>(d_in[0], d_in[1], d_in[2], d_in[4], (const unsigned short*)d_in[5], d_in[6],
                                               (const char*)d_in[7], (char*)d_out, (char*)d_ws);
}
```

```cpp
#define WARMH 8
#include <hip/hip_runtime.h>
#include <hip/hip_bf16.h>
#include <type_traits>
#include <utility>

typedef __attribute__((ext_vector_type(4))) short s16x4;
typedef __attribute__((ext_vector_type(8))) __bf16 bf16x8;
typedef __attribute__((ext_vector_type(2))) __bf16 bf16x2;
typedef __attribute__((ext_vector_type(4))) float f32x4;
typedef __attribute__((ext_vector_type(2))) float f32x2;
typedef __attribute__((ext_vector_type(4))) unsigned u32x4;
typedef __attribute__((ext_vector_type(2))) unsigned u32x2;
#define LAS __attribute__((address_space(3)))
#define GAS __attribute__((address_space(1)))
#define DEVI __device__ __forceinline__
typedef LAS char* lptr;
typedef unsigned long long u64;
#define IC(n) std::integral_constant<int, n>{}

constexpr int S_ = 2048, D_ = 640, F_ = 1728;
constexpr int KS_T = 74;
constexpr int WSLAB = 15360;
constexpr int XOFF = 2 * WSLAB;
constexpr int SLOT = 49152;
constexpr int LDS_TB = 86016;
constexpr int LDS_ST = 155648;
constexpr int LDS_CS = LDS_ST + 1152;
constexpr int LDS_B1 = LDS_CS + 1024;
constexpr int LDS_CNT = LDS_B1 + 1024;
constexpr int LDS_ZERO = LDS_CNT + 24;
constexpr int LDS_CONS = LDS_CNT + 64;
constexpr int LDS_PROD = LDS_CONS + 32;
constexpr int LDS_TAB = LDS_CNT + 128;
constexpr int LDS_TOTAL = 163840;
constexpr int WS_FLAG = 4096, WS_STAT = 8192, WS_BOX = 65536, WS_A2 = 1 << 20;
constexpr int A2_BYTES = 128 * KS_T * 1024;
#define AD3Y 3
#define AD3S 4
#define AD2Y 4
#define AD2S 5
#define ALN 0
#define YPRIO 0
#define DEXTRA 0
#define LATESET 0
#define L1PRIO 3
#define HOLD 0
#define THRA 32
#define MAP2 0
#define ORD 1
#define PRE9 0
#define LP1 0
#define INL0 5
#define INLR 2
#define INLN 0
#define WMLP 0
#define WNE 1
#define WSTEP 2
#define TLBW 0
#define LPM 3
#define LPA 3
#define LPP 0
#define SPRIO 0
#define THR 32
#define KTX (((idx >> 1) ^ sub) & 1)
#define ROT2 (((ug + nl) & 3) | (nl & 4))
constexpr int CODE_BYTES = 46464;
constexpr unsigned SPIN_CAP = 1u << 20;

#define STAMP(idx)
#define FINISH()
#define STAMPC(k)
#define STAMPA(k) STAMP(k)
#define STAMPB(k)
#define PR_ARGS
#define PR_PASS

DEVI void bar_lgkm() { asm volatile("s_waitcnt lgkmcnt(0)\n\ts_barrier" ::: "memory"); }
DEVI void bar() { asm volatile("s_barrier" ::: "memory"); }
template <int N> DEVI void bar_vm() { asm volatile("s_waitcnt vmcnt(%0)\n\ts_barrier" ::"i"(N) : "memory"); }
template <int N> DEVI void wait_vm() { asm volatile("s_waitcnt vmcnt(%0)" ::"i"(N) : "memory"); }
DEVI unsigned pk2(float a, float b) { f32x2 v = {a, b}; return __builtin_bit_cast(unsigned, __builtin_convertvector(v, bf16x2)); }
DEVI unsigned pk2(f32x2 v) { return __builtin_bit_cast(unsigned, __builtin_convertvector(v, bf16x2)); }
DEVI float asf(unsigned u) { return __builtin_bit_cast(float, u); }
DEVI unsigned asu(float f) { return __builtin_bit_cast(unsigned, f); }
DEVI float blo(unsigned u) { return __builtin_bit_cast(float, u << 16); }
DEVI float bhi(unsigned u) { return __builtin_bit_cast(float, u & 0xffff0000u); }
DEVI u32x2 tr_read(lptr p) { return __builtin_bit_cast(u32x2, __builtin_amdgcn_ds_read_tr16_b64_v4i16((LAS s16x4*)p)); }
DEVI f32x4 mfma(u32x4 a, u32x4 b, f32x4 c) {
  return __builtin_amdgcn_mfma_f32_16x16x32_bf16(__builtin_bit_cast(bf16x8, a), __builtin_bit_cast(bf16x8, b), c, 0, 0, 0);
}
DEVI u32x4 cat(u32x2 a, u32x2 b) { return __builtin_shufflevector(a, b, 0, 1, 2, 3); }
DEVI void mfma_a(f32x4& c, u32x4 a, u32x4 b) { asm volatile("v_mfma_f32_16x16x32_bf16 %0, %1, %2, %0" : "+v"(c) : "v"(a), "v"(b)); }
template <int OFF> DEVI void ds128(u32x4& d, int addr) { asm volatile("ds_read_b128 %0, %1 offset:%2" : "=v"(d) : "v"(addr), "i"(OFF)); }
template <int OFF> DEVI void dstr(u32x2& d, int addr) { asm volatile("ds_read_b64_tr_b16 %0, %1 offset:%2" : "=v"(d) : "v"(addr), "i"(OFF)); }
template <int OFF> DEVI void dsw128(int addr, u32x4 d) { asm volatile("ds_write_b128 %0, %1 offset:%2" ::"v"(addr), "v"(d), "i"(OFF) : "memory"); }
DEVI u32x4 mkr(const void* p, unsigned bytes) {
  u64 a = (u64)p;
  return u32x4{(unsigned)a, (unsigned)(a >> 32) & 0xffffu, bytes, 0x00020000u};
}
DEVI void st_wt(u32x4 r, int voff, int soff, u32x4 v) {
  asm volatile("buffer_store_dwordx4 %0, %1, %2, %3 offen sc1\n\ts_nop 1" ::"v"(v), "v"(voff), "s"(r), "s"(soff) : "memory");
}
template <int LSTEP>
DEVI void dma4(u32x4 r, u64 mask, int l, int va, int vb, int s0, int s1, int s2, int s3) {
  asm volatile(
      "s_mov_b64 exec, %0\n\t"
      "s_mov_b32 m0, %1\n\ts_nop 0\n\tbuffer_load_dwordx4 %2, %4, %5 offen lds\n\t"
      "s_add_u32 m0, %1, %9\n\ts_nop 0\n\tbuffer_load_dwordx4 %3, %4, %6 offen lds\n\t"
      "s_add_u32 m0, %1, %10\n\ts_nop 0\n\tbuffer_load_dwordx4 %2, %4, %7 offen lds\n\t"
      "s_add_u32 m0, %1, %11\n\ts_nop 0\n\tbuffer_load_dwordx4 %3, %4, %8 offen lds\n\t"
      "s_mov_b64 exec, -1"
      :: "s"(mask), "s"(l), "v"(va), "v"(vb), "s"(r), "s"(s0), "s"(s1), "s"(s2), "s"(s3), "i"(LSTEP), "i"(2 * LSTEP), "i"(3 * LSTEP)
      : "memory", "scc");
}
DEVI void dma1(u32x4 r, u64 mask, int l, int v, int s0) {
  asm volatile("s_mov_b64 exec, %0\n\ts_mov_b32 m0, %1\n\ts_nop 0\n\tbuffer_load_dwordx4 %2, %3, %4 offen lds\n\ts_mov_b64 exec, -1"
               :: "s"(mask), "s"(l), "v"(v), "s"(r), "s"(s0) : "memory");
}
DEVI void setup_loads(u32x2 (&xr)[5], unsigned& ep, unsigned& bv, u32x4 rx, int vx, u32x4 re, u32x4 rb, int vb) {
  asm volatile("buffer_load_dwordx2 %0, %7, %8, 0 offen\n\tbuffer_load_dwordx2 %1, %7, %8, 0 offen offset:256\n\tbuffer_load_dwordx2 %2, %7, %8, 0 offen offset:512\n\t"
               "buffer_load_dwordx2 %3, %7, %8, 0 offen offset:768\n\tbuffer_load_dwordx2 %4, %7, %8, 0 offen offset:1024\n\t"
               "buffer_load_dword %5, off, %9, 0 sc1\n\tbuffer_load_ushort %6, %11, %10, 0 offen"
               : "=&v"(xr[0]), "=&v"(xr[1]), "=&v"(xr[2]), "=&v"(xr[3]), "=&v"(xr[4]), "=&v"(ep), "=&v"(bv)
               : "v"(vx), "s"(rx), "s"(re), "s"(rb), "v"(vb) : "memory");
}
template <int WHEN>
DEVI void bload4_if(u32x4 (&d)[4], u32x4 r, int v, int s0, int s1, int s2, int s3, int flag) {
#define LDR_BODY "s_cmp_lg_u32 %10, %11\n\ts_cbranch_scc1 .Lr%=\n\tbuffer_load_dwordx4 %0, %4, %5, %6 offen\n\tbuffer_load_dwordx4 %1, %4, %5, %7 offen\n\t" \
                 "buffer_load_dwordx4 %2, %4, %5, %8 offen\n\tbuffer_load_dwordx4 %3, %4, %5, %9 offen\n.Lr%=:"
  if (WHEN) asm volatile(LDR_BODY : "+v"(d[0]), "+v"(d[1]), "+v"(d[2]), "+v"(d[3]) : "v"(v), "s"(r), "s"(s0), "s"(s1), "s"(s2), "s"(s3), "s"(flag), "i"(WHEN) : "scc");
  else asm volatile(LDR_BODY : "=&v"(d[0]), "=&v"(d[1]), "=&v"(d[2]), "=&v"(d[3]) : "v"(v), "s"(r), "s"(s0), "s"(s1), "s"(s2), "s"(s3), "s"(flag), "i"(WHEN) : "scc");
}
template <int N0, int N1>
DEVI void bar_vm_sel(int flag) {
  asm volatile("s_cmp_lg_u32 %0, 0\n\ts_cbranch_scc1 .La%=\n\ts_waitcnt vmcnt(%1)\n\ts_branch .Lb%=\n.La%=:\n\ts_waitcnt vmcnt(%2)\n.Lb%=:\n\ts_barrier"
               ::"s"(flag), "i"(N0), "i"(N1) : "memory", "scc");
}
template <class T> DEVI void pin(T& v) { asm volatile("" : "+v"(v)); }
DEVI void dsw_col0(int a0, f32x4 d0, int a1, f32x4 d1, int a2, f32x4 d2) {
  asm volatile("s_mov_b64 exec, %6\n\tds_write_b128 %0, %1\n\tds_write_b128 %2, %3\n\tds_write_b128 %4, %5\n\ts_mov_b64 exec, -1"
               ::"v"(a0), "v"(d0), "v"(a1), "v"(d1), "v"(a2), "v"(d2), "s"(0x0001000100010001ull) : "memory");
}
DEVI void reload_sc1(u32x4& d, u32x4 r, int v, int s0) { asm volatile("buffer_load_dwordx4 %0, %1, %2, %3 offen sc1" : "+v"(d) : "v"(v), "s"(r), "s"(s0) : "memory"); }
template <int OFF> DEVI void redstr(u32x2& d, int addr) { asm volatile("ds_read_b64_tr_b16 %0, %1 offset:%2" : "+v"(d) : "v"(addr), "i"(OFF)); }
DEVI void vinc(int& d, int s) { asm volatile("v_add_u32 %0, %1, %0" : "+v"(d) : "s"(s)); }
DEVI void vadd(int& d, int a, int s) { asm volatile("v_add_u32 %0, %2, %1" : "=v"(d) : "v"(a), "s"(s)); }
template <class F, int... I> DEVI void sfor_(F f, std::integer_sequence<int, I...>) { (f(std::integral_constant<int, I>{}), ...); }
template <int N, class F> DEVI void sfor(F f) { sfor_(f, std::make_integer_sequence<int, N>{}); }
DEVI void stouch1(unsigned& sink, const void* p, int off) { asm volatile("s_load_dword %0, %1, %2" : "+s"(sink) : "s"(p), "s"(off)); }
DEVI void touch_sc1(u32x4 r, int v) {
  asm volatile("s_mov_b32 m0, %2\n\ts_nop 0\n\tbuffer_load_dword %0, %1, 0 offen sc1 lds" ::"v"(v), "s"(r), "i"(LDS_TOTAL - 256) : "memory");
}
DEVI void touch(u32x4 r, int v) {
  asm volatile("s_mov_b32 m0, %2\n\ts_nop 0\n\tbuffer_load_dword %0, %1, 0 offen lds" ::"v"(v), "s"(r), "i"(LDS_TOTAL - 256) : "memory");
}
DEVI void bload(u32x4& d, u32x4 r, int v, int s0) { asm volatile("buffer_load_dwordx4 %0, %1, %2, %3 offen" : "=v"(d) : "v"(v), "s"(r), "s"(s0)); }
DEVI void bload_sc1(u32x4& d, u32x4 r, int v, int s0) { asm volatile("buffer_load_dwordx4 %0, %1, %2, %3 offen sc1" : "=v"(d) : "v"(v), "s"(r), "s"(s0) : "memory"); }
DEVI void arrive(int k) {
  asm volatile("s_mov_b64 exec, 1\n\tds_add_u32 %0, %1\n\ts_mov_b64 exec, -1" ::"v"(LDS_CNT + 4 * k), "v"(1) : "memory");
}
template <int SLEEP, unsigned N>
DEVI void await(int k) {
  unsigned v;
  for (;;) {
    asm volatile("ds_read_b32 %0, %1\n\ts_waitcnt lgkmcnt(0)" : "=v"(v) : "v"(LDS_CNT + 4 * k) : "memory");
    if (__builtin_amdgcn_readfirstlane(v) >= N) break;
    if (SLEEP) __builtin_amdgcn_s_sleep(SLEEP);
  }
}
DEVI void post(int addr, int val) { asm volatile("ds_write_b32 %0, %1" ::"v"(addr), "v"(val) : "memory"); }
DEVI void peek(unsigned& v, int addr) { asm volatile("ds_read_b32 %0, %1" : "=v"(v) : "v"(addr) : "memory"); }
DEVI void require(unsigned v, int addr, unsigned n) {
  unsigned t;
  asm volatile("v_readfirstlane_b32 %1, %0\n\ts_cmp_ge_u32 %1, %3\n\ts_cbranch_scc1 .Lq%=\n"
               ".Lw%=:\n\tds_read_b32 %0, %2\n\ts_waitcnt lgkmcnt(0)\n\tv_readfirstlane_b32 %1, %0\n\ts_cmp_ge_u32 %1, %3\n\ts_cbranch_scc0 .Lw%=\n.Lq%=:"
               : "+&v"(v), "=&s"(t) : "v"(addr), "s"(n) : "memory", "scc");
}
DEVI void await_pair(int addr, unsigned n) {
  for (;;) {
    u32x2 v;
    asm volatile("ds_read_b64 %0, %1\n\ts_waitcnt lgkmcnt(0)" : "=v"(v) : "v"(addr) : "memory");
    const unsigned a = v[0], b = v[1];
    if (__builtin_amdgcn_readfirstlane(a < b ? a : b) >= n) break;
    __builtin_amdgcn_s_sleep(1);
  }
}
DEVI int lane_id() { int l; asm volatile("v_mbcnt_lo_u32_b32 %0, -1, 0\n\tv_mbcnt_hi_u32_b32 %0, -1, %0" : "=v"(l)); return l; }
DEVI int sig(int i) { return 4 * (i & 3) + (i >> 2); }
DEVI u64 flagval(unsigned tag) { return ((u64)(tag ^ 0x5bd1e995u) << 32) | tag; }

DEVI float xmax16(float v) { auto r = __builtin_amdgcn_permlane16_swap(asu(v), asu(v), false, false); return fmaxf(asf(r[0]), asf(r[1])); }
DEVI float xmax32(float v) { auto r = __builtin_amdgcn_permlane32_swap(asu(v), asu(v), false, false); return fmaxf(asf(r[0]), asf(r[1])); }
DEVI float xadd16(float v) { auto r = __builtin_amdgcn_permlane16_swap(asu(v), asu(v), false, false); return asf(r[0]) + asf(r[1]); }
DEVI float xadd32(float v) { auto r = __builtin_amdgcn_permlane32_swap(asu(v), asu(v), false, false); return asf(r[0]) + asf(r[1]); }
template <int CTRL> DEVI float dpp(float v) { return asf(__builtin_amdgcn_mov_dpp(asu(v), CTRL, 0xf, 0xf, true)); }
DEVI float half_sum(float v) {
  v += dpp<0xb1>(v); v += dpp<0x4e>(v); v += dpp<0x141>(v); v += dpp<0x140>(v);
  return xadd16(v);
}

DEVI int h_slot(int j, int l) { const int t = l < 4 ? 4 * j + l : 28 + 3 * j + l, d = (t * 43) >> 7; return 18 * (t - 3 * d) + d; }

DEVI int row_token(int p_, int pos) {
  const int tau = pos >> 4, i = pos & 15;
  int tok = tau == 0 ? p_ * 128 - 16 + 8 * (i & 1) + 2 * (i >> 2) + ((i & 3) < 2) : p_ * 128 + 32 * ((tau - 1) >> 1) + 2 * sig(i) + ((tau - 1) & 1);
  return tok < 0 ? 0 : tok;
}

struct ColGeo { int A, B, C, ct; };
DEVI ColGeo colgeo(bool att, int nw, int mw, int c, bool heavy) {
  const int cc = heavy ? (c ^ (2 * mw)) : c;
  const int ct = 4 * nw + cc, sec = (ct * 13) >> 6, hh = ct - 5 * sec;
  const bool q4 = hh == 4, sp = !heavy && c == 2;
  ColGeo r;
  r.A = att ? sec * 160 + (q4 ? 32 : 40 * hh) : heavy ? 128 * nw + 32 * cc : (sp ? 352 + 16 * nw : 128 + 64 * nw + 32 * c);
  r.B = att && q4 ? 40 : 8;
  r.C = att ? (q4 ? 80 : 16) : (sp ? 32 : 16);
  r.ct = ct;
  return r;
}

template <int NC>
struct Frags { u32x4 x[5]; u32x2 w[NC][2]; };

template <int N> DEVI void wait_lg() { asm volatile("s_waitcnt lgkmcnt(%0)" ::"i"(N) : "memory"); }

template <int NC, int K>
DEVI void s1_rd(Frags<NC>& n, int xo, int xh, const int (&wa)[NC]) {
  if constexpr (K == 0) ds128<0>(n.x[0], xo);
  else if constexpr (K <= 2 * NC) { if ((K - 1) & 1) dstr<7680>(n.w[(K - 1) >> 1][1], wa[(K - 1) >> 1]); else dstr<0>(n.w[(K - 1) >> 1][0], wa[(K - 1) >> 1]); }
  else if constexpr (K < 2 * NC + 4) ds128<(K - 2 * NC) * 2048>(n.x[K - 2 * NC], xo);
  else if constexpr (K == 2 * NC + 4) ds128<0>(n.x[4], xh);
}
template <int NC, int NH, int K>
DEVI void s1_mm(const Frags<NC>& c, f32x4 (&acc)[4][NC], f32x4 (&hacc)[NH]) {
  if constexpr (K < 4 * NC) mfma_a(acc[K / NC][K % NC], cat(c.w[K % NC][0], c.w[K % NC][1]), c.x[K / NC]);
  else mfma_a(hacc[K - 4 * NC], cat(c.w[K - 4 * NC][0], c.w[K - 4 * NC][1]), c.x[4]);
}
template <int NC, int NH, bool BAR>
DEVI void s1_step(const Frags<NC>& c, Frags<NC>& n, int xo, int xh, const int (&wa)[NC], f32x4 (&acc)[4][NC], f32x4 (&hacc)[NH]) {
  constexpr int NM = 4 * NC + NH, NR = 2 * NC + 5, D = BAR ? NM - NR - DEXTRA : 0;
  sfor<NM>([&](auto KK) {
    constexpr int k = decltype(KK)::value;
    constexpr int need = k < NC ? 2 * k + 2 : (k % NC == 0 && k <= 4 * NC) ? 2 * NC + k / NC : -1;
    if constexpr (BAR && k == D) bar_lgkm();
    if constexpr (need >= 0 && (!BAR || k < D)) wait_lg<NR - 1 - need + (BAR ? 0 : (k < NR ? k : NR))>();
    s1_mm<NC, NH, k>(c, acc, hacc);
    if constexpr (k >= D) s1_rd<NC, k - D>(n, xo, xh, wa);
  });
}

template <int OFF> DEVI void dsw64(int addr, u32x2 d) { asm volatile("ds_write_b64 %0, %1 offset:%2" ::"v"(addr), "v"(d), "i"(OFF) : "memory"); }
template <int OFF> DEVI void ds64(u32x2& d, int addr) { asm volatile("ds_read_b64 %0, %1 offset:%2" : "=v"(d) : "v"(addr), "i"(OFF)); }

template <int NC>
DEVI void s1_compute(lptr shm, bool att, int w, int lane, int p_, int j_, int hf, char* ws PR_ARGS) {
  constexpr int NH = NC == 4 ? 2 : 3;
  const int mw = w & 1, nw = w >> 1, i = lane & 15, g = lane >> 4;
  f32x4 acc[4][NC], hacc[NH];
#pragma unroll
  for (int m = 0; m < 4; m++)
#pragma unroll
    for (int c = 0; c < NC; c++) { acc[m][c] = f32x4{0, 0, 0, 0}; asm volatile("" : "+v"(acc[m][c])); }
#pragma unroll
  for (int c = 0; c < NH; c++) { hacc[c] = f32x4{0, 0, 0, 0}; asm volatile("" : "+v"(hacc[c])); }
  int wb[NC], cb[NC], ct[NC];
#pragma unroll
  for (int c = 0; c < NC; c++) {
    const ColGeo cg = colgeo(att, nw, mw, c, NC == 4);
    wb[c] = (lane >> 2) * 480 + cg.A + cg.B * (lane & 1) + cg.C * ((lane >> 1) & 1);
    cb[c] = LDS_CS + 2 * (cg.A + cg.B * (g & 1) + cg.C * (g >> 1));
    ct[c] = cg.ct;
    asm volatile("" : "+v"(cb[c]), "+v"(wb[c]));
  }
  int xb = XOFF + i * 128 + ((g ^ (i >> 1) ^ (4 * hf)) << 4);
  int sta = LDS_ST + i * 8;
  asm volatile("" : "+v"(xb), "+v"(sta));
  const int own = (1 + 4 * mw) * 2048;
  Frags<NC> fa, fb;
  int wa[NC];
  STAMPB(12);
  if (YPRIO && w >= 4) __builtin_amdgcn_s_setprio(1);
  bar_lgkm();
  STAMP(1);
  sfor<2 * NC + 5>([&](auto K) { s1_rd<NC, decltype(K)::value>(fa, xb + own, xb, wb); });
#pragma unroll
  for (int c = 0; c < NC; c++) wa[c] = wb[c] + WSLAB;
  s1_step<NC, NH, true>(fa, fb, (xb ^ 64) + own, xb ^ 64, wa, acc, hacc);
  int sl = 0;
#pragma nounroll
  for (int T = 1; T < 10; T++) {
    sl = sl == 2 * SLOT ? 0 : sl + SLOT;
    if (YPRIO && T == 9) __builtin_amdgcn_s_setprio(0);
#pragma unroll
    for (int c = 0; c < NC; c++) wa[c] = wb[c] + sl;
    s1_step<NC, NH, true>(fb, fa, xb + sl + own, xb + sl, wa, acc, hacc);
#pragma unroll
    for (int c = 0; c < NC; c++) wa[c] = wb[c] + sl + WSLAB;
    s1_step<NC, NH, false>(fa, fb, (xb ^ 64) + sl + own, (xb ^ 64) + sl, wa, acc, hacc);
  }
  wait_lg<0>();
  arrive(2);
  sfor<NH>([&](auto K) { s1_mm<NC, NH, 4 * NC + decltype(K)::value>(fb, acc, hacc); });
  u32x2 stu[5];
  u32x4 b1u[NC], csu[NC];
  sfor<4 * NC>([&](auto KK) {
    constexpr int k = decltype(KK)::value;
    s1_mm<NC, NH, k>(fb, acc, hacc);
    if constexpr (k < 4) ds64<(1 + k) * 128>(stu[k], sta + mw * 512);
    if constexpr (k == 4) ds64<0>(stu[4], sta);
    if constexpr (k == 5) {
      dsw_col0(cb[0], hacc[0], cb[1], hacc[1], cb[NH - 1], hacc[NH - 1]);
      if (NC == 4) arrive(8 + nw);
    }
    if constexpr (k >= 6 && k < 6 + NC) ds128<LDS_B1 - LDS_CS>(b1u[k - 6], cb[k - 6]);
  });
  STAMP(2);
  if (NC == 4) await<0, 2>(8 + nw);
#pragma unroll
  for (int c = 0; c < NC; c++) ds128<0>(csu[c], cb[c]);
  asm volatile("s_waitcnt lgkmcnt(0)\n\ts_nop 7" ::: "memory");
  f32x2 st[5];
  f32x4 cs[NC], b1[NC];
#pragma unroll
  for (int m = 0; m < 5; m++) { asm volatile("" : "+v"(stu[m])); st[m] = __builtin_bit_cast(f32x2, stu[m]); }
#pragma unroll
  for (int c = 0; c < NC; c++) { asm volatile("" : "+v"(csu[c]), "+v"(b1u[c])); cs[c] = __builtin_bit_cast(f32x4, csu[c]); b1[c] = __builtin_bit_cast(f32x4, b1u[c]); }
#pragma unroll
  for (int m = 0; m < 4; m++)
#pragma unroll
    for (int c = 0; c < NC; c++) pin(acc[m][c]);
#pragma unroll
  for (int c = 0; c < NH; c++) pin(hacc[c]);
  STAMPA(12);
#define SB __builtin_amdgcn_sched_barrier(0)
  const int l2 = lane_id();
  if (att) {
    const int g2 = l2 >> 4, piece = LDS_TB + ((l2 & 48) + ((l2 & 15) ^ (8 * (g2 >> 1)))) * 8;
    sfor<NC>([&](auto C) {
      constexpr int c = decltype(C)::value;
      const int ah = piece + ct[c] * 4608, ao = ah + (1 + 4 * mw) * 512;
      u32x2 o[5];
      SB;
#pragma unroll
      for (int m = 0; m < (c < NH ? 5 : 4); m++) {
        f32x4 a = m < 4 ? acc[m < 4 ? m : 0][c] : hacc[c < NH ? c : 0];
        f32x4 v = a * st[m][0] + cs[c] * st[m][1];
        o[m] = u32x2{pk2(v[0], v[1]), pk2(v[2], v[3])};
      }
      dsw64<0>(ao, o[0]); dsw64<512>(ao, o[1]); dsw64<1024>(ao, o[2]); dsw64<1536>(ao, o[3]);
      if (c < NH) dsw64<0>(ah, o[4]);
    });
  } else {
    const u32x4 ra = mkr(ws + WS_A2, A2_BYTES);
    const int base = (8 * p_ + 4 * mw) * KS_T * 1024;
    const int o0 = base + (NC == 4 || j_ < 7 || nw == 2 ? h_slot(j_, NC == 4 ? 2 * nw + mw : 2 + nw) * 1024 : 0x70000000);
    const int o1 = base + (NC == 4 ? h_slot(j_, 2 * nw + (mw ^ 1)) * 1024 : j_ < 7 ? h_slot(j_, 6) * 1024 + (nw - 2) * 512 : 0x70000000);
    const float c1 = -2.f * 0.7978845608028654f * 1.4426950408889634f, c2 = c1 * 0.044715f;
    unsigned sp[2];
#pragma unroll
    for (int m = 0; m < 4; m++) {
      f32x4 v[NC], t[NC];
      SB;
#pragma unroll
      for (int c = 0; c < NC; c++) t[c] = cs[c] * st[m][1] + b1[c];
      SB;
#pragma unroll
      for (int c = 0; c < NC; c++) v[c] = acc[m][c] * st[m][0] + t[c];
      SB;
#pragma unroll
      for (int c = 0; c < NC; c++) t[c] = v[c] * v[c];
      SB;
#pragma unroll
      for (int c = 0; c < NC; c++) t[c] = t[c] * c2 + c1;
      SB;
#pragma unroll
      for (int c = 0; c < NC; c++) t[c] = t[c] * v[c];
      SB;
#pragma unroll
      for (int c = 0; c < NC; c++)
#pragma unroll
        for (int e = 0; e < 4; e++) t[c][e] = __builtin_amdgcn_exp2f(t[c][e]);
      SB;
#pragma unroll
      for (int c = 0; c < NC; c++) t[c] = t[c] + 1.f;
      SB;
#pragma unroll
      for (int c = 0; c < NC; c++)
#pragma unroll
        for (int e = 0; e < 4; e++) t[c][e] = __builtin_amdgcn_rcpf(t[c][e]);
      SB;
#pragma unroll
      for (int c = 0; c < NC; c++) v[c] = v[c] * t[c];
      SB;
      unsigned o[NC][2];
#pragma unroll
      for (int c = 0; c < NC; c++) { o[c][0] = pk2(v[c][0], v[c][1]); o[c][1] = pk2(v[c][2], v[c][3]); }
      st_wt(ra, l2 * 16, o0 + m * KS_T * 1024, u32x4{o[0][0], o[0][1], o[1][0], o[1][1]});
      if (NC == 4) st_wt(ra, l2 * 16, o1 + m * KS_T * 1024, u32x4{o[2][0], o[2][1], o[NC - 1][0], o[NC - 1][1]});
      else if (m & 1) {
        auto s0 = __builtin_amdgcn_permlane32_swap(sp[0], o[2][0], false, false);
        auto s1 = __builtin_amdgcn_permlane32_swap(sp[1], o[2][1], false, false);
        st_wt(ra, (l2 >> 5) * (KS_T * 1024) + (l2 & 31) * 16, o1 + (m - 1) * KS_T * 1024, u32x4{s0[0], s1[0], s0[1], s1[1]});
      } else { sp[0] = o[2][0]; sp[1] = o[2][1]; }
    }
  }
}

template <bool MAX> DEVI float red4(const float (&v)[4]) {
  auto op = [](unsigned a, unsigned b) { float r; if (MAX) asm("v_max_f32 %0, %1, %2" : "=v"(r) : "v"(a), "v"(b)); else r = asf(a) + asf(b); return r; };
  auto a = __builtin_amdgcn_permlane32_swap(asu(v[0]), asu(v[1]), false, false);
  auto b = __builtin_amdgcn_permlane32_swap(asu(v[2]), asu(v[3]), false, false);
  auto c = __builtin_amdgcn_permlane16_swap(asu(op(a[0], a[1])), asu(op(b[0], b[1])), false, false);
  return op(c[0], c[1]);
}
DEVI void bcast4(float r, float (&v)[4]) {
  auto e = __builtin_amdgcn_permlane16_swap(asu(r), asu(r), false, false);
  unsigned e0 = e[0], e1 = e[1];
  auto a = __builtin_amdgcn_permlane32_swap(e0, e0, false, false);
  auto b = __builtin_amdgcn_permlane32_swap(e1, e1, false, false);
  v[0] = asf(a[0]); v[1] = asf(a[1]); v[2] = asf(b[0]); v[3] = asf(b[1]);
}

DEVI void att_table(lptr shm, int lane) {
  const int i = lane & 15, g = lane >> 4, p = lane & 3;
  const int d = sig(i) - g;
  const unsigned NEG = 0xf149f2cau;
  bool vo[4], vp[4];
#pragma unroll
  for (int r = 0; r < 4; r++) { vo[r] = (unsigned)(d - 4 * r) <= 7u; vp[r] = d + 16 - 4 * r <= 7; }
  auto pm = [](bool lo, bool hi) { return (lo ? 0xffffu : 0u) | (hi ? 0xffff0000u : 0u); };
  LAS u32x4* t = (LAS u32x4*)(shm + LDS_TAB) + lane;
  t[0] = u32x4{vo[0] ? 0u : NEG, vo[1] ? 0u : NEG, vo[2] ? 0u : NEG, vo[3] ? 0u : NEG};
  t[64] = u32x4{pm(vp[2], vp[3]), pm(vo[0], vo[1]), vp[2] ? 0u : NEG, vp[3] ? 0u : NEG};
  t[128] = u32x4{pm(vo[2], vo[3]), (unsigned)(LDS_TB + (g * 16 + (i ^ (8 * (g >> 1)))) * 8), (unsigned)(LDS_TB + (p * 16 + ((lane >> 2) ^ (8 * (p >> 1)))) * 8), 0u};
}
DEVI int sel(u64 mask, int a, int b) { int r; asm("v_cndmask_b32_e64 %0, %2, %1, %3" : "=v"(r) : "v"(a), "v"(b), "s"(mask)); return r; }

DEVI void s1_attention(lptr shm, int w, int p_, int j_, char* ws PR_ARGS) {
  arrive(3);
  const int lane = lane_id();
  const int tau = (w + 1) * 512, taup = w >= 2 ? tau - 1024 : 0, flip = w == 1 ? 16 : 0;
  const LAS u32x4* t = (const LAS u32x4*)(shm + LDS_TAB) + lane;
  const u32x4 t0 = t[0], t2 = t[128];
  u32x4 t1 = t[64];
  if (p_ == 0 && w < 2) { t1[0] = 0u; t1[2] = 0xf149f2cau; t1[3] = 0xf149f2cau; }
  const f32x4 mO = __builtin_bit_cast(f32x4, t0), mP = __builtin_bit_cast(f32x4, t1);
  const unsigned M1 = t1[0], M2 = t1[1], M3 = t2[0];
  const int po = t2[1] + tau, pp = (t2[1] ^ flip) + taup, to = t2[2] + tau, tp = (t2[2] ^ flip) + taup;
  int aq[4], avo[4], avp[4];
#pragma unroll
  for (int h = 0; h < 4; h++) {
    aq[h] = sel(0xffffull << (16 * h), po, LDS_ZERO - 4 * 4608);
    avo[h] = sel(0x1111111111111111ull << h, to, LDS_ZERO - 14 * 4608);
    avp[h] = sel(0x1111111111111111ull << h, tp, LDS_ZERO - 14 * 4608);
    asm volatile("" : "+v"(aq[h]), "+v"(avo[h]), "+v"(avp[h]));
  }
  STAMPA(14);
  await<0, 8>(3);
  STAMPA(15);
  u32x2 qa[4][2], ko[4][2], kp[4][2], va[4][2], vb[4][2];
  f32x4 sp[4], so[4];
  auto rd = [&](auto H) {
    constexpr int h = decltype(H)::value;
    ds64<(5 + h) * 4608>(ko[h][0], po); ds64<9 * 4608>(ko[h][1], po); ds64<h * 4608>(qa[h][0], po); ds64<4 * 4608>(qa[h][1], aq[h]);
    ds64<(5 + h) * 4608>(kp[h][0], pp); ds64<9 * 4608>(kp[h][1], pp);
  };
  auto rv = [&](auto H) {
    constexpr int h = decltype(H)::value;
    dstr<(10 + h) * 4608>(va[h][0], tp); dstr<(10 + h) * 4608>(va[h][1], to); dstr<14 * 4608>(vb[h][0], avp[h]); dstr<14 * 4608>(vb[h][1], avo[h]);
  };
  auto qk = [&](auto H, auto N) {
    constexpr int h = decltype(H)::value;
    asm volatile("s_waitcnt lgkmcnt(%6)" : "+v"(ko[h][0]), "+v"(ko[h][1]), "+v"(qa[h][0]), "+v"(qa[h][1]), "+v"(kp[h][0]), "+v"(kp[h][1]) : "i"(decltype(N)::value));
    so[h] = mfma(cat(ko[h][0], ko[h][1]), cat(qa[h][0], qa[h][1]), mO);
    sp[h] = mfma(cat(kp[h][0], kp[h][1]), cat(qa[h][0], qa[h][1]), mP);
  };
  rd(IC(0)); rd(IC(1));
  qk(IC(0), IC(6)); rd(IC(2));
  qk(IC(1), IC(6)); rd(IC(3));
  qk(IC(2), IC(6)); rv(IC(0)); rv(IC(1));
  qk(IC(3), IC(8)); rv(IC(2));
  asm volatile("s_waitcnt lgkmcnt(8)");
  rv(IC(3));
  const float qs = 0.22360679774997896f * 1.4426950408889634f;
  float ee[4], eo[4], mx[4], l[4];
#pragma unroll
  for (int h = 0; h < 4; h++) { ee[h] = fmaxf(fmaxf(sp[h][2], so[h][0]), so[h][2]); eo[h] = fmaxf(fmaxf(sp[h][3], so[h][1]), so[h][3]); mx[h] = fmaxf(ee[h], eo[h]); }
  bcast4(red4<true>(mx) * -qs, mx);
#pragma unroll
  for (int h = 0; h < 4; h++) {
    f32x2 t = f32x2{ee[h], eo[h]} * qs + mx[h];
    ee[h] = __builtin_amdgcn_exp2f(t[0]); eo[h] = __builtin_amdgcn_exp2f(t[1]); l[h] = ee[h] + eo[h];
  }
  bcast4(__builtin_amdgcn_rcpf(red4<false>(l)), l);
  asm volatile("s_waitcnt lgkmcnt(0)" : "+v"(va[0][0]), "+v"(va[0][1]), "+v"(va[1][0]), "+v"(va[1][1]), "+v"(va[2][0]), "+v"(va[2][1]), "+v"(va[3][0]), "+v"(va[3][1]),
               "+v"(vb[0][0]), "+v"(vb[0][1]), "+v"(vb[1][0]), "+v"(vb[1][1]), "+v"(vb[2][0]), "+v"(vb[2][1]), "+v"(vb[3][0]), "+v"(vb[3][1]));
  f32x4 o4 = {0, 0, 0, 0}, oh[4];
#pragma unroll
  for (int h = 0; h < 4; h++) {
    const unsigned pk = pk2(f32x2{ee[h], eo[h]} * l[h]);
    u32x4 pf = {0u, pk & M1, pk & M2, pk & M3};
    o4 = mfma(cat(vb[h][0], vb[h][1]), pf, o4);
    oh[h] = mfma(cat(va[h][0], va[h][1]), pf, f32x4{0, 0, 0, 0});
  }
  const u32x4 ra = mkr(ws + WS_A2, A2_BYTES);
  const int base = ((8 * p_ + w) * KS_T + 54 + 5 * (j_ >> 1)) * 1024;
#pragma unroll
  for (int hp = 0; hp < 2; hp++)
    st_wt(ra, lane * 16, base + ((j_ & 1) * 3 + hp) * 1024,
          u32x4{pk2(oh[2 * hp][0], oh[2 * hp][1]), pk2(oh[2 * hp][2], oh[2 * hp][3]), pk2(oh[2 * hp + 1][0], oh[2 * hp + 1][1]), pk2(oh[2 * hp + 1][2], oh[2 * hp + 1][3])});
  unsigned a0 = pk2(o4[0], o4[1]), a1 = pk2(o4[2], o4[3]);
  auto s0 = __builtin_amdgcn_permlane32_swap(a0, a0, false, false);
  auto s1 = __builtin_amdgcn_permlane32_swap(a1, a1, false, false);
  st_wt(ra, lane < 32 ? lane * 16 : 0x70000000, base + 2 * 1024 + (j_ & 1) * 512, u32x4{s0[0], s1[0], s0[1], s1[1]});
  arrive(4);
}

DEVI int s2_perm(int s, bool o, int rot) { return s < 8 ? s ^ rot : (s < 10 || s >= 18) ? s ^ (rot & 1) : ((s - 10) ^ rot) + 10; }
DEVI int fin_off(int G0, int n_, int r0, int lane) {
  const int frow = r0 + (lane >> 3), fG = G0 + (frow >> 4);
  return (32 * (fG >> 1) + 2 * sig(frow & 15) + (fG & 1)) * 1280 + n_ * 128 + (lane & 7) * 16;
}
constexpr int s2_slot(int r) { return r < 18 ? (r % 9) * 16384 : 147456 + (r - 18) * 4096; }
DEVI void watch(char* ws, int Ga, int Gb, int n_, int kind, int word, u64 fv) {
  const int lane = lane_id();
  GAS u64* fp = (GAS u64*)(ws + WS_BOX) + ((((lane & 8) ? Gb : Ga) >> 3) * 10 + n_) * 32 + 8 * kind + (lane & 7);
#pragma nounroll
  for (unsigned spins = 0; spins < SPIN_CAP; spins++) {
    if (__all(__hip_atomic_load(fp, __ATOMIC_RELAXED, __HIP_MEMORY_SCOPE_AGENT) == fv)) break;
    __builtin_amdgcn_s_sleep(1);
  }
  post(LDS_CNT + 4 * word, 1);
}

template <int NS, int MT>
DEVI void s2_compute(int w, int kq, int G0, int n_, int rot, char* ws, u64 fv PR_ARGS) {
  constexpr int AD = MT == 3 ? (NS == 20 ? AD3S : AD3Y) : (NS == 20 ? AD2S : AD2Y);
  const int mh = w >> 2, lane = lane_id();
  const u32x4 ra = mkr(ws + WS_A2, A2_BYTES);
  int av = lane * 16;
  pin(av);
  const int ao = ((G0 + MT * mh) * KS_T + (NS == 20 ? 54 : 18 * (kq - 1))) * 1024;
  int bs[4];
#pragma unroll
  for (int c = 0; c < 4; c++) bs[c] = kq * 4096 + (lane >> 2) * 128 + ((c ^ ((lane >> 3) & 3)) << 5) + 8 * (lane & 3);
  f32x4 acc[MT][4];
#pragma unroll
  for (int m = 0; m < MT; m++)
#pragma unroll
    for (int c = 0; c < 4; c++) { acc[m][c] = f32x4{0, 0, 0, 0}; pin(acc[m][c]); }
  u32x4 a[AD][MT];
  u32x2 b[3][4][2];
  int ca = LDS_CONS + kq * 8 + mh * 4, pa = LDS_PROD + kq * 4;
  pin(ca); pin(pa);
  unsigned pv, lv;
  auto rdb = [&](auto R, auto K) {
    constexpr int r = decltype(R)::value, k = decltype(K)::value;
    constexpr int off = s2_slot(r), nxt = s2_slot(r + 1) / 65536 - off / 65536;
    if constexpr (r < 3) dstr<off % 65536 + (k & 1) * 2048>(b[r % 3][k >> 1][k & 1], bs[k >> 1]);
    else redstr<off % 65536 + (k & 1) * 2048>(b[r % 3][k >> 1][k & 1], bs[k >> 1]);
    if constexpr (nxt != 0 && (k & 1)) vinc(bs[k >> 1], nxt * 65536);
  };
  auto lda = [&](auto S, int m) {
    constexpr int s = decltype(S)::value;
    const int o = ao + m * KS_T * 1024 + (s2_perm(s, NS == 20, rot) << 10);
    if constexpr (s < AD) bload_sc1(a[s % AD][m], ra, av, o); else reload_sc1(a[s % AD][m], ra, av, o);
  };
  auto ldb = [&]() {
    peek(pv, pa); wait_lg<0>(); require(pv, pa, 1);
    sfor<3>([&](auto R) { sfor<8>([&](auto K) { rdb(R, K); }); wait_lg<(decltype(R)::value < 2 ? 4 : 0)>(); });
    post(ca, 1);
  };
  constexpr bool BFIRST = NS == 20 || MT == 3;
  if (BFIRST) ldb();
  STAMP(6);
  if (NS == 20) watch(ws, G0, G0 + 2 * MT - 1, n_, w == 0 ? 0 : 2, w == 0 ? 14 : 5, fv);
  await<0, 1>(NS == 20 ? 14 : 15);
  STAMP(7);
  sfor<AD - 1>([&](auto S) {
#pragma unroll
    for (int m = 0; m < MT; m++) lda(S, m);
  });
  if (!BFIRST) ldb();
  if (NS == 20) __builtin_amdgcn_s_setprio(SPRIO);
  sfor<NS - 3>([&](auto S) {
    constexpr int s = decltype(S)::value;
    constexpr bool LA = s + AD - 1 < NS;
    constexpr int later = NS - 1 - s < AD - 2 ? NS - 1 - s : AD - 2;
    if constexpr (s == 3) require(pv, pa, 2);
    if constexpr (s == 7) require(pv, pa, 3);
    if constexpr (s == 10) require(pv, pa, 4);
    if constexpr (s == 13) require(pv, pa, 5);
    if constexpr (s == 2 || s == 6 || s == 9 || s == 12) peek(pv, pa);
    if constexpr (NS == 18 && s == 11 - AD) require(lv, LDS_CNT + 4 * 5, 1);
    if constexpr (NS == 18 && s == 10 - AD) peek(lv, LDS_CNT + 4 * 5);
    if constexpr (s == NS - 4) peek(lv, ca ^ 4);
    sfor<MT>([&](auto M) {
      constexpr int m = decltype(M)::value;
      if constexpr (LA) lda(IC(s + AD - 1), m);
      wait_vm<MT * later + MT - 1 - m + (LA ? m + 1 : 0)>();
      if constexpr (m == 1 && s >= 2) wait_lg<4>();
      sfor<4>([&](auto C) {
        constexpr int c = decltype(C)::value;
        mfma_a(acc[m][c], cat(b[s % 3][c][0], b[s % 3][c][1]), a[s % AD][m]);
        if constexpr (s >= 1 && m < 2) rdb(IC(s + 2), IC(4 * m + c));
      });
    });
    if constexpr (s == 3 || s == 6 || s == 9 || s == 12) post(ca, s);
  });
  wait_lg<4>();
  sfor<8>([&](auto K) { rdb(IC(NS - 1), K); });
  if constexpr (AD == 3) {
#pragma unroll
    for (int m = 0; m < MT; m++) lda(IC(NS - 1), m);
  }
  int da[4];
#pragma unroll
  for (int c = 0; c < 4; c++) da[c] = mh * 49152 + kq * 4096 + (av & 0xf0) * 16 + ((((4 * c) << 4) + (av >> 8 << 4)) ^ (av & 0x70));
  require(lv, ca ^ 4, mh ? 12 : 9);
  sfor<MT>([&](auto M) {
    constexpr int m = decltype(M)::value;
    sfor<3>([&](auto U) {
      constexpr int u = decltype(U)::value, s = NS - 3 + u;
      if constexpr (m == 0) wait_vm<MT * (2 - u) + MT - 1>();
      if constexpr (m > 0 && u == 2) wait_vm<MT - 1 - m>();
      if constexpr (m == 0 && u == 1) wait_lg<8>();
      if constexpr (m == 0 && u == 2) wait_lg<0>();
      sfor<4>([&](auto C) {
        constexpr int c = decltype(C)::value;
        mfma_a(acc[m][c], cat(b[s % 3][c][0], b[s % 3][c][1]), a[s % AD][m]);
        if constexpr (m > 0 && u > 0 && (c & 1)) dsw128<(m - 1) * 16384>(da[2 * (u - 1) + (c >> 1)], __builtin_bit_cast(u32x4, acc[m - 1][2 * (u - 1) + (c >> 1)]));
      });
    });
  });
  STAMP(9);
  asm volatile("s_nop 4");
#pragma unroll
  for (int c = 0; c < 4; c++) dsw128<(MT - 1) * 16384>(da[c], __builtin_bit_cast(u32x4, acc[MT - 1][c]));
  arrive((NS == 20 ? 28 : 12) + mh);
}

constexpr int inl(int T) { const int n = (T - INL0) * INLR; return n < 0 ? 0 : n > INLN ? INLN : n; }
__global__ __launch_bounds__(768, 3) void fused_block(const void* __restrict__ x, const void* __restrict__ wqkv, const void* __restrict__ wo,
                                                      const void* __restrict__ w1, const unsigned short* __restrict__ b1,
                                                      const void* __restrict__ w2, const char* __restrict__ b2, char* __restrict__ out,
                                                      char* ws) {
  extern __shared__ __attribute__((aligned(16))) char shm_[];
  lptr shm = (lptr)shm_;
  const int tid = threadIdx.x, lane = tid & 63, w = __builtin_amdgcn_readfirstlane(tid >> 6);
  const int xcd = blockIdx.x & 7, idx = blockIdx.x >> 3;
  const int p_ = (xcd & 1) * 8 + (idx & 7), sub = idx >> 3;
  const bool att = !(sub & 1);
  const int j_ = (xcd >> 1) * 2 + (sub >> 1);
  const int uidx = (idx & 7) + 8 * (idx >> 4);
  const int u5 = uidx >= 11;
  const int nl = att ? (uidx < 6 ? uidx >> 1 : 3 + u5) : (uidx < 12 ? uidx >> 2 : 3 + ((uidx - 12) >> 1));
  const int G0 = 32 * (xcd >> 1) + (att ? (uidx < 6 ? 24 + 4 * (uidx & 1) : 12 + 4 * (uidx - 6 - 5 * u5)) : 6 * (uidx < 12 ? uidx & 3 : uidx & 1));
  const int n_ = (xcd & 1) * 5 + nl;
  const int kq = w < 8 ? (w + (w >> 2)) & 3 : w & 3;
  const int ug = att ? (uidx < 6 ? uidx & 1 : uidx - 6 - 5 * (uidx >= 11) + 2) : (uidx < 12 ? uidx & 3 : uidx & 1);
  const int rot = (ROT2);
  STAMP(0);
  if (w == 7) {
    u64 pc; asm volatile("s_getpc_b64 %0" : "=s"(pc));
    touch(mkr((const void*)(pc & ~255ull), CODE_BYTES), (idx + 32 * lane) * 128);
  }
  asm volatile("" : "+s"(x), "+s"(wqkv), "+s"(wo), "+s"(w1), "+s"(b1), "+s"(w2), "+s"(b2), "+s"(out), "+s"(ws));
  const int hf = idx & 1, tx = KTX;
  const int rid = (att ? 0 : 8) + j_;
  int fu;
  if (__builtin_expect(w >= 8, 1)) {
    __builtin_amdgcn_s_setprio(3);
    const int lw = w - 8;
    auto ktile = [&](int T) { return T ^ (T < 8 ? tx : tx & 1); };
    const int RS = att ? 3840 : 3456;
    const u32x4 rx = mkr(x, S_ * D_ * 2), rw1 = mkr(att ? wqkv : w1, D_ * RS);
    const int r8 = lane >> 3, cp = lane & 7, t0 = p_ * 128;
    int xo[2], hv, wv;
#pragma unroll
    for (int e = 0; e < 2; e++)
      xo[e] = (t0 + 32 * lw + 2 * (4 * (r8 & 3) + 2 * e + (r8 >> 2))) * 1280 + ((cp ^ (4 * e + (r8 >> 1))) << 4);
    auto ldx = [&](int T) { const int o = ktile(T) * 128; dma4<1024>(rx, ~0ull, (T % 3) * SLOT + XOFF + 2048 + 4 * lw * 1024, xo[0], xo[1], o, o, o + 1280, o + 1280); };
    STAMP(12);
    ldx(0);
    {
      const int hi = lane >= 30, ch = lane - 30 * hi, lim = j_ < 7 ? 27 : 19;
      wv = hi * RS + (att ? ((ch >= 10) + (ch >= 20)) * 1120 + j_ * 160 + ch * 16 : j_ * 448 + (ch < lim ? ch : lim) * 16);
    }
    const int wrow = (16 * (lw & 1) + 4 * (lw >> 1)) * RS;
    auto ldw = [&](int T, int h) {
      const int so = (64 * ktile(T) + 32 * (h ^ hf)) * RS + wrow;
      dma4<960>(rw1, 0x0fffffffffffffffull, (T % 3) * SLOT + h * WSLAB + 4 * lw * 960, wv, wv, so, so + 2 * RS, so + 8 * RS, so + 10 * RS);
    };
    ldw(0, 0);
    {
      int tok = t0 - 16 + 8 * (r8 & 1) + 2 * lw + ((r8 & 3) < 2);
      tok = tok < 0 ? 0 : tok;
      hv = att ? tok * 1280 + ((cp ^ ((2 * lw + ((r8 & 3) >> 1)) & 7)) << 4) : 0x70000000;
    }
    const u64 hmask = lw == 0 ? 0xffffff00ull : 0xffffffffull;
    auto ldh = [&](int T) { dma1(rx, hmask, (T % 3) * SLOT + XOFF + lw * 512, hv, ktile(T) * 128); };
    ldh(0); ldw(0, 1);
    STAMP(13);
    bar_vm<4>();
    STAMP(14);
    ldx(1); ldh(1);
    bar_vm<5>();
    ldw(1, 0); ldw(1, 1);
    auto warmh = [&](int ev = 0) {
      constexpr int PER = 24 * (WMLP ? 2 : 1) / WNE, NW2 = 17280 / PER;
      const int wv2 = ((WMLP ? (blockIdx.x >> 7) * 64 + (blockIdx.x & 63) : blockIdx.x) * 4 + lw) * WNE + ev;
      touch(mkr(wv2 < NW2 ? w2 : wo, WMLP && att ? 0 : wv2 < NW2 ? F_ * 1280 : D_ * 1280), ((wv2 < NW2 ? wv2 : wv2 - NW2) * PER + min(lane, PER - 1)) * 128);
    };
    ldx(2); ldh(2);
    if (WARMH == 2) warmh();
    bar_vm<5 + (WARMH == 2)>();
    __builtin_amdgcn_s_setprio(L1PRIO);
    if (WARMH == 3) warmh();
    const int srow = p_ * 128 + 8 * rid + 2 * lw + (lane >> 5);
    u32x2 xr[5];
    unsigned ep, b1v;
    setup_loads(xr, ep, b1v, rx, srow * 1280 + (lane & 31) * 8, mkr(ws, 4), mkr(b1, F_ * 2), (j_ * 224 + 64 * lw + lane) * 2);
    touch_sc1(mkr(ws + WS_BOX, lw == 0 ? 160 * 256 : 0), (((G0 >> 3) + (lane & 1)) * 10 + n_) * 256 + (lane & 2) * 64);
    ldw(2, 0); ldw(2, 1); ldx(3); ldh(3); ldw(3, 0);
    bar_vm<9>();
    pin(ep); pin(b1v);
#pragma unroll
    for (int k = 0; k < 5; k++) pin(xr[k]);
    const unsigned tag = __builtin_amdgcn_readfirstlane(ep) + 1u;
    ldw(3, 1); ldx(4); ldw(4, 0); ldh(4); ldw(4, 1);
    {
      float s = 0;
#pragma unroll
      for (int k = 0; k < 5; k++) s += (blo(xr[k][0]) + bhi(xr[k][0])) + (blo(xr[k][1]) + bhi(xr[k][1]));
      const float m = half_sum(s) * (1.f / D_);
      float q = 0;
#pragma unroll
      for (int k = 0; k < 10; k++) { const unsigned u = xr[k >> 1][k & 1]; const float a = blo(u) - m, b = bhi(u) - m; q = __builtin_fmaf(a, a, q); q = __builtin_fmaf(b, b, q); }
      const float rs = __builtin_amdgcn_rsqf(half_sum(q) * (1.f / D_) + 1e-5f);
      st_wt(mkr(ws + WS_STAT, S_ * 16), (lane & 31) == 0 ? srow * 16 : 0x70000000, 0, u32x4{asu(rs), tag, asu(-m * rs), tag});
      *(LAS float*)(shm + LDS_B1 + (64 * lw + lane) * 4) = blo(b1v);
      *(LAS unsigned*)(shm + LDS_CNT + 4) = tag;
    }
    bar_vm<14>();
    const u32x4 rw = mkr(kq == 0 ? wo : w2, kq == 0 ? D_ * 1280 : F_ * 1280);
    const int cbyte = n_ * 128 + ((cp ^ ((lane >> 4) << 1)) << 4);
    const int v1 = r8 * 1280 + cbyte, v2 = kq == 0 ? (20 * (r8 >> 2) + (r8 & 3)) * 1280 + cbyte : v1;
    int gtab;
    {
      const int so = s2_perm(lane, true, rot), P = (so * 13) >> 6, kp = so - 5 * P;
      const int r0 = 160 * P + (kp == 2 ? 16 : 40 * (kp - (kp > 2)));
      const int t = 3 * s2_perm(lane, false, rot) + kq - 1, u = t - 32;
      const int r1 = 32 * (t < 32 ? 7 * (t >> 2) + (t & 3) : 4 * ((u * 43) >> 7) + 4 + u);
      gtab = kq == 0 ? r0 * 1280 + (kp == 2) : r1 * 1280;
      pin(gtab);
    }
    const int sbn = 1280 * (kq == 0 ? 20 : 16);
    auto geom = [&](int s, int& rb, int& sa, int& sb, int& v) {
      const int e = __builtin_amdgcn_readlane(gtab, s), f = e & 1;
      v = v1 + ((v2 - v1) & -f);
      rb = e - f;
      sa = f ? 1280 * 80 : 1280 * 8;
      sb = f ? 1280 * 40 : sbn;
    };
    u32x4 pre[9][4];
    auto ldr = [&](auto R) {
      constexpr int r = decltype(R)::value;
      int rb, sa, sb, v; geom(9 + r, rb, sa, sb, v);
      bload(pre[r][0], rw, v, rb); bload(pre[r][1], rw, v, rb + sa); bload(pre[r][2], rw, v, rb + sb); bload(pre[r][3], rw, v, rb + sa + sb);
    };
    const int attf = att;
    auto ldc = [&](auto R, auto LATE) {
      constexpr int r = decltype(R)::value;
      int rb, sa, sb, v; geom(9 + r, rb, sa, sb, v);
      bload4_if<decltype(LATE)::value>(pre[r], rw, v, rb, rb + sa, rb + sb, rb + sa + sb, attf);
    };
    auto ldl = [&](auto S) {
      constexpr int s = decltype(S)::value;
      int rb, sa, sb, v; geom(s, rb, sa, sb, v);
      dma4<1024>(rw, ~0ull, s < 9 ? s * 16384 + kq * 4096 : 147456 + (s - 18) * 4096, v, v, rb, rb + sa, rb + sb, rb + sa + sb);
    };
    const u32x4 rst = mkr(ws + WS_STAT, S_ * 16);
    const int spos = 36 * lw + (lane < 36 ? lane : 35), sv = row_token(p_, spos) * 16;
    u32x4 sg;
    sfor<(LATESET ? 3 : 5)>([&](auto TT) {
      constexpr int T = decltype(TT)::value + (LATESET ? 7 : 5);
      if (T == 9) bload_sc1(sg, rst, sv, 0);
      if (T == 6) { STAMP(15); }
      constexpr bool WE = WARMH >= 5 && T >= WARMH && (T - WARMH) % WSTEP == 0 && (T - WARMH) / WSTEP < WNE;
      ldx(T); ldw(T, 0);
      if (WE) warmh((T - WARMH) / WSTEP);
      ldh(T); ldw(T, 1);
      constexpr int n0 = inl(T - 1), n1 = inl(T), n2 = inl(T + 1);
      sfor<n2 - n1>([&](auto R) { ldc(IC(1 + n1 + decltype(R)::value), IC(0)); });
      bar_vm_sel<13 + WE + 4 * (n2 - n1) + (T < 9 ? 4 * (n1 - n0) : 0), 13 + WE>(attf);
    });
    STAMP(1);
    asm volatile("" : "+v"(sg));
#pragma nounroll
    for (unsigned spins = 0; !__all(sg[1] == tag && sg[3] == tag) && spins < SPIN_CAP; spins++) {
      __builtin_amdgcn_s_sleep(2);
      bload_sc1(sg, rst, sv, 0);
      asm volatile("s_waitcnt vmcnt(0)" : "+v"(sg) :: "memory");
    }
    *(LAS u32x2*)(shm + LDS_ST + spos * 8) = u32x2{sg[0], sg[2]};
    ldl(IC(4));
    if (att) wait_vm<4>(); else wait_vm<4 + 4 * (inl(10) - inl(9))>();
    STAMP(2);
    bar_lgkm();
    __builtin_amdgcn_s_setprio(0);
    STAMP(3);
    const int pa = LDS_PROD + kq * 4, wa = kq * 4096 + lane * 16;
    auto ldq = [&](auto R) { if constexpr (decltype(R)::value <= inl(10)) ldc(R, IC(1)); else ldr(R); };
    if (!att) { ldl(IC(5)); ldl(IC(6)); ldl(IC(7)); }
    ldq(IC(1)); ldq(IC(2)); ldq(IC(3)); ldq(IC(4));
    STAMP(4);
    ldl(IC(3));
    await<1, 8>(2);
    ldl(IC(2)); ldl(IC(1)); ldl(IC(0));
    if (!att) { ldl(IC(8)); if (kq == 0) { ldl(IC(18)); ldl(IC(19)); } }
    ldr(IC(0)); wait_vm<4>();
    post(pa, att ? 1 : 2);
    STAMP(5);
    if (!att) __builtin_amdgcn_s_setprio(LPM);
    ldq(IC(5)); ldq(IC(6)); ldq(IC(7)); ldq(IC(8));
    if (att) {
      await<1, 8>(4);
      __builtin_amdgcn_s_setprio(LPA);
      ldl(IC(5)); ldl(IC(6)); ldl(IC(7)); ldl(IC(8));
      if (kq == 0) { ldl(IC(18)); ldl(IC(19)); }
    }
    if (lw == 3) touch(mkr(b2, D_ * 2), n_ * 128);
    wait_vm<0>();
    post(pa, 2);
#define PUT(r, off) dsw128<off>(wa + (r / 3) * 49152, pre[r][0]); dsw128<off + 1024>(wa + (r / 3) * 49152, pre[r][1]); dsw128<off + 2048>(wa + (r / 3) * 49152, pre[r][2]); dsw128<off + 3072>(wa + (r / 3) * 49152, pre[r][3]);
    await_pair(LDS_CONS + kq * 8, 1);
    PUT(0, 0) PUT(1, 16384) PUT(2, 32768)
    post(pa, 3);
    __builtin_amdgcn_s_setprio(LPP);
    STAMP(6);
    await_pair(LDS_CONS + kq * 8, 3);
    PUT(3, 0) PUT(4, 16384) PUT(5, 32768)
    post(pa, 4);
    await_pair(LDS_CONS + kq * 8, 6);
    PUT(6, 0) PUT(7, 16384) PUT(8, 32768)
    post(pa, 5);
    fu = lw;
  } else {
    if (tid < 24) *(LAS u32x4*)(shm + (tid >> 3) * SLOT + XOFF + (tid & 7) * 16) = u32x4{0x3f803f80u, 0x3f803f80u, 0x3f803f80u, 0x3f803f80u};
    if (tid >= 448 && tid < 480) *(LAS unsigned*)(shm + LDS_CNT + 4 * (tid - 448)) = 0u;
    if (w == 5 && att) att_table(shm, lane);
    const bool light = att ? (w >= 6) : (w >= 4);
    if (light) s1_compute<3>(shm, att, w, lane, p_, j_, hf, ws PR_PASS);
    else s1_compute<4>(shm, att, w, lane, p_, j_, hf, ws PR_PASS);
    STAMP(3);
    if (att) s1_attention(shm, w, p_, j_, ws PR_PASS);
    STAMP(4);
    const unsigned tag = __builtin_amdgcn_readfirstlane(*(LAS unsigned*)(shm + LDS_CNT + 4));
    GAS u64* flags = (GAS u64*)(ws + WS_FLAG);
    const u64 fv = flagval(tag);
    wait_vm<0>();
    {
      const int lane = lane_id(), kind = att ? 0 : 1 + (w >> 2);
      unsigned old = 0;
      if (lane == 0) old = __hip_atomic_fetch_add((LAS unsigned*)(shm + LDS_CNT + (kind == 1 ? 120 : 0)), 1u, __ATOMIC_RELAXED, __HIP_MEMORY_SCOPE_WORKGROUP);
      if (__builtin_amdgcn_readfirstlane(old) == (att ? 7u : 3u)) {
        GAS u64* dst = lane == 10 ? flags + p_ * 16 + rid : (GAS u64*)(ws + WS_BOX) + (p_ * 10 + lane) * 32 + 8 * kind + j_;
        if (lane <= 10) __hip_atomic_store(dst, fv, __ATOMIC_RELAXED, __HIP_MEMORY_SCOPE_AGENT);
      }
    }
    STAMP(5);
    if (w == 0) watch(ws, G0, G0 + (att ? 3 : 5), n_, 1, 15, fv);
    if (kq == 0) {
      if (att) s2_compute<20, 2>(w, kq, G0, n_, rot, ws, fv PR_PASS);
      else s2_compute<20, 3>(w, kq, G0, n_, rot, ws, fv PR_PASS);
    } else {
      if (att) s2_compute<18, 2>(w, kq, G0, n_, rot, ws, fv PR_PASS);
      else s2_compute<18, 3>(w, kq, G0, n_, rot, ws, fv PR_PASS);
    }
    if (blockIdx.x == 0 && w == 3) {
      const int lane = lane_id();
#pragma nounroll
      for (unsigned spins = 0; spins < SPIN_CAP; spins++) {
        bool ok = true;
#pragma unroll
        for (int k = 0; k < 4; k++) ok &= __hip_atomic_load(flags + lane + 64 * k, __ATOMIC_RELAXED, __HIP_MEMORY_SCOPE_AGENT) == fv;
        if (__all(ok)) break;
        __builtin_amdgcn_s_sleep(8);
      }
      if (lane == 0) __hip_atomic_store((GAS unsigned*)ws, tag, __ATOMIC_RELAXED, __HIP_MEMORY_SCOPE_AGENT);
    }
    if (att || w == 0 || w > 2) { STAMP(11); FINISH(); return; }
    fu = w + 3;
  }
  {
    const int lane = lane_id();
    const u32x4 bv = *(const GAS u32x4*)(b2 + n_ * 128 + (lane & 7) * 16);
    int foff[2], pr0[2];
    u32x4 xv[2];
    f32x2 gt[2][4];
#pragma unroll
    for (int hh = 0; hh < 2; hh++) {
      const int frow = 48 * hh + 8 * fu + (lane >> 3);
      foff[hh] = fin_off(G0 + (att ? 2 : 3) * hh, n_, 8 * fu, lane);
      xv[hh] = *(const GAS u32x4*)((const char*)x + foff[hh]);
      pr0[hh] = (frow >> 4) * 16384 + (frow & 15) * 256 + (((2 * (lane & 7)) ^ (frow & 7)) << 4);
      pin(pr0[hh]);
    }
#pragma unroll
    for (int hh = 0; hh < 2; hh++) {
      await<1, 3>(12 + hh);
      f32x4 y[2];
#pragma unroll
      for (int e = 0; e < 2; e++) {
        lptr pr = shm + (pr0[hh] ^ (16 * e));
        y[e] = *(LAS f32x4*)(pr + 4096) + *(LAS f32x4*)(pr + 8192) + *(LAS f32x4*)(pr + 12288);
      }
#pragma unroll
      for (int k = 0; k < 4; k++) {
        f32x2 yy = f32x2{y[k >> 1][2 * (k & 1)], y[k >> 1][2 * (k & 1) + 1]} + f32x2{blo(bv[k]), bhi(bv[k])};
        f32x2 t = yy * -1.4426950408889634f;
        f32x2 e = f32x2{__builtin_amdgcn_exp2f(t[0]), __builtin_amdgcn_exp2f(t[1])} + 1.f;
        gt[hh][k] = yy * f32x2{__builtin_amdgcn_rcpf(e[0]), __builtin_amdgcn_rcpf(e[1])};
        pin(gt[hh][k]);
      }
    }
    STAMP(10);
#pragma unroll
    for (int hh = 0; hh < 2; hh++) {
      await<0, 1>(28 + hh);
      u32x4 o;
#pragma unroll
      for (int e = 0; e < 2; e++) {
        const f32x4 s = *(LAS f32x4*)(shm + (pr0[hh] ^ (16 * e)));
        o[2 * e] = pk2(f32x2{s[0], s[1]} * gt[hh][2 * e] + f32x2{blo(xv[hh][2 * e]), bhi(xv[hh][2 * e])});
        o[2 * e + 1] = pk2(f32x2{s[2], s[3]} * gt[hh][2 * e + 1] + f32x2{blo(xv[hh][2 * e + 1]), bhi(xv[hh][2 * e + 1])});
      }
      __builtin_nontemporal_store(o, (GAS u32x4*)(out + foff[hh]));
    }
  }
  STAMP(11);
  FINISH();
}

extern "C" void kernel_launch(void* const* d_in, const int* in_sizes, int n_in, void* d_out, int out_size, void* d_ws,
                              size_t ws_size, hipStream_t stream) {
  (void)hipFuncSetAttribute((const void*)fused_block, hipFuncAttributeMaxDynamicSharedMemorySize, LDS_TOTAL);
  fused_block<<<256, 768, LDS_TOTAL, stream>>>(d_in[0], d_in[1], d_in[2], d_in[4], (const unsigned short*)d_in[5], d_in[6],
                                               (const char*)d_in[7], (char*)d_out, (char*)d_ws);
}
```

```cpp
#define WARMH 9
#define WEND 1
#define WMLP 1
#include <hip/hip_runtime.h>
#include <hip/hip_bf16.h>
#include <type_traits>
#include <utility>

typedef __attribute__((ext_vector_type(4))) short s16x4;
typedef __attribute__((ext_vector_type(8))) __bf16 bf16x8;
typedef __attribute__((ext_vector_type(2))) __bf16 bf16x2;
typedef __attribute__((ext_vector_type(4))) float f32x4;
typedef __attribute__((ext_vector_type(2))) float f32x2;
typedef __attribute__((ext_vector_type(4))) unsigned u32x4;
typedef __attribute__((ext_vector_type(2))) unsigned u32x2;
#define LAS __attribute__((address_space(3)))
#define GAS __attribute__((address_space(1)))
#define DEVI __device__ __forceinline__
typedef LAS char* lptr;
typedef unsigned long long u64;
#define IC(n) std::integral_constant<int, n>{}

constexpr int S_ = 2048, D_ = 640, F_ = 1728;
constexpr int KS_T = 74;
constexpr int WSLAB = 15360;
constexpr int XOFF = 2 * WSLAB;
constexpr int SLOT = 49152;
constexpr int LDS_TB = 86016;
constexpr int LDS_ST = 155648;
constexpr int LDS_CS = LDS_ST + 1152;
constexpr int LDS_B1 = LDS_CS + 1024;
constexpr int LDS_CNT = LDS_B1 + 1024;
constexpr int LDS_ZERO = LDS_CNT + 24;
constexpr int LDS_CONS = LDS_CNT + 64;
constexpr int LDS_PROD = LDS_CONS + 32;
constexpr int LDS_TAB = LDS_CNT + 128;
constexpr int LDS_TOTAL = 163840;
constexpr int WS_FLAG = 4096, WS_STAT = 8192, WS_BOX = 65536, WS_A2 = 1 << 20;
constexpr int A2_BYTES = 128 * KS_T * 1024;
#define AD3Y 3
#define AD3S 4
#define AD2Y 4
#define AD2S 5
#define ALN 0
#define YPRIO 0
#define DEXTRA 0
#define LATESET 0
#define L1PRIO 3
#define HOLD 0
#define THRA 32
#define MAP2 0
#define ORD 1
#define PRE9 0
#define LP1 0
#define INL0 5
#define INLR 2
#define INLN 0
#define WNE 1
#define WSTEP 2
#define TLBW 0
#define LPM 3
#define LPA 3
#define LPP 0
#define SPRIO 0
#define THR 32
#define KTX (((idx >> 1) ^ sub) & 1)
#define ROT2 (((ug + nl) & 3) | (nl & 4))
constexpr int CODE_BYTES = 46848;
constexpr unsigned SPIN_CAP = 1u << 20;

#define STAMP(idx)
#define FINISH()
#define STAMPC(k)
#define STAMPA(k) STAMP(k)
#define STAMPB(k)
#define PR_ARGS
#define PR_PASS

DEVI void bar_lgkm() { asm volatile("s_waitcnt lgkmcnt(0)\n\ts_barrier" ::: "memory"); }
DEVI void bar() { asm volatile("s_barrier" ::: "memory"); }
template <int N> DEVI void bar_vm() { asm volatile("s_waitcnt vmcnt(%0)\n\ts_barrier" ::"i"(N) : "memory"); }
template <int N> DEVI void wait_vm() { asm volatile("s_waitcnt vmcnt(%0)" ::"i"(N) : "memory"); }
DEVI unsigned pk2(float a, float b) { f32x2 v = {a, b}; return __builtin_bit_cast(unsigned, __builtin_convertvector(v, bf16x2)); }
DEVI unsigned pk2(f32x2 v) { return __builtin_bit_cast(unsigned, __builtin_convertvector(v, bf16x2)); }
DEVI float asf(unsigned u) { return __builtin_bit_cast(float, u); }
DEVI unsigned asu(float f) { return __builtin_bit_cast(unsigned, f); }
DEVI float blo(unsigned u) { return __builtin_bit_cast(float, u << 16); }
DEVI float bhi(unsigned u) { return __builtin_bit_cast(float, u & 0xffff0000u); }
DEVI u32x2 tr_read(lptr p) { return __builtin_bit_cast(u32x2, __builtin_amdgcn_ds_read_tr16_b64_v4i16((LAS s16x4*)p)); }
DEVI f32x4 mfma(u32x4 a, u32x4 b, f32x4 c) {
  return __builtin_amdgcn_mfma_f32_16x16x32_bf16(__builtin_bit_cast(bf16x8, a), __builtin_bit_cast(bf16x8, b), c, 0, 0, 0);
}
DEVI u32x4 cat(u32x2 a, u32x2 b) { return __builtin_shufflevector(a, b, 0, 1, 2, 3); }
DEVI void mfma_a(f32x4& c, u32x4 a, u32x4 b) { asm volatile("v_mfma_f32_16x16x32_bf16 %0, %1, %2, %0" : "+v"(c) : "v"(a), "v"(b)); }
template <int OFF> DEVI void ds128(u32x4& d, int addr) { asm volatile("ds_read_b128 %0, %1 offset:%2" : "=v"(d) : "v"(addr), "i"(OFF)); }
template <int OFF> DEVI void dstr(u32x2& d, int addr) { asm volatile("ds_read_b64_tr_b16 %0, %1 offset:%2" : "=v"(d) : "v"(addr), "i"(OFF)); }
template <int OFF> DEVI void dsw128(int addr, u32x4 d) { asm volatile("ds_write_b128 %0, %1 offset:%2" ::"v"(addr), "v"(d), "i"(OFF) : "memory"); }
DEVI u32x4 mkr(const void* p, unsigned bytes) {
  u64 a = (u64)p;
  return u32x4{(unsigned)a, (unsigned)(a >> 32) & 0xffffu, bytes, 0x00020000u};
}
DEVI void st_wt(u32x4 r, int voff, int soff, u32x4 v) {
  asm volatile("buffer_store_dwordx4 %0, %1, %2, %3 offen sc1\n\ts_nop 1" ::"v"(v), "v"(voff), "s"(r), "s"(soff) : "memory");
}
template <int LSTEP>
DEVI void dma4(u32x4 r, u64 mask, int l, int va, int vb, int s0, int s1, int s2, int s3) {
  asm volatile(
      "s_mov_b64 exec, %0\n\t"
      "s_mov_b32 m0, %1\n\ts_nop 0\n\tbuffer_load_dwordx4 %2, %4, %5 offen lds\n\t"
      "s_add_u32 m0, %1, %9\n\ts_nop 0\n\tbuffer_load_dwordx4 %3, %4, %6 offen lds\n\t"
      "s_add_u32 m0, %1, %10\n\ts_nop 0\n\tbuffer_load_dwordx4 %2, %4, %7 offen lds\n\t"
      "s_add_u32 m0, %1, %11\n\ts_nop 0\n\tbuffer_load_dwordx4 %3, %4, %8 offen lds\n\t"
      "s_mov_b64 exec, -1"
      :: "s"(mask), "s"(l), "v"(va), "v"(vb), "s"(r), "s"(s0), "s"(s1), "s"(s2), "s"(s3), "i"(LSTEP), "i"(2 * LSTEP), "i"(3 * LSTEP)
      : "memory", "scc");
}
DEVI void dma1(u32x4 r, u64 mask, int l, int v, int s0) {
  asm volatile("s_mov_b64 exec, %0\n\ts_mov_b32 m0, %1\n\ts_nop 0\n\tbuffer_load_dwordx4 %2, %3, %4 offen lds\n\ts_mov_b64 exec, -1"
               :: "s"(mask), "s"(l), "v"(v), "s"(r), "s"(s0) : "memory");
}
DEVI void setup_loads(u32x2 (&xr)[5], unsigned& ep, unsigned& bv, u32x4 rx, int vx, u32x4 re, u32x4 rb, int vb) {
  asm volatile("buffer_load_dwordx2 %0, %7, %8, 0 offen\n\tbuffer_load_dwordx2 %1, %7, %8, 0 offen offset:256\n\tbuffer_load_dwordx2 %2, %7, %8, 0 offen offset:512\n\t"
               "buffer_load_dwordx2 %3, %7, %8, 0 offen offset:768\n\tbuffer_load_dwordx2 %4, %7, %8, 0 offen offset:1024\n\t"
               "buffer_load_dword %5, off, %9, 0 sc1\n\tbuffer_load_ushort %6, %11, %10, 0 offen"
               : "=&v"(xr[0]), "=&v"(xr[1]), "=&v"(xr[2]), "=&v"(xr[3]), "=&v"(xr[4]), "=&v"(ep), "=&v"(bv)
               : "v"(vx), "s"(rx), "s"(re), "s"(rb), "v"(vb) : "memory");
}
template <int WHEN>
DEVI void bload4_if(u32x4 (&d)[4], u32x4 r, int v, int s0, int s1, int s2, int s3, int flag) {
#define LDR_BODY "s_cmp_lg_u32 %10, %11\n\ts_cbranch_scc1 .Lr%=\n\tbuffer_load_dwordx4 %0, %4, %5, %6 offen\n\tbuffer_load_dwordx4 %1, %4, %5, %7 offen\n\t" \
                 "buffer_load_dwordx4 %2, %4, %5, %8 offen\n\tbuffer_load_dwordx4 %3, %4, %5, %9 offen\n.Lr%=:"
  if (WHEN) asm volatile(LDR_BODY : "+v"(d[0]), "+v"(d[1]), "+v"(d[2]), "+v"(d[3]) : "v"(v), "s"(r), "s"(s0), "s"(s1), "s"(s2), "s"(s3), "s"(flag), "i"(WHEN) : "scc");
  else asm volatile(LDR_BODY : "=&v"(d[0]), "=&v"(d[1]), "=&v"(d[2]), "=&v"(d[3]) : "v"(v), "s"(r), "s"(s0), "s"(s1), "s"(s2), "s"(s3), "s"(flag), "i"(WHEN) : "scc");
}
template <int N0, int N1>
DEVI void bar_vm_sel(int flag) {
  asm volatile("s_cmp_lg_u32 %0, 0\n\ts_cbranch_scc1 .La%=\n\ts_waitcnt vmcnt(%1)\n\ts_branch .Lb%=\n.La%=:\n\ts_waitcnt vmcnt(%2)\n.Lb%=:\n\ts_barrier"
               ::"s"(flag), "i"(N0), "i"(N1) : "memory", "scc");
}
template <class T> DEVI void pin(T& v) { asm volatile("" : "+v"(v)); }
DEVI void dsw_col0(int a0, f32x4 d0, int a1, f32x4 d1, int a2, f32x4 d2) {
  asm volatile("s_mov_b64 exec, %6\n\tds_write_b128 %0, %1\n\tds_write_b128 %2, %3\n\tds_write_b128 %4, %5\n\ts_mov_b64 exec, -1"
               ::"v"(a0), "v"(d0), "v"(a1), "v"(d1), "v"(a2), "v"(d2), "s"(0x0001000100010001ull) : "memory");
}
DEVI void reload_sc1(u32x4& d, u32x4 r, int v, int s0) { asm volatile("buffer_load_dwordx4 %0, %1, %2, %3 offen sc1" : "+v"(d) : "v"(v), "s"(r), "s"(s0) : "memory"); }
template <int OFF> DEVI void redstr(u32x2& d, int addr) { asm volatile("ds_read_b64_tr_b16 %0, %1 offset:%2" : "+v"(d) : "v"(addr), "i"(OFF)); }
DEVI void vinc(int& d, int s) { asm volatile("v_add_u32 %0, %1, %0" : "+v"(d) : "s"(s)); }
DEVI void vadd(int& d, int a, int s) { asm volatile("v_add_u32 %0, %2, %1" : "=v"(d) : "v"(a), "s"(s)); }
template <class F, int... I> DEVI void sfor_(F f, std::integer_sequence<int, I...>) { (f(std::integral_constant<int, I>{}), ...); }
template <int N, class F> DEVI void sfor(F f) { sfor_(f, std::make_integer_sequence<int, N>{}); }
DEVI void stouch1(unsigned& sink, const void* p, int off) { asm volatile("s_load_dword %0, %1, %2" : "+s"(sink) : "s"(p), "s"(off)); }
DEVI void touch_sc1(u32x4 r, int v) {
  asm volatile("s_mov_b32 m0, %2\n\ts_nop 0\n\tbuffer_load_dword %0, %1, 0 offen sc1 lds" ::"v"(v), "s"(r), "i"(LDS_TOTAL - 256) : "memory");
}
DEVI void touch(u32x4 r, int v) {
  asm volatile("s_mov_b32 m0, %2\n\ts_nop 0\n\tbuffer_load_dword %0, %1, 0 offen lds" ::"v"(v), "s"(r), "i"(LDS_TOTAL - 256) : "memory");
}
DEVI void bload(u32x4& d, u32x4 r, int v, int s0) { asm volatile("buffer_load_dwordx4 %0, %1, %2, %3 offen" : "=v"(d) : "v"(v), "s"(r), "s"(s0)); }
DEVI void bload_sc1(u32x4& d, u32x4 r, int v, int s0) { asm volatile("buffer_load_dwordx4 %0, %1, %2, %3 offen sc1" : "=v"(d) : "v"(v), "s"(r), "s"(s0) : "memory"); }
DEVI void arrive(int k) {
  asm volatile("s_mov_b64 exec, 1\n\tds_add_u32 %0, %1\n\ts_mov_b64 exec, -1" ::"v"(LDS_CNT + 4 * k), "v"(1) : "memory");
}
template <int SLEEP, unsigned N>
DEVI void await(int k) {
  unsigned v;
  for (;;) {
    asm volatile("ds_read_b32 %0, %1\n\ts_waitcnt lgkmcnt(0)" : "=v"(v) : "v"(LDS_CNT + 4 * k) : "memory");
    if (__builtin_amdgcn_readfirstlane(v) >= N) break;
    if (SLEEP) __builtin_amdgcn_s_sleep(SLEEP);
  }
}
DEVI void post(int addr, int val) { asm volatile("ds_write_b32 %0, %1" ::"v"(addr), "v"(val) : "memory"); }
DEVI void peek(unsigned& v, int addr) { asm volatile("ds_read_b32 %0, %1" : "=v"(v) : "v"(addr) : "memory"); }
DEVI void require(unsigned v, int addr, unsigned n) {
  unsigned t;
  asm volatile("v_readfirstlane_b32 %1, %0\n\ts_cmp_ge_u32 %1, %3\n\ts_cbranch_scc1 .Lq%=\n"
               ".Lw%=:\n\tds_read_b32 %0, %2\n\ts_waitcnt lgkmcnt(0)\n\tv_readfirstlane_b32 %1, %0\n\ts_cmp_ge_u32 %1, %3\n\ts_cbranch_scc0 .Lw%=\n.Lq%=:"
               : "+&v"(v), "=&s"(t) : "v"(addr), "s"(n) : "memory", "scc");
}
DEVI void await_pair(int addr, unsigned n) {
  for (;;) {
    u32x2 v;
    asm volatile("ds_read_b64 %0, %1\n\ts_waitcnt lgkmcnt(0)" : "=v"(v) : "v"(addr) : "memory");
    const unsigned a = v[0], b = v[1];
    if (__builtin_amdgcn_readfirstlane(a < b ? a : b) >= n) break;
    __builtin_amdgcn_s_sleep(1);
  }
}
DEVI int lane_id() { int l; asm volatile("v_mbcnt_lo_u32_b32 %0, -1, 0\n\tv_mbcnt_hi_u32_b32 %0, -1, %0" : "=v"(l)); return l; }
DEVI int sig(int i) { return 4 * (i & 3) + (i >> 2); }
DEVI u64 flagval(unsigned tag) { return ((u64)(tag ^ 0x5bd1e995u) << 32) | tag; }

DEVI float xmax16(float v) { auto r = __builtin_amdgcn_permlane16_swap(asu(v), asu(v), false, false); return fmaxf(asf(r[0]), asf(r[1])); }
DEVI float xmax32(float v) { auto r = __builtin_amdgcn_permlane32_swap(asu(v), asu(v), false, false); return fmaxf(asf(r[0]), asf(r[1])); }
DEVI float xadd16(float v) { auto r = __builtin_amdgcn_permlane16_swap(asu(v), asu(v), false, false); return asf(r[0]) + asf(r[1]); }
DEVI float xadd32(float v) { auto r = __builtin_amdgcn_permlane32_swap(asu(v), asu(v), false, false); return asf(r[0]) + asf(r[1]); }
template <int CTRL> DEVI float dpp(float v) { return asf(__builtin_amdgcn_mov_dpp(asu(v), CTRL, 0xf, 0xf, true)); }
DEVI float half_sum(float v) {
  v += dpp<0xb1>(v); v += dpp<0x4e>(v); v += dpp<0x141>(v); v += dpp<0x140>(v);
  return xadd16(v);
}

DEVI int h_slot(int j, int l) { const int t = l < 4 ? 4 * j + l : 28 + 3 * j + l, d = (t * 43) >> 7; return 18 * (t - 3 * d) + d; }

DEVI int row_token(int p_, int pos) {
  const int tau = pos >> 4, i = pos & 15;
  int tok = tau == 0 ? p_ * 128 - 16 + 8 * (i & 1) + 2 * (i >> 2) + ((i & 3) < 2) : p_ * 128 + 32 * ((tau - 1) >> 1) + 2 * sig(i) + ((tau - 1) & 1);
  return tok < 0 ? 0 : tok;
}

struct ColGeo { int A, B, C, ct; };
DEVI ColGeo colgeo(bool att, int nw, int mw, int c, bool heavy) {
  const int cc = heavy ? (c ^ (2 * mw)) : c;
  const int ct = 4 * nw + cc, sec = (ct * 13) >> 6, hh = ct - 5 * sec;
  const bool q4 = hh == 4, sp = !heavy && c == 2;
  ColGeo r;
  r.A = att ? sec * 160 + (q4 ? 32 : 40 * hh) : heavy ? 128 * nw + 32 * cc : (sp ? 352 + 16 * nw : 128 + 64 * nw + 32 * c);
  r.B = att && q4 ? 40 : 8;
  r.C = att ? (q4 ? 80 : 16) : (sp ? 32 : 16);
  r.ct = ct;
  return r;
}

template <int NC>
struct Frags { u32x4 x[5]; u32x2 w[NC][2]; };

template <int N> DEVI void wait_lg() { asm volatile("s_waitcnt lgkmcnt(%0)" ::"i"(N) : "memory"); }

template <int NC, int K>
DEVI void s1_rd(Frags<NC>& n, int xo, int xh, const int (&wa)[NC]) {
  if constexpr (K == 0) ds128<0>(n.x[0], xo);
  else if constexpr (K <= 2 * NC) { if ((K - 1) & 1) dstr<7680>(n.w[(K - 1) >> 1][1], wa[(K - 1) >> 1]); else dstr<0>(n.w[(K - 1) >> 1][0], wa[(K - 1) >> 1]); }
  else if constexpr (K < 2 * NC + 4) ds128<(K - 2 * NC) * 2048>(n.x[K - 2 * NC], xo);
  else if constexpr (K == 2 * NC + 4) ds128<0>(n.x[4], xh);
}
template <int NC, int NH, int K>
DEVI void s1_mm(const Frags<NC>& c, f32x4 (&acc)[4][NC], f32x4 (&hacc)[NH]) {
  if constexpr (K < 4 * NC) mfma_a(acc[K / NC][K % NC], cat(c.w[K % NC][0], c.w[K % NC][1]), c.x[K / NC]);
  else mfma_a(hacc[K - 4 * NC], cat(c.w[K - 4 * NC][0], c.w[K - 4 * NC][1]), c.x[4]);
}
template <int NC, int NH, bool BAR>
DEVI void s1_step(const Frags<NC>& c, Frags<NC>& n, int xo, int xh, const int (&wa)[NC], f32x4 (&acc)[4][NC], f32x4 (&hacc)[NH]) {
  constexpr int NM = 4 * NC + NH, NR = 2 * NC + 5, D = BAR ? NM - NR - DEXTRA : 0;
  sfor<NM>([&](auto KK) {
    constexpr int k = decltype(KK)::value;
    constexpr int need = k < NC ? 2 * k + 2 : (k % NC == 0 && k <= 4 * NC) ? 2 * NC + k / NC : -1;
    if constexpr (BAR && k == D) bar_lgkm();
    if constexpr (need >= 0 && (!BAR || k < D)) wait_lg<NR - 1 - need + (BAR ? 0 : (k < NR ? k : NR))>();
    s1_mm<NC, NH, k>(c, acc, hacc);
    if constexpr (k >= D) s1_rd<NC, k - D>(n, xo, xh, wa);
  });
}

template <int OFF> DEVI void dsw64(int addr, u32x2 d) { asm volatile("ds_write_b64 %0, %1 offset:%2" ::"v"(addr), "v"(d), "i"(OFF) : "memory"); }
template <int OFF> DEVI void ds64(u32x2& d, int addr) { asm volatile("ds_read_b64 %0, %1 offset:%2" : "=v"(d) : "v"(addr), "i"(OFF)); }

template <int NC>
DEVI void s1_compute(lptr shm, bool att, int w, int lane, int p_, int j_, int hf, char* ws PR_ARGS) {
  constexpr int NH = NC == 4 ? 2 : 3;
  const int mw = w & 1, nw = w >> 1, i = lane & 15, g = lane >> 4;
  f32x4 acc[4][NC], hacc[NH];
#pragma unroll
  for (int m = 0; m < 4; m++)
#pragma unroll
    for (int c = 0; c < NC; c++) { acc[m][c] = f32x4{0, 0, 0, 0}; asm volatile("" : "+v"(acc[m][c])); }
#pragma unroll
  for (int c = 0; c < NH; c++) { hacc[c] = f32x4{0, 0, 0, 0}; asm volatile("" : "+v"(hacc[c])); }
  int wb[NC], cb[NC], ct[NC];
#pragma unroll
  for (int c = 0; c < NC; c++) {
    const ColGeo cg = colgeo(att, nw, mw, c, NC == 4);
    wb[c] = (lane >> 2) * 480 + cg.A + cg.B * (lane & 1) + cg.C * ((lane >> 1) & 1);
    cb[c] = LDS_CS + 2 * (cg.A + cg.B * (g & 1) + cg.C * (g >> 1));
    ct[c] = cg.ct;
    asm volatile("" : "+v"(cb[c]), "+v"(wb[c]));
  }
  int xb = XOFF + i * 128 + ((g ^ (i >> 1) ^ (4 * hf)) << 4);
  int sta = LDS_ST + i * 8;
  asm volatile("" : "+v"(xb), "+v"(sta));
  const int own = (1 + 4 * mw) * 2048;
  Frags<NC> fa, fb;
  int wa[NC];
  STAMPB(12);
  if (YPRIO && w >= 4) __builtin_amdgcn_s_setprio(1);
  bar_lgkm();
  STAMP(1);
  sfor<2 * NC + 5>([&](auto K) { s1_rd<NC, decltype(K)::value>(fa, xb + own, xb, wb); });
#pragma unroll
  for (int c = 0; c < NC; c++) wa[c] = wb[c] + WSLAB;
  s1_step<NC, NH, true>(fa, fb, (xb ^ 64) + own, xb ^ 64, wa, acc, hacc);
  int sl = 0;
#pragma nounroll
  for (int T = 1; T < 10; T++) {
    sl = sl == 2 * SLOT ? 0 : sl + SLOT;
    if (YPRIO && T == 9) __builtin_amdgcn_s_setprio(0);
#pragma unroll
    for (int c = 0; c < NC; c++) wa[c] = wb[c] + sl;
    s1_step<NC, NH, true>(fb, fa, xb + sl + own, xb + sl, wa, acc, hacc);
#pragma unroll
    for (int c = 0; c < NC; c++) wa[c] = wb[c] + sl + WSLAB;
    s1_step<NC, NH, false>(fa, fb, (xb ^ 64) + sl + own, (xb ^ 64) + sl, wa, acc, hacc);
  }
  wait_lg<0>();
  arrive(2);
  sfor<NH>([&](auto K) { s1_mm<NC, NH, 4 * NC + decltype(K)::value>(fb, acc, hacc); });
  u32x2 stu[5];
  u32x4 b1u[NC], csu[NC];
  sfor<4 * NC>([&](auto KK) {
    constexpr int k = decltype(KK)::value;
    s1_mm<NC, NH, k>(fb, acc, hacc);
    if constexpr (k < 4) ds64<(1 + k) * 128>(stu[k], sta + mw * 512);
    if constexpr (k == 4) ds64<0>(stu[4], sta);
    if constexpr (k == 5) {
      dsw_col0(cb[0], hacc[0], cb[1], hacc[1], cb[NH - 1], hacc[NH - 1]);
      if (NC == 4) arrive(8 + nw);
    }
    if constexpr (k >= 6 && k < 6 + NC) ds128<LDS_B1 - LDS_CS>(b1u[k - 6], cb[k - 6]);
  });
  STAMP(2);
  if (NC == 4) await<0, 2>(8 + nw);
#pragma unroll
  for (int c = 0; c < NC; c++) ds128<0>(csu[c], cb[c]);
  asm volatile("s_waitcnt lgkmcnt(0)\n\ts_nop 7" ::: "memory");
  f32x2 st[5];
  f32x4 cs[NC], b1[NC];
#pragma unroll
  for (int m = 0; m < 5; m++) { asm volatile("" : "+v"(stu[m])); st[m] = __builtin_bit_cast(f32x2, stu[m]); }
#pragma unroll
  for (int c = 0; c < NC; c++) { asm volatile("" : "+v"(csu[c]), "+v"(b1u[c])); cs[c] = __builtin_bit_cast(f32x4, csu[c]); b1[c] = __builtin_bit_cast(f32x4, b1u[c]); }
#pragma unroll
  for (int m = 0; m < 4; m++)
#pragma unroll
    for (int c = 0; c < NC; c++) pin(acc[m][c]);
#pragma unroll
  for (int c = 0; c < NH; c++) pin(hacc[c]);
  STAMPA(12);
#define SB __builtin_amdgcn_sched_barrier(0)
  const int l2 = lane_id();
  if (att) {
    const int g2 = l2 >> 4, piece = LDS_TB + ((l2 & 48) + ((l2 & 15) ^ (8 * (g2 >> 1)))) * 8;
    sfor<NC>([&](auto C) {
      constexpr int c = decltype(C)::value;
      const int ah = piece + ct[c] * 4608, ao = ah + (1 + 4 * mw) * 512;
      u32x2 o[5];
      SB;
#pragma unroll
      for (int m = 0; m < (c < NH ? 5 : 4); m++) {
        f32x4 a = m < 4 ? acc[m < 4 ? m : 0][c] : hacc[c < NH ? c : 0];
        f32x4 v = a * st[m][0] + cs[c] * st[m][1];
        o[m] = u32x2{pk2(v[0], v[1]), pk2(v[2], v[3])};
      }
      dsw64<0>(ao, o[0]); dsw64<512>(ao, o[1]); dsw64<1024>(ao, o[2]); dsw64<1536>(ao, o[3]);
      if (c < NH) dsw64<0>(ah, o[4]);
    });
  } else {
    const u32x4 ra = mkr(ws + WS_A2, A2_BYTES);
    const int base = (8 * p_ + 4 * mw) * KS_T * 1024;
    const int o0 = base + (NC == 4 || j_ < 7 || nw == 2 ? h_slot(j_, NC == 4 ? 2 * nw + mw : 2 + nw) * 1024 : 0x70000000);
    const int o1 = base + (NC == 4 ? h_slot(j_, 2 * nw + (mw ^ 1)) * 1024 : j_ < 7 ? h_slot(j_, 6) * 1024 + (nw - 2) * 512 : 0x70000000);
    const float c1 = -2.f * 0.7978845608028654f * 1.4426950408889634f, c2 = c1 * 0.044715f;
    unsigned sp[2];
#pragma unroll
    for (int m = 0; m < 4; m++) {
      f32x4 v[NC], t[NC];
      SB;
#pragma unroll
      for (int c = 0; c < NC; c++) t[c] = cs[c] * st[m][1] + b1[c];
      SB;
#pragma unroll
      for (int c = 0; c < NC; c++) v[c] = acc[m][c] * st[m][0] + t[c];
      SB;
#pragma unroll
      for (int c = 0; c < NC; c++) t[c] = v[c] * v[c];
      SB;
#pragma unroll
      for (int c = 0; c < NC; c++) t[c] = t[c] * c2 + c1;
      SB;
#pragma unroll
      for (int c = 0; c < NC; c++) t[c] = t[c] * v[c];
      SB;
#pragma unroll
      for (int c = 0; c < NC; c++)
#pragma unroll
        for (int e = 0; e < 4; e++) t[c][e] = __builtin_amdgcn_exp2f(t[c][e]);
      SB;
#pragma unroll
      for (int c = 0; c < NC; c++) t[c] = t[c] + 1.f;
      SB;
#pragma unroll
      for (int c = 0; c < NC; c++)
#pragma unroll
        for (int e = 0; e < 4; e++) t[c][e] = __builtin_amdgcn_rcpf(t[c][e]);
      SB;
#pragma unroll
      for (int c = 0; c < NC; c++) v[c] = v[c] * t[c];
      SB;
      unsigned o[NC][2];
#pragma unroll
      for (int c = 0; c < NC; c++) { o[c][0] = pk2(v[c][0], v[c][1]); o[c][1] = pk2(v[c][2], v[c][3]); }
      st_wt(ra, l2 * 16, o0 + m * KS_T * 1024, u32x4{o[0][0], o[0][1], o[1][0], o[1][1]});
      if (NC == 4) st_wt(ra, l2 * 16, o1 + m * KS_T * 1024, u32x4{o[2][0], o[2][1], o[NC - 1][0], o[NC - 1][1]});
      else if (m & 1) {
        auto s0 = __builtin_amdgcn_permlane32_swap(sp[0], o[2][0], false, false);
        auto s1 = __builtin_amdgcn_permlane32_swap(sp[1], o[2][1], false, false);
        st_wt(ra, (l2 >> 5) * (KS_T * 1024) + (l2 & 31) * 16, o1 + (m - 1) * KS_T * 1024, u32x4{s0[0], s1[0], s0[1], s1[1]});
      } else { sp[0] = o[2][0]; sp[1] = o[2][1]; }
    }
  }
}

template <bool MAX> DEVI float red4(const float (&v)[4]) {
  auto op = [](unsigned a, unsigned b) { float r; if (MAX) asm("v_max_f32 %0, %1, %2" : "=v"(r) : "v"(a), "v"(b)); else r = asf(a) + asf(b); return r; };
  auto a = __builtin_amdgcn_permlane32_swap(asu(v[0]), asu(v[1]), false, false);
  auto b = __builtin_amdgcn_permlane32_swap(asu(v[2]), asu(v[3]), false, false);
  auto c = __builtin_amdgcn_permlane16_swap(asu(op(a[0], a[1])), asu(op(b[0], b[1])), false, false);
  return op(c[0], c[1]);
}
DEVI void bcast4(float r, float (&v)[4]) {
  auto e = __builtin_amdgcn_permlane16_swap(asu(r), asu(r), false, false);
  unsigned e0 = e[0], e1 = e[1];
  auto a = __builtin_amdgcn_permlane32_swap(e0, e0, false, false);
  auto b = __builtin_amdgcn_permlane32_swap(e1, e1, false, false);
  v[0] = asf(a[0]); v[1] = asf(a[1]); v[2] = asf(b[0]); v[3] = asf(b[1]);
}

DEVI void att_table(lptr shm, int lane) {
  const int i = lane & 15, g = lane >> 4, p = lane & 3;
  const int d = sig(i) - g;
  const unsigned NEG = 0xf149f2cau;
  bool vo[4], vp[4];
#pragma unroll
  for (int r = 0; r < 4; r++) { vo[r] = (unsigned)(d - 4 * r) <= 7u; vp[r] = d + 16 - 4 * r <= 7; }
  auto pm = [](bool lo, bool hi) { return (lo ? 0xffffu : 0u) | (hi ? 0xffff0000u : 0u); };
  LAS u32x4* t = (LAS u32x4*)(shm + LDS_TAB) + lane;
  t[0] = u32x4{vo[0] ? 0u : NEG, vo[1] ? 0u : NEG, vo[2] ? 0u : NEG, vo[3] ? 0u : NEG};
  t[64] = u32x4{pm(vp[2], vp[3]), pm(vo[0], vo[1]), vp[2] ? 0u : NEG, vp[3] ? 0u : NEG};
  t[128] = u32x4{pm(vo[2], vo[3]), (unsigned)(LDS_TB + (g * 16 + (i ^ (8 * (g >> 1)))) * 8), (unsigned)(LDS_TB + (p * 16 + ((lane >> 2) ^ (8 * (p >> 1)))) * 8), 0u};
}
DEVI int sel(u64 mask, int a, int b) { int r; asm("v_cndmask_b32_e64 %0, %2, %1, %3" : "=v"(r) : "v"(a), "v"(b), "s"(mask)); return r; }

DEVI void s1_attention(lptr shm, int w, int p_, int j_, char* ws PR_ARGS) {
  arrive(3);
  const int lane = lane_id();
  const int tau = (w + 1) * 512, taup = w >= 2 ? tau - 1024 : 0, flip = w == 1 ? 16 : 0;
  const LAS u32x4* t = (const LAS u32x4*)(shm + LDS_TAB) + lane;
  const u32x4 t0 = t[0], t2 = t[128];
  u32x4 t1 = t[64];
  if (p_ == 0 && w < 2) { t1[0] = 0u; t1[2] = 0xf149f2cau; t1[3] = 0xf149f2cau; }
  const f32x4 mO = __builtin_bit_cast(f32x4, t0), mP = __builtin_bit_cast(f32x4, t1);
  const unsigned M1 = t1[0], M2 = t1[1], M3 = t2[0];
  const int po = t2[1] + tau, pp = (t2[1] ^ flip) + taup, to = t2[2] + tau, tp = (t2[2] ^ flip) + taup;
  int aq[4], avo[4], avp[4];
#pragma unroll
  for (int h = 0; h < 4; h++) {
    aq[h] = sel(0xffffull << (16 * h), po, LDS_ZERO - 4 * 4608);
    avo[h] = sel(0x1111111111111111ull << h, to, LDS_ZERO - 14 * 4608);
    avp[h] = sel(0x1111111111111111ull << h, tp, LDS_ZERO - 14 * 4608);
    asm volatile("" : "+v"(aq[h]), "+v"(avo[h]), "+v"(avp[h]));
  }
  STAMPA(14);
  await<0, 8>(3);
  STAMPA(15);
  u32x2 qa[4][2], ko[4][2], kp[4][2], va[4][2], vb[4][2];
  f32x4 sp[4], so[4];
  auto rd = [&](auto H) {
    constexpr int h = decltype(H)::value;
    ds64<(5 + h) * 4608>(ko[h][0], po); ds64<9 * 4608>(ko[h][1], po); ds64<h * 4608>(qa[h][0], po); ds64<4 * 4608>(qa[h][1], aq[h]);
    ds64<(5 + h) * 4608>(kp[h][0], pp); ds64<9 * 4608>(kp[h][1], pp);
  };
  auto rv = [&](auto H) {
    constexpr int h = decltype(H)::value;
    dstr<(10 + h) * 4608>(va[h][0], tp); dstr<(10 + h) * 4608>(va[h][1], to); dstr<14 * 4608>(vb[h][0], avp[h]); dstr<14 * 4608>(vb[h][1], avo[h]);
  };
  auto qk = [&](auto H, auto N) {
    constexpr int h = decltype(H)::value;
    asm volatile("s_waitcnt lgkmcnt(%6)" : "+v"(ko[h][0]), "+v"(ko[h][1]), "+v"(qa[h][0]), "+v"(qa[h][1]), "+v"(kp[h][0]), "+v"(kp[h][1]) : "i"(decltype(N)::value));
    so[h] = mfma(cat(ko[h][0], ko[h][1]), cat(qa[h][0], qa[h][1]), mO);
    sp[h] = mfma(cat(kp[h][0], kp[h][1]), cat(qa[h][0], qa[h][1]), mP);
  };
  rd(IC(0)); rd(IC(1));
  qk(IC(0), IC(6)); rd(IC(2));
  qk(IC(1), IC(6)); rd(IC(3));
  qk(IC(2), IC(6)); rv(IC(0)); rv(IC(1));
  qk(IC(3), IC(8)); rv(IC(2));
  asm volatile("s_waitcnt lgkmcnt(8)");
  rv(IC(3));
  const float qs = 0.22360679774997896f * 1.4426950408889634f;
  float ee[4], eo[4], mx[4], l[4];
#pragma unroll
  for (int h = 0; h < 4; h++) { ee[h] = fmaxf(fmaxf(sp[h][2], so[h][0]), so[h][2]); eo[h] = fmaxf(fmaxf(sp[h][3], so[h][1]), so[h][3]); mx[h] = fmaxf(ee[h], eo[h]); }
  bcast4(red4<true>(mx) * -qs, mx);
#pragma unroll
  for (int h = 0; h < 4; h++) {
    f32x2 t = f32x2{ee[h], eo[h]} * qs + mx[h];
    ee[h] = __builtin_amdgcn_exp2f(t[0]); eo[h] = __builtin_amdgcn_exp2f(t[1]); l[h] = ee[h] + eo[h];
  }
  bcast4(__builtin_amdgcn_rcpf(red4<false>(l)), l);
  asm volatile("s_waitcnt lgkmcnt(0)" : "+v"(va[0][0]), "+v"(va[0][1]), "+v"(va[1][0]), "+v"(va[1][1]), "+v"(va[2][0]), "+v"(va[2][1]), "+v"(va[3][0]), "+v"(va[3][1]),
               "+v"(vb[0][0]), "+v"(vb[0][1]), "+v"(vb[1][0]), "+v"(vb[1][1]), "+v"(vb[2][0]), "+v"(vb[2][1]), "+v"(vb[3][0]), "+v"(vb[3][1]));
  f32x4 o4 = {0, 0, 0, 0}, oh[4];
#pragma unroll
  for (int h = 0; h < 4; h++) {
    const unsigned pk = pk2(f32x2{ee[h], eo[h]} * l[h]);
    u32x4 pf = {0u, pk & M1, pk & M2, pk & M3};
    o4 = mfma(cat(vb[h][0], vb[h][1]), pf, o4);
    oh[h] = mfma(cat(va[h][0], va[h][1]), pf, f32x4{0, 0, 0, 0});
  }
  const u32x4 ra = mkr(ws + WS_A2, A2_BYTES);
  const int base = ((8 * p_ + w) * KS_T + 54 + 5 * (j_ >> 1)) * 1024;
#pragma unroll
  for (int hp = 0; hp < 2; hp++)
    st_wt(ra, lane * 16, base + ((j_ & 1) * 3 + hp) * 1024,
          u32x4{pk2(oh[2 * hp][0], oh[2 * hp][1]), pk2(oh[2 * hp][2], oh[2 * hp][3]), pk2(oh[2 * hp + 1][0], oh[2 * hp + 1][1]), pk2(oh[2 * hp + 1][2], oh[2 * hp + 1][3])});
  unsigned a0 = pk2(o4[0], o4[1]), a1 = pk2(o4[2], o4[3]);
  auto s0 = __builtin_amdgcn_permlane32_swap(a0, a0, false, false);
  auto s1 = __builtin_amdgcn_permlane32_swap(a1, a1, false, false);
  st_wt(ra, lane < 32 ? lane * 16 : 0x70000000, base + 2 * 1024 + (j_ & 1) * 512, u32x4{s0[0], s1[0], s0[1], s1[1]});
  arrive(4);
}

DEVI int s2_perm(int s, bool o, int rot) { return s < 8 ? s ^ rot : (s < 10 || s >= 18) ? s ^ (rot & 1) : ((s - 10) ^ rot) + 10; }
DEVI int fin_off(int G0, int n_, int r0, int lane) {
  const int frow = r0 + (lane >> 3), fG = G0 + (frow >> 4);
  return (32 * (fG >> 1) + 2 * sig(frow & 15) + (fG & 1)) * 1280 + n_ * 128 + (lane & 7) * 16;
}
constexpr int s2_slot(int r) { return r < 18 ? (r % 9) * 16384 : 147456 + (r - 18) * 4096; }
DEVI void watch(char* ws, int Ga, int Gb, int n_, int kind, int word, u64 fv) {
  const int lane = lane_id();
  GAS u64* fp = (GAS u64*)(ws + WS_BOX) + ((((lane & 8) ? Gb : Ga) >> 3) * 10 + n_) * 32 + 8 * kind + (lane & 7);
#pragma nounroll
  for (unsigned spins = 0; spins < SPIN_CAP; spins++) {
    if (__all(__hip_atomic_load(fp, __ATOMIC_RELAXED, __HIP_MEMORY_SCOPE_AGENT) == fv)) break;
    __builtin_amdgcn_s_sleep(1);
  }
  post(LDS_CNT + 4 * word, 1);
}

template <int NS, int MT>
DEVI void s2_compute(int w, int kq, int G0, int n_, int rot, char* ws, u64 fv PR_ARGS) {
  constexpr int AD = MT == 3 ? (NS == 20 ? AD3S : AD3Y) : (NS == 20 ? AD2S : AD2Y);
  const int mh = w >> 2, lane = lane_id();
  const u32x4 ra = mkr(ws + WS_A2, A2_BYTES);
  int av = lane * 16;
  pin(av);
  const int ao = ((G0 + MT * mh) * KS_T + (NS == 20 ? 54 : 18 * (kq - 1))) * 1024;
  int bs[4];
#pragma unroll
  for (int c = 0; c < 4; c++) bs[c] = kq * 4096 + (lane >> 2) * 128 + ((c ^ ((lane >> 3) & 3)) << 5) + 8 * (lane & 3);
  f32x4 acc[MT][4];
#pragma unroll
  for (int m = 0; m < MT; m++)
#pragma unroll
    for (int c = 0; c < 4; c++) { acc[m][c] = f32x4{0, 0, 0, 0}; pin(acc[m][c]); }
  u32x4 a[AD][MT];
  u32x2 b[3][4][2];
  int ca = LDS_CONS + kq * 8 + mh * 4, pa = LDS_PROD + kq * 4;
  pin(ca); pin(pa);
  unsigned pv, lv;
  auto rdb = [&](auto R, auto K) {
    constexpr int r = decltype(R)::value, k = decltype(K)::value;
    constexpr int off = s2_slot(r), nxt = s2_slot(r + 1) / 65536 - off / 65536;
    if constexpr (r < 3) dstr<off % 65536 + (k & 1) * 2048>(b[r % 3][k >> 1][k & 1], bs[k >> 1]);
    else redstr<off % 65536 + (k & 1) * 2048>(b[r % 3][k >> 1][k & 1], bs[k >> 1]);
    if constexpr (nxt != 0 && (k & 1)) vinc(bs[k >> 1], nxt * 65536);
  };
  auto lda = [&](auto S, int m) {
    constexpr int s = decltype(S)::value;
    const int o = ao + m * KS_T * 1024 + (s2_perm(s, NS == 20, rot) << 10);
    if constexpr (s < AD) bload_sc1(a[s % AD][m], ra, av, o); else reload_sc1(a[s % AD][m], ra, av, o);
  };
  auto ldb = [&]() {
    peek(pv, pa); wait_lg<0>(); require(pv, pa, 1);
    sfor<3>([&](auto R) { sfor<8>([&](auto K) { rdb(R, K); }); wait_lg<(decltype(R)::value < 2 ? 4 : 0)>(); });
    post(ca, 1);
  };
  constexpr bool BFIRST = NS == 20 || MT == 3;
  if (BFIRST) ldb();
  STAMP(6);
  if (NS == 20) watch(ws, G0, G0 + 2 * MT - 1, n_, w == 0 ? 0 : 2, w == 0 ? 14 : 5, fv);
  await<0, 1>(NS == 20 ? 14 : 15);
  STAMP(7);
  sfor<AD - 1>([&](auto S) {
#pragma unroll
    for (int m = 0; m < MT; m++) lda(S, m);
  });
  if (!BFIRST) ldb();
  if (NS == 20) __builtin_amdgcn_s_setprio(SPRIO);
  sfor<NS - 3>([&](auto S) {
    constexpr int s = decltype(S)::value;
    constexpr bool LA = s + AD - 1 < NS;
    constexpr int later = NS - 1 - s < AD - 2 ? NS - 1 - s : AD - 2;
    if constexpr (s == 3) require(pv, pa, 2);
    if constexpr (s == 7) require(pv, pa, 3);
    if constexpr (s == 10) require(pv, pa, 4);
    if constexpr (s == 13) require(pv, pa, 5);
    if constexpr (s == 2 || s == 6 || s == 9 || s == 12) peek(pv, pa);
    if constexpr (NS == 18 && s == 11 - AD) require(lv, LDS_CNT + 4 * 5, 1);
    if constexpr (NS == 18 && s == 10 - AD) peek(lv, LDS_CNT + 4 * 5);
    if constexpr (s == NS - 4) peek(lv, ca ^ 4);
    sfor<MT>([&](auto M) {
      constexpr int m = decltype(M)::value;
      if constexpr (LA) lda(IC(s + AD - 1), m);
      wait_vm<MT * later + MT - 1 - m + (LA ? m + 1 : 0)>();
      if constexpr (m == 1 && s >= 2) wait_lg<4>();
      sfor<4>([&](auto C) {
        constexpr int c = decltype(C)::value;
        mfma_a(acc[m][c], cat(b[s % 3][c][0], b[s % 3][c][1]), a[s % AD][m]);
        if constexpr (s >= 1 && m < 2) rdb(IC(s + 2), IC(4 * m + c));
      });
    });
    if constexpr (s == 3 || s == 6 || s == 9 || s == 12) post(ca, s);
  });
  wait_lg<4>();
  sfor<8>([&](auto K) { rdb(IC(NS - 1), K); });
  if constexpr (AD == 3) {
#pragma unroll
    for (int m = 0; m < MT; m++) lda(IC(NS - 1), m);
  }
  int da[4];
#pragma unroll
  for (int c = 0; c < 4; c++) da[c] = mh * 49152 + kq * 4096 + (av & 0xf0) * 16 + ((((4 * c) << 4) + (av >> 8 << 4)) ^ (av & 0x70));
  require(lv, ca ^ 4, mh ? 12 : 9);
  sfor<MT>([&](auto M) {
    constexpr int m = decltype(M)::value;
    sfor<3>([&](auto U) {
      constexpr int u = decltype(U)::value, s = NS - 3 + u;
      if constexpr (m == 0) wait_vm<MT * (2 - u) + MT - 1>();
      if constexpr (m > 0 && u == 2) wait_vm<MT - 1 - m>();
      if constexpr (m == 0 && u == 1) wait_lg<8>();
      if constexpr (m == 0 && u == 2) wait_lg<0>();
      sfor<4>([&](auto C) {
        constexpr int c = decltype(C)::value;
        mfma_a(acc[m][c], cat(b[s % 3][c][0], b[s % 3][c][1]), a[s % AD][m]);
        if constexpr (m > 0 && u > 0 && (c & 1)) dsw128<(m - 1) * 16384>(da[2 * (u - 1) + (c >> 1)], __builtin_bit_cast(u32x4, acc[m - 1][2 * (u - 1) + (c >> 1)]));
      });
    });
  });
  STAMP(9);
  asm volatile("s_nop 4");
#pragma unroll
  for (int c = 0; c < 4; c++) dsw128<(MT - 1) * 16384>(da[c], __builtin_bit_cast(u32x4, acc[MT - 1][c]));
  arrive((NS == 20 ? 28 : 12) + mh);
}

constexpr int inl(int T) { const int n = (T - INL0) * INLR; return n < 0 ? 0 : n > INLN ? INLN : n; }
__global__ __launch_bounds__(768, 3) void fused_block(const void* __restrict__ x, const void* __restrict__ wqkv, const void* __restrict__ wo,
                                                      const void* __restrict__ w1, const unsigned short* __restrict__ b1,
                                                      const void* __restrict__ w2, const char* __restrict__ b2, char* __restrict__ out,
                                                      char* ws) {
  extern __shared__ __attribute__((aligned(16))) char shm_[];
  lptr shm = (lptr)shm_;
  const int tid = threadIdx.x, lane = tid & 63, w = __builtin_amdgcn_readfirstlane(tid >> 6);
  const int xcd = blockIdx.x & 7, idx = blockIdx.x >> 3;
  const int p_ = (xcd & 1) * 8 + (idx & 7), sub = idx >> 3;
  const bool att = !(sub & 1);
  const int j_ = (xcd >> 1) * 2 + (sub >> 1);
  const int uidx = (idx & 7) + 8 * (idx >> 4);
  const int u5 = uidx >= 11;
  const int nl = att ? (uidx < 6 ? uidx >> 1 : 3 + u5) : (uidx < 12 ? uidx >> 2 : 3 + ((uidx - 12) >> 1));
  const int G0 = 32 * (xcd >> 1) + (att ? (uidx < 6 ? 24 + 4 * (uidx & 1) : 12 + 4 * (uidx - 6 - 5 * u5)) : 6 * (uidx < 12 ? uidx & 3 : uidx & 1));
  const int n_ = (xcd & 1) * 5 + nl;
  const int kq = w < 8 ? (w + (w >> 2)) & 3 : w & 3;
  const int ug = att ? (uidx < 6 ? uidx & 1 : uidx - 6 - 5 * (uidx >= 11) + 2) : (uidx < 12 ? uidx & 3 : uidx & 1);
  const int rot = (ROT2);
  STAMP(0);
  if (w == 7) {
    u64 pc; asm volatile("s_getpc_b64 %0" : "=s"(pc));
    touch(mkr((const void*)(pc & ~255ull), CODE_BYTES), (idx + 32 * lane) * 128);
  }
  asm volatile("" : "+s"(x), "+s"(wqkv), "+s"(wo), "+s"(w1), "+s"(b1), "+s"(w2), "+s"(b2), "+s"(out), "+s"(ws));
  const int hf = idx & 1, tx = KTX;
  const int rid = (att ? 0 : 8) + j_;
  int fu;
  if (__builtin_expect(w >= 8, 1)) {
    __builtin_amdgcn_s_setprio(3);
    const int lw = w - 8;
    auto ktile = [&](int T) { return T ^ (T < 8 ? tx : tx & 1); };
    const int RS = att ? 3840 : 3456;
    const u32x4 rx = mkr(x, S_ * D_ * 2), rw1 = mkr(att ? wqkv : w1, D_ * RS);
    const int r8 = lane >> 3, cp = lane & 7, t0 = p_ * 128;
    int xo[2], hv, wv;
#pragma unroll
    for (int e = 0; e < 2; e++)
      xo[e] = (t0 + 32 * lw + 2 * (4 * (r8 & 3) + 2 * e + (r8 >> 2))) * 1280 + ((cp ^ (4 * e + (r8 >> 1))) << 4);
    auto ldx = [&](int T) { const int o = ktile(T) * 128; dma4<1024>(rx, ~0ull, (T % 3) * SLOT + XOFF + 2048 + 4 * lw * 1024, xo[0], xo[1], o, o, o + 1280, o + 1280); };
    STAMP(12);
    ldx(0);
    {
      const int hi = lane >= 30, ch = lane - 30 * hi, lim = j_ < 7 ? 27 : 19;
      wv = hi * RS + (att ? ((ch >= 10) + (ch >= 20)) * 1120 + j_ * 160 + ch * 16 : j_ * 448 + (ch < lim ? ch : lim) * 16);
    }
    const int wrow = (16 * (lw & 1) + 4 * (lw >> 1)) * RS;
    auto ldw = [&](int T, int h) {
      const int so = (64 * ktile(T) + 32 * (h ^ hf)) * RS + wrow;
      dma4<960>(rw1, 0x0fffffffffffffffull, (T % 3) * SLOT + h * WSLAB + 4 * lw * 960, wv, wv, so, so + 2 * RS, so + 8 * RS, so + 10 * RS);
    };
    ldw(0, 0);
    {
      int tok = t0 - 16 + 8 * (r8 & 1) + 2 * lw + ((r8 & 3) < 2);
      tok = tok < 0 ? 0 : tok;
      hv = att ? tok * 1280 + ((cp ^ ((2 * lw + ((r8 & 3) >> 1)) & 7)) << 4) : 0x70000000;
    }
    const u64 hmask = lw == 0 ? 0xffffff00ull : 0xffffffffull;
    auto ldh = [&](int T) { dma1(rx, hmask, (T % 3) * SLOT + XOFF + lw * 512, hv, ktile(T) * 128); };
    ldh(0); ldw(0, 1);
    STAMP(13);
    bar_vm<4>();
    STAMP(14);
    ldx(1); ldh(1);
    bar_vm<5>();
    ldw(1, 0); ldw(1, 1);
    auto warmh = [&](int ev = 0) {
      constexpr int PER = 24 * (WMLP ? 2 : 1) / WNE, NW2 = 17280 / PER;
      const int wv2 = ((WMLP ? (blockIdx.x >> 7) * 64 + (blockIdx.x & 63) : blockIdx.x) * 4 + lw) * WNE + ev;
      touch(mkr(wv2 < NW2 ? w2 : wo, WMLP && att ? 0 : wv2 < NW2 ? F_ * 1280 : D_ * 1280), ((wv2 < NW2 ? wv2 : wv2 - NW2) * PER + min(lane, PER - 1)) * 128);
    };
    ldx(2); ldh(2);
    if (WARMH == 2) warmh();
    bar_vm<5 + (WARMH == 2)>();
    __builtin_amdgcn_s_setprio(L1PRIO);
    if (WARMH == 3) warmh();
    const int srow = p_ * 128 + 8 * rid + 2 * lw + (lane >> 5);
    u32x2 xr[5];
    unsigned ep, b1v;
    setup_loads(xr, ep, b1v, rx, srow * 1280 + (lane & 31) * 8, mkr(ws, 4), mkr(b1, F_ * 2), (j_ * 224 + 64 * lw + lane) * 2);
    touch_sc1(mkr(ws + WS_BOX, lw == 0 ? 160 * 256 : 0), (((G0 >> 3) + (lane & 1)) * 10 + n_) * 256 + (lane & 2) * 64);
    ldw(2, 0); ldw(2, 1); ldx(3); ldh(3); ldw(3, 0);
    bar_vm<9>();
    pin(ep); pin(b1v);
#pragma unroll
    for (int k = 0; k < 5; k++) pin(xr[k]);
    const unsigned tag = __builtin_amdgcn_readfirstlane(ep) + 1u;
    ldw(3, 1); ldx(4); ldw(4, 0); ldh(4); ldw(4, 1);
    {
      float s = 0;
#pragma unroll
      for (int k = 0; k < 5; k++) s += (blo(xr[k][0]) + bhi(xr[k][0])) + (blo(xr[k][1]) + bhi(xr[k][1]));
      const float m = half_sum(s) * (1.f / D_);
      float q = 0;
#pragma unroll
      for (int k = 0; k < 10; k++) { const unsigned u = xr[k >> 1][k & 1]; const float a = blo(u) - m, b = bhi(u) - m; q = __builtin_fmaf(a, a, q); q = __builtin_fmaf(b, b, q); }
      const float rs = __builtin_amdgcn_rsqf(half_sum(q) * (1.f / D_) + 1e-5f);
      st_wt(mkr(ws + WS_STAT, S_ * 16), (lane & 31) == 0 ? srow * 16 : 0x70000000, 0, u32x4{asu(rs), tag, asu(-m * rs), tag});
      *(LAS float*)(shm + LDS_B1 + (64 * lw + lane) * 4) = blo(b1v);
      *(LAS unsigned*)(shm + LDS_CNT + 4) = tag;
    }
    bar_vm<14>();
    const u32x4 rw = mkr(kq == 0 ? wo : w2, kq == 0 ? D_ * 1280 : F_ * 1280);
    const int cbyte = n_ * 128 + ((cp ^ ((lane >> 4) << 1)) << 4);
    const int v1 = r8 * 1280 + cbyte, v2 = kq == 0 ? (20 * (r8 >> 2) + (r8 & 3)) * 1280 + cbyte : v1;
    int gtab;
    {
      const int so = s2_perm(lane, true, rot), P = (so * 13) >> 6, kp = so - 5 * P;
      const int r0 = 160 * P + (kp == 2 ? 16 : 40 * (kp - (kp > 2)));
      const int t = 3 * s2_perm(lane, false, rot) + kq - 1, u = t - 32;
      const int r1 = 32 * (t < 32 ? 7 * (t >> 2) + (t & 3) : 4 * ((u * 43) >> 7) + 4 + u);
      gtab = kq == 0 ? r0 * 1280 + (kp == 2) : r1 * 1280;
      pin(gtab);
    }
    const int sbn = 1280 * (kq == 0 ? 20 : 16);
    auto geom = [&](int s, int& rb, int& sa, int& sb, int& v) {
      const int e = __builtin_amdgcn_readlane(gtab, s), f = e & 1;
      v = v1 + ((v2 - v1) & -f);
      rb = e - f;
      sa = f ? 1280 * 80 : 1280 * 8;
      sb = f ? 1280 * 40 : sbn;
    };
    u32x4 pre[9][4];
    auto ldr = [&](auto R) {
      constexpr int r = decltype(R)::value;
      int rb, sa, sb, v; geom(9 + r, rb, sa, sb, v);
      bload(pre[r][0], rw, v, rb); bload(pre[r][1], rw, v, rb + sa); bload(pre[r][2], rw, v, rb + sb); bload(pre[r][3], rw, v, rb + sa + sb);
    };
    const int attf = att;
    auto ldc = [&](auto R, auto LATE) {
      constexpr int r = decltype(R)::value;
      int rb, sa, sb, v; geom(9 + r, rb, sa, sb, v);
      bload4_if<decltype(LATE)::value>(pre[r], rw, v, rb, rb + sa, rb + sb, rb + sa + sb, attf);
    };
    auto ldl = [&](auto S) {
      constexpr int s = decltype(S)::value;
      int rb, sa, sb, v; geom(s, rb, sa, sb, v);
      dma4<1024>(rw, ~0ull, s < 9 ? s * 16384 + kq * 4096 : 147456 + (s - 18) * 4096, v, v, rb, rb + sa, rb + sb, rb + sa + sb);
    };
    const u32x4 rst = mkr(ws + WS_STAT, S_ * 16);
    const int spos = 36 * lw + (lane < 36 ? lane : 35), sv = row_token(p_, spos) * 16;
    u32x4 sg;
    sfor<(LATESET ? 3 : 5)>([&](auto TT) {
      constexpr int T = decltype(TT)::value + (LATESET ? 7 : 5);
      if (T == 9) bload_sc1(sg, rst, sv, 0);
      if (T == 6) { STAMP(15); }
      constexpr bool WE = WARMH >= 5 && T >= WARMH && (T - WARMH) % WSTEP == 0 && (T - WARMH) / WSTEP < WNE;
      ldx(T); ldw(T, 0);
      if (WE && !WEND) warmh((T - WARMH) / WSTEP);
      ldh(T); ldw(T, 1);
      if (WE && WEND) warmh((T - WARMH) / WSTEP);
      constexpr int n0 = inl(T - 1), n1 = inl(T), n2 = inl(T + 1);
      sfor<n2 - n1>([&](auto R) { ldc(IC(1 + n1 + decltype(R)::value), IC(0)); });
      bar_vm_sel<13 + WE + 4 * (n2 - n1) + (T < 9 ? 4 * (n1 - n0) : 0), 13 + WE>(attf);
    });
    STAMP(1);
    asm volatile("" : "+v"(sg));
#pragma nounroll
    for (unsigned spins = 0; !__all(sg[1] == tag && sg[3] == tag) && spins < SPIN_CAP; spins++) {
      __builtin_amdgcn_s_sleep(2);
      bload_sc1(sg, rst, sv, 0);
      asm volatile("s_waitcnt vmcnt(0)" : "+v"(sg) :: "memory");
    }
    *(LAS u32x2*)(shm + LDS_ST + spos * 8) = u32x2{sg[0], sg[2]};
    ldl(IC(4));
    if (att) wait_vm<4 + (WEND && WARMH == 9)>(); else wait_vm<4 + (WEND && WARMH == 9) + 4 * (inl(10) - inl(9))>();
    STAMP(2);
    bar_lgkm();
    __builtin_amdgcn_s_setprio(0);
    STAMP(3);
    const int pa = LDS_PROD + kq * 4, wa = kq * 4096 + lane * 16;
    auto ldq = [&](auto R) { if constexpr (decltype(R)::value <= inl(10)) ldc(R, IC(1)); else ldr(R); };
    if (!att) { ldl(IC(5)); ldl(IC(6)); ldl(IC(7)); }
    ldq(IC(1)); ldq(IC(2)); ldq(IC(3)); ldq(IC(4));
    STAMP(4);
    ldl(IC(3));
    await<1, 8>(2);
    ldl(IC(2)); ldl(IC(1)); ldl(IC(0));
    if (!att) { ldl(IC(8)); if (kq == 0) { ldl(IC(18)); ldl(IC(19)); } }
    ldr(IC(0)); wait_vm<4>();
    post(pa, att ? 1 : 2);
    STAMP(5);
    if (!att) __builtin_amdgcn_s_setprio(LPM);
    ldq(IC(5)); ldq(IC(6)); ldq(IC(7)); ldq(IC(8));
    if (att) {
      await<1, 8>(4);
      __builtin_amdgcn_s_setprio(LPA);
      ldl(IC(5)); ldl(IC(6)); ldl(IC(7)); ldl(IC(8));
      if (kq == 0) { ldl(IC(18)); ldl(IC(19)); }
    }
    if (lw == 3) touch(mkr(b2, D_ * 2), n_ * 128);
    wait_vm<0>();
    post(pa, 2);
#define PUT(r, off) dsw128<off>(wa + (r / 3) * 49152, pre[r][0]); dsw128<off + 1024>(wa + (r / 3) * 49152, pre[r][1]); dsw128<off + 2048>(wa + (r / 3) * 49152, pre[r][2]); dsw128<off + 3072>(wa + (r / 3) * 49152, pre[r][3]);
    await_pair(LDS_CONS + kq * 8, 1);
    PUT(0, 0) PUT(1, 16384) PUT(2, 32768)
    post(pa, 3);
    __builtin_amdgcn_s_setprio(LPP);
    STAMP(6);
    await_pair(LDS_CONS + kq * 8, 3);
    PUT(3, 0) PUT(4, 16384) PUT(5, 32768)
    post(pa, 4);
    await_pair(LDS_CONS + kq * 8, 6);
    PUT(6, 0) PUT(7, 16384) PUT(8, 32768)
    post(pa, 5);
    fu = lw;
  } else {
    if (tid < 24) *(LAS u32x4*)(shm + (tid >> 3) * SLOT + XOFF + (tid & 7) * 16) = u32x4{0x3f803f80u, 0x3f803f80u, 0x3f803f80u, 0x3f803f80u};
    if (tid >= 448 && tid < 480) *(LAS unsigned*)(shm + LDS_CNT + 4 * (tid - 448)) = 0u;
    if (w == 5 && att) att_table(shm, lane);
    const bool light = att ? (w >= 6) : (w >= 4);
    if (light) s1_compute<3>(shm, att, w, lane, p_, j_, hf, ws PR_PASS);
    else s1_compute<4>(shm, att, w, lane, p_, j_, hf, ws PR_PASS);
    STAMP(3);
    if (att) s1_attention(shm, w, p_, j_, ws PR_PASS);
    STAMP(4);
    const unsigned tag = __builtin_amdgcn_readfirstlane(*(LAS unsigned*)(shm + LDS_CNT + 4));
    GAS u64* flags = (GAS u64*)(ws + WS_FLAG);
    const u64 fv = flagval(tag);
    wait_vm<0>();
    {
      const int lane = lane_id(), kind = att ? 0 : 1 + (w >> 2);
      unsigned old = 0;
      if (lane == 0) old = __hip_atomic_fetch_add((LAS unsigned*)(shm + LDS_CNT + (kind == 1 ? 120 : 0)), 1u, __ATOMIC_RELAXED, __HIP_MEMORY_SCOPE_WORKGROUP);
      if (__builtin_amdgcn_readfirstlane(old) == (att ? 7u : 3u)) {
        GAS u64* dst = lane == 10 ? flags + p_ * 16 + rid : (GAS u64*)(ws + WS_BOX) + (p_ * 10 + lane) * 32 + 8 * kind + j_;
        if (lane <= 10) __hip_atomic_store(dst, fv, __ATOMIC_RELAXED, __HIP_MEMORY_SCOPE_AGENT);
      }
    }
    STAMP(5);
    if (w == 0) watch(ws, G0, G0 + (att ? 3 : 5), n_, 1, 15, fv);
    if (kq == 0) {
      if (att) s2_compute<20, 2>(w, kq, G0, n_, rot, ws, fv PR_PASS);
      else s2_compute<20, 3>(w, kq, G0, n_, rot, ws, fv PR_PASS);
    } else {
      if (att) s2_compute<18, 2>(w, kq, G0, n_, rot, ws, fv PR_PASS);
      else s2_compute<18, 3>(w, kq, G0, n_, rot, ws, fv PR_PASS);
    }
    if (blockIdx.x == 0 && w == 3) {
      const int lane = lane_id();
#pragma nounroll
      for (unsigned spins = 0; spins < SPIN_CAP; spins++) {
        bool ok = true;
#pragma unroll
        for (int k = 0; k < 4; k++) ok &= __hip_atomic_load(flags + lane + 64 * k, __ATOMIC_RELAXED, __HIP_MEMORY_SCOPE_AGENT) == fv;
        if (__all(ok)) break;
        __builtin_amdgcn_s_sleep(8);
      }
      if (lane == 0) __hip_atomic_store((GAS unsigned*)ws, tag, __ATOMIC_RELAXED, __HIP_MEMORY_SCOPE_AGENT);
    }
    if (att || w == 0 || w > 2) { STAMP(11); FINISH(); return; }
    fu = w + 3;
  }
  {
    const int lane = lane_id();
    const u32x4 bv = *(const GAS u32x4*)(b2 + n_ * 128 + (lane & 7) * 16);
    int foff[2], pr0[2];
    u32x4 xv[2];
    f32x2 gt[2][4];
#pragma unroll
    for (int hh = 0; hh < 2; hh++) {
      const int frow = 48 * hh + 8 * fu + (lane >> 3);
      foff[hh] = fin_off(G0 + (att ? 2 : 3) * hh, n_, 8 * fu, lane);
      xv[hh] = *(const GAS u32x4*)((const char*)x + foff[hh]);
      pr0[hh] = (frow >> 4) * 16384 + (frow & 15) * 256 + (((2 * (lane & 7)) ^ (frow & 7)) << 4);
      pin(pr0[hh]);
    }
#pragma unroll
    for (int hh = 0; hh < 2; hh++) {
      await<1, 3>(12 + hh);
      f32x4 y[2];
#pragma unroll
      for (int e = 0; e < 2; e++) {
        lptr pr = shm + (pr0[hh] ^ (16 * e));
        y[e] = *(LAS f32x4*)(pr + 4096) + *(LAS f32x4*)(pr + 8192) + *(LAS f32x4*)(pr + 12288);
      }
#pragma unroll
      for (int k = 0; k < 4; k++) {
        f32x2 yy = f32x2{y[k >> 1][2 * (k & 1)], y[k >> 1][2 * (k & 1) + 1]} + f32x2{blo(bv[k]), bhi(bv[k])};
        f32x2 t = yy * -1.4426950408889634f;
        f32x2 e = f32x2{__builtin_amdgcn_exp2f(t[0]), __builtin_amdgcn_exp2f(t[1])} + 1.f;
        gt[hh][k] = yy * f32x2{__builtin_amdgcn_rcpf(e[0]), __builtin_amdgcn_rcpf(e[1])};
        pin(gt[hh][k]);
      }
    }
    STAMP(10);
#pragma unroll
    for (int hh = 0; hh < 2; hh++) {
      await<0, 1>(28 + hh);
      u32x4 o;
#pragma unroll
      for (int e = 0; e < 2; e++) {
        const f32x4 s = *(LAS f32x4*)(shm + (pr0[hh] ^ (16 * e)));
        o[2 * e] = pk2(f32x2{s[0], s[1]} * gt[hh][2 * e] + f32x2{blo(xv[hh][2 * e]), bhi(xv[hh][2 * e])});
        o[2 * e + 1] = pk2(f32x2{s[2], s[3]} * gt[hh][2 * e + 1] + f32x2{blo(xv[hh][2 * e + 1]), bhi(xv[hh][2 * e + 1])});
      }
      __builtin_nontemporal_store(o, (GAS u32x4*)(out + foff[hh]));
    }
  }
  STAMP(11);
  FINISH();
}

extern "C" void kernel_launch(void* const* d_in, const int* in_sizes, int n_in, void* d_out, int out_size, void* d_ws,
                              size_t ws_size, hipStream_t stream) {
  (void)hipFuncSetAttribute((const void*)fused_block, hipFuncAttributeMaxDynamicSharedMemorySize, LDS_TOTAL);
  fused_block<<<256, 768, LDS_TOTAL, stream>>>(d_in[0], d_in[1], d_in[2], d_in[4], (const unsigned short*)d_in[5], d_in[6],
                                               (const char*)d_in[7], (char*)d_out, (char*)d_ws);
}
```

```cpp
#include <hip/hip_runtime.h>
#include <hip/hip_bf16.h>
#include <type_traits>
#include <utility>

typedef __attribute__((ext_vector_type(8))) __bf16 bf16x8;
typedef __attribute__((ext_vector_type(2))) __bf16 bf16x2;
typedef __attribute__((ext_vector_type(4))) float f32x4;
typedef __attribute__((ext_vector_type(2))) float f32x2;
typedef __attribute__((ext_vector_type(4))) unsigned u32x4;
typedef __attribute__((ext_vector_type(2))) unsigned u32x2;
#define LAS __attribute__((address_space(3)))
#define GAS __attribute__((address_space(1)))
#define DEVI __device__ __forceinline__
typedef LAS char* lptr;
typedef unsigned long long u64;
#define SB __builtin_amdgcn_sched_barrier(0)
#define IC(n) std::integral_constant<int, n>{}

constexpr int S_ = 2048, D_ = 640, F_ = 1728;
constexpr int KS_T = 74;
constexpr int WSLAB = 15360;
constexpr int XOFF = 2 * WSLAB;
constexpr int SLOT = 49152;
constexpr int LDS_TB = 86016;
constexpr int LDS_ST = 155648;
constexpr int LDS_CS = LDS_ST + 1152;
constexpr int LDS_B1 = LDS_CS + 1024;
constexpr int LDS_CNT = LDS_B1 + 1024;
constexpr int LDS_ZERO = LDS_CNT + 24;
constexpr int LDS_CONS = LDS_CNT + 64;
constexpr int LDS_PROD = LDS_CONS + 32;
constexpr int LDS_TAB = LDS_CNT + 128;
constexpr int LDS_TOTAL = 163840;
constexpr int WS_FLAG = 4096, WS_STAT = 8192, WS_BOX = 65536, WS_A2 = 1 << 20;
constexpr int A2_BYTES = 128 * KS_T * 1024;
constexpr int CODE_BYTES = 46464;
constexpr unsigned SPIN_CAP = 1u << 20;

#define STAMP(idx)
#define FINISH()
#define STAMPC(k)
#define STAMPA(k) STAMP(k)
#define STAMPB(k)
#define PR_ARGS
#define PR_PASS

DEVI void bar_lgkm() { asm volatile("s_waitcnt lgkmcnt(0)\n\ts_barrier" ::: "memory"); }
template <int N> DEVI void bar_vm() { asm volatile("s_waitcnt vmcnt(%0)\n\ts_barrier" ::"i"(N) : "memory"); }
template <int N> DEVI void wait_vm() { asm volatile("s_waitcnt vmcnt(%0)" ::"i"(N) : "memory"); }
DEVI unsigned pk2(float a, float b) { f32x2 v = {a, b}; return __builtin_bit_cast(unsigned, __builtin_convertvector(v, bf16x2)); }
DEVI unsigned pk2(f32x2 v) { return __builtin_bit_cast(unsigned, __builtin_convertvector(v, bf16x2)); }
DEVI float asf(unsigned u) { return __builtin_bit_cast(float, u); }
DEVI unsigned asu(float f) { return __builtin_bit_cast(unsigned, f); }
DEVI float blo(unsigned u) { return __builtin_bit_cast(float, u << 16); }
DEVI float bhi(unsigned u) { return __builtin_bit_cast(float, u & 0xffff0000u); }
DEVI f32x4 mfma(u32x4 a, u32x4 b, f32x4 c) {
  return __builtin_amdgcn_mfma_f32_16x16x32_bf16(__builtin_bit_cast(bf16x8, a), __builtin_bit_cast(bf16x8, b), c, 0, 0, 0);
}
DEVI u32x4 cat(u32x2 a, u32x2 b) { return __builtin_shufflevector(a, b, 0, 1, 2, 3); }
DEVI void mfma_a(f32x4& c, u32x4 a, u32x4 b) { asm volatile("v_mfma_f32_16x16x32_bf16 %0, %1, %2, %0" : "+v"(c) : "v"(a), "v"(b)); }
template <int OFF> DEVI void ds128(u32x4& d, int addr) { asm volatile("ds_read_b128 %0, %1 offset:%2" : "=v"(d) : "v"(addr), "i"(OFF)); }
template <int OFF> DEVI void dstr(u32x2& d, int addr) { asm volatile("ds_read_b64_tr_b16 %0, %1 offset:%2" : "=v"(d) : "v"(addr), "i"(OFF)); }
template <int OFF> DEVI void dsw128(int addr, u32x4 d) { asm volatile("ds_write_b128 %0, %1 offset:%2" ::"v"(addr), "v"(d), "i"(OFF) : "memory"); }
DEVI u32x4 mkr(const void* p, unsigned bytes) {
  u64 a = (u64)p;
  return u32x4{(unsigned)a, (unsigned)(a >> 32) & 0xffffu, bytes, 0x00020000u};
}
DEVI void st_wt(u32x4 r, int voff, int soff, u32x4 v) {
  asm volatile("buffer_store_dwordx4 %0, %1, %2, %3 offen sc1\n\ts_nop 1" ::"v"(v), "v"(voff), "s"(r), "s"(soff) : "memory");
}
template <int LSTEP>
DEVI void dma4(u32x4 r, u64 mask, int l, int va, int vb, int s0, int s1, int s2, int s3) {
  asm volatile(
      "s_mov_b64 exec, %0\n\t"
      "s_mov_b32 m0, %1\n\ts_nop 0\n\tbuffer_load_dwordx4 %2, %4, %5 offen lds\n\t"
      "s_add_u32 m0, %1, %9\n\ts_nop 0\n\tbuffer_load_dwordx4 %3, %4, %6 offen lds\n\t"
      "s_add_u32 m0, %1, %10\n\ts_nop 0\n\tbuffer_load_dwordx4 %2, %4, %7 offen lds\n\t"
      "s_add_u32 m0, %1, %11\n\ts_nop 0\n\tbuffer_load_dwordx4 %3, %4, %8 offen lds\n\t"
      "s_mov_b64 exec, -1"
      :: "s"(mask), "s"(l), "v"(va), "v"(vb), "s"(r), "s"(s0), "s"(s1), "s"(s2), "s"(s3), "i"(LSTEP), "i"(2 * LSTEP), "i"(3 * LSTEP)
      : "memory", "scc");
}
DEVI void dma1(u32x4 r, u64 mask, int l, int v, int s0) {
  asm volatile("s_mov_b64 exec, %0\n\ts_mov_b32 m0, %1\n\ts_nop 0\n\tbuffer_load_dwordx4 %2, %3, %4 offen lds\n\ts_mov_b64 exec, -1"
               :: "s"(mask), "s"(l), "v"(v), "s"(r), "s"(s0) : "memory");
}
DEVI void setup_loads(u32x2 (&xr)[5], unsigned& ep, unsigned& bv, u32x4 rx, int vx, u32x4 re, u32x4 rb, int vb) {
  asm volatile("buffer_load_dwordx2 %0, %7, %8, 0 offen\n\tbuffer_load_dwordx2 %1, %7, %8, 0 offen offset:256\n\tbuffer_load_dwordx2 %2, %7, %8, 0 offen offset:512\n\t"
               "buffer_load_dwordx2 %3, %7, %8, 0 offen offset:768\n\tbuffer_load_dwordx2 %4, %7, %8, 0 offen offset:1024\n\t"
               "buffer_load_dword %5, off, %9, 0 sc1\n\tbuffer_load_ushort %6, %11, %10, 0 offen"
               : "=&v"(xr[0]), "=&v"(xr[1]), "=&v"(xr[2]), "=&v"(xr[3]), "=&v"(xr[4]), "=&v"(ep), "=&v"(bv)
               : "v"(vx), "s"(rx), "s"(re), "s"(rb), "v"(vb) : "memory");
}
template <class T> DEVI void pin(T& v) { asm volatile("" : "+v"(v)); }
DEVI void dsw_col0(int a0, f32x4 d0, int a1, f32x4 d1, int a2, f32x4 d2) {
  asm volatile("s_mov_b64 exec, %6\n\tds_write_b128 %0, %1\n\tds_write_b128 %2, %3\n\tds_write_b128 %4, %5\n\ts_mov_b64 exec, -1"
               ::"v"(a0), "v"(d0), "v"(a1), "v"(d1), "v"(a2), "v"(d2), "s"(0x0001000100010001ull) : "memory");
}
DEVI void reload_sc1(u32x4& d, u32x4 r, int v, int s0) { asm volatile("buffer_load_dwordx4 %0, %1, %2, %3 offen sc1" : "+v"(d) : "v"(v), "s"(r), "s"(s0) : "memory"); }
template <int OFF> DEVI void redstr(u32x2& d, int addr) { asm volatile("ds_read_b64_tr_b16 %0, %1 offset:%2" : "+v"(d) : "v"(addr), "i"(OFF)); }
DEVI void vinc(int& d, int s) { asm volatile("v_add_u32 %0, %1, %0" : "+v"(d) : "s"(s)); }
template <class F, int... I> DEVI void sfor_(F f, std::integer_sequence<int, I...>) { (f(std::integral_constant<int, I>{}), ...); }
template <int N, class F> DEVI void sfor(F f) { sfor_(f, std::make_integer_sequence<int, N>{}); }
DEVI void touch_sc1(u32x4 r, int v) {
  asm volatile("s_mov_b32 m0, %2\n\ts_nop 0\n\tbuffer_load_dword %0, %1, 0 offen sc1 lds" ::"v"(v), "s"(r), "i"(LDS_TOTAL - 256) : "memory");
}
DEVI void touch(u32x4 r, int v) {
  asm volatile("s_mov_b32 m0, %2\n\ts_nop 0\n\tbuffer_load_dword %0, %1, 0 offen lds" ::"v"(v), "s"(r), "i"(LDS_TOTAL - 256) : "memory");
}
DEVI void bload(u32x4& d, u32x4 r, int v, int s0) { asm volatile("buffer_load_dwordx4 %0, %1, %2, %3 offen" : "=v"(d) : "v"(v), "s"(r), "s"(s0)); }
DEVI void bload_sc1(u32x4& d, u32x4 r, int v, int s0) { asm volatile("buffer_load_dwordx4 %0, %1, %2, %3 offen sc1" : "=v"(d) : "v"(v), "s"(r), "s"(s0) : "memory"); }
DEVI void arrive(int k) {
  asm volatile("s_mov_b64 exec, 1\n\tds_add_u32 %0, %1\n\ts_mov_b64 exec, -1" ::"v"(LDS_CNT + 4 * k), "v"(1) : "memory");
}
template <int SLEEP, unsigned N>
DEVI void await(int k) {
  unsigned v;
  for (;;) {
    asm volatile("ds_read_b32 %0, %1\n\ts_waitcnt lgkmcnt(0)" : "=v"(v) : "v"(LDS_CNT + 4 * k) : "memory");
    if (__builtin_amdgcn_readfirstlane(v) >= N) break;
    if (SLEEP) __builtin_amdgcn_s_sleep(SLEEP);
  }
}
DEVI void post(int addr, int val) { asm volatile("ds_write_b32 %0, %1" ::"v"(addr), "v"(val) : "memory"); }
DEVI void peek(unsigned& v, int addr) { asm volatile("ds_read_b32 %0, %1" : "=v"(v) : "v"(addr) : "memory"); }
DEVI void require(unsigned v, int addr, unsigned n) {
  unsigned t;
  asm volatile("v_readfirstlane_b32 %1, %0\n\ts_cmp_ge_u32 %1, %3\n\ts_cbranch_scc1 .Lq%=\n"
               ".Lw%=:\n\tds_read_b32 %0, %2\n\ts_waitcnt lgkmcnt(0)\n\tv_readfirstlane_b32 %1, %0\n\ts_cmp_ge_u32 %1, %3\n\ts_cbranch_scc0 .Lw%=\n.Lq%=:"
               : "+&v"(v), "=&s"(t) : "v"(addr), "s"(n) : "memory", "scc");
}
DEVI void await_pair(int addr, unsigned n) {
  for (;;) {
    u32x2 v;
    asm volatile("ds_read_b64 %0, %1\n\ts_waitcnt lgkmcnt(0)" : "=v"(v) : "v"(addr) : "memory");
    const unsigned a = v[0], b = v[1];
    if (__builtin_amdgcn_readfirstlane(a < b ? a : b) >= n) break;
    __builtin_amdgcn_s_sleep(1);
  }
}
DEVI int lane_id() { int l; asm volatile("v_mbcnt_lo_u32_b32 %0, -1, 0\n\tv_mbcnt_hi_u32_b32 %0, -1, %0" : "=v"(l)); return l; }
DEVI int sig(int i) { return 4 * (i & 3) + (i >> 2); }
DEVI u64 flagval(unsigned tag) { return ((u64)(tag ^ 0x5bd1e995u) << 32) | tag; }

DEVI float xadd16(float v) { auto r = __builtin_amdgcn_permlane16_swap(asu(v), asu(v), false, false); return asf(r[0]) + asf(r[1]); }
template <int CTRL> DEVI float dpp(float v) { return asf(__builtin_amdgcn_mov_dpp(asu(v), CTRL, 0xf, 0xf, true)); }
DEVI float half_sum(float v) {
  v += dpp<0xb1>(v); v += dpp<0x4e>(v); v += dpp<0x141>(v); v += dpp<0x140>(v);
  return xadd16(v);
}

DEVI int h_slot(int j, int l) { const int t = l < 4 ? 4 * j + l : 28 + 3 * j + l, d = (t * 43) >> 7; return 18 * (t - 3 * d) + d; }

DEVI int row_token(int p_, int pos) {
  const int tau = pos >> 4, i = pos & 15;
  int tok = tau == 0 ? p_ * 128 - 16 + 8 * (i & 1) + 2 * (i >> 2) + ((i & 3) < 2) : p_ * 128 + 32 * ((tau - 1) >> 1) + 2 * sig(i) + ((tau - 1) & 1);
  return tok < 0 ? 0 : tok;
}

struct ColGeo { int A, B, C, ct; };
DEVI ColGeo colgeo(bool att, int nw, int mw, int c, bool heavy) {
  const int cc = heavy ? (c ^ (2 * mw)) : c;
  const int ct = 4 * nw + cc, sec = (ct * 13) >> 6, hh = ct - 5 * sec;
  const bool q4 = hh == 4, sp = !heavy && c == 2;
  ColGeo r;
  r.A = att ? sec * 160 + (q4 ? 32 : 40 * hh) : heavy ? 128 * nw + 32 * cc : (sp ? 352 + 16 * nw : 128 + 64 * nw + 32 * c);
  r.B = att && q4 ? 40 : 8;
  r.C = att ? (q4 ? 80 : 16) : (sp ? 32 : 16);
  r.ct = ct;
  return r;
}

template <int NC>
struct Frags { u32x4 x[5]; u32x2 w[NC][2]; };

template <int N> DEVI void wait_lg() { asm volatile("s_waitcnt lgkmcnt(%0)" ::"i"(N) : "memory"); }

template <int NC, int K>
DEVI void s1_rd(Frags<NC>& n, int xo, int xh, const int (&wa)[NC]) {
  if constexpr (K == 0) ds128<0>(n.x[0], xo);
  else if constexpr (K <= 2 * NC) { if ((K - 1) & 1) dstr<7680>(n.w[(K - 1) >> 1][1], wa[(K - 1) >> 1]); else dstr<0>(n.w[(K - 1) >> 1][0], wa[(K - 1) >> 1]); }
  else if constexpr (K < 2 * NC + 4) ds128<(K - 2 * NC) * 2048>(n.x[K - 2 * NC], xo);
  else if constexpr (K == 2 * NC + 4) ds128<0>(n.x[4], xh);
}
template <int NC, int NH, int K>
DEVI void s1_mm(const Frags<NC>& c, f32x4 (&acc)[4][NC], f32x4 (&hacc)[NH]) {
  if constexpr (K < 4 * NC) mfma_a(acc[K / NC][K % NC], cat(c.w[K % NC][0], c.w[K % NC][1]), c.x[K / NC]);
  else mfma_a(hacc[K - 4 * NC], cat(c.w[K - 4 * NC][0], c.w[K - 4 * NC][1]), c.x[4]);
}
template <int NC, int NH, bool BAR>
DEVI void s1_step(const Frags<NC>& c, Frags<NC>& n, int xo, int xh, const int (&wa)[NC], f32x4 (&acc)[4][NC], f32x4 (&hacc)[NH]) {
  constexpr int NM = 4 * NC + NH, NR = 2 * NC + 5, D = BAR ? NM - NR : 0;
  sfor<NM>([&](auto KK) {
    constexpr int k = decltype(KK)::value;
    constexpr int need = k < NC ? 2 * k + 2 : (k % NC == 0 && k <= 4 * NC) ? 2 * NC + k / NC : -1;
    if constexpr (BAR && k == D) bar_lgkm();
    if constexpr (need >= 0 && (!BAR || k < D)) wait_lg<NR - 1 - need + (BAR ? 0 : (k < NR ? k : NR))>();
    s1_mm<NC, NH, k>(c, acc, hacc);
    if constexpr (k >= D) s1_rd<NC, k - D>(n, xo, xh, wa);
  });
}

template <int OFF> DEVI void dsw64(int addr, u32x2 d) { asm volatile("ds_write_b64 %0, %1 offset:%2" ::"v"(addr), "v"(d), "i"(OFF) : "memory"); }
template <int OFF> DEVI void ds64(u32x2& d, int addr) { asm volatile("ds_read_b64 %0, %1 offset:%2" : "=v"(d) : "v"(addr), "i"(OFF)); }

template <int NC>
DEVI void s1_compute(lptr shm, bool att, int w, int lane, int p_, int j_, int hf, char* ws PR_ARGS) {
  constexpr int NH = NC == 4 ? 2 : 3;
  const int mw = w & 1, nw = w >> 1, i = lane & 15, g = lane >> 4;
  f32x4 acc[4][NC], hacc[NH];
#pragma unroll
  for (int m = 0; m < 4; m++)
#pragma unroll
    for (int c = 0; c < NC; c++) { acc[m][c] = f32x4{0, 0, 0, 0}; asm volatile("" : "+v"(acc[m][c])); }
#pragma unroll
  for (int c = 0; c < NH; c++) { hacc[c] = f32x4{0, 0, 0, 0}; asm volatile("" : "+v"(hacc[c])); }
  int wb[NC], cb[NC], ct[NC];
#pragma unroll
  for (int c = 0; c < NC; c++) {
    const ColGeo cg = colgeo(att, nw, mw, c, NC == 4);
    wb[c] = (lane >> 2) * 480 + cg.A + cg.B * (lane & 1) + cg.C * ((lane >> 1) & 1);
    cb[c] = LDS_CS + 2 * (cg.A + cg.B * (g & 1) + cg.C * (g >> 1));
    ct[c] = cg.ct;
    asm volatile("" : "+v"(cb[c]), "+v"(wb[c]));
  }
  int xb = XOFF + i * 128 + ((g ^ (i >> 1) ^ (4 * hf)) << 4);
  int sta = LDS_ST + i * 8;
  asm volatile("" : "+v"(xb), "+v"(sta));
  const int own = (1 + 4 * mw) * 2048;
  Frags<NC> fa, fb;
  int wa[NC];
  STAMPB(12);
  bar_lgkm();
  STAMP(1);
  sfor<2 * NC + 5>([&](auto K) { s1_rd<NC, decltype(K)::value>(fa, xb + own, xb, wb); });
#pragma unroll
  for (int c = 0; c < NC; c++) wa[c] = wb[c] + WSLAB;
  s1_step<NC, NH, true>(fa, fb, (xb ^ 64) + own, xb ^ 64, wa, acc, hacc);
  int sl = 0;
#pragma nounroll
  for (int T = 1; T < 10; T++) {
    sl = sl == 2 * SLOT ? 0 : sl + SLOT;
#pragma unroll
    for (int c = 0; c < NC; c++) wa[c] = wb[c] + sl;
    s1_step<NC, NH, true>(fb, fa, xb + sl + own, xb + sl, wa, acc, hacc);
#pragma unroll
    for (int c = 0; c < NC; c++) wa[c] = wb[c] + sl + WSLAB;
    s1_step<NC, NH, false>(fa, fb, (xb ^ 64) + sl + own, (xb ^ 64) + sl, wa, acc, hacc);
  }
  wait_lg<0>();
  arrive(2);
  sfor<NH>([&](auto K) { s1_mm<NC, NH, 4 * NC + decltype(K)::value>(fb, acc, hacc); });
  u32x2 stu[5];
  u32x4 b1u[NC], csu[NC];
  sfor<4 * NC>([&](auto KK) {
    constexpr int k = decltype(KK)::value;
    s1_mm<NC, NH, k>(fb, acc, hacc);
    if constexpr (k < 4) ds64<(1 + k) * 128>(stu[k], sta + mw * 512);
    if constexpr (k == 4) ds64<0>(stu[4], sta);
    if constexpr (k == 5) {
      dsw_col0(cb[0], hacc[0], cb[1], hacc[1], cb[NH - 1], hacc[NH - 1]);
      if (NC == 4) arrive(8 + nw);
    }
    if constexpr (k >= 6 && k < 6 + NC) ds128<LDS_B1 - LDS_CS>(b1u[k - 6], cb[k - 6]);
  });
  STAMP(2);
  if (NC == 4) await<0, 2>(8 + nw);
#pragma unroll
  for (int c = 0; c < NC; c++) ds128<0>(csu[c], cb[c]);
  asm volatile("s_waitcnt lgkmcnt(0)\n\ts_nop 7" ::: "memory");
  f32x2 st[5];
  f32x4 cs[NC], b1[NC];
#pragma unroll
  for (int m = 0; m < 5; m++) { asm volatile("" : "+v"(stu[m])); st[m] = __builtin_bit_cast(f32x2, stu[m]); }
#pragma unroll
  for (int c = 0; c < NC; c++) { asm volatile("" : "+v"(csu[c]), "+v"(b1u[c])); cs[c] = __builtin_bit_cast(f32x4, csu[c]); b1[c] = __builtin_bit_cast(f32x4, b1u[c]); }
#pragma unroll
  for (int m = 0; m < 4; m++)
#pragma unroll
    for (int c = 0; c < NC; c++) pin(acc[m][c]);
#pragma unroll
  for (int c = 0; c < NH; c++) pin(hacc[c]);
  STAMPA(12);
  const int l2 = lane_id();
  if (att) {
    const int g2 = l2 >> 4, piece = LDS_TB + ((l2 & 48) + ((l2 & 15) ^ (8 * (g2 >> 1)))) * 8;
    sfor<NC>([&](auto C) {
      constexpr int c = decltype(C)::value;
      const int ah = piece + ct[c] * 4608, ao = ah + (1 + 4 * mw) * 512;
      u32x2 o[5];
      SB;
#pragma unroll
      for (int m = 0; m < (c < NH ? 5 : 4); m++) {
        f32x4 a = m < 4 ? acc[m < 4 ? m : 0][c] : hacc[c < NH ? c : 0];
        f32x4 v = a * st[m][0] + cs[c] * st[m][1];
        o[m] = u32x2{pk2(v[0], v[1]), pk2(v[2], v[3])};
      }
      dsw64<0>(ao, o[0]); dsw64<512>(ao, o[1]); dsw64<1024>(ao, o[2]); dsw64<1536>(ao, o[3]);
      if (c < NH) dsw64<0>(ah, o[4]);
    });
  } else {
    const u32x4 ra = mkr(ws + WS_A2, A2_BYTES);
    const int base = (8 * p_ + 4 * mw) * KS_T * 1024;
    const int o0 = base + (NC == 4 || j_ < 7 || nw == 2 ? h_slot(j_, NC == 4 ? 2 * nw + mw : 2 + nw) * 1024 : 0x70000000);
    const int o1 = base + (NC == 4 ? h_slot(j_, 2 * nw + (mw ^ 1)) * 1024 : j_ < 7 ? h_slot(j_, 6) * 1024 + (nw - 2) * 512 : 0x70000000);
    const float c1 = -2.f * 0.7978845608028654f * 1.4426950408889634f, c2 = c1 * 0.044715f;
    unsigned sp[2];
#pragma unroll
    for (int m = 0; m < 4; m++) {
      f32x4 v[NC], t[NC];
      SB;
#pragma unroll
      for (int c = 0; c < NC; c++) t[c] = cs[c] * st[m][1] + b1[c];
      SB;
#pragma unroll
      for (int c = 0; c < NC; c++) v[c] = acc[m][c] * st[m][0] + t[c];
      SB;
#pragma unroll
      for (int c = 0; c < NC; c++) t[c] = v[c] * v[c];
      SB;
#pragma unroll
      for (int c = 0; c < NC; c++) t[c] = t[c] * c2 + c1;
      SB;
#pragma unroll
      for (int c = 0; c < NC; c++) t[c] = t[c] * v[c];
      SB;
#pragma unroll
      for (int c = 0; c < NC; c++)
#pragma unroll
        for (int e = 0; e < 4; e++) t[c][e] = __builtin_amdgcn_exp2f(t[c][e]);
      SB;
#pragma unroll
      for (int c = 0; c < NC; c++) t[c] = t[c] + 1.f;
      SB;
#pragma unroll
      for (int c = 0; c < NC; c++)
#pragma unroll
        for (int e = 0; e < 4; e++) t[c][e] = __builtin_amdgcn_rcpf(t[c][e]);
      SB;
#pragma unroll
      for (int c = 0; c < NC; c++) v[c] = v[c] * t[c];
      SB;
      unsigned o[NC][2];
#pragma unroll
      for (int c = 0; c < NC; c++) { o[c][0] = pk2(v[c][0], v[c][1]); o[c][1] = pk2(v[c][2], v[c][3]); }
      st_wt(ra, l2 * 16, o0 + m * KS_T * 1024, u32x4{o[0][0], o[0][1], o[1][0], o[1][1]});
      if (NC == 4) st_wt(ra, l2 * 16, o1 + m * KS_T * 1024, u32x4{o[2][0], o[2][1], o[NC - 1][0], o[NC - 1][1]});
      else if (m & 1) {
        auto s0 = __builtin_amdgcn_permlane32_swap(sp[0], o[2][0], false, false);
        auto s1 = __builtin_amdgcn_permlane32_swap(sp[1], o[2][1], false, false);
        st_wt(ra, (l2 >> 5) * (KS_T * 1024) + (l2 & 31) * 16, o1 + (m - 1) * KS_T * 1024, u32x4{s0[0], s1[0], s0[1], s1[1]});
      } else { sp[0] = o[2][0]; sp[1] = o[2][1]; }
    }
  }
}

template <bool MAX> DEVI float red4(const float (&v)[4]) {
  auto op = [](unsigned a, unsigned b) { float r; if (MAX) asm("v_max_f32 %0, %1, %2" : "=v"(r) : "v"(a), "v"(b)); else r = asf(a) + asf(b); return r; };
  auto a = __builtin_amdgcn_permlane32_swap(asu(v[0]), asu(v[1]), false, false);
  auto b = __builtin_amdgcn_permlane32_swap(asu(v[2]), asu(v[3]), false, false);
  auto c = __builtin_amdgcn_permlane16_swap(asu(op(a[0], a[1])), asu(op(b[0], b[1])), false, false);
  return op(c[0], c[1]);
}
DEVI void bcast4(float r, float (&v)[4]) {
  auto e = __builtin_amdgcn_permlane16_swap(asu(r), asu(r), false, false);
  unsigned e0 = e[0], e1 = e[1];
  auto a = __builtin_amdgcn_permlane32_swap(e0, e0, false, false);
  auto b = __builtin_amdgcn_permlane32_swap(e1, e1, false, false);
  v[0] = asf(a[0]); v[1] = asf(a[1]); v[2] = asf(b[0]); v[3] = asf(b[1]);
}

DEVI void att_table(lptr shm, int lane) {
  const int i = lane & 15, g = lane >> 4, p = lane & 3;
  const int d = sig(i) - g;
  const unsigned NEG = 0xf149f2cau;
  bool vo[4], vp[4];
#pragma unroll
  for (int r = 0; r < 4; r++) { vo[r] = (unsigned)(d - 4 * r) <= 7u; vp[r] = d + 16 - 4 * r <= 7; }
  auto pm = [](bool lo, bool hi) { return (lo ? 0xffffu : 0u) | (hi ? 0xffff0000u : 0u); };
  LAS u32x4* t = (LAS u32x4*)(shm + LDS_TAB) + lane;
  t[0] = u32x4{vo[0] ? 0u : NEG, vo[1] ? 0u : NEG, vo[2] ? 0u : NEG, vo[3] ? 0u : NEG};
  t[64] = u32x4{pm(vp[2], vp[3]), pm(vo[0], vo[1]), vp[2] ? 0u : NEG, vp[3] ? 0u : NEG};
  t[128] = u32x4{pm(vo[2], vo[3]), (unsigned)(LDS_TB + (g * 16 + (i ^ (8 * (g >> 1)))) * 8), (unsigned)(LDS_TB + (p * 16 + ((lane >> 2) ^ (8 * (p >> 1)))) * 8), 0u};
}
DEVI int sel(u64 mask, int a, int b) { int r; asm("v_cndmask_b32_e64 %0, %2, %1, %3" : "=v"(r) : "v"(a), "v"(b), "s"(mask)); return r; }

DEVI void s1_attention(lptr shm, int w, int p_, int j_, char* ws PR_ARGS) {
  arrive(3);
  const int lane = lane_id();
  const int tau = (w + 1) * 512, taup = w >= 2 ? tau - 1024 : 0, flip = w == 1 ? 16 : 0;
  const LAS u32x4* t = (const LAS u32x4*)(shm + LDS_TAB) + lane;
  const u32x4 t0 = t[0], t2 = t[128];
  u32x4 t1 = t[64];
  if (p_ == 0 && w < 2) { t1[0] = 0u; t1[2] = 0xf149f2cau; t1[3] = 0xf149f2cau; }
  const f32x4 mO = __builtin_bit_cast(f32x4, t0), mP = __builtin_bit_cast(f32x4, t1);
  const unsigned M1 = t1[0], M2 = t1[1], M3 = t2[0];
  const int po = t2[1] + tau, pp = (t2[1] ^ flip) + taup, to = t2[2] + tau, tp = (t2[2] ^ flip) + taup;
  int aq[4], avo[4], avp[4];
#pragma unroll
  for (int h = 0; h < 4; h++) {
    aq[h] = sel(0xffffull << (16 * h), po, LDS_ZERO - 4 * 4608);
    avo[h] = sel(0x1111111111111111ull << h, to, LDS_ZERO - 14 * 4608);
    avp[h] = sel(0x1111111111111111ull << h, tp, LDS_ZERO - 14 * 4608);
    asm volatile("" : "+v"(aq[h]), "+v"(avo[h]), "+v"(avp[h]));
  }
  STAMPA(14);
  await<0, 8>(3);
  STAMPA(15);
  u32x2 qa[4][2], ko[4][2], kp[4][2], va[4][2], vb[4][2];
  f32x4 sp[4], so[4];
  auto rd = [&](auto H) {
    constexpr int h = decltype(H)::value;
    ds64<(5 + h) * 4608>(ko[h][0], po); ds64<9 * 4608>(ko[h][1], po); ds64<h * 4608>(qa[h][0], po); ds64<4 * 4608>(qa[h][1], aq[h]);
    ds64<(5 + h) * 4608>(kp[h][0], pp); ds64<9 * 4608>(kp[h][1], pp);
  };
  auto rv = [&](auto H) {
    constexpr int h = decltype(H)::value;
    dstr<(10 + h) * 4608>(va[h][0], tp); dstr<(10 + h) * 4608>(va[h][1], to); dstr<14 * 4608>(vb[h][0], avp[h]); dstr<14 * 4608>(vb[h][1], avo[h]);
  };
  auto qk = [&](auto H, auto N) {
    constexpr int h = decltype(H)::value;
    asm volatile("s_waitcnt lgkmcnt(%6)" : "+v"(ko[h][0]), "+v"(ko[h][1]), "+v"(qa[h][0]), "+v"(qa[h][1]), "+v"(kp[h][0]), "+v"(kp[h][1]) : "i"(decltype(N)::value));
    so[h] = mfma(cat(ko[h][0], ko[h][1]), cat(qa[h][0], qa[h][1]), mO);
    sp[h] = mfma(cat(kp[h][0], kp[h][1]), cat(qa[h][0], qa[h][1]), mP);
  };
  rd(IC(0)); rd(IC(1));
  qk(IC(0), IC(6)); rd(IC(2));
  qk(IC(1), IC(6)); rd(IC(3));
  qk(IC(2), IC(6)); rv(IC(0)); rv(IC(1));
  qk(IC(3), IC(8)); rv(IC(2));
  asm volatile("s_waitcnt lgkmcnt(8)");
  rv(IC(3));
  const float qs = 0.22360679774997896f * 1.4426950408889634f;
  float ee[4], eo[4], mx[4], l[4];
#pragma unroll
  for (int h = 0; h < 4; h++) { ee[h] = fmaxf(fmaxf(sp[h][2], so[h][0]), so[h][2]); eo[h] = fmaxf(fmaxf(sp[h][3], so[h][1]), so[h][3]); mx[h] = fmaxf(ee[h], eo[h]); }
  bcast4(red4<true>(mx) * -qs, mx);
#pragma unroll
  for (int h = 0; h < 4; h++) {
    f32x2 t = f32x2{ee[h], eo[h]} * qs + mx[h];
    ee[h] = __builtin_amdgcn_exp2f(t[0]); eo[h] = __builtin_amdgcn_exp2f(t[1]); l[h] = ee[h] + eo[h];
  }
  bcast4(__builtin_amdgcn_rcpf(red4<false>(l)), l);
  asm volatile("s_waitcnt lgkmcnt(0)" : "+v"(va[0][0]), "+v"(va[0][1]), "+v"(va[1][0]), "+v"(va[1][1]), "+v"(va[2][0]), "+v"(va[2][1]), "+v"(va[3][0]), "+v"(va[3][1]),
               "+v"(vb[0][0]), "+v"(vb[0][1]), "+v"(vb[1][0]), "+v"(vb[1][1]), "+v"(vb[2][0]), "+v"(vb[2][1]), "+v"(vb[3][0]), "+v"(vb[3][1]));
  f32x4 o4 = {0, 0, 0, 0}, oh[4];
#pragma unroll
  for (int h = 0; h < 4; h++) {
    const unsigned pk = pk2(f32x2{ee[h], eo[h]} * l[h]);
    u32x4 pf = {0u, pk & M1, pk & M2, pk & M3};
    o4 = mfma(cat(vb[h][0], vb[h][1]), pf, o4);
    oh[h] = mfma(cat(va[h][0], va[h][1]), pf, f32x4{0, 0, 0, 0});
  }
  const u32x4 ra = mkr(ws + WS_A2, A2_BYTES);
  const int base = ((8 * p_ + w) * KS_T + 54 + 5 * (j_ >> 1)) * 1024;
#pragma unroll
  for (int hp = 0; hp < 2; hp++)
    st_wt(ra, lane * 16, base + ((j_ & 1) * 3 + hp) * 1024,
          u32x4{pk2(oh[2 * hp][0], oh[2 * hp][1]), pk2(oh[2 * hp][2], oh[2 * hp][3]), pk2(oh[2 * hp + 1][0], oh[2 * hp + 1][1]), pk2(oh[2 * hp + 1][2], oh[2 * hp + 1][3])});
  unsigned a0 = pk2(o4[0], o4[1]), a1 = pk2(o4[2], o4[3]);
  auto s0 = __builtin_amdgcn_permlane32_swap(a0, a0, false, false);
  auto s1 = __builtin_amdgcn_permlane32_swap(a1, a1, false, false);
  st_wt(ra, lane < 32 ? lane * 16 : 0x70000000, base + 2 * 1024 + (j_ & 1) * 512, u32x4{s0[0], s1[0], s0[1], s1[1]});
  arrive(4);
}

DEVI int s2_perm(int s, int rot) { return s < 8 ? s ^ rot : (s < 10 || s >= 18) ? s ^ (rot & 1) : ((s - 10) ^ rot) + 10; }
DEVI int fin_off(int G0, int n_, int r0, int lane) {
  const int frow = r0 + (lane >> 3), fG = G0 + (frow >> 4);
  return (32 * (fG >> 1) + 2 * sig(frow & 15) + (fG & 1)) * 1280 + n_ * 128 + (lane & 7) * 16;
}
constexpr int s2_slot(int r) { return r < 18 ? (r % 9) * 16384 : 147456 + (r - 18) * 4096; }
DEVI void watch(char* ws, int Ga, int Gb, int n_, int kind, int word, u64 fv) {
  const int lane = lane_id();
  GAS u64* fp = (GAS u64*)(ws + WS_BOX) + ((((lane & 8) ? Gb : Ga) >> 3) * 10 + n_) * 32 + 8 * kind + (lane & 7);
#pragma nounroll
  for (unsigned spins = 0; spins < SPIN_CAP; spins++) {
    if (__all(__hip_atomic_load(fp, __ATOMIC_RELAXED, __HIP_MEMORY_SCOPE_AGENT) == fv)) break;
    __builtin_amdgcn_s_sleep(1);
  }
  post(LDS_CNT + 4 * word, 1);
}

template <int NS, int MT>
DEVI void s2_compute(int w, int kq, int G0, int n_, int rot, char* ws, u64 fv PR_ARGS) {
  constexpr int AD = (MT == 3 ? 3 : 4) + (NS == 20);
  const int mh = w >> 2, lane = lane_id();
  const u32x4 ra = mkr(ws + WS_A2, A2_BYTES);
  int av = lane * 16;
  pin(av);
  const int ao = ((G0 + MT * mh) * KS_T + (NS == 20 ? 54 : 18 * (kq - 1))) * 1024;
  int bs[4];
#pragma unroll
  for (int c = 0; c < 4; c++) bs[c] = kq * 4096 + (lane >> 2) * 128 + ((c ^ ((lane >> 3) & 3)) << 5) + 8 * (lane & 3);
  f32x4 acc[MT][4];
#pragma unroll
  for (int m = 0; m < MT; m++)
#pragma unroll
    for (int c = 0; c < 4; c++) { acc[m][c] = f32x4{0, 0, 0, 0}; pin(acc[m][c]); }
  u32x4 a[AD][MT];
  u32x2 b[3][4][2];
  int ca = LDS_CONS + kq * 8 + mh * 4, pa = LDS_PROD + kq * 4;
  pin(ca); pin(pa);
  unsigned pv, lv;
  auto rdb = [&](auto R, auto K) {
    constexpr int r = decltype(R)::value, k = decltype(K)::value;
    constexpr int off = s2_slot(r), nxt = s2_slot(r + 1) / 65536 - off / 65536;
    if constexpr (r < 3) dstr<off % 65536 + (k & 1) * 2048>(b[r % 3][k >> 1][k & 1], bs[k >> 1]);
    else redstr<off % 65536 + (k & 1) * 2048>(b[r % 3][k >> 1][k & 1], bs[k >> 1]);
    if constexpr (nxt != 0 && (k & 1)) vinc(bs[k >> 1], nxt * 65536);
  };
  auto lda = [&](auto S, int m) {
    constexpr int s = decltype(S)::value;
    const int o = ao + m * KS_T * 1024 + (s2_perm(s, rot) << 10);
    if constexpr (s < AD) bload_sc1(a[s % AD][m], ra, av, o); else reload_sc1(a[s % AD][m], ra, av, o);
  };
  auto ldb = [&]() {
    peek(pv, pa); wait_lg<0>(); require(pv, pa, 1);
    sfor<3>([&](auto R) { sfor<8>([&](auto K) { rdb(R, K); }); wait_lg<(decltype(R)::value < 2 ? 4 : 0)>(); });
    post(ca, 1);
  };
  constexpr bool BFIRST = NS == 20 || MT == 3;
  if (BFIRST) ldb();
  STAMP(6);
  if (NS == 20) watch(ws, G0, G0 + 2 * MT - 1, n_, w == 0 ? 0 : 2, w == 0 ? 14 : 5, fv);
  await<0, 1>(NS == 20 ? 14 : 15);
  STAMP(7);
  sfor<AD - 1>([&](auto S) {
#pragma unroll
    for (int m = 0; m < MT; m++) lda(S, m);
  });
  if (!BFIRST) ldb();
  sfor<NS - 3>([&](auto S) {
    constexpr int s = decltype(S)::value;
    constexpr bool LA = s + AD - 1 < NS;
    constexpr int later = NS - 1 - s < AD - 2 ? NS - 1 - s : AD - 2;
    if constexpr (s == 3) require(pv, pa, 2);
    if constexpr (s == 7) require(pv, pa, 3);
    if constexpr (s == 10) require(pv, pa, 4);
    if constexpr (s == 13) require(pv, pa, 5);
    if constexpr (s == 2 || s == 6 || s == 9 || s == 12) peek(pv, pa);
    if constexpr (NS == 18 && s == 11 - AD) require(lv, LDS_CNT + 4 * 5, 1);
    if constexpr (NS == 18 && s == 10 - AD) peek(lv, LDS_CNT + 4 * 5);
    if constexpr (s == NS - 4) peek(lv, ca ^ 4);
    sfor<MT>([&](auto M) {
      constexpr int m = decltype(M)::value;
      if constexpr (LA) lda(IC(s + AD - 1), m);
      wait_vm<MT * later + MT - 1 - m + (LA ? m + 1 : 0)>();
      if constexpr (m == 1 && s >= 2) wait_lg<4>();
      sfor<4>([&](auto C) {
        constexpr int c = decltype(C)::value;
        mfma_a(acc[m][c], cat(b[s % 3][c][0], b[s % 3][c][1]), a[s % AD][m]);
        if constexpr (s >= 1 && m < 2) rdb(IC(s + 2), IC(4 * m + c));
      });
    });
    if constexpr (s == 3 || s == 6 || s == 9 || s == 12) post(ca, s);
  });
  wait_lg<4>();
  sfor<8>([&](auto K) { rdb(IC(NS - 1), K); });
  if constexpr (AD == 3) {
#pragma unroll
    for (int m = 0; m < MT; m++) lda(IC(NS - 1), m);
  }
  int da[4];
#pragma unroll
  for (int c = 0; c < 4; c++) da[c] = mh * 49152 + kq * 4096 + (av & 0xf0) * 16 + ((((4 * c) << 4) + (av >> 8 << 4)) ^ (av & 0x70));
  require(lv, ca ^ 4, mh ? 12 : 9);
  sfor<MT>([&](auto M) {
    constexpr int m = decltype(M)::value;
    sfor<3>([&](auto U) {
      constexpr int u = decltype(U)::value, s = NS - 3 + u;
      if constexpr (m == 0) wait_vm<MT * (2 - u) + MT - 1>();
      if constexpr (m > 0 && u == 2) wait_vm<MT - 1 - m>();
      if constexpr (m == 0 && u == 1) wait_lg<8>();
      if constexpr (m == 0 && u == 2) wait_lg<0>();
      sfor<4>([&](auto C) {
        constexpr int c = decltype(C)::value;
        mfma_a(acc[m][c], cat(b[s % 3][c][0], b[s % 3][c][1]), a[s % AD][m]);
        if constexpr (m > 0 && u > 0 && (c & 1)) dsw128<(m - 1) * 16384>(da[2 * (u - 1) + (c >> 1)], __builtin_bit_cast(u32x4, acc[m - 1][2 * (u - 1) + (c >> 1)]));
      });
    });
  });
  STAMP(9);
  asm volatile("s_nop 4");
#pragma unroll
  for (int c = 0; c < 4; c++) dsw128<(MT - 1) * 16384>(da[c], __builtin_bit_cast(u32x4, acc[MT - 1][c]));
  arrive((NS == 20 ? 28 : 12) + mh);
}

typedef __attribute__((ext_vector_type(8))) u64 u64x8;
__global__ __launch_bounds__(768, 3) void fused_block(const void*, const void*, const void*, const void*, const void*, const void*, const void*,
                                                      void*, void*) {
  u64x8 ka; u64 ka8, pc;
  asm volatile("s_load_dwordx16 %0, %3, 0x0\n\ts_load_dwordx2 %1, %3, 0x40\n\ts_getpc_b64 %2"
               : "=&s"(ka), "=&s"(ka8), "=&s"(pc) : "s"(__builtin_amdgcn_kernarg_segment_ptr()));
  extern __shared__ __attribute__((aligned(16))) char shm_[];
  lptr shm = (lptr)shm_;
  const int tid = threadIdx.x, lane = tid & 63, w = __builtin_amdgcn_readfirstlane(tid >> 6);
  const int xcd = blockIdx.x & 7, idx = blockIdx.x >> 3;
  const int p_ = (xcd & 1) * 8 + (idx & 7), sub = idx >> 3;
  const bool att = !(sub & 1);
  const int j_ = (xcd >> 1) * 2 + (sub >> 1);
  const int uidx = (idx & 7) + 8 * (idx >> 4);
  const int n_ = (xcd & 1) * 5 + (int)(((att ? 0x4444433333221100ull : 0x4433222211110000ull) >> (4 * uidx)) & 15);
  const int G0 = 32 * (xcd >> 1) + 2 * (int)(((att ? 0xeca86eca86ecececull : 0x3030963096309630ull) >> (4 * uidx)) & 15);
  const int rot = (int)(((att ? 0x6547610321322110ull : 0x5403103203213210ull) >> (4 * uidx)) & 15);
  const int kq = (w + ((w >> 2) & 1)) & 3;
  STAMP(0);
  if (w == 7) {
    touch(mkr((const void*)(pc & ~255ull), CODE_BYTES), (idx + 32 * lane) * 128);
  }
  const int hf = idx & 1, tx = ((idx >> 1) ^ sub) & 1;
  const int rid = (att ? 0 : 8) + j_;
  if (w >= 8) __builtin_amdgcn_s_setprio(3);
  const int lw = w & 3;
  const int RS = att ? 3840 : 3456;
  const int r8 = lane >> 3, cp = lane & 7, t0 = p_ * 128;
  int xo[2], hv, wv;
#pragma unroll
  for (int e = 0; e < 2; e++)
    xo[e] = (t0 + 32 * lw + 2 * (4 * (r8 & 3) + 2 * e + (r8 >> 2))) * 1280 + ((cp ^ (4 * e + (r8 >> 1))) << 4);
  {
    const int hi = lane >= 30, ch = lane - 30 * hi, lim = att ? 63 : j_ < 7 ? 27 : 19;
    wv = hi * RS + ((ch >= 10) + (ch >= 20)) * (att ? 1120 : 0) + j_ * (att ? 160 : 448) + min(ch, lim) * 16;
    const int tok = max(t0 - 16 + 8 * (r8 & 1) + 2 * lw + ((r8 & 3) < 2), 0);
    hv = (tok * 1280 + ((cp ^ ((2 * lw + ((r8 & 3) >> 1)) & 7)) << 4)) | (att ? 0 : 0x70000000);
  }
  pin(xo[0]); pin(xo[1]); pin(wv); pin(hv);
  asm volatile("s_waitcnt lgkmcnt(0)" : "+s"(ka), "+s"(ka8));
  const char *x = (const char*)ka[0], *wqkv = (const char*)ka[1], *wo = (const char*)ka[2], *w1 = (const char*)ka[3], *b1 = (const char*)ka[4],
             *w2 = (const char*)ka[5], *b2 = (const char*)ka[6];
  char *out = (char*)ka[7], *ws = (char*)ka8;
  int fu;
  if (__builtin_expect(w >= 8, 1)) {
    auto ktile = [&](int T) { return T ^ tx; };
    const u32x4 rx = mkr(x, S_ * D_ * 2), rw1 = mkr(att ? wqkv : w1, D_ * RS);
    const int wrow = (16 * (lw & 1) + 4 * (lw >> 1)) * RS;
    const u64 hmask = lw == 0 ? 0xffffff00ull : 0xffffffffull;
    auto ldx = [&](int T) { const int o = ktile(T) * 128; dma4<1024>(rx, ~0ull, (T % 3) * SLOT + XOFF + 2048 + 4 * lw * 1024, xo[0], xo[1], o, o, o + 1280, o + 1280); };
    auto ldw = [&](int T, int h) {
      const int so = (64 * ktile(T) + 32 * (h ^ hf)) * RS + wrow;
      dma4<960>(rw1, 0x0fffffffffffffffull, (T % 3) * SLOT + h * WSLAB + 4 * lw * 960, wv, wv, so, so + 2 * RS, so + 8 * RS, so + 10 * RS);
    };
    auto ldh = [&](int T) { dma1(rx, hmask, (T % 3) * SLOT + XOFF + lw * 512, hv, ktile(T) * 128); };
    STAMP(12);
    ldx(0); ldw(0, 0); ldh(0); ldw(0, 1);
    STAMP(13);
    bar_vm<4>();
    STAMP(14);
    ldx(1); ldh(1);
    bar_vm<4>();
    ldw(1, 0); ldw(1, 1);
    ldx(2); ldh(2);
    bar_vm<4>();
    const int srow = p_ * 128 + 8 * rid + 2 * lw + (lane >> 5);
    u32x2 xr[5];
    unsigned ep, b1v;
    setup_loads(xr, ep, b1v, rx, srow * 1280 + (lane & 31) * 8, mkr(ws, 4), mkr(b1, F_ * 2), (j_ * 224 + 64 * lw + lane) * 2);
    touch_sc1(mkr(ws + WS_BOX, lw == 0 ? 160 * 256 : 0), (((G0 >> 3) + (lane & 1)) * 10 + n_) * 256 + (lane & 2) * 64);
    ldw(2, 0); ldw(2, 1); ldx(3); ldh(3); ldw(3, 0);
    bar_vm<8>();
    pin(ep); pin(b1v);
#pragma unroll
    for (int k = 0; k < 5; k++) pin(xr[k]);
    const unsigned tag = __builtin_amdgcn_readfirstlane(ep) + 1u;
    ldw(3, 1); ldx(4); ldw(4, 0); ldh(4); ldw(4, 1);
    {
      float s = 0;
#pragma unroll
      for (int k = 0; k < 5; k++) s += (blo(xr[k][0]) + bhi(xr[k][0])) + (blo(xr[k][1]) + bhi(xr[k][1]));
      const float m = half_sum(s) * (1.f / D_);
      float q = 0;
#pragma unroll
      for (int k = 0; k < 10; k++) { const unsigned u = xr[k >> 1][k & 1]; const float a = blo(u) - m, b = bhi(u) - m; q = __builtin_fmaf(a, a, q); q = __builtin_fmaf(b, b, q); }
      const float rs = __builtin_amdgcn_rsqf(half_sum(q) * (1.f / D_) + 1e-5f);
      st_wt(mkr(ws + WS_STAT, S_ * 16), (lane & 31) == 0 ? srow * 16 : 0x70000000, 0, u32x4{asu(rs), tag, asu(-m * rs), tag});
      *(LAS float*)(shm + LDS_B1 + (64 * lw + lane) * 4) = blo(b1v);
      *(LAS unsigned*)(shm + LDS_CNT + 4) = tag;
    }
    bar_vm<12>();
    const u32x4 rw = mkr(kq == 0 ? wo : w2, kq == 0 ? D_ * 1280 : F_ * 1280);
    const int cbyte = n_ * 128 + ((cp ^ ((lane >> 4) << 1)) << 4);
    const int v1 = r8 * 1280 + cbyte, v2 = kq == 0 ? (20 * (r8 >> 2) + (r8 & 3)) * 1280 + cbyte : v1;
    int gtab;
    {
      const int so = s2_perm(lane, rot), P = (so * 13) >> 6, kp = so - 5 * P;
      const int r0 = 160 * P + (kp == 2 ? 16 : 40 * (kp - (kp > 2)));
      const int t = 3 * so + kq - 1, u = t - 32;
      const int r1 = 32 * (t < 32 ? 7 * (t >> 2) + (t & 3) : 4 * ((u * 43) >> 7) + 4 + u);
      gtab = kq == 0 ? r0 * 1280 + (kp == 2) : r1 * 1280;
      pin(gtab);
    }
    const int sbn = 1280 * (kq == 0 ? 20 : 16);
    auto geom = [&](int s, int& rb, int& sa, int& sb, int& v) {
      const int e = __builtin_amdgcn_readlane(gtab, s), f = e & 1;
      v = v1 + ((v2 - v1) & -f);
      rb = e - f;
      sa = f ? 1280 * 80 : 1280 * 8;
      sb = f ? 1280 * 40 : sbn;
    };
    u32x4 pre[9][4];
    auto ldr = [&](auto R) {
      constexpr int r = decltype(R)::value;
      int rb, sa, sb, v; geom(9 + r, rb, sa, sb, v);
      bload(pre[r][0], rw, v, rb); bload(pre[r][1], rw, v, rb + sa); bload(pre[r][2], rw, v, rb + sb); bload(pre[r][3], rw, v, rb + sa + sb);
    };
    auto ldl = [&](auto S) {
      constexpr int s = decltype(S)::value;
      int rb, sa, sb, v; geom(s, rb, sa, sb, v);
      dma4<1024>(rw, ~0ull, s < 9 ? s * 16384 + kq * 4096 : 147456 + (s - 18) * 4096, v, v, rb, rb + sa, rb + sb, rb + sa + sb);
    };
    const u32x4 rst = mkr(ws + WS_STAT, S_ * 16);
    const int spos = 36 * lw + (lane < 36 ? lane : 35), sv = row_token(p_, spos) * 16;
    u32x4 sg;
    sfor<5>([&](auto TT) {
      constexpr int T = decltype(TT)::value + 5;
      if (T == 9) bload_sc1(sg, rst, sv, 0);
      if (T == 6) { STAMP(15); }
      ldx(T); ldw(T, 0); ldh(T); ldw(T, 1);
      if (T == 9) {
        const int wv2 = ((blockIdx.x >> 7) * 64 + (blockIdx.x & 63)) * 4 + lw;
        touch(mkr(wv2 < 360 ? w2 : wo, att ? 0 : wv2 < 360 ? F_ * 1280 : D_ * 1280), ((wv2 < 360 ? wv2 : wv2 - 360) * 48 + min(lane, 47)) * 128);
      }
      bar_vm<12 + (T == 9)>();
    });
    STAMP(1);
    asm volatile("" : "+v"(sg));
#pragma nounroll
    for (unsigned spins = 0; !__all(sg[1] == tag && sg[3] == tag) && spins < SPIN_CAP; spins++) {
      __builtin_amdgcn_s_sleep(2);
      bload_sc1(sg, rst, sv, 0);
      asm volatile("s_waitcnt vmcnt(0)" : "+v"(sg) :: "memory");
    }
    *(LAS u32x2*)(shm + LDS_ST + spos * 8) = u32x2{sg[0], sg[2]};
    ldl(IC(4));
    if (att) wait_vm<4>(); else wait_vm<5>();
    STAMP(2);
    bar_lgkm();
    __builtin_amdgcn_s_setprio(0);
    STAMP(3);
    const int pa = LDS_PROD + kq * 4, wa = kq * 4096 + lane * 16;
    if (!att) { ldl(IC(5)); ldl(IC(6)); ldl(IC(7)); }
    ldr(IC(1)); ldr(IC(2)); ldr(IC(3)); ldr(IC(4));
    STAMP(4);
    ldl(IC(3));
    await<1, 8>(2);
    ldl(IC(2)); ldl(IC(1)); ldl(IC(0));
    if (!att) { ldl(IC(8)); if (kq == 0) { ldl(IC(18)); ldl(IC(19)); } }
    ldr(IC(0)); wait_vm<4>();
    post(pa, att ? 1 : 2);
    STAMP(5);
    if (!att) __builtin_amdgcn_s_setprio(3);
    ldr(IC(5)); ldr(IC(6)); ldr(IC(7)); ldr(IC(8));
    if (att) {
      await<1, 8>(4);
      __builtin_amdgcn_s_setprio(3);
      ldl(IC(5)); ldl(IC(6)); ldl(IC(7)); ldl(IC(8));
      if (kq == 0) { ldl(IC(18)); ldl(IC(19)); }
    }
    if (lw == 3) touch(mkr(b2, D_ * 2), n_ * 128);
    wait_vm<0>();
    post(pa, 2);
#define PUT(r, off) dsw128<off>(wa + (r / 3) * 49152, pre[r][0]); dsw128<off + 1024>(wa + (r / 3) * 49152, pre[r][1]); dsw128<off + 2048>(wa + (r / 3) * 49152, pre[r][2]); dsw128<off + 3072>(wa + (r / 3) * 49152, pre[r][3]);
    await_pair(LDS_CONS + kq * 8, 1);
    PUT(0, 0) PUT(1, 16384) PUT(2, 32768)
    post(pa, 3);
    __builtin_amdgcn_s_setprio(0);
    STAMP(6);
    await_pair(LDS_CONS + kq * 8, 3);
    PUT(3, 0) PUT(4, 16384) PUT(5, 32768)
    post(pa, 4);
    await_pair(LDS_CONS + kq * 8, 6);
    PUT(6, 0) PUT(7, 16384) PUT(8, 32768)
    post(pa, 5);
    fu = lw;
  } else {
    if (tid < 24) *(LAS u32x4*)(shm + (tid >> 3) * SLOT + XOFF + (tid & 7) * 16) = u32x4{0x3f803f80u, 0x3f803f80u, 0x3f803f80u, 0x3f803f80u};
    if (tid >= 448 && tid < 480) *(LAS unsigned*)(shm + LDS_CNT + 4 * (tid - 448)) = 0u;
    if (w == 5 && att) att_table(shm, lane);
    const bool light = att ? (w >= 6) : (w >= 4);
    if (light) s1_compute<3>(shm, att, w, lane, p_, j_, hf, ws PR_PASS);
    else s1_compute<4>(shm, att, w, lane, p_, j_, hf, ws PR_PASS);
    STAMP(3);
    if (att) s1_attention(shm, w, p_, j_, ws PR_PASS);
    STAMP(4);
    const unsigned tag = __builtin_amdgcn_readfirstlane(*(LAS unsigned*)(shm + LDS_CNT + 4));
    GAS u64* flags = (GAS u64*)(ws + WS_FLAG);
    const u64 fv = flagval(tag);
    wait_vm<0>();
    {
      const int lane = lane_id(), kind = att ? 0 : 1 + (w >> 2);
      unsigned old = 0;
      if (lane == 0) old = __hip_atomic_fetch_add((LAS unsigned*)(shm + LDS_CNT + (kind == 1 ? 120 : 0)), 1u, __ATOMIC_RELAXED, __HIP_MEMORY_SCOPE_WORKGROUP);
      if (__builtin_amdgcn_readfirstlane(old) == (att ? 7u : 3u)) {
        GAS u64* dst = lane == 10 ? flags + p_ * 16 + rid : (GAS u64*)(ws + WS_BOX) + (p_ * 10 + lane) * 32 + 8 * kind + j_;
        if (lane <= 10) __hip_atomic_store(dst, fv, __ATOMIC_RELAXED, __HIP_MEMORY_SCOPE_AGENT);
      }
    }
    STAMP(5);
    if (w == 0) watch(ws, G0, G0 + (att ? 3 : 5), n_, 1, 15, fv);
    if (kq == 0) {
      if (att) s2_compute<20, 2>(w, kq, G0, n_, rot, ws, fv PR_PASS);
      else s2_compute<20, 3>(w, kq, G0, n_, rot, ws, fv PR_PASS);
    } else {
      if (att) s2_compute<18, 2>(w, kq, G0, n_, rot, ws, fv PR_PASS);
      else s2_compute<18, 3>(w, kq, G0, n_, rot, ws, fv PR_PASS);
    }
    if (blockIdx.x == 0 && w == 3) {
      const int lane = lane_id();
#pragma nounroll
      for (unsigned spins = 0; spins < SPIN_CAP; spins++) {
        bool ok = true;
#pragma unroll
        for (int k = 0; k < 4; k++) ok &= __hip_atomic_load(flags + lane + 64 * k, __ATOMIC_RELAXED, __HIP_MEMORY_SCOPE_AGENT) == fv;
        if (__all(ok)) break;
        __builtin_amdgcn_s_sleep(8);
      }
      if (lane == 0) __hip_atomic_store((GAS unsigned*)ws, tag, __ATOMIC_RELAXED, __HIP_MEMORY_SCOPE_AGENT);
    }
    if (att || w == 0 || w > 2) { STAMP(11); FINISH(); return; }
    fu = w + 3;
  }
  {
    const int lane = lane_id();
    const u32x4 bv = *(const GAS u32x4*)(b2 + n_ * 128 + (lane & 7) * 16);
    int foff[2], pr0[2];
    u32x4 xv[2];
    f32x2 gt[2][4];
#pragma unroll
    for (int hh = 0; hh < 2; hh++) {
      const int frow = 48 * hh + 8 * fu + (lane >> 3);
      foff[hh] = fin_off(G0 + (att ? 2 : 3) * hh, n_, 8 * fu, lane);
      xv[hh] = *(const GAS u32x4*)((const char*)x + foff[hh]);
      pr0[hh] = (frow >> 4) * 16384 + (frow & 15) * 256 + (((2 * (lane & 7)) ^ (frow & 7)) << 4);
      pin(pr0[hh]);
    }
#pragma unroll
    for (int hh = 0; hh < 2; hh++) {
      await<1, 3>(12 + hh);
      f32x4 y[2];
#pragma unroll
      for (int e = 0; e < 2; e++) {
        lptr pr = shm + (pr0[hh] ^ (16 * e));
        y[e] = *(LAS f32x4*)(pr + 4096) + *(LAS f32x4*)(pr + 8192) + *(LAS f32x4*)(pr + 12288);
      }
#pragma unroll
      for (int k = 0; k < 4; k++) {
        f32x2 yy = f32x2{y[k >> 1][2 * (k & 1)], y[k >> 1][2 * (k & 1) + 1]} + f32x2{blo(bv[k]), bhi(bv[k])};
        f32x2 t = yy * -1.4426950408889634f;
        f32x2 e = f32x2{__builtin_amdgcn_exp2f(t[0]), __builtin_amdgcn_exp2f(t[1])} + 1.f;
        gt[hh][k] = yy * f32x2{__builtin_amdgcn_rcpf(e[0]), __builtin_amdgcn_rcpf(e[1])};
        pin(gt[hh][k]);
      }
    }
    STAMP(10);
#pragma unroll
    for (int hh = 0; hh < 2; hh++) {
      await<0, 1>(28 + hh);
      u32x4 o;
#pragma unroll
      for (int e = 0; e < 2; e++) {
        const f32x4 s = *(LAS f32x4*)(shm + (pr0[hh] ^ (16 * e)));
        o[2 * e] = pk2(f32x2{s[0], s[1]} * gt[hh][2 * e] + f32x2{blo(xv[hh][2 * e]), bhi(xv[hh][2 * e])});
        o[2 * e + 1] = pk2(f32x2{s[2], s[3]} * gt[hh][2 * e + 1] + f32x2{blo(xv[hh][2 * e + 1]), bhi(xv[hh][2 * e + 1])});
      }
      __builtin_nontemporal_store(o, (GAS u32x4*)(out + foff[hh]));
    }
  }
  STAMP(11);
  FINISH();
}

extern "C" void kernel_launch(void* const* d_in, const int* in_sizes, int n_in, void* d_out, int out_size, void* d_ws,
                              size_t ws_size, hipStream_t stream) {
  (void)hipFuncSetAttribute((const void*)fused_block, hipFuncAttributeMaxDynamicSharedMemorySize, LDS_TOTAL);
  fused_block<<<256, 768, LDS_TOTAL, stream>>>(d_in[0], d_in[1], d_in[2], d_in[4], d_in[5], d_in[6],
                                               d_in[7], d_out, d_ws);
}
```

```cpp
#include <hip/hip_runtime.h>
#include <hip/hip_bf16.h>
#include <type_traits>
#include <utility>

typedef __attribute__((ext_vector_type(8))) __bf16 bf16x8;
typedef __attribute__((ext_vector_type(2))) __bf16 bf16x2;
typedef __attribute__((ext_vector_type(4))) float f32x4;
typedef __attribute__((ext_vector_type(2))) float f32x2;
typedef __attribute__((ext_vector_type(4))) unsigned u32x4;
typedef __attribute__((ext_vector_type(2))) unsigned u32x2;
#define LAS __attribute__((address_space(3)))
#define GAS __attribute__((address_space(1)))
#define DEVI __device__ __forceinline__
typedef LAS char* lptr;
typedef unsigned long long u64;
#define SB __builtin_amdgcn_sched_barrier(0)
#define IC(n) std::integral_constant<int, n>{}

constexpr int S_ = 2048, D_ = 640, F_ = 1728;
constexpr int KS_T = 74;
constexpr int WSLAB = 15360;
constexpr int XOFF = 2 * WSLAB;
constexpr int SLOT = 49152;
constexpr int LDS_TB = 86016;
constexpr int LDS_ST = 155648;
constexpr int LDS_CS = LDS_ST + 1152;
constexpr int LDS_B1 = LDS_CS + 1024;
constexpr int LDS_CNT = LDS_B1 + 1024;
constexpr int LDS_ZERO = LDS_CNT + 24;
constexpr int LDS_CONS = LDS_CNT + 64;
constexpr int LDS_PROD = LDS_CONS + 32;
constexpr int LDS_TAB = LDS_CNT + 128;
constexpr int LDS_TOTAL = 163840;
constexpr int WS_FLAG = 4096, WS_STAT = 8192, WS_BOX = 65536, WS_A2 = 1 << 20;
constexpr int A2_BYTES = 128 * KS_T * 1024;
constexpr int CODE_BYTES = 46464;
constexpr unsigned SPIN_CAP = 1u << 20;

DEVI void bar_lgkm() { asm volatile("s_waitcnt lgkmcnt(0)\n\ts_barrier" ::: "memory"); }
template <int N> DEVI void bar_vm() { asm volatile("s_waitcnt vmcnt(%0)\n\ts_barrier" ::"i"(N) : "memory"); }
template <int N> DEVI void wait_vm() { asm volatile("s_waitcnt vmcnt(%0)" ::"i"(N) : "memory"); }
DEVI unsigned pk2(float a, float b) { f32x2 v = {a, b}; return __builtin_bit_cast(unsigned, __builtin_convertvector(v, bf16x2)); }
DEVI unsigned pk2(f32x2 v) { return __builtin_bit_cast(unsigned, __builtin_convertvector(v, bf16x2)); }
DEVI float asf(unsigned u) { return __builtin_bit_cast(float, u); }
DEVI unsigned asu(float f) { return __builtin_bit_cast(unsigned, f); }
DEVI float blo(unsigned u) { return __builtin_bit_cast(float, u << 16); }
DEVI float bhi(unsigned u) { return __builtin_bit_cast(float, u & 0xffff0000u); }
DEVI f32x4 mfma(u32x4 a, u32x4 b, f32x4 c) {
  return __builtin_amdgcn_mfma_f32_16x16x32_bf16(__builtin_bit_cast(bf16x8, a), __builtin_bit_cast(bf16x8, b), c, 0, 0, 0);
}
DEVI u32x4 cat(u32x2 a, u32x2 b) { return __builtin_shufflevector(a, b, 0, 1, 2, 3); }
DEVI void mfma_a(f32x4& c, u32x4 a, u32x4 b) { asm volatile("v_mfma_f32_16x16x32_bf16 %0, %1, %2, %0" : "+v"(c) : "v"(a), "v"(b)); }
template <int OFF> DEVI void ds128(u32x4& d, int addr) { asm volatile("ds_read_b128 %0, %1 offset:%2" : "=v"(d) : "v"(addr), "i"(OFF)); }
template <int OFF> DEVI void dstr(u32x2& d, int addr) { asm volatile("ds_read_b64_tr_b16 %0, %1 offset:%2" : "=v"(d) : "v"(addr), "i"(OFF)); }
template <int OFF> DEVI void dsw128(int addr, u32x4 d) { asm volatile("ds_write_b128 %0, %1 offset:%2" ::"v"(addr), "v"(d), "i"(OFF) : "memory"); }
DEVI u32x4 mkr(const void* p, unsigned bytes) {
  u64 a = (u64)p;
  return u32x4{(unsigned)a, (unsigned)(a >> 32) & 0xffffu, bytes, 0x00020000u};
}
DEVI void st_wt(u32x4 r, int voff, int soff, u32x4 v) {
  asm volatile("buffer_store_dwordx4 %0, %1, %2, %3 offen sc1\n\ts_nop 1" ::"v"(v), "v"(voff), "s"(r), "s"(soff) : "memory");
}
template <int LSTEP>
DEVI void dma4(u32x4 r, u64 mask, int l, int va, int vb, int s0, int s1, int s2, int s3) {
  asm volatile(
      "s_mov_b64 exec, %0\n\t"
      "s_mov_b32 m0, %1\n\ts_nop 0\n\tbuffer_load_dwordx4 %2, %4, %5 offen lds\n\t"
      "s_add_u32 m0, %1, %9\n\ts_nop 0\n\tbuffer_load_dwordx4 %3, %4, %6 offen lds\n\t"
      "s_add_u32 m0, %1, %10\n\ts_nop 0\n\tbuffer_load_dwordx4 %2, %4, %7 offen lds\n\t"
      "s_add_u32 m0, %1, %11\n\ts_nop 0\n\tbuffer_load_dwordx4 %3, %4, %8 offen lds\n\t"
      "s_mov_b64 exec, -1"
      :: "s"(mask), "s"(l), "v"(va), "v"(vb), "s"(r), "s"(s0), "s"(s1), "s"(s2), "s"(s3), "i"(LSTEP), "i"(2 * LSTEP), "i"(3 * LSTEP)
      : "memory", "scc");
}
DEVI void dma1(u32x4 r, u64 mask, int l, int v, int s0) {
  asm volatile("s_mov_b64 exec, %0\n\ts_mov_b32 m0, %1\n\ts_nop 0\n\tbuffer_load_dwordx4 %2, %3, %4 offen lds\n\ts_mov_b64 exec, -1"
               :: "s"(mask), "s"(l), "v"(v), "s"(r), "s"(s0) : "memory");
}
DEVI void setup_loads(u32x2 (&xr)[5], unsigned& ep, unsigned& bv, u32x4 rx, int vx, u32x4 re, u32x4 rb, int vb) {
  asm volatile("buffer_load_dwordx2 %0, %7, %8, 0 offen\n\tbuffer_load_dwordx2 %1, %7, %8, 0 offen offset:256\n\tbuffer_load_dwordx2 %2, %7, %8, 0 offen offset:512\n\t"
               "buffer_load_dwordx2 %3, %7, %8, 0 offen offset:768\n\tbuffer_load_dwordx2 %4, %7, %8, 0 offen offset:1024\n\t"
               "buffer_load_dword %5, off, %9, 0 sc1\n\tbuffer_load_ushort %6, %11, %10, 0 offen"
               : "=&v"(xr[0]), "=&v"(xr[1]), "=&v"(xr[2]), "=&v"(xr[3]), "=&v"(xr[4]), "=&v"(ep), "=&v"(bv)
               : "v"(vx), "s"(rx), "s"(re), "s"(rb), "v"(vb) : "memory");
}
template <int N>
DEVI void bar_vm1(int flag) {
  asm volatile("s_waitcnt vmcnt(%2)\n\ts_cmp_lg_u32 %0, 0\n\ts_cbranch_scc1 .La%=\n\ts_waitcnt vmcnt(%1)\n.La%=:\n\ts_barrier" ::"s"(flag), "i"(N), "i"(N + 1) : "memory", "scc");
}
template <class T> DEVI void pin(T& v) { asm volatile("" : "+v"(v)); }
DEVI void dsw_col0(int a0, f32x4 d0, int a1, f32x4 d1, int a2, f32x4 d2) {
  asm volatile("s_mov_b64 exec, %6\n\tds_write_b128 %0, %1\n\tds_write_b128 %2, %3\n\tds_write_b128 %4, %5\n\ts_mov_b64 exec, -1"
               ::"v"(a0), "v"(d0), "v"(a1), "v"(d1), "v"(a2), "v"(d2), "s"(0x0001000100010001ull) : "memory");
}
DEVI void reload_sc1(u32x4& d, u32x4 r, int v, int s0) { asm volatile("buffer_load_dwordx4 %0, %1, %2, %3 offen sc1" : "+v"(d) : "v"(v), "s"(r), "s"(s0) : "memory"); }
template <int OFF> DEVI void redstr(u32x2& d, int addr) { asm volatile("ds_read_b64_tr_b16 %0, %1 offset:%2" : "+v"(d) : "v"(addr), "i"(OFF)); }
DEVI void vinc(int& d, int s) { asm volatile("v_add_u32 %0, %1, %0" : "+v"(d) : "s"(s)); }
template <class F, int... I> DEVI void sfor_(F f, std::integer_sequence<int, I...>) { (f(std::integral_constant<int, I>{}), ...); }
template <int N, class F> DEVI void sfor(F f) { sfor_(f, std::make_integer_sequence<int, N>{}); }
DEVI void touch_sc1(u32x4 r, int v) {
  asm volatile("s_mov_b32 m0, %2\n\ts_nop 0\n\tbuffer_load_dword %0, %1, 0 offen sc1 lds" ::"v"(v), "s"(r), "i"(LDS_TOTAL - 256) : "memory");
}
DEVI void touch(u32x4 r, int v) {
  asm volatile("s_mov_b32 m0, %2\n\ts_nop 0\n\tbuffer_load_dword %0, %1, 0 offen lds" ::"v"(v), "s"(r), "i"(LDS_TOTAL - 256) : "memory");
}
DEVI void bload(u32x4& d, u32x4 r, int v, int s0) { asm volatile("buffer_load_dwordx4 %0, %1, %2, %3 offen" : "=v"(d) : "v"(v), "s"(r), "s"(s0)); }
DEVI void bload_sc1(u32x4& d, u32x4 r, int v, int s0) { asm volatile("buffer_load_dwordx4 %0, %1, %2, %3 offen sc1" : "=v"(d) : "v"(v), "s"(r), "s"(s0) : "memory"); }
DEVI void arrive(int k) {
  asm volatile("s_mov_b64 exec, 1\n\tds_add_u32 %0, %1\n\ts_mov_b64 exec, -1" ::"v"(LDS_CNT + 4 * k), "v"(1) : "memory");
}
template <int SLEEP, unsigned N>
DEVI void await(int k) {
  unsigned v;
  for (;;) {
    asm volatile("ds_read_b32 %0, %1\n\ts_waitcnt lgkmcnt(0)" : "=v"(v) : "v"(LDS_CNT + 4 * k) : "memory");
    if (__builtin_amdgcn_readfirstlane(v) >= N) break;
    if (SLEEP) __builtin_amdgcn_s_sleep(SLEEP);
  }
}
DEVI void post(int addr, int val) { asm volatile("ds_write_b32 %0, %1" ::"v"(addr), "v"(val) : "memory"); }
DEVI void peek(unsigned& v, int addr) { asm volatile("ds_read_b32 %0, %1" : "=v"(v) : "v"(addr) : "memory"); }
DEVI void require(unsigned v, int addr, unsigned n) {
  unsigned t;
  asm volatile("v_readfirstlane_b32 %1, %0\n\ts_cmp_ge_u32 %1, %3\n\ts_cbranch_scc1 .Lq%=\n"
               ".Lw%=:\n\tds_read_b32 %0, %2\n\ts_waitcnt lgkmcnt(0)\n\tv_readfirstlane_b32 %1, %0\n\ts_cmp_ge_u32 %1, %3\n\ts_cbranch_scc0 .Lw%=\n.Lq%=:"
               : "+&v"(v), "=&s"(t) : "v"(addr), "s"(n) : "memory", "scc");
}
DEVI void await_pair(int addr, unsigned n) {
  for (;;) {
    u32x2 v;
    asm volatile("ds_read_b64 %0, %1\n\ts_waitcnt lgkmcnt(0)" : "=v"(v) : "v"(addr) : "memory");
    const unsigned a = v[0], b = v[1];
    if (__builtin_amdgcn_readfirstlane(a < b ? a : b) >= n) break;
    __builtin_amdgcn_s_sleep(1);
  }
}
DEVI int lane_id() { int l; asm volatile("v_mbcnt_lo_u32_b32 %0, -1, 0\n\tv_mbcnt_hi_u32_b32 %0, -1, %0" : "=v"(l)); return l; }
DEVI int sig(int i) { return 4 * (i & 3) + (i >> 2); }
DEVI u64 flagval(unsigned tag) { return ((u64)(tag ^ 0x5bd1e995u) << 32) | tag; }

DEVI float xadd16(float v) { auto r = __builtin_amdgcn_permlane16_swap(asu(v), asu(v), false, false); return asf(r[0]) + asf(r[1]); }
template <int CTRL> DEVI float dpp(float v) { return asf(__builtin_amdgcn_mov_dpp(asu(v), CTRL, 0xf, 0xf, true)); }
DEVI float half_sum(float v) {
  v += dpp<0xb1>(v); v += dpp<0x4e>(v); v += dpp<0x141>(v); v += dpp<0x140>(v);
  return xadd16(v);
}

DEVI int h_slot(int j, int l) { const int t = l < 4 ? 4 * j + l : 28 + 3 * j + l, d = (t * 43) >> 7; return 18 * (t - 3 * d) + d; }

DEVI int row_token(int p_, int pos) {
  const int tau = pos >> 4, i = pos & 15;
  int tok = tau == 0 ? p_ * 128 - 16 + 8 * (i & 1) + 2 * (i >> 2) + ((i & 3) < 2) : p_ * 128 + 32 * ((tau - 1) >> 1) + 2 * sig(i) + ((tau - 1) & 1);
  return tok < 0 ? 0 : tok;
}

struct ColGeo { int A, B, C, ct; };
DEVI ColGeo colgeo(bool att, int nw, int mw, int c, bool heavy) {
  const int cc = heavy ? (c ^ (2 * mw)) : c;
  const int ct = 4 * nw + cc, sec = (ct * 13) >> 6, hh = ct - 5 * sec;
  const bool q4 = hh == 4, sp = !heavy && c == 2;
  ColGeo r;
  r.A = att ? sec * 160 + (q4 ? 32 : 40 * hh) : heavy ? 128 * nw + 32 * cc : (sp ? 352 + 16 * nw : 128 + 64 * nw + 32 * c);
  r.B = att && q4 ? 40 : 8;
  r.C = att ? (q4 ? 80 : 16) : (sp ? 32 : 16);
  r.ct = ct;
  return r;
}

template <int NC>
struct Frags { u32x4 x[5]; u32x2 w[NC][2]; };

template <int N> DEVI void wait_lg() { asm volatile("s_waitcnt lgkmcnt(%0)" ::"i"(N) : "memory"); }

template <int NC, int K>
DEVI void s1_rd(Frags<NC>& n, int xo, int xh, const int (&wa)[NC]) {
  if constexpr (K == 0) ds128<0>(n.x[0], xo);
  else if constexpr (K <= 2 * NC) { if ((K - 1) & 1) dstr<7680>(n.w[(K - 1) >> 1][1], wa[(K - 1) >> 1]); else dstr<0>(n.w[(K - 1) >> 1][0], wa[(K - 1) >> 1]); }
  else if constexpr (K < 2 * NC + 4) ds128<(K - 2 * NC) * 2048>(n.x[K - 2 * NC], xo);
  else if constexpr (K == 2 * NC + 4) ds128<0>(n.x[4], xh);
}
template <int NC, int NH, int K>
DEVI void s1_mm(const Frags<NC>& c, f32x4 (&acc)[4][NC], f32x4 (&hacc)[NH]) {
  if constexpr (K < 4 * NC) mfma_a(acc[K / NC][K % NC], cat(c.w[K % NC][0], c.w[K % NC][1]), c.x[K / NC]);
  else mfma_a(hacc[K - 4 * NC], cat(c.w[K - 4 * NC][0], c.w[K - 4 * NC][1]), c.x[4]);
}
template <int NC, int NH, bool BAR>
DEVI void s1_step(const Frags<NC>& c, Frags<NC>& n, int xo, int xh, const int (&wa)[NC], f32x4 (&acc)[4][NC], f32x4 (&hacc)[NH]) {
  constexpr int NM = 4 * NC + NH, NR = 2 * NC + 5, D = BAR ? NM - NR : 0;
  sfor<NM>([&](auto KK) {
    constexpr int k = decltype(KK)::value;
    constexpr int need = k < NC ? 2 * k + 2 : (k % NC == 0 && k <= 4 * NC) ? 2 * NC + k / NC : -1;
    if constexpr (BAR && k == D) bar_lgkm();
    if constexpr (need >= 0 && (!BAR || k < D)) wait_lg<NR - 1 - need + (BAR ? 0 : (k < NR ? k : NR))>();
    s1_mm<NC, NH, k>(c, acc, hacc);
    if constexpr (k >= D) s1_rd<NC, k - D>(n, xo, xh, wa);
  });
}

template <int OFF> DEVI void dsw64(int addr, u32x2 d) { asm volatile("ds_write_b64 %0, %1 offset:%2" ::"v"(addr), "v"(d), "i"(OFF) : "memory"); }
template <int OFF> DEVI void ds64(u32x2& d, int addr) { asm volatile("ds_read_b64 %0, %1 offset:%2" : "=v"(d) : "v"(addr), "i"(OFF)); }

template <int NC>
DEVI void s1_compute(lptr shm, bool att, int w, int lane, int p_, int j_, int hf, char* ws) {
  constexpr int NH = NC == 4 ? 2 : 3;
  const int mw = w & 1, nw = w >> 1, i = lane & 15, g = lane >> 4;
  f32x4 acc[4][NC], hacc[NH];
#pragma unroll
  for (int m = 0; m < 4; m++)
#pragma unroll
    for (int c = 0; c < NC; c++) { acc[m][c] = f32x4{0, 0, 0, 0}; asm volatile("" : "+v"(acc[m][c])); }
#pragma unroll
  for (int c = 0; c < NH; c++) { hacc[c] = f32x4{0, 0, 0, 0}; asm volatile("" : "+v"(hacc[c])); }
  int wb[NC], cb[NC], ct[NC];
#pragma unroll
  for (int c = 0; c < NC; c++) {
    const ColGeo cg = colgeo(att, nw, mw, c, NC == 4);
    wb[c] = (lane >> 2) * 480 + cg.A + cg.B * (lane & 1) + cg.C * ((lane >> 1) & 1);
    cb[c] = LDS_CS + 2 * (cg.A + cg.B * (g & 1) + cg.C * (g >> 1));
    ct[c] = cg.ct;
    asm volatile("" : "+v"(cb[c]), "+v"(wb[c]));
  }
  int xb = XOFF + i * 128 + ((g ^ (i >> 1) ^ (4 * hf)) << 4);
  int sta = LDS_ST + i * 8;
  asm volatile("" : "+v"(xb), "+v"(sta));
  const int own = (1 + 4 * mw) * 2048;
  Frags<NC> fa, fb;
  int wa[NC];
  bar_lgkm();
  sfor<2 * NC + 5>([&](auto K) { s1_rd<NC, decltype(K)::value>(fa, xb + own, xb, wb); });
#pragma unroll
  for (int c = 0; c < NC; c++) wa[c] = wb[c] + WSLAB;
  s1_step<NC, NH, true>(fa, fb, (xb ^ 64) + own, xb ^ 64, wa, acc, hacc);
  int sl = 0;
#pragma nounroll
  for (int T = 1; T < 10; T++) {
    sl = sl == 2 * SLOT ? 0 : sl + SLOT;
#pragma unroll
    for (int c = 0; c < NC; c++) wa[c] = wb[c] + sl;
    s1_step<NC, NH, true>(fb, fa, xb + sl + own, xb + sl, wa, acc, hacc);
#pragma unroll
    for (int c = 0; c < NC; c++) wa[c] = wb[c] + sl + WSLAB;
    s1_step<NC, NH, false>(fa, fb, (xb ^ 64) + sl + own, (xb ^ 64) + sl, wa, acc, hacc);
  }
  wait_lg<0>();
  arrive(2);
  sfor<NH>([&](auto K) { s1_mm<NC, NH, 4 * NC + decltype(K)::value>(fb, acc, hacc); });
  u32x2 stu[5];
  u32x4 b1u[NC], csu[NC];
  sfor<4 * NC>([&](auto KK) {
    constexpr int k = decltype(KK)::value;
    s1_mm<NC, NH, k>(fb, acc, hacc);
    if constexpr (k < 4) ds64<(1 + k) * 128>(stu[k], sta + mw * 512);
    if constexpr (k == 4) ds64<0>(stu[4], sta);
    if constexpr (k == 5) {
      dsw_col0(cb[0], hacc[0], cb[1], hacc[1], cb[NH - 1], hacc[NH - 1]);
      if (NC == 4) arrive(8 + nw);
    }
    if constexpr (k >= 6 && k < 6 + NC) ds128<LDS_B1 - LDS_CS>(b1u[k - 6], cb[k - 6]);
  });
  if (NC == 4) await<0, 2>(8 + nw);
#pragma unroll
  for (int c = 0; c < NC; c++) ds128<0>(csu[c], cb[c]);
  asm volatile("s_waitcnt lgkmcnt(0)\n\ts_nop 7" ::: "memory");
  f32x2 st[5];
  f32x4 cs[NC], b1[NC];
#pragma unroll
  for (int m = 0; m < 5; m++) { asm volatile("" : "+v"(stu[m])); st[m] = __builtin_bit_cast(f32x2, stu[m]); }
#pragma unroll
  for (int c = 0; c < NC; c++) { asm volatile("" : "+v"(csu[c]), "+v"(b1u[c])); cs[c] = __builtin_bit_cast(f32x4, csu[c]); b1[c] = __builtin_bit_cast(f32x4, b1u[c]); }
#pragma unroll
  for (int m = 0; m < 4; m++)
#pragma unroll
    for (int c = 0; c < NC; c++) pin(acc[m][c]);
#pragma unroll
  for (int c = 0; c < NH; c++) pin(hacc[c]);
  const int l2 = lane_id();
  if (att) {
    const int g2 = l2 >> 4, piece = LDS_TB + ((l2 & 48) + ((l2 & 15) ^ (8 * (g2 >> 1)))) * 8;
    sfor<NC>([&](auto C) {
      constexpr int c = decltype(C)::value;
      const int ah = piece + ct[c] * 4608, ao = ah + (1 + 4 * mw) * 512;
      u32x2 o[5];
      SB;
#pragma unroll
      for (int m = 0; m < (c < NH ? 5 : 4); m++) {
        f32x4 a = m < 4 ? acc[m < 4 ? m : 0][c] : hacc[c < NH ? c : 0];
        f32x4 v = a * st[m][0] + cs[c] * st[m][1];
        o[m] = u32x2{pk2(v[0], v[1]), pk2(v[2], v[3])};
      }
      dsw64<0>(ao, o[0]); dsw64<512>(ao, o[1]); dsw64<1024>(ao, o[2]); dsw64<1536>(ao, o[3]);
      if (c < NH) dsw64<0>(ah, o[4]);
    });
  } else {
    const u32x4 ra = mkr(ws + WS_A2, A2_BYTES);
    const int base = (8 * p_ + 4 * mw) * KS_T * 1024;
    const int o0 = base + (NC == 4 || j_ < 7 || nw == 2 ? h_slot(j_, NC == 4 ? 2 * nw + mw : 2 + nw) * 1024 : 0x70000000);
    const int o1 = base + (NC == 4 ? h_slot(j_, 2 * nw + (mw ^ 1)) * 1024 : j_ < 7 ? h_slot(j_, 6) * 1024 + (nw - 2) * 512 : 0x70000000);
    const float c1 = -2.f * 0.7978845608028654f * 1.4426950408889634f, c2 = c1 * 0.044715f;
    unsigned sp[2];
#pragma unroll
    for (int m = 0; m < 4; m++) {
      f32x4 v[NC], t[NC];
      SB;
#pragma unroll
      for (int c = 0; c < NC; c++) t[c] = cs[c] * st[m][1] + b1[c];
      SB;
#pragma unroll
      for (int c = 0; c < NC; c++) v[c] = acc[m][c] * st[m][0] + t[c];
      SB;
#pragma unroll
      for (int c = 0; c < NC; c++) t[c] = v[c] * v[c];
      SB;
#pragma unroll
      for (int c = 0; c < NC; c++) t[c] = t[c] * c2 + c1;
      SB;
#pragma unroll
      for (int c = 0; c < NC; c++) t[c] = t[c] * v[c];
      SB;
#pragma unroll
      for (int c = 0; c < NC; c++)
#pragma unroll
        for (int e = 0; e < 4; e++) t[c][e] = __builtin_amdgcn_exp2f(t[c][e]);
      SB;
#pragma unroll
      for (int c = 0; c < NC; c++) t[c] = t[c] + 1.f;
      SB;
#pragma unroll
      for (int c = 0; c < NC; c++)
#pragma unroll
        for (int e = 0; e < 4; e++) t[c][e] = __builtin_amdgcn_rcpf(t[c][e]);
      SB;
#pragma unroll
      for (int c = 0; c < NC; c++) v[c] = v[c] * t[c];
      SB;
      unsigned o[NC][2];
#pragma unroll
      for (int c = 0; c < NC; c++) { o[c][0] = pk2(v[c][0], v[c][1]); o[c][1] = pk2(v[c][2], v[c][3]); }
      st_wt(ra, l2 * 16, o0 + m * KS_T * 1024, u32x4{o[0][0], o[0][1], o[1][0], o[1][1]});
      if (NC == 4) st_wt(ra, l2 * 16, o1 + m * KS_T * 1024, u32x4{o[2][0], o[2][1], o[NC - 1][0], o[NC - 1][1]});
      else if (m & 1) {
        auto s0 = __builtin_amdgcn_permlane32_swap(sp[0], o[2][0], false, false);
        auto s1 = __builtin_amdgcn_permlane32_swap(sp[1], o[2][1], false, false);
        st_wt(ra, (l2 >> 5) * (KS_T * 1024) + (l2 & 31) * 16, o1 + (m - 1) * KS_T * 1024, u32x4{s0[0], s1[0], s0[1], s1[1]});
      } else { sp[0] = o[2][0]; sp[1] = o[2][1]; }
    }
  }
}

template <bool MAX> DEVI float red4(const float (&v)[4]) {
  auto op = [](unsigned a, unsigned b) { float r; if (MAX) asm("v_max_f32 %0, %1, %2\n\ts_nop 1" : "=v"(r) : "v"(a), "v"(b)); else r = asf(a) + asf(b); return r; };
  auto a = __builtin_amdgcn_permlane32_swap(asu(v[0]), asu(v[1]), false, false);
  auto b = __builtin_amdgcn_permlane32_swap(asu(v[2]), asu(v[3]), false, false);
  auto c = __builtin_amdgcn_permlane16_swap(asu(op(a[0], a[1])), asu(op(b[0], b[1])), false, false);
  return op(c[0], c[1]);
}
DEVI void bcast4(float r, float (&v)[4]) {
  auto e = __builtin_amdgcn_permlane16_swap(asu(r), asu(r), false, false);
  unsigned e0 = e[0], e1 = e[1];
  auto a = __builtin_amdgcn_permlane32_swap(e0, e0, false, false);
  auto b = __builtin_amdgcn_permlane32_swap(e1, e1, false, false);
  v[0] = asf(a[0]); v[1] = asf(a[1]); v[2] = asf(b[0]); v[3] = asf(b[1]);
}

DEVI void att_table(lptr shm, int lane) {
  const int i = lane & 15, g = lane >> 4, p = lane & 3;
  const int d = sig(i) - g;
  const unsigned NEG = 0xf149f2cau;
  bool vo[4], vp[4];
#pragma unroll
  for (int r = 0; r < 4; r++) { vo[r] = (unsigned)(d - 4 * r) <= 7u; vp[r] = d + 16 - 4 * r <= 7; }
  auto pm = [](bool lo, bool hi) { return (lo ? 0xffffu : 0u) | (hi ? 0xffff0000u : 0u); };
  LAS u32x4* t = (LAS u32x4*)(shm + LDS_TAB) + lane;
  t[0] = u32x4{vo[0] ? 0u : NEG, vo[1] ? 0u : NEG, vo[2] ? 0u : NEG, vo[3] ? 0u : NEG};
  t[64] = u32x4{pm(vp[2], vp[3]), pm(vo[0], vo[1]), vp[2] ? 0u : NEG, vp[3] ? 0u : NEG};
  t[128] = u32x4{pm(vo[2], vo[3]), (unsigned)(LDS_TB + (g * 16 + (i ^ (8 * (g >> 1)))) * 8), (unsigned)(LDS_TB + (p * 16 + ((lane >> 2) ^ (8 * (p >> 1)))) * 8), 0u};
}
DEVI int sel(u64 mask, int a, int b) { int r; asm("v_cndmask_b32_e64 %0, %2, %1, %3" : "=v"(r) : "v"(a), "v"(b), "s"(mask)); return r; }

DEVI void s1_attention(lptr shm, int w, int p_, int j_, char* ws) {
  arrive(3);
  const int lane = lane_id();
  const int tau = (w + 1) * 512, taup = w >= 2 ? tau - 1024 : 0, flip = w == 1 ? 16 : 0;
  const LAS u32x4* t = (const LAS u32x4*)(shm + LDS_TAB) + lane;
  const u32x4 t0 = t[0], t2 = t[128];
  u32x4 t1 = t[64];
  if (p_ == 0 && w < 2) { t1[0] = 0u; t1[2] = 0xf149f2cau; t1[3] = 0xf149f2cau; }
  const f32x4 mO = __builtin_bit_cast(f32x4, t0), mP = __builtin_bit_cast(f32x4, t1);
  const unsigned M1 = t1[0], M2 = t1[1], M3 = t2[0];
  const int po = t2[1] + tau, pp = (t2[1] ^ flip) + taup, to = t2[2] + tau, tp = (t2[2] ^ flip) + taup;
  int aq[4], avo[4], avp[4];
#pragma unroll
  for (int h = 0; h < 4; h++) {
    aq[h] = sel(0xffffull << (16 * h), po, LDS_ZERO - 4 * 4608);
    avo[h] = sel(0x1111111111111111ull << h, to, LDS_ZERO - 14 * 4608);
    avp[h] = sel(0x1111111111111111ull << h, tp, LDS_ZERO - 14 * 4608);
    asm volatile("" : "+v"(aq[h]), "+v"(avo[h]), "+v"(avp[h]));
  }
  await<0, 8>(3);
  u32x2 qa[4][2], ko[4][2], kp[4][2], va[4][2], vb[4][2];
  f32x4 sp[4], so[4];
  auto rd = [&](auto H) {
    constexpr int h = decltype(H)::value;
    ds64<(5 + h) * 4608>(ko[h][0], po); ds64<9 * 4608>(ko[h][1], po); ds64<h * 4608>(qa[h][0], po); ds64<4 * 4608>(qa[h][1], aq[h]);
    ds64<(5 + h) * 4608>(kp[h][0], pp); ds64<9 * 4608>(kp[h][1], pp);
  };
  auto rv = [&](auto H) {
    constexpr int h = decltype(H)::value;
    dstr<(10 + h) * 4608>(va[h][0], tp); dstr<(10 + h) * 4608>(va[h][1], to); dstr<14 * 4608>(vb[h][0], avp[h]); dstr<14 * 4608>(vb[h][1], avo[h]);
  };
  auto qk = [&](auto H, auto N) {
    constexpr int h = decltype(H)::value;
    asm volatile("s_waitcnt lgkmcnt(%6)" : "+v"(ko[h][0]), "+v"(ko[h][1]), "+v"(qa[h][0]), "+v"(qa[h][1]), "+v"(kp[h][0]), "+v"(kp[h][1]) : "i"(decltype(N)::value));
    so[h] = mfma(cat(ko[h][0], ko[h][1]), cat(qa[h][0], qa[h][1]), mO);
    sp[h] = mfma(cat(kp[h][0], kp[h][1]), cat(qa[h][0], qa[h][1]), mP);
  };
  rd(IC(0)); rd(IC(1));
  qk(IC(0), IC(6)); rd(IC(2));
  qk(IC(1), IC(6)); rd(IC(3));
  qk(IC(2), IC(6)); rv(IC(0)); rv(IC(1));
  qk(IC(3), IC(8)); rv(IC(2));
  asm volatile("s_waitcnt lgkmcnt(8)");
  rv(IC(3));
  const float qs = 0.22360679774997896f * 1.4426950408889634f;
  float ee[4], eo[4], mx[4], l[4];
#pragma unroll
  for (int h = 0; h < 4; h++) { ee[h] = fmaxf(fmaxf(sp[h][2], so[h][0]), so[h][2]); eo[h] = fmaxf(fmaxf(sp[h][3], so[h][1]), so[h][3]); mx[h] = fmaxf(ee[h], eo[h]); }
  bcast4(red4<true>(mx) * -qs, mx);
#pragma unroll
  for (int h = 0; h < 4; h++) {
    f32x2 t = f32x2{ee[h], eo[h]} * qs + mx[h];
    ee[h] = __builtin_amdgcn_exp2f(t[0]); eo[h] = __builtin_amdgcn_exp2f(t[1]); l[h] = ee[h] + eo[h];
  }
  bcast4(__builtin_amdgcn_rcpf(red4<false>(l)), l);
  asm volatile("s_waitcnt lgkmcnt(0)" : "+v"(va[0][0]), "+v"(va[0][1]), "+v"(va[1][0]), "+v"(va[1][1]), "+v"(va[2][0]), "+v"(va[2][1]), "+v"(va[3][0]), "+v"(va[3][1]),
               "+v"(vb[0][0]), "+v"(vb[0][1]), "+v"(vb[1][0]), "+v"(vb[1][1]), "+v"(vb[2][0]), "+v"(vb[2][1]), "+v"(vb[3][0]), "+v"(vb[3][1]));
  f32x4 o4 = {0, 0, 0, 0}, oh[4];
#pragma unroll
  for (int h = 0; h < 4; h++) {
    const unsigned pk = pk2(f32x2{ee[h], eo[h]} * l[h]);
    u32x4 pf = {0u, pk & M1, pk & M2, pk & M3};
    o4 = mfma(cat(vb[h][0], vb[h][1]), pf, o4);
    oh[h] = mfma(cat(va[h][0], va[h][1]), pf, f32x4{0, 0, 0, 0});
  }
  const u32x4 ra = mkr(ws + WS_A2, A2_BYTES);
  const int base = ((8 * p_ + w) * KS_T + 54 + 5 * (j_ >> 1)) * 1024;
#pragma unroll
  for (int hp = 0; hp < 2; hp++)
    st_wt(ra, lane * 16, base + ((j_ & 1) * 3 + hp) * 1024,
          u32x4{pk2(oh[2 * hp][0], oh[2 * hp][1]), pk2(oh[2 * hp][2], oh[2 * hp][3]), pk2(oh[2 * hp + 1][0], oh[2 * hp + 1][1]), pk2(oh[2 * hp + 1][2], oh[2 * hp + 1][3])});
  unsigned a0 = pk2(o4[0], o4[1]), a1 = pk2(o4[2], o4[3]);
  auto s0 = __builtin_amdgcn_permlane32_swap(a0, a0, false, false);
  auto s1 = __builtin_amdgcn_permlane32_swap(a1, a1, false, false);
  st_wt(ra, lane < 32 ? lane * 16 : 0x70000000, base + 2 * 1024 + (j_ & 1) * 512, u32x4{s0[0], s1[0], s0[1], s1[1]});
  arrive(4);
}

DEVI int s2_perm(int s, int rot) { return s < 8 ? s ^ rot : (s < 10 || s >= 18) ? s ^ (rot & 1) : ((s - 10) ^ rot) + 10; }
DEVI int fin_off(int G0, int n_, int r0, int lane) {
  const int frow = r0 + (lane >> 3), fG = G0 + (frow >> 4);
  return (32 * (fG >> 1) + 2 * sig(frow & 15) + (fG & 1)) * 1280 + n_ * 128 + (lane & 7) * 16;
}
constexpr int s2_slot(int r) { return r < 18 ? (r % 9) * 16384 : 147456 + (r - 18) * 4096; }
DEVI void watch(char* ws, int Ga, int Gb, int n_, int kind, int word, u64 fv) {
  const int lane = lane_id();
  GAS u64* fp = (GAS u64*)(ws + WS_BOX) + ((((lane & 8) ? Gb : Ga) >> 3) * 10 + n_) * 32 + 8 * kind + (lane & 7);
#pragma nounroll
  for (unsigned spins = 0; spins < SPIN_CAP; spins++) {
    if (__all(__hip_atomic_load(fp, __ATOMIC_RELAXED, __HIP_MEMORY_SCOPE_AGENT) == fv)) break;
    __builtin_amdgcn_s_sleep(1);
  }
  post(LDS_CNT + 4 * word, 1);
}

template <int NS, int MT>
DEVI void s2_compute(int w, int kq, int G0, int n_, int rot, char* ws, u64 fv) {
  constexpr int AD = (MT == 3 ? 3 : 4) + (NS == 20);
  const int mh = w >> 2, lane = lane_id();
  const u32x4 ra = mkr(ws + WS_A2, A2_BYTES);
  int av = lane * 16;
  pin(av);
  const int ao = ((G0 + MT * mh) * KS_T + (NS == 20 ? 54 : 18 * (kq - 1))) * 1024;
  int bs[4];
#pragma unroll
  for (int c = 0; c < 4; c++) bs[c] = kq * 4096 + (lane >> 2) * 128 + ((c ^ ((lane >> 3) & 3)) << 5) + 8 * (lane & 3);
  f32x4 acc[MT][4];
#pragma unroll
  for (int m = 0; m < MT; m++)
#pragma unroll
    for (int c = 0; c < 4; c++) { acc[m][c] = f32x4{0, 0, 0, 0}; pin(acc[m][c]); }
  u32x4 a[AD][MT];
  u32x2 b[3][4][2];
  int ca = LDS_CONS + kq * 8 + mh * 4, pa = LDS_PROD + kq * 4;
  pin(ca); pin(pa);
  unsigned pv, lv;
  auto rdb = [&](auto R, auto K) {
    constexpr int r = decltype(R)::value, k = decltype(K)::value;
    constexpr int off = s2_slot(r), nxt = s2_slot(r + 1) / 65536 - off / 65536;
    if constexpr (r < 3) dstr<off % 65536 + (k & 1) * 2048>(b[r % 3][k >> 1][k & 1], bs[k >> 1]);
    else redstr<off % 65536 + (k & 1) * 2048>(b[r % 3][k >> 1][k & 1], bs[k >> 1]);
    if constexpr (nxt != 0 && (k & 1)) vinc(bs[k >> 1], nxt * 65536);
  };
  auto lda = [&](auto S, int m) {
    constexpr int s = decltype(S)::value;
    const int o = ao + m * KS_T * 1024 + (s2_perm(s, rot) << 10);
    if constexpr (s < AD) bload_sc1(a[s % AD][m], ra, av, o); else reload_sc1(a[s % AD][m], ra, av, o);
  };
  auto ldb = [&]() {
    peek(pv, pa); wait_lg<0>(); require(pv, pa, 1);
    sfor<3>([&](auto R) { sfor<8>([&](auto K) { rdb(R, K); }); wait_lg<(decltype(R)::value < 2 ? 4 : 0)>(); });
    post(ca, 1);
  };
  constexpr bool BFIRST = NS == 20 || MT == 3;
  if (BFIRST) ldb();
  if (NS == 20) watch(ws, G0, G0 + 2 * MT - 1, n_, w == 0 ? 0 : 2, w == 0 ? 14 : 5, fv);
  await<0, 1>(NS == 20 ? 14 : 15);
  sfor<AD - 1>([&](auto S) {
#pragma unroll
    for (int m = 0; m < MT; m++) lda(S, m);
  });
  if (!BFIRST) ldb();
  sfor<NS - 3>([&](auto S) {
    constexpr int s = decltype(S)::value;
    constexpr bool LA = s + AD - 1 < NS;
    constexpr int later = NS - 1 - s < AD - 2 ? NS - 1 - s : AD - 2;
    if constexpr (s == 3) require(pv, pa, 2);
    if constexpr (s == 7) require(pv, pa, 3);
    if constexpr (s == 10) require(pv, pa, 4);
    if constexpr (s == 13) require(pv, pa, 5);
    if constexpr (s == 2 || s == 6 || s == 9 || s == 12) peek(pv, pa);
    if constexpr (NS == 18 && s == 11 - AD) require(lv, LDS_CNT + 4 * 5, 1);
    if constexpr (NS == 18 && s == 10 - AD) peek(lv, LDS_CNT + 4 * 5);
    if constexpr (s == NS - 4) peek(lv, ca ^ 4);
    sfor<MT>([&](auto M) {
      constexpr int m = decltype(M)::value;
      if constexpr (LA) lda(IC(s + AD - 1), m);
      wait_vm<MT * later + MT - 1 - m + (LA ? m + 1 : 0)>();
      if constexpr (m == 1 && s >= 2) wait_lg<4>();
      sfor<4>([&](auto C) {
        constexpr int c = decltype(C)::value;
        mfma_a(acc[m][c], cat(b[s % 3][c][0], b[s % 3][c][1]), a[s % AD][m]);
        if constexpr (s >= 1 && m < 2) rdb(IC(s + 2), IC(4 * m + c));
      });
    });
    if constexpr (s == 3 || s == 6 || s == 9 || s == 12) post(ca, s);
  });
  wait_lg<4>();
  sfor<8>([&](auto K) { rdb(IC(NS - 1), K); });
  if constexpr (AD == 3) {
#pragma unroll
    for (int m = 0; m < MT; m++) lda(IC(NS - 1), m);
  }
  int da[4];
#pragma unroll
  for (int c = 0; c < 4; c++) da[c] = mh * 49152 + kq * 4096 + (av & 0xf0) * 16 + ((((4 * c) << 4) + (av >> 8 << 4)) ^ (av & 0x70));
  require(lv, ca ^ 4, mh ? 12 : 9);
  sfor<MT>([&](auto M) {
    constexpr int m = decltype(M)::value;
    sfor<3>([&](auto U) {
      constexpr int u = decltype(U)::value, s = NS - 3 + u;
      if constexpr (m == 0) wait_vm<MT * (2 - u) + MT - 1>();
      if constexpr (m > 0 && u == 2) wait_vm<MT - 1 - m>();
      if constexpr (m == 0 && u == 1) wait_lg<8>();
      if constexpr (m == 0 && u == 2) wait_lg<0>();
      sfor<4>([&](auto C) {
        constexpr int c = decltype(C)::value;
        mfma_a(acc[m][c], cat(b[s % 3][c][0], b[s % 3][c][1]), a[s % AD][m]);
        if constexpr (m > 0 && u > 0 && (c & 1)) dsw128<(m - 1) * 16384>(da[2 * (u - 1) + (c >> 1)], __builtin_bit_cast(u32x4, acc[m - 1][2 * (u - 1) + (c >> 1)]));
      });
    });
  });
  asm volatile("s_nop 4");
#pragma unroll
  for (int c = 0; c < 4; c++) dsw128<(MT - 1) * 16384>(da[c], __builtin_bit_cast(u32x4, acc[MT - 1][c]));
  arrive((NS == 20 ? 28 : 12) + mh);
}

typedef __attribute__((ext_vector_type(8))) u64 u64x8;
__global__ __launch_bounds__(768, 3) void fused_block(const void*, const void*, const void*, const void*, const void*, const void*, const void*,
                                                      void*, void*) {
  u64x8 ka; u64 ka8, pc;
  asm volatile("s_load_dwordx16 %0, %3, 0x0\n\ts_load_dwordx2 %1, %3, 0x40\n\ts_getpc_b64 %2"
               : "=&s"(ka), "=&s"(ka8), "=&s"(pc) : "s"(__builtin_amdgcn_kernarg_segment_ptr()));
  extern __shared__ __attribute__((aligned(16))) char shm_[];
  lptr shm = (lptr)shm_;
  const int tid = threadIdx.x, lane = tid & 63, w = __builtin_amdgcn_readfirstlane(tid >> 6);
  const int xcd = blockIdx.x & 7, idx = blockIdx.x >> 3;
  const int p_ = (xcd & 1) * 8 + (idx & 7), sub = idx >> 3;
  const bool att = !(sub & 1);
  const int j_ = (xcd >> 1) * 2 + (sub >> 1);
  const int uidx = (idx & 7) + 8 * (idx >> 4);
  const int n_ = (xcd & 1) * 5 + (int)(((att ? 0x4444433333221100ull : 0x4433222211110000ull) >> (4 * uidx)) & 15);
  const int G0 = 32 * (xcd >> 1) + 2 * (int)(((att ? 0xeca86eca86ecececull : 0x3030963096309630ull) >> (4 * uidx)) & 15);
  const int rot = (int)(((att ? 0x6547610321322110ull : 0x5403103203213210ull) >> (4 * uidx)) & 15);
  const int kq = (w + ((w >> 2) & 1)) & 3;
  if (w == 7) {
    touch(mkr((const void*)(pc & ~255ull), CODE_BYTES), (idx + 32 * lane) * 128);
  }
  const int hf = idx & 1, tx = ((idx >> 1) ^ sub) & 1;
  const int rid = (att ? 0 : 8) + j_;
  if (w >= 8) __builtin_amdgcn_s_setprio(3);
  const int lw = w & 3;
  const int RS = att ? 3840 : 3456;
  const int r8 = lane >> 3, cp = lane & 7, t0 = p_ * 128;
  int xo[2], hv, wv;
#pragma unroll
  for (int e = 0; e < 2; e++)
    xo[e] = (t0 + 32 * lw + 2 * (4 * (r8 & 3) + 2 * e + (r8 >> 2))) * 1280 + ((cp ^ (4 * e + (r8 >> 1))) << 4);
  {
    const int hi = lane >= 30, ch = lane - 30 * hi, lim = att ? 63 : j_ < 7 ? 27 : 19;
    wv = hi * RS + ((ch >= 10) + (ch >= 20)) * (att ? 1120 : 0) + j_ * (att ? 160 : 448) + min(ch, lim) * 16;
    const int tok = max(t0 - 16 + 8 * (r8 & 1) + 2 * lw + ((r8 & 3) < 2), 0);
    hv = (tok * 1280 + ((cp ^ ((2 * lw + ((r8 & 3) >> 1)) & 7)) << 4)) | (att ? 0 : 0x70000000);
  }
  pin(xo[0]); pin(xo[1]); pin(wv); pin(hv);
  asm volatile("s_waitcnt lgkmcnt(0)" : "+s"(ka), "+s"(ka8));
  const char *x = (const char*)ka[0], *wqkv = (const char*)ka[1], *wo = (const char*)ka[2], *w1 = (const char*)ka[3], *b1 = (const char*)ka[4],
             *w2 = (const char*)ka[5], *b2 = (const char*)ka[6];
  char *out = (char*)ka[7], *ws = (char*)ka8;
  int fu;
  if (__builtin_expect(w >= 8, 1)) {
    auto ktile = [&](int T) { return T ^ tx; };
    const u32x4 rx = mkr(x, S_ * D_ * 2), rw1 = mkr(att ? wqkv : w1, D_ * RS);
    const int wrow = (16 * (lw & 1) + 4 * (lw >> 1)) * RS;
    const u64 hmask = lw == 0 ? 0xffffff00ull : 0xffffffffull;
    auto ldx = [&](int T) { const int o = ktile(T) * 128; dma4<1024>(rx, ~0ull, (T % 3) * SLOT + XOFF + 2048 + 4 * lw * 1024, xo[0], xo[1], o, o, o + 1280, o + 1280); };
    auto ldw = [&](int T, int h) {
      const int so = (64 * ktile(T) + 32 * (h ^ hf)) * RS + wrow;
      dma4<960>(rw1, 0x0fffffffffffffffull, (T % 3) * SLOT + h * WSLAB + 4 * lw * 960, wv, wv, so, so + 2 * RS, so + 8 * RS, so + 10 * RS);
    };
    auto ldh = [&](int T) { dma1(rx, hmask, (T % 3) * SLOT + XOFF + lw * 512, hv, ktile(T) * 128); };
    const int attf = att;
    ldx(0); ldw(0, 0); ldh(0); ldw(0, 1);
    bar_vm<4>();
    ldh(1); ldx(1);
    bar_vm1<4>(attf);
    ldw(1, 0); ldw(1, 1);
    ldh(2); ldx(2);
    bar_vm1<4>(attf);
    const int srow = p_ * 128 + 8 * rid + 2 * lw + (lane >> 5);
    u32x2 xr[5];
    unsigned ep, b1v;
    setup_loads(xr, ep, b1v, rx, srow * 1280 + (lane & 31) * 8, mkr(ws, 4), mkr(b1, F_ * 2), (j_ * 224 + 64 * lw + lane) * 2);
    touch_sc1(mkr(ws + WS_BOX, lw == 0 ? 160 * 256 : 0), (((G0 >> 3) + (lane & 1)) * 10 + n_) * 256 + (lane & 2) * 64);
    ldw(2, 0); ldw(2, 1); ldh(3); ldx(3); ldw(3, 0);
    bar_vm1<8>(attf);
    pin(ep); pin(b1v);
#pragma unroll
    for (int k = 0; k < 5; k++) pin(xr[k]);
    const unsigned tag = __builtin_amdgcn_readfirstlane(ep) + 1u;
    ldw(3, 1); ldh(4); ldx(4); ldw(4, 0); ldw(4, 1);
    {
      float s = 0;
#pragma unroll
      for (int k = 0; k < 5; k++) s += (blo(xr[k][0]) + bhi(xr[k][0])) + (blo(xr[k][1]) + bhi(xr[k][1]));
      const float m = half_sum(s) * (1.f / D_);
      float q = 0;
#pragma unroll
      for (int k = 0; k < 10; k++) { const unsigned u = xr[k >> 1][k & 1]; const float a = blo(u) - m, b = bhi(u) - m; q = __builtin_fmaf(a, a, q); q = __builtin_fmaf(b, b, q); }
      const float rs = __builtin_amdgcn_rsqf(half_sum(q) * (1.f / D_) + 1e-5f);
      st_wt(mkr(ws + WS_STAT, S_ * 16), (lane & 31) == 0 ? srow * 16 : 0x70000000, 0, u32x4{asu(rs), tag, asu(-m * rs), tag});
      *(LAS float*)(shm + LDS_B1 + (64 * lw + lane) * 4) = blo(b1v);
      *(LAS unsigned*)(shm + LDS_CNT + 4) = tag;
    }
    bar_vm1<13>(attf);
    const u32x4 rw = mkr(kq == 0 ? wo : w2, kq == 0 ? D_ * 1280 : F_ * 1280);
    const int cbyte = n_ * 128 + ((cp ^ ((lane >> 4) << 1)) << 4);
    const int v1 = r8 * 1280 + cbyte, v2 = kq == 0 ? (20 * (r8 >> 2) + (r8 & 3)) * 1280 + cbyte : v1;
    int gtab;
    {
      const int so = s2_perm(lane, rot), P = (so * 13) >> 6, kp = so - 5 * P;
      const int r0 = 160 * P + (kp == 2 ? 16 : 40 * (kp - (kp > 2)));
      const int t = 3 * so + kq - 1, u = t - 32;
      const int r1 = 32 * (t < 32 ? 7 * (t >> 2) + (t & 3) : 4 * ((u * 43) >> 7) + 4 + u);
      gtab = kq == 0 ? r0 * 1280 + (kp == 2) : r1 * 1280;
      pin(gtab);
    }
    const int sbn = 1280 * (kq == 0 ? 20 : 16);
    auto geom = [&](int s, int& rb, int& sa, int& sb, int& v) {
      const int e = __builtin_amdgcn_readlane(gtab, s), f = e & 1;
      v = v1 + ((v2 - v1) & -f);
      rb = e - f;
      sa = f ? 1280 * 80 : 1280 * 8;
      sb = f ? 1280 * 40 : sbn;
    };
    u32x4 pre[9][4];
    auto ldr = [&](auto R) {
      constexpr int r = decltype(R)::value;
      int rb, sa, sb, v; geom(9 + r, rb, sa, sb, v);
      bload(pre[r][0], rw, v, rb); bload(pre[r][1], rw, v, rb + sa); bload(pre[r][2], rw, v, rb + sb); bload(pre[r][3], rw, v, rb + sa + sb);
    };
    auto ldl = [&](auto S) {
      constexpr int s = decltype(S)::value;
      int rb, sa, sb, v; geom(s, rb, sa, sb, v);
      dma4<1024>(rw, ~0ull, s < 9 ? s * 16384 + kq * 4096 : 147456 + (s - 18) * 4096, v, v, rb, rb + sa, rb + sb, rb + sa + sb);
    };
    const u32x4 rst = mkr(ws + WS_STAT, S_ * 16);
    const int spos = 36 * lw + (lane < 36 ? lane : 35), sv = row_token(p_, spos) * 16;
    u32x4 sg;
    sfor<5>([&](auto TT) {
      constexpr int T = decltype(TT)::value + 5;
      if (T == 9) bload_sc1(sg, rst, sv, 0);
      ldh(T); ldx(T); ldw(T, 0); ldw(T, 1);
      if (T == 9 && !att) {
        const int wv2 = ((blockIdx.x >> 7) * 64 + (blockIdx.x & 63)) * 4 + lw;
        touch(mkr(wv2 < 360 ? w2 : wo, wv2 < 360 ? F_ * 1280 : D_ * 1280), min((wv2 < 360 ? wv2 : wv2 - 360) * 48 + min(lane, 47), wv2 < 360 ? F_ * 10 - 1 : D_ * 10 - 1) * 128);
      }
      if (T < 9) bar_vm1<12>(attf); else bar_vm<13>();
    });
    asm volatile("" : "+v"(sg));
#pragma nounroll
    for (unsigned spins = 0; !__all(sg[1] == tag && sg[3] == tag) && spins < SPIN_CAP; spins++) {
      __builtin_amdgcn_s_sleep(2);
      bload_sc1(sg, rst, sv, 0);
      asm volatile("s_waitcnt vmcnt(0)" : "+v"(sg) :: "memory");
    }
    *(LAS u32x2*)(shm + LDS_ST + spos * 8) = u32x2{sg[0], sg[2]};
    ldl(IC(4));
    if (att) wait_vm<4>(); else wait_vm<5>();
    bar_lgkm();
    __builtin_amdgcn_s_setprio(0);
    const int pa = LDS_PROD + kq * 4, wa = kq * 4096 + lane * 16;
    if (!att) { ldl(IC(5)); ldl(IC(6)); ldl(IC(7)); }
    ldr(IC(1)); ldr(IC(2)); ldr(IC(3)); ldr(IC(4));
    ldl(IC(3));
    await<1, 8>(2);
    ldl(IC(2)); ldl(IC(1)); ldl(IC(0));
    if (!att) { ldl(IC(8)); if (kq == 0) { ldl(IC(18)); ldl(IC(19)); } }
    ldr(IC(0)); wait_vm<4>();
    post(pa, att ? 1 : 2);
    if (!att) __builtin_amdgcn_s_setprio(3);
    ldr(IC(5)); ldr(IC(6)); ldr(IC(7)); ldr(IC(8));
    if (att) {
      await<1, 8>(4);
      __builtin_amdgcn_s_setprio(3);
      ldl(IC(5)); ldl(IC(6)); ldl(IC(7)); ldl(IC(8));
      if (kq == 0) { ldl(IC(18)); ldl(IC(19)); }
    }
    if (lw == 3) touch(mkr(b2, D_ * 2), n_ * 128);
    wait_vm<0>();
    post(pa, 2);
#define PUT(r, off) dsw128<off>(wa + (r / 3) * 49152, pre[r][0]); dsw128<off + 1024>(wa + (r / 3) * 49152, pre[r][1]); dsw128<off + 2048>(wa + (r / 3) * 49152, pre[r][2]); dsw128<off + 3072>(wa + (r / 3) * 49152, pre[r][3]);
    await_pair(LDS_CONS + kq * 8, 1);
    PUT(0, 0) PUT(1, 16384) PUT(2, 32768)
    post(pa, 3);
    __builtin_amdgcn_s_setprio(0);
    await_pair(LDS_CONS + kq * 8, 3);
    PUT(3, 0) PUT(4, 16384) PUT(5, 32768)
    post(pa, 4);
    await_pair(LDS_CONS + kq * 8, 6);
    PUT(6, 0) PUT(7, 16384) PUT(8, 32768)
    post(pa, 5);
    fu = lw;
  } else {
    if (tid < 24) *(LAS u32x4*)(shm + (tid >> 3) * SLOT + XOFF + (tid & 7) * 16) = u32x4{0x3f803f80u, 0x3f803f80u, 0x3f803f80u, 0x3f803f80u};
    if (tid >= 448 && tid < 480) *(LAS unsigned*)(shm + LDS_CNT + 4 * (tid - 448)) = 0u;
    if (w == 5 && att) att_table(shm, lane);
    const bool light = att ? (w >= 6) : (w >= 4);
    if (light) s1_compute<3>(shm, att, w, lane, p_, j_, hf, ws);
    else s1_compute<4>(shm, att, w, lane, p_, j_, hf, ws);
    if (att) s1_attention(shm, w, p_, j_, ws);
    const unsigned tag = __builtin_amdgcn_readfirstlane(*(LAS unsigned*)(shm + LDS_CNT + 4));
    GAS u64* flags = (GAS u64*)(ws + WS_FLAG);
    const u64 fv = flagval(tag);
    wait_vm<0>();
    {
      const int lane = lane_id(), kind = att ? 0 : 1 + (w >> 2);
      unsigned old = 0;
      if (lane == 0) old = __hip_atomic_fetch_add((LAS unsigned*)(shm + LDS_CNT + (kind == 1 ? 120 : 0)), 1u, __ATOMIC_RELAXED, __HIP_MEMORY_SCOPE_WORKGROUP);
      if (__builtin_amdgcn_readfirstlane(old) == (att ? 7u : 3u)) {
        GAS u64* dst = lane == 10 ? flags + p_ * 16 + rid : (GAS u64*)(ws + WS_BOX) + (p_ * 10 + lane) * 32 + 8 * kind + j_;
        if (lane <= 10) __hip_atomic_store(dst, fv, __ATOMIC_RELAXED, __HIP_MEMORY_SCOPE_AGENT);
      }
    }
    if (w == 0) watch(ws, G0, G0 + (att ? 3 : 5), n_, 1, 15, fv);
    if (kq == 0) {
      if (att) s2_compute<20, 2>(w, kq, G0, n_, rot, ws, fv);
      else s2_compute<20, 3>(w, kq, G0, n_, rot, ws, fv);
    } else {
      if (att) s2_compute<18, 2>(w, kq, G0, n_, rot, ws, fv);
      else s2_compute<18, 3>(w, kq, G0, n_, rot, ws, fv);
    }
    if (blockIdx.x == 0 && w == 3) {
      const int lane = lane_id();
#pragma nounroll
      for (unsigned spins = 0; spins < SPIN_CAP; spins++) {
        bool ok = true;
#pragma unroll
        for (int k = 0; k < 4; k++) ok &= __hip_atomic_load(flags + lane + 64 * k, __ATOMIC_RELAXED, __HIP_MEMORY_SCOPE_AGENT) == fv;
        if (__all(ok)) break;
        __builtin_amdgcn_s_sleep(8);
      }
      if (lane == 0) __hip_atomic_store((GAS unsigned*)ws, tag, __ATOMIC_RELAXED, __HIP_MEMORY_SCOPE_AGENT);
    }
    if (att || w == 0 || w > 2) return;
    fu = w + 3;
  }
  {
    const int lane = lane_id();
    const u32x4 bv = *(const GAS u32x4*)(b2 + n_ * 128 + (lane & 7) * 16);
    int foff[2], pr0[2];
    u32x4 xv[2];
    f32x2 gt[2][4];
#pragma unroll
    for (int hh = 0; hh < 2; hh++) {
      const int frow = 48 * hh + 8 * fu + (lane >> 3);
      foff[hh] = fin_off(G0 + (att ? 2 : 3) * hh, n_, 8 * fu, lane);
      xv[hh] = *(const GAS u32x4*)((const char*)x + foff[hh]);
      pr0[hh] = (frow >> 4) * 16384 + (frow & 15) * 256 + (((2 * (lane & 7)) ^ (frow & 7)) << 4);
      pin(pr0[hh]);
    }
#pragma unroll
    for (int hh = 0; hh < 2; hh++) {
      await<1, 3>(12 + hh);
      f32x4 y[2];
#pragma unroll
      for (int e = 0; e < 2; e++) {
        lptr pr = shm + (pr0[hh] ^ (16 * e));
        y[e] = *(LAS f32x4*)(pr + 4096) + *(LAS f32x4*)(pr + 8192) + *(LAS f32x4*)(pr + 12288);
      }
#pragma unroll
      for (int k = 0; k < 4; k++) {
        f32x2 yy = f32x2{y[k >> 1][2 * (k & 1)], y[k >> 1][2 * (k & 1) + 1]} + f32x2{blo(bv[k]), bhi(bv[k])};
        f32x2 t = yy * -1.4426950408889634f;
        f32x2 e = f32x2{__builtin_amdgcn_exp2f(t[0]), __builtin_amdgcn_exp2f(t[1])} + 1.f;
        gt[hh][k] = yy * f32x2{__builtin_amdgcn_rcpf(e[0]), __builtin_amdgcn_rcpf(e[1])};
        pin(gt[hh][k]);
      }
    }
#pragma unroll
    for (int hh = 0; hh < 2; hh++) {
      await<0, 1>(28 + hh);
      u32x4 o;
#pragma unroll
      for (int e = 0; e < 2; e++) {
        const f32x4 s = *(LAS f32x4*)(shm + (pr0[hh] ^ (16 * e)));
        o[2 * e] = pk2(f32x2{s[0], s[1]} * gt[hh][2 * e] + f32x2{blo(xv[hh][2 * e]), bhi(xv[hh][2 * e])});
        o[2 * e + 1] = pk2(f32x2{s[2], s[3]} * gt[hh][2 * e + 1] + f32x2{blo(xv[hh][2 * e + 1]), bhi(xv[hh][2 * e + 1])});
      }
      __builtin_nontemporal_store(o, (GAS u32x4*)(out + foff[hh]));
    }
  }
}

extern "C" void kernel_launch(void* const* d_in, const int* in_sizes, int n_in, void* d_out, int out_size, void* d_ws,
                              size_t ws_size, hipStream_t stream) {
  (void)hipFuncSetAttribute((const void*)fused_block, hipFuncAttributeMaxDynamicSharedMemorySize, LDS_TOTAL);
  fused_block<<<256, 768, LDS_TOTAL, stream>>>(d_in[0], d_in[1], d_in[2], d_in[4], d_in[5], d_in[6],
                                               d_in[7], d_out, d_ws);
}
```

```cpp
#include <hip/hip_runtime.h>
#include <hip/hip_bf16.h>
#include <type_traits>
#include <utility>

typedef __attribute__((ext_vector_type(8))) __bf16 bf16x8;
typedef __attribute__((ext_vector_type(2))) __bf16 bf16x2;
typedef __attribute__((ext_vector_type(4))) float f32x4;
typedef __attribute__((ext_vector_type(2))) float f32x2;
typedef __attribute__((ext_vector_type(4))) unsigned u32x4;
typedef __attribute__((ext_vector_type(2))) unsigned u32x2;
#define LAS __attribute__((address_space(3)))
#define GAS __attribute__((address_space(1)))
#define DEVI __device__ __forceinline__
typedef LAS char* lptr;
typedef unsigned long long u64;
#define SB __builtin_amdgcn_sched_barrier(0)
#define IC(n) std::integral_constant<int, n>{}

constexpr int S_ = 2048, D_ = 640, F_ = 1728;
constexpr int KS_T = 74;
constexpr int WSLAB = 15360;
constexpr int XOFF = 2 * WSLAB;
constexpr int SLOT = 49152;
constexpr int LDS_TB = 86016;
constexpr int LDS_ST = 155648;
constexpr int LDS_CS = LDS_ST + 1152;
constexpr int LDS_B1 = LDS_CS + 1024;
constexpr int LDS_CNT = LDS_B1 + 1024;
constexpr int LDS_ZERO = LDS_CNT + 24;
constexpr int LDS_CONS = LDS_CNT + 64;
constexpr int LDS_PROD = LDS_CONS + 32;
constexpr int LDS_TAB = LDS_CNT + 128;
constexpr int LDS_TOTAL = 163840;
constexpr int WS_FLAG = 4096, WS_STAT = 8192, WS_BOX = 65536, WS_A2 = 1 << 20;
constexpr int A2_BYTES = 128 * KS_T * 1024;
constexpr int CODE_BYTES = 48896;
constexpr unsigned SPIN_CAP = 1u << 20;

#define STAMP(idx)
#define FINISH()
#define STAMPC(k)
#define STAMPA(k) STAMP(k)
#define STAMPB(k)
#define PR_ARGS
#define PR_PASS

DEVI void bar_lgkm() { asm volatile("s_waitcnt lgkmcnt(0)\n\ts_barrier" ::: "memory"); }
template <int N> DEVI void bar_vm() { asm volatile("s_waitcnt vmcnt(%0)\n\ts_barrier" ::"i"(N) : "memory"); }
template <int N> DEVI void wait_vm() { asm volatile("s_waitcnt vmcnt(%0)" ::"i"(N) : "memory"); }
DEVI unsigned pk2(float a, float b) { f32x2 v = {a, b}; return __builtin_bit_cast(unsigned, __builtin_convertvector(v, bf16x2)); }
DEVI unsigned pk2(f32x2 v) { return __builtin_bit_cast(unsigned, __builtin_convertvector(v, bf16x2)); }
DEVI float asf(unsigned u) { return __builtin_bit_cast(float, u); }
DEVI unsigned asu(float f) { return __builtin_bit_cast(unsigned, f); }
DEVI float blo(unsigned u) { return __builtin_bit_cast(float, u << 16); }
DEVI float bhi(unsigned u) { return __builtin_bit_cast(float, u & 0xffff0000u); }
DEVI f32x4 mfma(u32x4 a, u32x4 b, f32x4 c) {
  return __builtin_amdgcn_mfma_f32_16x16x32_bf16(__builtin_bit_cast(bf16x8, a), __builtin_bit_cast(bf16x8, b), c, 0, 0, 0);
}
DEVI u32x4 cat(u32x2 a, u32x2 b) { return __builtin_shufflevector(a, b, 0, 1, 2, 3); }
DEVI void mfma_a(f32x4& c, u32x4 a, u32x4 b) { asm volatile("v_mfma_f32_16x16x32_bf16 %0, %1, %2, %0" : "+v"(c) : "v"(a), "v"(b)); }
template <int OFF> DEVI void ds128(u32x4& d, int addr) { asm volatile("ds_read_b128 %0, %1 offset:%2" : "=v"(d) : "v"(addr), "i"(OFF)); }
template <int OFF> DEVI void dstr(u32x2& d, int addr) { asm volatile("ds_read_b64_tr_b16 %0, %1 offset:%2" : "=v"(d) : "v"(addr), "i"(OFF)); }
template <int OFF> DEVI void dsw128(int addr, u32x4 d) { asm volatile("ds_write_b128 %0, %1 offset:%2" ::"v"(addr), "v"(d), "i"(OFF) : "memory"); }
DEVI u32x4 mkr(const void* p, unsigned bytes) {
  u64 a = (u64)p;
  return u32x4{(unsigned)a, (unsigned)(a >> 32) & 0xffffu, bytes, 0x00020000u};
}
DEVI void st_wt(u32x4 r, int voff, int soff, u32x4 v) {
  asm volatile("buffer_store_dwordx4 %0, %1, %2, %3 offen sc1\n\ts_nop 1" ::"v"(v), "v"(voff), "s"(r), "s"(soff) : "memory");
}
template <int LSTEP>
DEVI void dma4(u32x4 r, u64 mask, int l, int va, int vb, int s0, int s1, int s2, int s3) {
  asm volatile(
      "s_mov_b64 exec, %0\n\t"
      "s_mov_b32 m0, %1\n\ts_nop 0\n\tbuffer_load_dwordx4 %2, %4, %5 offen lds\n\t"
      "s_add_u32 m0, %1, %9\n\ts_nop 0\n\tbuffer_load_dwordx4 %3, %4, %6 offen lds\n\t"
      "s_add_u32 m0, %1, %10\n\ts_nop 0\n\tbuffer_load_dwordx4 %2, %4, %7 offen lds\n\t"
      "s_add_u32 m0, %1, %11\n\ts_nop 0\n\tbuffer_load_dwordx4 %3, %4, %8 offen lds\n\t"
      "s_mov_b64 exec, -1"
      :: "s"(mask), "s"(l), "v"(va), "v"(vb), "s"(r), "s"(s0), "s"(s1), "s"(s2), "s"(s3), "i"(LSTEP), "i"(2 * LSTEP), "i"(3 * LSTEP)
      : "memory", "scc");
}
DEVI void dma1(u32x4 r, u64 mask, int l, int v, int s0) {
  asm volatile("s_mov_b64 exec, %0\n\ts_mov_b32 m0, %1\n\ts_nop 0\n\tbuffer_load_dwordx4 %2, %3, %4 offen lds\n\ts_mov_b64 exec, -1"
               :: "s"(mask), "s"(l), "v"(v), "s"(r), "s"(s0) : "memory");
}
DEVI void setup_loads(u32x2 (&xr)[5], unsigned& ep, unsigned& bv, u32x4 rx, int vx, u32x4 re, u32x4 rb, int vb) {
  asm volatile("buffer_load_dwordx2 %0, %7, %8, 0 offen\n\tbuffer_load_dwordx2 %1, %7, %8, 0 offen offset:256\n\tbuffer_load_dwordx2 %2, %7, %8, 0 offen offset:512\n\t"
               "buffer_load_dwordx2 %3, %7, %8, 0 offen offset:768\n\tbuffer_load_dwordx2 %4, %7, %8, 0 offen offset:1024\n\t"
               "buffer_load_dword %5, off, %9, 0 sc1\n\tbuffer_load_ushort %6, %11, %10, 0 offen"
               : "=&v"(xr[0]), "=&v"(xr[1]), "=&v"(xr[2]), "=&v"(xr[3]), "=&v"(xr[4]), "=&v"(ep), "=&v"(bv)
               : "v"(vx), "s"(rx), "s"(re), "s"(rb), "v"(vb) : "memory");
}
template <int N>
DEVI void bar_vm1(int flag) {
  asm volatile("s_waitcnt vmcnt(%2)\n\ts_cmp_lg_u32 %0, 0\n\ts_cbranch_scc1 .La%=\n\ts_waitcnt vmcnt(%1)\n.La%=:\n\ts_barrier" ::"s"(flag), "i"(N), "i"(N + 1) : "memory", "scc");
}
template <class T> DEVI void pin(T& v) { asm volatile("" : "+v"(v)); }
DEVI void dsw_col0(int a0, f32x4 d0, int a1, f32x4 d1, int a2, f32x4 d2) {
  asm volatile("s_mov_b64 exec, %6\n\tds_write_b128 %0, %1\n\tds_write_b128 %2, %3\n\tds_write_b128 %4, %5\n\ts_mov_b64 exec, -1"
               ::"v"(a0), "v"(d0), "v"(a1), "v"(d1), "v"(a2), "v"(d2), "s"(0x0001000100010001ull) : "memory");
}
DEVI void reload_sc1(u32x4& d, u32x4 r, int v, int s0) { asm volatile("buffer_load_dwordx4 %0, %1, %2, %3 offen sc1" : "+v"(d) : "v"(v), "s"(r), "s"(s0) : "memory"); }
template <int OFF> DEVI void redstr(u32x2& d, int addr) { asm volatile("ds_read_b64_tr_b16 %0, %1 offset:%2" : "+v"(d) : "v"(addr), "i"(OFF)); }
DEVI void vinc(int& d, int s) { asm volatile("v_add_u32 %0, %1, %0" : "+v"(d) : "s"(s)); }
template <class F, int... I> DEVI void sfor_(F f, std::integer_sequence<int, I...>) { (f(std::integral_constant<int, I>{}), ...); }
template <int N, class F> DEVI void sfor(F f) { sfor_(f, std::make_integer_sequence<int, N>{}); }
DEVI void touch_sc1(u32x4 r, int v) {
  asm volatile("s_mov_b32 m0, %2\n\ts_nop 0\n\tbuffer_load_dword %0, %1, 0 offen sc1 lds" ::"v"(v), "s"(r), "i"(LDS_TOTAL - 256) : "memory");
}
DEVI void touch(u32x4 r, int v) {
  asm volatile("s_mov_b32 m0, %2\n\ts_nop 0\n\tbuffer_load_dword %0, %1, 0 offen lds" ::"v"(v), "s"(r), "i"(LDS_TOTAL - 256) : "memory");
}
DEVI void bload(u32x4& d, u32x4 r, int v, int s0) { asm volatile("buffer_load_dwordx4 %0, %1, %2, %3 offen" : "=v"(d) : "v"(v), "s"(r), "s"(s0)); }
DEVI void bload_sc1(u32x4& d, u32x4 r, int v, int s0) { asm volatile("buffer_load_dwordx4 %0, %1, %2, %3 offen sc1" : "=v"(d) : "v"(v), "s"(r), "s"(s0) : "memory"); }
DEVI void arrive(int k) {
  asm volatile("s_mov_b64 exec, 1\n\tds_add_u32 %0, %1\n\ts_mov_b64 exec, -1" ::"v"(LDS_CNT + 4 * k), "v"(1) : "memory");
}
template <int SLEEP, unsigned N>
DEVI void await(int k) {
  unsigned v, t;
  if (SLEEP)
    asm volatile("s_branch .Ly%=\n.Lx%=:\n\ts_sleep 1\n.Ly%=:\n\tds_read_b32 %0, %2\n\ts_waitcnt lgkmcnt(0)\n\tv_readfirstlane_b32 %1, %0\n\ts_cmp_ge_u32 %1, %3\n\ts_cbranch_scc0 .Lx%="
                 : "=&v"(v), "=&s"(t) : "v"(LDS_CNT + 4 * k), "i"(N) : "memory", "scc");
  else
    asm volatile(".Lx%=:\n\tds_read_b32 %0, %2\n\ts_waitcnt lgkmcnt(0)\n\tv_readfirstlane_b32 %1, %0\n\ts_cmp_ge_u32 %1, %3\n\ts_cbranch_scc0 .Lx%="
                 : "=&v"(v), "=&s"(t) : "v"(LDS_CNT + 4 * k), "i"(N) : "memory", "scc");
}
DEVI void publish(int cnt, unsigned last, u32x4 r, int voff, u32x2 fv) {
  unsigned v, t;
  asm volatile("s_waitcnt vmcnt(0)\n\ts_mov_b64 exec, 1\n\tds_add_rtn_u32 %0, %2, %3\n\ts_waitcnt lgkmcnt(0)\n\tv_readfirstlane_b32 %1, %0\n\t"
               "s_cmp_lg_u32 %1, %4\n\ts_cbranch_scc1 .Lp%=\n\ts_mov_b64 exec, 0x7ff\n\tbuffer_store_dwordx2 %7, %6, %5, 0 offen sc1\n.Lp%=:\n\ts_mov_b64 exec, -1"
               : "=&v"(v), "=&s"(t) : "v"(cnt), "v"(1), "s"(last), "s"(r), "v"(voff), "v"(fv) : "memory", "scc");
}
DEVI void post(int addr, int val) { asm volatile("ds_write_b32 %0, %1" ::"v"(addr), "v"(val) : "memory"); }
DEVI void peek(unsigned& v, int addr) { asm volatile("ds_read_b32 %0, %1" : "=v"(v) : "v"(addr) : "memory"); }
DEVI void require(unsigned v, int addr, unsigned n) {
  unsigned t;
  asm volatile("v_readfirstlane_b32 %1, %0\n\ts_cmp_ge_u32 %1, %3\n\ts_cbranch_scc1 .Lq%=\n"
               ".Lw%=:\n\tds_read_b32 %0, %2\n\ts_waitcnt lgkmcnt(0)\n\tv_readfirstlane_b32 %1, %0\n\ts_cmp_ge_u32 %1, %3\n\ts_cbranch_scc0 .Lw%=\n.Lq%=:"
               : "+&v"(v), "=&s"(t) : "v"(addr), "s"(n) : "memory", "scc");
}
DEVI void await_pair(int addr, unsigned n) {
  for (;;) {
    u32x2 v;
    asm volatile("ds_read_b64 %0, %1\n\ts_waitcnt lgkmcnt(0)" : "=v"(v) : "v"(addr) : "memory");
    const unsigned a = v[0], b = v[1];
    if (__builtin_amdgcn_readfirstlane(a < b ? a : b) >= n) break;
    __builtin_amdgcn_s_sleep(1);
  }
}
DEVI int lane_id() { int l; asm volatile("v_mbcnt_lo_u32_b32 %0, -1, 0\n\tv_mbcnt_hi_u32_b32 %0, -1, %0" : "=v"(l)); return l; }
DEVI int sig(int i) { return 4 * (i & 3) + (i >> 2); }
DEVI u64 flagval(unsigned tag) { return ((u64)(tag ^ 0x5bd1e995u) << 32) | tag; }

DEVI float xadd16(float v) { auto r = __builtin_amdgcn_permlane16_swap(asu(v), asu(v), false, false); return asf(r[0]) + asf(r[1]); }
template <int CTRL> DEVI float dpp(float v) { return asf(__builtin_amdgcn_mov_dpp(asu(v), CTRL, 0xf, 0xf, true)); }
DEVI float half_sum(float v) {
  v += dpp<0xb1>(v); v += dpp<0x4e>(v); v += dpp<0x141>(v); v += dpp<0x140>(v);
  return xadd16(v);
}

DEVI int h_slot(int j, int l) { const int t = l < 4 ? 4 * j + l : 28 + 3 * j + l, d = (t * 43) >> 7; return 18 * (t - 3 * d) + d; }

DEVI int row_token(int p_, int pos) {
  const int tau = pos >> 4, i = pos & 15;
  int tok = tau == 0 ? p_ * 128 - 16 + 8 * (i & 1) + 2 * (i >> 2) + ((i & 3) < 2) : p_ * 128 + 32 * ((tau - 1) >> 1) + 2 * sig(i) + ((tau - 1) & 1);
  return tok < 0 ? 0 : tok;
}

struct ColGeo { int A, B, C, ct; };
DEVI ColGeo colgeo(bool att, int nw, int mw, int c, bool heavy) {
  const int cc = heavy ? (c ^ (2 * mw)) : c;
  const int ct = 4 * nw + cc, sec = (ct * 13) >> 6, hh = ct - 5 * sec;
  const bool q4 = hh == 4, sp = !heavy && c == 2;
  ColGeo r;
  r.A = att ? sec * 160 + (q4 ? 32 : 40 * hh) : heavy ? 128 * nw + 32 * cc : (sp ? 352 + 16 * nw : 128 + 64 * nw + 32 * c);
  r.B = att && q4 ? 40 : 8;
  r.C = att ? (q4 ? 80 : 16) : (sp ? 32 : 16);
  r.ct = ct;
  return r;
}

template <int NC>
struct Frags { u32x4 x[5]; u32x2 w[NC][2]; };

template <int N> DEVI void wait_lg() { asm volatile("s_waitcnt lgkmcnt(%0)" ::"i"(N) : "memory"); }

template <int NC, int K>
DEVI void s1_rd(Frags<NC>& n, int xo, int xh, const int (&wa)[NC]) {
  if constexpr (K == 0) ds128<0>(n.x[0], xo);
  else if constexpr (K <= 2 * NC) { if ((K - 1) & 1) dstr<7680>(n.w[(K - 1) >> 1][1], wa[(K - 1) >> 1]); else dstr<0>(n.w[(K - 1) >> 1][0], wa[(K - 1) >> 1]); }
  else if constexpr (K < 2 * NC + 4) ds128<(K - 2 * NC) * 2048>(n.x[K - 2 * NC], xo);
  else if constexpr (K == 2 * NC + 4) ds128<0>(n.x[4], xh);
}
template <int NC, int NH, int K>
DEVI void s1_mm(const Frags<NC>& c, f32x4 (&acc)[4][NC], f32x4 (&hacc)[NH]) {
  if constexpr (K < 4 * NC) mfma_a(acc[K / NC][K % NC], cat(c.w[K % NC][0], c.w[K % NC][1]), c.x[K / NC]);
  else mfma_a(hacc[K - 4 * NC], cat(c.w[K - 4 * NC][0], c.w[K - 4 * NC][1]), c.x[4]);
}
template <int NC, int NH, bool BAR, bool HX>
DEVI void s1_step(const Frags<NC>& c, Frags<NC>& n, int xo, int xh, const int (&wa)[NC], f32x4 (&acc)[4][NC], f32x4 (&hacc)[NH]) {
  constexpr int NM = 4 * NC + NH, NR = 2 * NC + 4 + HX, D = BAR ? NM - NR : 0;
  sfor<NM>([&](auto KK) {
    constexpr int k = decltype(KK)::value;
    constexpr int need = k < NC ? 2 * k + 2 : (k % NC == 0 && k < 4 * NC + HX) ? 2 * NC + k / NC : -1;
    if constexpr (BAR && k == D) bar_lgkm();
    if constexpr (need >= 0 && (!BAR || k < D)) wait_lg<NR - 1 - need + (BAR ? 0 : (k < NR ? k : NR))>();
    s1_mm<NC, NH, k>(c, acc, hacc);
    if constexpr (k >= D && k - D < NR) s1_rd<NC, k - D>(n, xo, xh, wa);
  });
}

template <int OFF> DEVI void dsw64(int addr, u32x2 d) { asm volatile("ds_write_b64 %0, %1 offset:%2" ::"v"(addr), "v"(d), "i"(OFF) : "memory"); }
template <int OFF> DEVI void ds64(u32x2& d, int addr) { asm volatile("ds_read_b64 %0, %1 offset:%2" : "=v"(d) : "v"(addr), "i"(OFF)); }

template <int NC, bool att>
DEVI void s1_compute(lptr shm, int w, int lane, int p_, int j_, int hf, char* ws PR_ARGS) {
  constexpr int NH = NC == 4 ? 2 : 3;
  const int mw = w & 1, nw = w >> 1, i = lane & 15, g = lane >> 4;
  f32x4 acc[4][NC], hacc[NH];
#pragma unroll
  for (int m = 0; m < 4; m++)
#pragma unroll
    for (int c = 0; c < NC; c++) { acc[m][c] = f32x4{0, 0, 0, 0}; asm volatile("" : "+v"(acc[m][c])); }
#pragma unroll
  for (int c = 0; c < NH; c++) { hacc[c] = f32x4{0, 0, 0, 0}; asm volatile("" : "+v"(hacc[c])); }
  int wb[NC], cb[NC], ct[NC];
#pragma unroll
  for (int c = 0; c < NC; c++) {
    const ColGeo cg = colgeo(att, nw, mw, c, NC == 4);
    wb[c] = (lane >> 2) * 480 + cg.A + cg.B * (lane & 1) + cg.C * ((lane >> 1) & 1);
    cb[c] = LDS_CS + 2 * (cg.A + cg.B * (g & 1) + cg.C * (g >> 1));
    ct[c] = cg.ct;
    asm volatile("" : "+v"(cb[c]), "+v"(wb[c]));
  }
  int xb = XOFF + i * 128 + ((g ^ (i >> 1) ^ (4 * hf)) << 4);
  int sta = LDS_ST + i * 8;
  asm volatile("" : "+v"(xb), "+v"(sta));
  const int own = (1 + 4 * mw) * 2048;
  Frags<NC> fa, fb;
  if (!att) {
    const unsigned one = i == 0 ? 0x3f803f80u : 0u;
    fa.x[4] = u32x4{one, one, one, one}; pin(fa.x[4]); fb.x[4] = fa.x[4];
  }
  int wa[NC];
  STAMPB(12);
  bar_lgkm();
  STAMP(1);
  sfor<2 * NC + 4 + att>([&](auto K) { s1_rd<NC, decltype(K)::value>(fa, xb + own, xb, wb); });
#pragma unroll
  for (int c = 0; c < NC; c++) wa[c] = wb[c] + WSLAB;
  s1_step<NC, NH, true, att>(fa, fb, (xb ^ 64) + own, xb ^ 64, wa, acc, hacc);
  int sl = 0;
#pragma nounroll
  for (int T = 1; T < 10; T++) {
    sl = sl == 2 * SLOT ? 0 : sl + SLOT;
#pragma unroll
    for (int c = 0; c < NC; c++) wa[c] = wb[c] + sl;
    s1_step<NC, NH, true, att>(fb, fa, xb + sl + own, xb + sl, wa, acc, hacc);
#pragma unroll
    for (int c = 0; c < NC; c++) wa[c] = wb[c] + sl + WSLAB;
    s1_step<NC, NH, false, att>(fa, fb, (xb ^ 64) + sl + own, (xb ^ 64) + sl, wa, acc, hacc);
  }
  wait_lg<0>();
  arrive(2);
  sfor<NH>([&](auto K) { s1_mm<NC, NH, 4 * NC + decltype(K)::value>(fb, acc, hacc); });
  u32x2 stu[5];
  u32x4 b1u[NC], csu[NC];
  sfor<4 * NC>([&](auto KK) {
    constexpr int k = decltype(KK)::value;
    s1_mm<NC, NH, k>(fb, acc, hacc);
    if constexpr (k < 4) ds64<(1 + k) * 128>(stu[k], sta + mw * 512);
    if constexpr (k == 4 && att) ds64<0>(stu[4], sta);
    if constexpr (k == 5) {
      dsw_col0(cb[0], hacc[0], cb[1], hacc[1], cb[NH - 1], hacc[NH - 1]);
      if (NC == 4) arrive(8 + nw);
    }
    if constexpr (k >= 6 && k < 6 + NC && !att) ds128<LDS_B1 - LDS_CS>(b1u[k - 6], cb[k - 6]);
  });
  STAMP(2);
  if (NC == 4) await<0, 2>(8 + nw);
#pragma unroll
  for (int c = 0; c < NC; c++) ds128<0>(csu[c], cb[c]);
  asm volatile("s_waitcnt lgkmcnt(0)\n\ts_nop 7" ::: "memory");
  f32x2 st[5];
  f32x4 cs[NC], b1[NC];
#pragma unroll
  for (int m = 0; m < 4 + att; m++) { asm volatile("" : "+v"(stu[m])); st[m] = __builtin_bit_cast(f32x2, stu[m]); }
#pragma unroll
  for (int c = 0; c < NC; c++) {
    pin(csu[c]); cs[c] = __builtin_bit_cast(f32x4, csu[c]);
    if (!att) { pin(b1u[c]); b1[c] = __builtin_bit_cast(f32x4, b1u[c]); }
  }
#pragma unroll
  for (int m = 0; m < 4; m++)
#pragma unroll
    for (int c = 0; c < NC; c++) pin(acc[m][c]);
#pragma unroll
  for (int c = 0; c < NH; c++) pin(hacc[c]);
  STAMPA(12);
  const int l2 = lane_id();
  if (!att) {
    const u32x4 ra = mkr(ws + WS_A2, A2_BYTES);
    const int base = (8 * p_ + 4 * mw) * KS_T * 1024;
    const int o0 = base + (NC == 4 || j_ < 7 || nw == 2 ? h_slot(j_, NC == 4 ? 2 * nw + mw : 2 + nw) * 1024 : 0x70000000);
    const int o1 = base + (NC == 4 ? h_slot(j_, 2 * nw + (mw ^ 1)) * 1024 : j_ < 7 ? h_slot(j_, 6) * 1024 + (nw - 2) * 512 : 0x70000000);
    const float c1 = -2.f * 0.7978845608028654f * 1.4426950408889634f, c2 = c1 * 0.044715f;
    unsigned sp[2];
#pragma unroll
    for (int m = 0; m < 4; m++) {
      f32x4 v[NC], t[NC];
      SB;
#pragma unroll
      for (int c = 0; c < NC; c++) t[c] = cs[c] * st[m][1] + b1[c];
      SB;
#pragma unroll
      for (int c = 0; c < NC; c++) v[c] = acc[m][c] * st[m][0] + t[c];
      SB;
#pragma unroll
      for (int c = 0; c < NC; c++) t[c] = v[c] * v[c];
      SB;
#pragma unroll
      for (int c = 0; c < NC; c++) t[c] = t[c] * c2 + c1;
      SB;
#pragma unroll
      for (int c = 0; c < NC; c++) t[c] = t[c] * v[c];
      SB;
#pragma unroll
      for (int c = 0; c < NC; c++)
#pragma unroll
        for (int e = 0; e < 4; e++) t[c][e] = __builtin_amdgcn_exp2f(t[c][e]);
      SB;
#pragma unroll
      for (int c = 0; c < NC; c++) t[c] = t[c] + 1.f;
      SB;
#pragma unroll
      for (int c = 0; c < NC; c++)
#pragma unroll
        for (int e = 0; e < 4; e++) t[c][e] = __builtin_amdgcn_rcpf(t[c][e]);
      SB;
#pragma unroll
      for (int c = 0; c < NC; c++) v[c] = v[c] * t[c];
      SB;
      unsigned o[NC][2];
#pragma unroll
      for (int c = 0; c < NC; c++) { o[c][0] = pk2(v[c][0], v[c][1]); o[c][1] = pk2(v[c][2], v[c][3]); }
      st_wt(ra, l2 * 16, o0 + m * KS_T * 1024, u32x4{o[0][0], o[0][1], o[1][0], o[1][1]});
      if (NC == 4) st_wt(ra, l2 * 16, o1 + m * KS_T * 1024, u32x4{o[2][0], o[2][1], o[NC - 1][0], o[NC - 1][1]});
      else if (m & 1) {
        auto s0 = __builtin_amdgcn_permlane32_swap(sp[0], o[2][0], false, false);
        auto s1 = __builtin_amdgcn_permlane32_swap(sp[1], o[2][1], false, false);
        st_wt(ra, (l2 >> 5) * (KS_T * 1024) + (l2 & 31) * 16, o1 + (m - 1) * KS_T * 1024, u32x4{s0[0], s1[0], s0[1], s1[1]});
      } else { sp[0] = o[2][0]; sp[1] = o[2][1]; }
    }
  } else {
    const int g2 = l2 >> 4, piece = LDS_TB + ((l2 & 48) + ((l2 & 15) ^ (8 * (g2 >> 1)))) * 8;
    sfor<NC>([&](auto C) {
      constexpr int c = decltype(C)::value;
      const int ah = piece + ct[c] * 4608, ao = ah + (1 + 4 * mw) * 512;
      u32x2 o[5];
      SB;
#pragma unroll
      for (int m = 0; m < (c < NH ? 5 : 4); m++) {
        f32x4 a = m < 4 ? acc[m < 4 ? m : 0][c] : hacc[c < NH ? c : 0];
        f32x4 v = a * st[m][0] + cs[c] * st[m][1];
        o[m] = u32x2{pk2(v[0], v[1]), pk2(v[2], v[3])};
      }
      dsw64<0>(ao, o[0]); dsw64<512>(ao, o[1]); dsw64<1024>(ao, o[2]); dsw64<1536>(ao, o[3]);
      if (c < NH) dsw64<0>(ah, o[4]);
    });
  }
}

template <bool MAX> DEVI float red4(const float (&v)[4]) {
  auto op = [](unsigned a, unsigned b) { float r; if (MAX) asm("v_max_f32 %0, %1, %2\n\ts_nop 1" : "=v"(r) : "v"(a), "v"(b)); else r = asf(a) + asf(b); return r; };
  auto a = __builtin_amdgcn_permlane32_swap(asu(v[0]), asu(v[1]), false, false);
  auto b = __builtin_amdgcn_permlane32_swap(asu(v[2]), asu(v[3]), false, false);
  auto c = __builtin_amdgcn_permlane16_swap(asu(op(a[0], a[1])), asu(op(b[0], b[1])), false, false);
  return op(c[0], c[1]);
}
DEVI void bcast4(float r, float (&v)[4]) {
  auto e = __builtin_amdgcn_permlane16_swap(asu(r), asu(r), false, false);
  unsigned e0 = e[0], e1 = e[1];
  auto a = __builtin_amdgcn_permlane32_swap(e0, e0, false, false);
  auto b = __builtin_amdgcn_permlane32_swap(e1, e1, false, false);
  v[0] = asf(a[0]); v[1] = asf(a[1]); v[2] = asf(b[0]); v[3] = asf(b[1]);
}

DEVI void att_table(lptr shm, int lane) {
  const int i = lane & 15, g = lane >> 4, p = lane & 3;
  const int d = sig(i) - g;
  const unsigned NEG = 0xf149f2cau;
  bool vo[4], vp[4];
#pragma unroll
  for (int r = 0; r < 4; r++) { vo[r] = (unsigned)(d - 4 * r) <= 7u; vp[r] = d + 16 - 4 * r <= 7; }
  auto pm = [](bool lo, bool hi) { return (lo ? 0xffffu : 0u) | (hi ? 0xffff0000u : 0u); };
  LAS u32x4* t = (LAS u32x4*)(shm + LDS_TAB) + lane;
  t[0] = u32x4{vo[0] ? 0u : NEG, vo[1] ? 0u : NEG, vo[2] ? 0u : NEG, vo[3] ? 0u : NEG};
  t[64] = u32x4{pm(vp[2], vp[3]), pm(vo[0], vo[1]), vp[2] ? 0u : NEG, vp[3] ? 0u : NEG};
  t[128] = u32x4{pm(vo[2], vo[3]), (unsigned)(LDS_TB + (g * 16 + (i ^ (8 * (g >> 1)))) * 8), (unsigned)(LDS_TB + (p * 16 + ((lane >> 2) ^ (8 * (p >> 1)))) * 8), 0u};
}
DEVI int sel(u64 mask, int a, int b) { int r; asm("v_cndmask_b32_e64 %0, %2, %1, %3" : "=v"(r) : "v"(a), "v"(b), "s"(mask)); return r; }

DEVI void s1_attention(lptr shm, int w, int p_, int j_, char* ws PR_ARGS) {
  arrive(3);
  const int lane = lane_id();
  const int tau = (w + 1) * 512, taup = w >= 2 ? tau - 1024 : 0, flip = w == 1 ? 16 : 0;
  const LAS u32x4* t = (const LAS u32x4*)(shm + LDS_TAB) + lane;
  const u32x4 t0 = t[0], t2 = t[128];
  u32x4 t1 = t[64];
  if (p_ == 0 && w < 2) { t1[0] = 0u; t1[2] = 0xf149f2cau; t1[3] = 0xf149f2cau; }
  const f32x4 mO = __builtin_bit_cast(f32x4, t0), mP = __builtin_bit_cast(f32x4, t1);
  const unsigned M1 = t1[0], M2 = t1[1], M3 = t2[0];
  const int po = t2[1] + tau, pp = (t2[1] ^ flip) + taup, to = t2[2] + tau, tp = (t2[2] ^ flip) + taup;
  int aq[4], avo[4], avp[4];
#pragma unroll
  for (int h = 0; h < 4; h++) {
    aq[h] = sel(0xffffull << (16 * h), po, LDS_ZERO - 4 * 4608);
    avo[h] = sel(0x1111111111111111ull << h, to, LDS_ZERO - 14 * 4608);
    avp[h] = sel(0x1111111111111111ull << h, tp, LDS_ZERO - 14 * 4608);
    asm volatile("" : "+v"(aq[h]), "+v"(avo[h]), "+v"(avp[h]));
  }
  STAMPA(14);
  await<0, 8>(3);
  STAMPA(15);
  u32x2 qa[4][2], ko[4][2], kp[4][2], va[4][2], vb[4][2];
  f32x4 sp[4], so[4];
  auto rd = [&](auto H) {
    constexpr int h = decltype(H)::value;
    ds64<(5 + h) * 4608>(ko[h][0], po); ds64<9 * 4608>(ko[h][1], po); ds64<h * 4608>(qa[h][0], po); ds64<4 * 4608>(qa[h][1], aq[h]);
    ds64<(5 + h) * 4608>(kp[h][0], pp); ds64<9 * 4608>(kp[h][1], pp);
  };
  auto rv = [&](auto H) {
    constexpr int h = decltype(H)::value;
    dstr<(10 + h) * 4608>(va[h][0], tp); dstr<(10 + h) * 4608>(va[h][1], to); dstr<14 * 4608>(vb[h][0], avp[h]); dstr<14 * 4608>(vb[h][1], avo[h]);
  };
  auto qk = [&](auto H, auto N) {
    constexpr int h = decltype(H)::value;
    asm volatile("s_waitcnt lgkmcnt(%6)" : "+v"(ko[h][0]), "+v"(ko[h][1]), "+v"(qa[h][0]), "+v"(qa[h][1]), "+v"(kp[h][0]), "+v"(kp[h][1]) : "i"(decltype(N)::value));
    so[h] = mfma(cat(ko[h][0], ko[h][1]), cat(qa[h][0], qa[h][1]), mO);
    sp[h] = mfma(cat(kp[h][0], kp[h][1]), cat(qa[h][0], qa[h][1]), mP);
  };
  rd(IC(0)); rd(IC(1));
  qk(IC(0), IC(6)); rd(IC(2));
  qk(IC(1), IC(6)); rd(IC(3));
  qk(IC(2), IC(6)); rv(IC(0)); rv(IC(1));
  qk(IC(3), IC(8)); rv(IC(2));
  asm volatile("s_waitcnt lgkmcnt(8)");
  rv(IC(3));
  const float qs = 0.22360679774997896f * 1.4426950408889634f;
  float ee[4], eo[4], mx[4], l[4];
#pragma unroll
  for (int h = 0; h < 4; h++) { ee[h] = fmaxf(fmaxf(sp[h][2], so[h][0]), so[h][2]); eo[h] = fmaxf(fmaxf(sp[h][3], so[h][1]), so[h][3]); mx[h] = fmaxf(ee[h], eo[h]); }
  bcast4(red4<true>(mx) * -qs, mx);
#pragma unroll
  for (int h = 0; h < 4; h++) {
    f32x2 t = f32x2{ee[h], eo[h]} * qs + mx[h];
    ee[h] = __builtin_amdgcn_exp2f(t[0]); eo[h] = __builtin_amdgcn_exp2f(t[1]); l[h] = ee[h] + eo[h];
  }
  bcast4(__builtin_amdgcn_rcpf(red4<false>(l)), l);
  asm volatile("s_waitcnt lgkmcnt(0)" : "+v"(va[0][0]), "+v"(va[0][1]), "+v"(va[1][0]), "+v"(va[1][1]), "+v"(va[2][0]), "+v"(va[2][1]), "+v"(va[3][0]), "+v"(va[3][1]),
               "+v"(vb[0][0]), "+v"(vb[0][1]), "+v"(vb[1][0]), "+v"(vb[1][1]), "+v"(vb[2][0]), "+v"(vb[2][1]), "+v"(vb[3][0]), "+v"(vb[3][1]));
  f32x4 o4 = {0, 0, 0, 0}, oh[4];
#pragma unroll
  for (int h = 0; h < 4; h++) {
    const unsigned pk = pk2(f32x2{ee[h], eo[h]} * l[h]);
    u32x4 pf = {0u, pk & M1, pk & M2, pk & M3};
    o4 = mfma(cat(vb[h][0], vb[h][1]), pf, o4);
    oh[h] = mfma(cat(va[h][0], va[h][1]), pf, f32x4{0, 0, 0, 0});
  }
  const u32x4 ra = mkr(ws + WS_A2, A2_BYTES);
  const int base = ((8 * p_ + w) * KS_T + 54 + 5 * (j_ >> 1)) * 1024;
#pragma unroll
  for (int hp = 0; hp < 2; hp++)
    st_wt(ra, lane * 16, base + ((j_ & 1) * 3 + hp) * 1024,
          u32x4{pk2(oh[2 * hp][0], oh[2 * hp][1]), pk2(oh[2 * hp][2], oh[2 * hp][3]), pk2(oh[2 * hp + 1][0], oh[2 * hp + 1][1]), pk2(oh[2 * hp + 1][2], oh[2 * hp + 1][3])});
  unsigned a0 = pk2(o4[0], o4[1]), a1 = pk2(o4[2], o4[3]);
  auto s0 = __builtin_amdgcn_permlane32_swap(a0, a0, false, false);
  auto s1 = __builtin_amdgcn_permlane32_swap(a1, a1, false, false);
  st_wt(ra, lane < 32 ? lane * 16 : 0x70000000, base + 2 * 1024 + (j_ & 1) * 512, u32x4{s0[0], s1[0], s0[1], s1[1]});
  arrive(4);
}

DEVI int s2_perm(int s, int rot) { return s < 8 ? s ^ rot : (s < 10 || s >= 18) ? s ^ (rot & 1) : ((s - 10) ^ rot) + 10; }
DEVI int fin_off(int G0, int n_, int r0, int lane) {
  const int frow = r0 + (lane >> 3), fG = G0 + (frow >> 4);
  return (32 * (fG >> 1) + 2 * sig(frow & 15) + (fG & 1)) * 1280 + n_ * 128 + (lane & 7) * 16;
}
constexpr int s2_slot(int r) { return r < 18 ? (r % 9) * 16384 : 147456 + (r - 18) * 4096; }
DEVI void watch(char* ws, int Ga, int Gb, int n_, int kind, int word, u64 fv) {
  const int lane = lane_id();
  const int off = WS_BOX + ((((lane & 8) ? Gb : Ga) >> 3) * 10 + n_) * 256 + (8 * kind + (lane & 7)) * 8;
  unsigned n = SPIN_CAP;
  u32x2 d;
  asm volatile(".Lg%=:\n\tbuffer_load_dwordx2 %0, %2, %3, 0 offen sc1\n\ts_sub_u32 %1, %1, 1\n\ts_waitcnt vmcnt(0)\n\ts_cbranch_scc1 .Lh%=\n\t"
               "v_cmp_ne_u64 vcc, %4, %0\n\ts_cbranch_vccnz .Lg%=\n.Lh%=:\n\tds_write_b32 %5, %6"
               : "=&v"(d), "+&s"(n) : "v"(off), "s"(mkr(ws, WS_A2)), "s"(fv), "v"(LDS_CNT + 4 * word), "v"(1) : "memory", "scc", "vcc");
}

template <int NS, int MT>
DEVI void s2_compute(int w, int kq, int G0, int n_, int rot, char* ws, u64 fv PR_ARGS) {
  constexpr int AD = (MT == 3 ? 3 : 4) + (NS == 20);
  const int mh = w >> 2, lane = lane_id();
  const u32x4 ra = mkr(ws + WS_A2, A2_BYTES);
  int av = lane * 16;
  pin(av);
  const int ao = ((G0 + MT * mh) * KS_T + (NS == 20 ? 54 : 18 * (kq - 1))) * 1024;
  int bs[4];
#pragma unroll
  for (int c = 0; c < 4; c++) bs[c] = kq * 4096 + (lane >> 2) * 128 + ((c ^ ((lane >> 3) & 3)) << 5) + 8 * (lane & 3);
  f32x4 acc[MT][4];
#pragma unroll
  for (int m = 0; m < MT; m++)
#pragma unroll
    for (int c = 0; c < 4; c++) { acc[m][c] = f32x4{0, 0, 0, 0}; pin(acc[m][c]); }
  u32x4 a[AD][MT];
  u32x2 b[3][4][2];
  int ca = LDS_CONS + kq * 8 + mh * 4, pa = LDS_PROD + kq * 4;
  pin(ca); pin(pa);
  unsigned pv, lv;
  auto rdb = [&](auto R, auto K) {
    constexpr int r = decltype(R)::value, k = decltype(K)::value;
    constexpr int off = s2_slot(r), nxt = s2_slot(r + 1) / 65536 - off / 65536;
    if constexpr (r < 3) dstr<off % 65536 + (k & 1) * 2048>(b[r % 3][k >> 1][k & 1], bs[k >> 1]);
    else redstr<off % 65536 + (k & 1) * 2048>(b[r % 3][k >> 1][k & 1], bs[k >> 1]);
    if constexpr (nxt != 0 && (k & 1)) vinc(bs[k >> 1], nxt * 65536);
  };
  auto lda = [&](auto S, int m) {
    constexpr int s = decltype(S)::value;
    const int o = ao + m * KS_T * 1024 + (s2_perm(s, rot) << 10);
    if constexpr (s < AD) bload_sc1(a[s % AD][m], ra, av, o); else reload_sc1(a[s % AD][m], ra, av, o);
  };
  auto ldb = [&]() {
    peek(pv, pa); wait_lg<0>(); require(pv, pa, 1);
    sfor<3>([&](auto R) { sfor<8>([&](auto K) { rdb(R, K); }); wait_lg<(decltype(R)::value < 2 ? 4 : 0)>(); });
    post(ca, 1);
  };
  constexpr bool BFIRST = NS == 20 || MT == 3;
  if (BFIRST) ldb();
  STAMP(6);
  if (NS == 20) watch(ws, G0, G0 + 2 * MT - 1, n_, w == 0 ? 0 : 2, w == 0 ? 14 : 5, fv);
  await<0, 1>(NS == 20 ? 14 : 15);
  STAMP(7);
  sfor<AD - 1>([&](auto S) {
#pragma unroll
    for (int m = 0; m < MT; m++) lda(S, m);
  });
  if (!BFIRST) ldb();
  sfor<NS - 3>([&](auto S) {
    constexpr int s = decltype(S)::value;
    constexpr bool LA = s + AD - 1 < NS;
    constexpr int later = NS - 1 - s < AD - 2 ? NS - 1 - s : AD - 2;
    if constexpr (s == 3) require(pv, pa, 2);
    if constexpr (s == 7) require(pv, pa, 3);
    if constexpr (s == 10) require(pv, pa, 4);
    if constexpr (s == 13) require(pv, pa, 5);
    if constexpr (s == 2 || s == 6 || s == 9 || s == 12) peek(pv, pa);
    if constexpr (NS == 18 && s == 11 - AD) require(lv, LDS_CNT + 4 * 5, 1);
    if constexpr (NS == 18 && s == 10 - AD) peek(lv, LDS_CNT + 4 * 5);
    if constexpr (s == NS - 4) peek(lv, ca ^ 4);
    sfor<MT>([&](auto M) {
      constexpr int m = decltype(M)::value;
      if constexpr (LA) lda(IC(s + AD - 1), m);
      wait_vm<MT * later + MT - 1 - m + (LA ? m + 1 : 0)>();
      if constexpr (m == 1 && s >= 2) wait_lg<4>();
      sfor<4>([&](auto C) {
        constexpr int c = decltype(C)::value;
        mfma_a(acc[m][c], cat(b[s % 3][c][0], b[s % 3][c][1]), a[s % AD][m]);
        if constexpr (s >= 1 && m < 2) rdb(IC(s + 2), IC(4 * m + c));
      });
    });
    if constexpr (s == 3 || s == 6 || s == 9 || s == 12) post(ca, s);
  });
  wait_lg<4>();
  sfor<8>([&](auto K) { rdb(IC(NS - 1), K); });
  if constexpr (AD == 3) {
#pragma unroll
    for (int m = 0; m < MT; m++) lda(IC(NS - 1), m);
  }
  int da[4];
#pragma unroll
  for (int c = 0; c < 4; c++) da[c] = mh * 49152 + kq * 4096 + (av & 0xf0) * 16 + ((((4 * c) << 4) + (av >> 8 << 4)) ^ (av & 0x70));
  require(lv, ca ^ 4, mh ? 12 : 9);
  sfor<MT>([&](auto M) {
    constexpr int m = decltype(M)::value;
    sfor<3>([&](auto U) {
      constexpr int u = decltype(U)::value, s = NS - 3 + u;
      if constexpr (m == 0) wait_vm<MT * (2 - u) + MT - 1>();
      if constexpr (m > 0 && u == 2) wait_vm<MT - 1 - m>();
      if constexpr (m == 0 && u == 1) wait_lg<8>();
      if constexpr (m == 0 && u == 2) wait_lg<0>();
      sfor<4>([&](auto C) {
        constexpr int c = decltype(C)::value;
        mfma_a(acc[m][c], cat(b[s % 3][c][0], b[s % 3][c][1]), a[s % AD][m]);
        if constexpr (m > 0 && u > 0 && (c & 1)) dsw128<(m - 1) * 16384>(da[2 * (u - 1) + (c >> 1)], __builtin_bit_cast(u32x4, acc[m - 1][2 * (u - 1) + (c >> 1)]));
      });
    });
  });
  STAMP(9);
  asm volatile("s_nop 4");
#pragma unroll
  for (int c = 0; c < 4; c++) dsw128<(MT - 1) * 16384>(da[c], __builtin_bit_cast(u32x4, acc[MT - 1][c]));
  arrive((NS == 20 ? 28 : 12) + mh);
}

typedef __attribute__((ext_vector_type(8))) u64 u64x8;
__global__ __launch_bounds__(768, 3) void fused_block(const void*, const void*, const void*, const void*, const void*, const void*, const void*,
                                                      void*, void*) {
  u64x8 ka; u64 ka8, pc;
  asm volatile("s_load_dwordx16 %0, %3, 0x0\n\ts_load_dwordx2 %1, %3, 0x40\n\ts_getpc_b64 %2"
               : "=&s"(ka), "=&s"(ka8), "=&s"(pc) : "s"(__builtin_amdgcn_kernarg_segment_ptr()));
  extern __shared__ __attribute__((aligned(16))) char shm_[];
  lptr shm = (lptr)shm_;
  const int tid = threadIdx.x, lane = tid & 63, w = __builtin_amdgcn_readfirstlane(tid >> 6);
  const int xcd = blockIdx.x & 7, idx = blockIdx.x >> 3;
  const int p_ = (xcd & 1) * 8 + (idx & 7), sub = idx >> 3;
  const bool att = !(sub & 1);
  const int j_ = (xcd >> 1) * 2 + (sub >> 1);
  const int uidx = (idx & 7) + 8 * (idx >> 4);
  const int n_ = (xcd & 1) * 5 + (int)(((att ? 0x4444433333221100ull : 0x4433222211110000ull) >> (4 * uidx)) & 15);
  const int G0 = 32 * (xcd >> 1) + 2 * (int)(((att ? 0xeca86eca86ecececull : 0x3030963096309630ull) >> (4 * uidx)) & 15);
  const int rot = (int)(((att ? 0x6547610321322110ull : 0x5403103203213210ull) >> (4 * uidx)) & 15);
  const int kq = (w + ((w >> 2) & 1)) & 3;
  STAMP(0);
  if (w == 7) {
    touch(mkr((const void*)(pc & ~255ull), CODE_BYTES), (idx + 32 * lane) * 128);
  }
  const int hf = idx & 1, tx = ((idx >> 1) ^ sub) & 1;
  const int rid = (att ? 0 : 8) + j_;
  if (w >= 8) __builtin_amdgcn_s_setprio(3);
  const int lw = w & 3;
  const int RS = att ? 3840 : 3456;
  const int r8 = lane >> 3, cp = lane & 7, t0 = p_ * 128;
  int xo[2], hv, wv;
#pragma unroll
  for (int e = 0; e < 2; e++)
    xo[e] = (t0 + 32 * lw + 2 * (4 * (r8 & 3) + 2 * e + (r8 >> 2))) * 1280 + ((cp ^ (4 * e + (r8 >> 1))) << 4);
  {
    const int hi = lane >= 30, ch = lane - 30 * hi, lim = att ? 63 : j_ < 7 ? 27 : 19;
    wv = hi * RS + ((ch >= 10) + (ch >= 20)) * (att ? 1120 : 0) + j_ * (att ? 160 : 448) + min(ch, lim) * 16;
    const int tok = max(t0 - 16 + 8 * (r8 & 1) + 2 * lw + ((r8 & 3) < 2), 0);
    hv = (tok * 1280 + ((cp ^ ((2 * lw + ((r8 & 3) >> 1)) & 7)) << 4));
  }
  pin(xo[0]); pin(xo[1]); pin(wv); pin(hv);
  asm volatile("s_waitcnt lgkmcnt(0)" : "+s"(ka), "+s"(ka8));
  const char *x = (const char*)ka[0], *wqkv = (const char*)ka[1], *wo = (const char*)ka[2], *w1 = (const char*)ka[3], *b1 = (const char*)ka[4],
             *w2 = (const char*)ka[5], *b2 = (const char*)ka[6];
  char *out = (char*)ka[7], *ws = (char*)ka8;
  int fu;
  if (__builtin_expect(w < 8, 0)) {
    if (tid < 24) *(LAS u32x4*)(shm + (tid >> 3) * SLOT + XOFF + (tid & 7) * 16) = u32x4{0x3f803f80u, 0x3f803f80u, 0x3f803f80u, 0x3f803f80u};
    if (tid >= 448 && tid < 480) *(LAS unsigned*)(shm + LDS_CNT + 4 * (tid - 448)) = 0u;
    if (w == 5 && att) att_table(shm, lane);
    const bool light = att ? (w >= 6) : (w >= 4);
    if (att) {
      if (light) s1_compute<3, true>(shm, w, lane, p_, j_, hf, ws PR_PASS);
      else s1_compute<4, true>(shm, w, lane, p_, j_, hf, ws PR_PASS);
    } else {
      if (light) s1_compute<3, false>(shm, w, lane, p_, j_, hf, ws PR_PASS);
      else s1_compute<4, false>(shm, w, lane, p_, j_, hf, ws PR_PASS);
    }
    STAMP(3);
    if (att) s1_attention(shm, w, p_, j_, ws PR_PASS);
    STAMP(4);
    const unsigned tag = __builtin_amdgcn_readfirstlane(*(LAS unsigned*)(shm + LDS_CNT + 4));
    GAS u64* flags = (GAS u64*)(ws + WS_FLAG);
    const u64 fv = flagval(tag);
    {
      const int lane = lane_id(), kind = att ? 0 : 1 + (w >> 2);
      const int dst = lane == 10 ? WS_FLAG + (p_ * 16 + rid) * 8 : WS_BOX + (p_ * 10 + lane) * 256 + (8 * kind + j_) * 8;
      publish(LDS_CNT + (kind == 1 ? 120 : 0), att ? 7u : 3u, mkr(ws, WS_A2), dst, u32x2{(unsigned)fv, (unsigned)(fv >> 32)});
    }
    STAMP(5);
    if (w == 0) watch(ws, G0, G0 + (att ? 3 : 5), n_, 1, 15, fv);
    if (kq == 0) {
      if (att) s2_compute<20, 2>(w, kq, G0, n_, rot, ws, fv PR_PASS);
      else s2_compute<20, 3>(w, kq, G0, n_, rot, ws, fv PR_PASS);
    } else {
      if (att) s2_compute<18, 2>(w, kq, G0, n_, rot, ws, fv PR_PASS);
      else s2_compute<18, 3>(w, kq, G0, n_, rot, ws, fv PR_PASS);
    }
    if (blockIdx.x == 0 && w == 3) {
      const int lane = lane_id();
#pragma nounroll
      for (unsigned spins = 0; spins < SPIN_CAP; spins++) {
        bool ok = true;
#pragma unroll
        for (int k = 0; k < 4; k++) ok &= __hip_atomic_load(flags + lane + 64 * k, __ATOMIC_RELAXED, __HIP_MEMORY_SCOPE_AGENT) == fv;
        if (__all(ok)) break;
        __builtin_amdgcn_s_sleep(8);
      }
      if (lane == 0) __hip_atomic_store((GAS unsigned*)ws, tag, __ATOMIC_RELAXED, __HIP_MEMORY_SCOPE_AGENT);
    }
    if (att || w == 0 || w > 2) { STAMP(11); FINISH(); return; }
    fu = w + 3;
  } else {
    auto ktile = [&](int T) { return T ^ tx; };
    const u32x4 rx = mkr(x, S_ * D_ * 2), rw1 = mkr(att ? wqkv : w1, D_ * RS);
    const int wrow = (16 * (lw & 1) + 4 * (lw >> 1)) * RS;
    const u64 hmask = lw == 0 ? 0xffffff00ull : 0xffffffffull;
    auto ldx = [&](int T) { const int o = ktile(T) * 128; dma4<1024>(rx, ~0ull, (T % 3) * SLOT + XOFF + 2048 + 4 * lw * 1024, xo[0], xo[1], o, o, o + 1280, o + 1280); };
    auto ldw = [&](int T, int h) {
      const int so = (64 * ktile(T) + 32 * (h ^ hf)) * RS + wrow;
      dma4<960>(rw1, 0x0fffffffffffffffull, (T % 3) * SLOT + h * WSLAB + 4 * lw * 960, wv, wv, so, so + 2 * RS, so + 8 * RS, so + 10 * RS);
    };
    auto ldh = [&](int T) { if (att) dma1(rx, hmask, (T % 3) * SLOT + XOFF + lw * 512, hv, ktile(T) * 128); };
    const int attf = att;
    STAMP(12);
    ldx(0); ldw(0, 0); ldh(0); ldw(0, 1);
    STAMP(13);
    bar_vm<4>();
    STAMP(14);
    ldh(1); ldx(1);
    bar_vm1<4>(attf);
    ldw(1, 0); ldw(1, 1);
    ldh(2); ldx(2);
    bar_vm1<4>(attf);
    const int srow = p_ * 128 + 8 * rid + 2 * lw + (lane >> 5);
    u32x2 xr[5];
    unsigned ep, b1v;
    setup_loads(xr, ep, b1v, rx, srow * 1280 + (lane & 31) * 8, mkr(ws, 4), mkr(b1, F_ * 2), (j_ * 224 + 64 * lw + lane) * 2);
    touch_sc1(mkr(ws + WS_BOX, lw == 0 ? 160 * 256 : 0), (((G0 >> 3) + (lane & 1)) * 10 + n_) * 256 + (lane & 2) * 64);
    ldw(2, 0); ldw(2, 1); ldh(3); ldx(3); ldw(3, 0);
    bar_vm1<8>(attf);
    pin(ep); pin(b1v);
#pragma unroll
    for (int k = 0; k < 5; k++) pin(xr[k]);
    const unsigned tag = __builtin_amdgcn_readfirstlane(ep) + 1u;
    ldw(3, 1); ldh(4); ldx(4); ldw(4, 0); ldw(4, 1);
    {
      float s = 0;
#pragma unroll
      for (int k = 0; k < 5; k++) s += (blo(xr[k][0]) + bhi(xr[k][0])) + (blo(xr[k][1]) + bhi(xr[k][1]));
      const float m = half_sum(s) * (1.f / D_);
      float q = 0;
#pragma unroll
      for (int k = 0; k < 10; k++) { const unsigned u = xr[k >> 1][k & 1]; const float a = blo(u) - m, b = bhi(u) - m; q = __builtin_fmaf(a, a, q); q = __builtin_fmaf(b, b, q); }
      const float rs = __builtin_amdgcn_rsqf(half_sum(q) * (1.f / D_) + 1e-5f);
      st_wt(mkr(ws + WS_STAT, S_ * 16), (lane & 31) == 0 ? srow * 16 : 0x70000000, 0, u32x4{asu(rs), tag, asu(-m * rs), tag});
      *(LAS float*)(shm + LDS_B1 + (64 * lw + lane) * 4) = blo(b1v);
      *(LAS unsigned*)(shm + LDS_CNT + 4) = tag;
    }
    bar_vm1<13>(attf);
    const u32x4 rw = mkr(kq == 0 ? wo : w2, kq == 0 ? D_ * 1280 : F_ * 1280);
    const int cbyte = n_ * 128 + ((cp ^ ((lane >> 4) << 1)) << 4);
    const int v1 = r8 * 1280 + cbyte, v2 = kq == 0 ? (20 * (r8 >> 2) + (r8 & 3)) * 1280 + cbyte : v1;
    int gtab;
    {
      const int so = s2_perm(lane, rot), P = (so * 13) >> 6, kp = so - 5 * P;
      const int r0 = 160 * P + (kp == 2 ? 16 : 40 * (kp - (kp > 2)));
      const int t = 3 * so + kq - 1, u = t - 32;
      const int r1 = 32 * (t < 32 ? 7 * (t >> 2) + (t & 3) : 4 * ((u * 43) >> 7) + 4 + u);
      gtab = kq == 0 ? r0 * 1280 + (kp == 2) : r1 * 1280;
      pin(gtab);
    }
    const int sbn = 1280 * (kq == 0 ? 20 : 16);
    auto geom = [&](int s, int& rb, int& sa, int& sb, int& v) {
      const int e = __builtin_amdgcn_readlane(gtab, s), f = e & 1;
      v = v1 + ((v2 - v1) & -f);
      rb = e - f;
      sa = f ? 1280 * 80 : 1280 * 8;
      sb = f ? 1280 * 40 : sbn;
    };
    u32x4 pre[9][4];
    auto ldr = [&](auto R) {
      constexpr int r = decltype(R)::value;
      int rb, sa, sb, v; geom(9 + r, rb, sa, sb, v);
      bload(pre[r][0], rw, v, rb); bload(pre[r][1], rw, v, rb + sa); bload(pre[r][2], rw, v, rb + sb); bload(pre[r][3], rw, v, rb + sa + sb);
    };
    auto ldl = [&](auto S) {
      constexpr int s = decltype(S)::value;
      int rb, sa, sb, v; geom(s, rb, sa, sb, v);
      dma4<1024>(rw, ~0ull, s < 9 ? s * 16384 + kq * 4096 : 147456 + (s - 18) * 4096, v, v, rb, rb + sa, rb + sb, rb + sa + sb);
    };
    const u32x4 rst = mkr(ws + WS_STAT, S_ * 16);
    const int spos = 36 * lw + (lane < 36 ? lane : 35), sv = row_token(p_, spos) * 16;
    u32x4 sg;
    sfor<5>([&](auto TT) {
      constexpr int T = decltype(TT)::value + 5;
      if (T == 9) bload_sc1(sg, rst, sv, 0);
      if (T == 6) { STAMP(15); }
      ldh(T); ldx(T); ldw(T, 0); ldw(T, 1);
      if (T == 9 && !att) {
        const int wv2 = ((blockIdx.x >> 7) * 64 + (blockIdx.x & 63)) * 4 + lw;
        touch(mkr(wv2 < 360 ? w2 : wo, wv2 < 360 ? F_ * 1280 : D_ * 1280), min((wv2 < 360 ? wv2 : wv2 - 360) * 48 + min(lane, 47), wv2 < 360 ? F_ * 10 - 1 : D_ * 10 - 1) * 128);
      }
      if (T < 9) bar_vm1<12>(attf); else bar_vm<13>();
    });
    STAMP(1);
    asm volatile("" : "+v"(sg));
#pragma nounroll
    for (unsigned spins = 0; !__all(sg[1] == tag && sg[3] == tag) && spins < SPIN_CAP; spins++) {
      __builtin_amdgcn_s_sleep(2);
      bload_sc1(sg, rst, sv, 0);
      asm volatile("s_waitcnt vmcnt(0)" : "+v"(sg) :: "memory");
    }
    *(LAS u32x2*)(shm + LDS_ST + spos * 8) = u32x2{sg[0], sg[2]};
    ldl(IC(4));
    if (att) wait_vm<4>(); else wait_vm<5>();
    STAMP(2);
    bar_lgkm();
    __builtin_amdgcn_s_setprio(0);
    STAMP(3);
    const int pa = LDS_PROD + kq * 4, wa = kq * 4096 + lane * 16;
    if (!att) { ldl(IC(5)); ldl(IC(6)); ldl(IC(7)); }
    ldr(IC(1)); ldr(IC(2)); ldr(IC(3)); ldr(IC(4));
    STAMP(4);
    ldl(IC(3));
    await<1, 8>(2);
    ldl(IC(2)); ldl(IC(1)); ldl(IC(0));
    if (!att) { ldl(IC(8)); if (kq == 0) { ldl(IC(18)); ldl(IC(19)); } }
    ldr(IC(0)); wait_vm<4>();
    post(pa, att ? 1 : 2);
    STAMP(5);
    if (!att) __builtin_amdgcn_s_setprio(3);
    ldr(IC(5)); ldr(IC(6)); ldr(IC(7)); ldr(IC(8));
    if (att) {
      await<1, 8>(4);
      __builtin_amdgcn_s_setprio(3);
      ldl(IC(5)); ldl(IC(6)); ldl(IC(7)); ldl(IC(8));
      if (kq == 0) { ldl(IC(18)); ldl(IC(19)); }
    }
    if (lw == 3) touch(mkr(b2, D_ * 2), n_ * 128);
    wait_vm<0>();
    post(pa, 2);
#define PUT(r, off) dsw128<off>(wa + (r / 3) * 49152, pre[r][0]); dsw128<off + 1024>(wa + (r / 3) * 49152, pre[r][1]); dsw128<off + 2048>(wa + (r / 3) * 49152, pre[r][2]); dsw128<off + 3072>(wa + (r / 3) * 49152, pre[r][3]);
    await_pair(LDS_CONS + kq * 8, 1);
    PUT(0, 0) PUT(1, 16384) PUT(2, 32768)
    post(pa, 3);
    __builtin_amdgcn_s_setprio(0);
    STAMP(6);
    await_pair(LDS_CONS + kq * 8, 3);
    PUT(3, 0) PUT(4, 16384) PUT(5, 32768)
    post(pa, 4);
    await_pair(LDS_CONS + kq * 8, 6);
    PUT(6, 0) PUT(7, 16384) PUT(8, 32768)
    post(pa, 5);
    fu = lw;
  }
  {
    const int lane = lane_id();
    const u32x4 bv = *(const GAS u32x4*)(b2 + n_ * 128 + (lane & 7) * 16);
    int foff[2], pr0[2];
    u32x4 xv[2];
    f32x2 gt[2][4];
#pragma unroll
    for (int hh = 0; hh < 2; hh++) {
      const int frow = 48 * hh + 8 * fu + (lane >> 3);
      foff[hh] = fin_off(G0 + (att ? 2 : 3) * hh, n_, 8 * fu, lane);
      xv[hh] = *(const GAS u32x4*)((const char*)x + foff[hh]);
      pr0[hh] = (frow >> 4) * 16384 + (frow & 15) * 256 + (((2 * (lane & 7)) ^ (frow & 7)) << 4);
      pin(pr0[hh]);
    }
#pragma unroll
    for (int hh = 0; hh < 2; hh++) {
      await<1, 3>(12 + hh);
      f32x4 y[2];
#pragma unroll
      for (int e = 0; e < 2; e++) {
        lptr pr = shm + (pr0[hh] ^ (16 * e));
        y[e] = *(LAS f32x4*)(pr + 4096) + *(LAS f32x4*)(pr + 8192) + *(LAS f32x4*)(pr + 12288);
      }
#pragma unroll
      for (int k = 0; k < 4; k++) {
        f32x2 yy = f32x2{y[k >> 1][2 * (k & 1)], y[k >> 1][2 * (k & 1) + 1]} + f32x2{blo(bv[k]), bhi(bv[k])};
        f32x2 t = yy * -1.4426950408889634f;
        f32x2 e = f32x2{__builtin_amdgcn_exp2f(t[0]), __builtin_amdgcn_exp2f(t[1])} + 1.f;
        gt[hh][k] = yy * f32x2{__builtin_amdgcn_rcpf(e[0]), __builtin_amdgcn_rcpf(e[1])};
        pin(gt[hh][k]);
      }
    }
    STAMP(10);
#pragma unroll
    for (int hh = 0; hh < 2; hh++) {
      await<0, 1>(28 + hh);
      u32x4 o;
#pragma unroll
      for (int e = 0; e < 2; e++) {
        const f32x4 s = *(LAS f32x4*)(shm + (pr0[hh] ^ (16 * e)));
        o[2 * e] = pk2(f32x2{s[0], s[1]} * gt[hh][2 * e] + f32x2{blo(xv[hh][2 * e]), bhi(xv[hh][2 * e])});
        o[2 * e + 1] = pk2(f32x2{s[2], s[3]} * gt[hh][2 * e + 1] + f32x2{blo(xv[hh][2 * e + 1]), bhi(xv[hh][2 * e + 1])});
      }
      __builtin_nontemporal_store(o, (GAS u32x4*)(out + foff[hh]));
    }
  }
  STAMP(11);
  FINISH();
}

extern "C" void kernel_launch(void* const* d_in, const int* in_sizes, int n_in, void* d_out, int out_size, void* d_ws,
                              size_t ws_size, hipStream_t stream) {
  (void)hipFuncSetAttribute((const void*)fused_block, hipFuncAttributeMaxDynamicSharedMemorySize, LDS_TOTAL);
  fused_block<<<256, 768, LDS_TOTAL, stream>>>(d_in[0], d_in[1], d_in[2], d_in[4], d_in[5], d_in[6],
                                               d_in[7], d_out, d_ws);
}
```

```cpp
#include <hip/hip_runtime.h>
#include <hip/hip_bf16.h>
#include <type_traits>
#include <utility>

typedef __attribute__((ext_vector_type(8))) __bf16 bf16x8;
typedef __attribute__((ext_vector_type(2))) __bf16 bf16x2;
typedef __attribute__((ext_vector_type(4))) float f32x4;
typedef __attribute__((ext_vector_type(2))) float f32x2;
typedef __attribute__((ext_vector_type(4))) unsigned u32x4;
typedef __attribute__((ext_vector_type(2))) unsigned u32x2;
#define LAS __attribute__((address_space(3)))
#define GAS __attribute__((address_space(1)))
#define DEVI __device__ __forceinline__
typedef LAS char* lptr;
typedef unsigned long long u64;
#define SB __builtin_amdgcn_sched_barrier(0)
#define IC(n) std::integral_constant<int, n>{}

constexpr int S_ = 2048, D_ = 640, F_ = 1728;
constexpr int KS_T = 74;
constexpr int WSLAB = 15360;
constexpr int XOFF = 2 * WSLAB;
constexpr int SLOT = 49152;
constexpr int LDS_TB = 86016;
constexpr int LDS_ST = 155648;
constexpr int LDS_CS = LDS_ST + 1152;
constexpr int LDS_B1 = LDS_CS + 1024;
constexpr int LDS_CNT = LDS_B1 + 1024;
constexpr int LDS_ZERO = LDS_CNT + 24;
constexpr int LDS_CONS = LDS_CNT + 64;
constexpr int LDS_PROD = LDS_CONS + 32;
constexpr int LDS_TAB = LDS_CNT + 128;
constexpr int LDS_TOTAL = 163840;
constexpr int WS_FLAG = 4096, WS_STAT = 8192, WS_BOX = 65536, WS_A2 = 1 << 20;
constexpr int A2_BYTES = 128 * KS_T * 1024;
#define WARM1 0
#define ROTM 15
#define KLIM 10
#define WPOS 1
#define HELPQ 0
#define HELPM 0
#define WATT 0
constexpr int CODE_BYTES = 48896;
constexpr unsigned SPIN_CAP = 1u << 20;

#define STAMP(idx)
#define FINISH()
#define STAMPC(k)
#define STAMPA(k) STAMP(k)
#define STAMPB(k)
#define PR_ARGS
#define PR_PASS

DEVI void bar_lgkm() { asm volatile("s_waitcnt lgkmcnt(0)\n\ts_barrier" ::: "memory"); }
template <int N> DEVI void bar_vm() { asm volatile("s_waitcnt vmcnt(%0)\n\ts_barrier" ::"i"(N) : "memory"); }
template <int N> DEVI void wait_vm() { asm volatile("s_waitcnt vmcnt(%0)" ::"i"(N) : "memory"); }
DEVI unsigned pk2(float a, float b) { f32x2 v = {a, b}; return __builtin_bit_cast(unsigned, __builtin_convertvector(v, bf16x2)); }
DEVI unsigned pk2(f32x2 v) { return __builtin_bit_cast(unsigned, __builtin_convertvector(v, bf16x2)); }
DEVI float asf(unsigned u) { return __builtin_bit_cast(float, u); }
DEVI unsigned asu(float f) { return __builtin_bit_cast(unsigned, f); }
DEVI float blo(unsigned u) { return __builtin_bit_cast(float, u << 16); }
DEVI float bhi(unsigned u) { return __builtin_bit_cast(float, u & 0xffff0000u); }
DEVI f32x4 mfma(u32x4 a, u32x4 b, f32x4 c) {
  return __builtin_amdgcn_mfma_f32_16x16x32_bf16(__builtin_bit_cast(bf16x8, a), __builtin_bit_cast(bf16x8, b), c, 0, 0, 0);
}
DEVI u32x4 cat(u32x2 a, u32x2 b) { return __builtin_shufflevector(a, b, 0, 1, 2, 3); }
DEVI void mfma_a(f32x4& c, u32x4 a, u32x4 b) { asm volatile("v_mfma_f32_16x16x32_bf16 %0, %1, %2, %0" : "+v"(c) : "v"(a), "v"(b)); }
template <int OFF> DEVI void ds128(u32x4& d, int addr) { asm volatile("ds_read_b128 %0, %1 offset:%2" : "=v"(d) : "v"(addr), "i"(OFF)); }
template <int OFF> DEVI void dstr(u32x2& d, int addr) { asm volatile("ds_read_b64_tr_b16 %0, %1 offset:%2" : "=v"(d) : "v"(addr), "i"(OFF)); }
template <int OFF> DEVI void dsw128(int addr, u32x4 d) { asm volatile("ds_write_b128 %0, %1 offset:%2" ::"v"(addr), "v"(d), "i"(OFF) : "memory"); }
DEVI u32x4 mkr(const void* p, unsigned bytes) {
  u64 a = (u64)p;
  return u32x4{(unsigned)a, (unsigned)(a >> 32) & 0xffffu, bytes, 0x00020000u};
}
DEVI void st_wt(u32x4 r, int voff, int soff, u32x4 v) {
  asm volatile("buffer_store_dwordx4 %0, %1, %2, %3 offen sc1\n\ts_nop 1" ::"v"(v), "v"(voff), "s"(r), "s"(soff) : "memory");
}
template <int LSTEP>
DEVI void dma4(u32x4 r, u64 mask, int l, int va, int vb, int s0, int s1, int s2, int s3) {
  asm volatile(
      "s_mov_b64 exec, %0\n\t"
      "s_mov_b32 m0, %1\n\ts_nop 0\n\tbuffer_load_dwordx4 %2, %4, %5 offen lds\n\t"
      "s_add_u32 m0, %1, %9\n\ts_nop 0\n\tbuffer_load_dwordx4 %3, %4, %6 offen lds\n\t"
      "s_add_u32 m0, %1, %10\n\ts_nop 0\n\tbuffer_load_dwordx4 %2, %4, %7 offen lds\n\t"
      "s_add_u32 m0, %1, %11\n\ts_nop 0\n\tbuffer_load_dwordx4 %3, %4, %8 offen lds\n\t"
      "s_mov_b64 exec, -1"
      :: "s"(mask), "s"(l), "v"(va), "v"(vb), "s"(r), "s"(s0), "s"(s1), "s"(s2), "s"(s3), "i"(LSTEP), "i"(2 * LSTEP), "i"(3 * LSTEP)
      : "memory", "scc");
}
DEVI void dma1(u32x4 r, u64 mask, int l, int v, int s0) {
  asm volatile("s_mov_b64 exec, %0\n\ts_mov_b32 m0, %1\n\ts_nop 0\n\tbuffer_load_dwordx4 %2, %3, %4 offen lds\n\ts_mov_b64 exec, -1"
               :: "s"(mask), "s"(l), "v"(v), "s"(r), "s"(s0) : "memory");
}
DEVI void setup_loads(u32x2 (&xr)[5], unsigned& ep, unsigned& bv, u32x4 rx, int vx, u32x4 re, u32x4 rb, int vb) {
  asm volatile("buffer_load_dwordx2 %0, %7, %8, 0 offen\n\tbuffer_load_dwordx2 %1, %7, %8, 0 offen offset:256\n\tbuffer_load_dwordx2 %2, %7, %8, 0 offen offset:512\n\t"
               "buffer_load_dwordx2 %3, %7, %8, 0 offen offset:768\n\tbuffer_load_dwordx2 %4, %7, %8, 0 offen offset:1024\n\t"
               "buffer_load_dword %5, off, %9, 0 sc1\n\tbuffer_load_ushort %6, %11, %10, 0 offen"
               : "=&v"(xr[0]), "=&v"(xr[1]), "=&v"(xr[2]), "=&v"(xr[3]), "=&v"(xr[4]), "=&v"(ep), "=&v"(bv)
               : "v"(vx), "s"(rx), "s"(re), "s"(rb), "v"(vb) : "memory");
}
template <int N>
DEVI void bar_vm1(int flag) {
  asm volatile("s_waitcnt vmcnt(%2)\n\ts_cmp_lg_u32 %0, 0\n\ts_cbranch_scc1 .La%=\n\ts_waitcnt vmcnt(%1)\n.La%=:\n\ts_barrier" ::"s"(flag), "i"(N), "i"(N + 1) : "memory", "scc");
}
DEVI void spin64(u64& v) { asm volatile("" : "+s"(v)); }
template <class T> DEVI void pin(T& v) { asm volatile("" : "+v"(v)); }
DEVI void dsw_col0(int a0, f32x4 d0, int a1, f32x4 d1, int a2, f32x4 d2) {
  asm volatile("s_mov_b64 exec, %6\n\tds_write_b128 %0, %1\n\tds_write_b128 %2, %3\n\tds_write_b128 %4, %5\n\ts_mov_b64 exec, -1"
               ::"v"(a0), "v"(d0), "v"(a1), "v"(d1), "v"(a2), "v"(d2), "s"(0x0001000100010001ull) : "memory");
}
DEVI void reload_sc1(u32x4& d, u32x4 r, int v, int s0) { asm volatile("buffer_load_dwordx4 %0, %1, %2, %3 offen sc1" : "+v"(d) : "v"(v), "s"(r), "s"(s0) : "memory"); }
template <int OFF> DEVI void redstr(u32x2& d, int addr) { asm volatile("ds_read_b64_tr_b16 %0, %1 offset:%2" : "+v"(d) : "v"(addr), "i"(OFF)); }
DEVI void vinc(int& d, int s) { asm volatile("v_add_u32 %0, %1, %0" : "+v"(d) : "s"(s)); }
template <class F, int... I> DEVI void sfor_(F f, std::integer_sequence<int, I...>) { (f(std::integral_constant<int, I>{}), ...); }
template <int N, class F> DEVI void sfor(F f) { sfor_(f, std::make_integer_sequence<int, N>{}); }
DEVI void touch_sc1(u32x4 r, int v) {
  asm volatile("s_mov_b32 m0, %2\n\ts_nop 0\n\tbuffer_load_dword %0, %1, 0 offen sc1 lds" ::"v"(v), "s"(r), "i"(LDS_TOTAL - 256) : "memory");
}
DEVI void touch(u32x4 r, int v) {
  asm volatile("s_mov_b32 m0, %2\n\ts_nop 0\n\tbuffer_load_dword %0, %1, 0 offen lds" ::"v"(v), "s"(r), "i"(LDS_TOTAL - 256) : "memory");
}
DEVI void bload(u32x4& d, u32x4 r, int v, int s0) { asm volatile("buffer_load_dwordx4 %0, %1, %2, %3 offen" : "=v"(d) : "v"(v), "s"(r), "s"(s0)); }
DEVI void bload_sc1(u32x4& d, u32x4 r, int v, int s0) { asm volatile("buffer_load_dwordx4 %0, %1, %2, %3 offen sc1" : "=v"(d) : "v"(v), "s"(r), "s"(s0) : "memory"); }
DEVI void arrive(int k) {
  asm volatile("s_mov_b64 exec, 1\n\tds_add_u32 %0, %1\n\ts_mov_b64 exec, -1" ::"v"(LDS_CNT + 4 * k), "v"(1) : "memory");
}
template <int SLEEP, unsigned N>
DEVI void await(int k) {
  unsigned v, t;
  if (SLEEP)
    asm volatile("s_branch .Ly%=\n.Lx%=:\n\ts_sleep 1\n.Ly%=:\n\tds_read_b32 %0, %2\n\ts_waitcnt lgkmcnt(0)\n\tv_readfirstlane_b32 %1, %0\n\ts_cmp_ge_u32 %1, %3\n\ts_cbranch_scc0 .Lx%="
                 : "=&v"(v), "=&s"(t) : "v"(LDS_CNT + 4 * k), "i"(N) : "memory", "scc");
  else
    asm volatile(".Lx%=:\n\tds_read_b32 %0, %2\n\ts_waitcnt lgkmcnt(0)\n\tv_readfirstlane_b32 %1, %0\n\ts_cmp_ge_u32 %1, %3\n\ts_cbranch_scc0 .Lx%="
                 : "=&v"(v), "=&s"(t) : "v"(LDS_CNT + 4 * k), "i"(N) : "memory", "scc");
}
DEVI void publish(int cnt, unsigned last, u32x4 r, int voff, u32x2 fv) {
  unsigned v, t;
  asm volatile("s_waitcnt vmcnt(0)\n\ts_mov_b64 exec, 1\n\tds_add_rtn_u32 %0, %2, %3\n\ts_waitcnt lgkmcnt(0)\n\tv_readfirstlane_b32 %1, %0\n\t"
               "s_cmp_lg_u32 %1, %4\n\ts_cbranch_scc1 .Lp%=\n\ts_mov_b64 exec, 0x7ff\n\tbuffer_store_dwordx2 %7, %6, %5, 0 offen sc1\n.Lp%=:\n\ts_mov_b64 exec, -1"
               : "=&v"(v), "=&s"(t) : "v"(cnt), "v"(1), "s"(last), "s"(r), "v"(voff), "v"(fv) : "memory", "scc");
}
DEVI void post(int addr, int val) { asm volatile("ds_write_b32 %0, %1" ::"v"(addr), "v"(val) : "memory"); }
DEVI void peek(unsigned& v, int addr) { asm volatile("ds_read_b32 %0, %1" : "=v"(v) : "v"(addr) : "memory"); }
DEVI void require(unsigned v, int addr, unsigned n) {
  unsigned t;
  asm volatile("v_readfirstlane_b32 %1, %0\n\ts_cmp_ge_u32 %1, %3\n\ts_cbranch_scc1 .Lq%=\n"
               ".Lw%=:\n\tds_read_b32 %0, %2\n\ts_waitcnt lgkmcnt(0)\n\tv_readfirstlane_b32 %1, %0\n\ts_cmp_ge_u32 %1, %3\n\ts_cbranch_scc0 .Lw%=\n.Lq%=:"
               : "+&v"(v), "=&s"(t) : "v"(addr), "s"(n) : "memory", "scc");
}
DEVI void await_pair(int addr, unsigned n) {
  for (;;) {
    u32x2 v;
    asm volatile("ds_read_b64 %0, %1\n\ts_waitcnt lgkmcnt(0)" : "=v"(v) : "v"(addr) : "memory");
    const unsigned a = v[0], b = v[1];
    if (__builtin_amdgcn_readfirstlane(a < b ? a : b) >= n) break;
    __builtin_amdgcn_s_sleep(1);
  }
}
DEVI int lane_id() { int l; asm volatile("v_mbcnt_lo_u32_b32 %0, -1, 0\n\tv_mbcnt_hi_u32_b32 %0, -1, %0" : "=v"(l)); return l; }
DEVI int sig(int i) { return 4 * (i & 3) + (i >> 2); }
DEVI u64 flagval(unsigned tag) { return ((u64)(tag ^ 0x5bd1e995u) << 32) | tag; }

DEVI float xadd16(float v) { auto r = __builtin_amdgcn_permlane16_swap(asu(v), asu(v), false, false); return asf(r[0]) + asf(r[1]); }
template <int CTRL> DEVI float dpp(float v) { return asf(__builtin_amdgcn_mov_dpp(asu(v), CTRL, 0xf, 0xf, true)); }
DEVI float half_sum(float v) {
  v += dpp<0xb1>(v); v += dpp<0x4e>(v); v += dpp<0x141>(v); v += dpp<0x140>(v);
  return xadd16(v);
}

DEVI int h_slot(int j, int l) { const int t = l < 4 ? 4 * j + l : 28 + 3 * j + l, d = (t * 43) >> 7; return 18 * (t - 3 * d) + d; }

DEVI int row_token(int p_, int pos) {
  const int tau = pos >> 4, i = pos & 15;
  int tok = tau == 0 ? p_ * 128 - 16 + 8 * (i & 1) + 2 * (i >> 2) + ((i & 3) < 2) : p_ * 128 + 32 * ((tau - 1) >> 1) + 2 * sig(i) + ((tau - 1) & 1);
  return tok < 0 ? 0 : tok;
}

struct ColGeo { int A, B, C, ct; };
DEVI ColGeo colgeo(bool att, int nw, int mw, int c, bool heavy) {
  const int cc = heavy ? (c ^ (2 * mw)) : c;
  const int ct = 4 * nw + cc, sec = (ct * 13) >> 6, hh = ct - 5 * sec;
  const bool q4 = hh == 4, sp = !heavy && c == 2;
  ColGeo r;
  r.A = att ? sec * 160 + (q4 ? 32 : 40 * hh) : heavy ? 128 * nw + 32 * cc : (sp ? 352 + 16 * nw : 128 + 64 * nw + 32 * c);
  r.B = att && q4 ? 40 : 8;
  r.C = att ? (q4 ? 80 : 16) : (sp ? 32 : 16);
  r.ct = ct;
  return r;
}

template <int NC>
struct Frags { u32x4 x[5]; u32x2 w[NC][2]; };

template <int N> DEVI void wait_lg() { asm volatile("s_waitcnt lgkmcnt(%0)" ::"i"(N) : "memory"); }

template <int NC, int K>
DEVI void s1_rd(Frags<NC>& n, int xo, int xh, const int (&wa)[NC]) {
  if constexpr (K == 0) ds128<0>(n.x[0], xo);
  else if constexpr (K <= 2 * NC) { if ((K - 1) & 1) dstr<7680>(n.w[(K - 1) >> 1][1], wa[(K - 1) >> 1]); else dstr<0>(n.w[(K - 1) >> 1][0], wa[(K - 1) >> 1]); }
  else if constexpr (K < 2 * NC + 4) ds128<(K - 2 * NC) * 2048>(n.x[K - 2 * NC], xo);
  else if constexpr (K == 2 * NC + 4) ds128<0>(n.x[4], xh);
}
template <int NC, int NH, int K>
DEVI void s1_mm(const Frags<NC>& c, f32x4 (&acc)[4][NC], f32x4 (&hacc)[NH]) {
  if constexpr (K < 4 * NC) mfma_a(acc[K / NC][K % NC], cat(c.w[K % NC][0], c.w[K % NC][1]), c.x[K / NC]);
  else mfma_a(hacc[K - 4 * NC], cat(c.w[K - 4 * NC][0], c.w[K - 4 * NC][1]), c.x[4]);
}
template <int NC, int NH, bool BAR, bool HX>
DEVI void s1_step(const Frags<NC>& c, Frags<NC>& n, int xo, int xh, const int (&wa)[NC], f32x4 (&acc)[4][NC], f32x4 (&hacc)[NH]) {
  constexpr int NM = 4 * NC + NH, NR = 2 * NC + 4 + HX, D = BAR ? NM - NR : 0;
  sfor<NM>([&](auto KK) {
    constexpr int k = decltype(KK)::value;
    constexpr int need = k < NC ? 2 * k + 2 : (k % NC == 0 && k < 4 * NC + HX) ? 2 * NC + k / NC : -1;
    if constexpr (BAR && k == D) bar_lgkm();
    if constexpr (need >= 0 && (!BAR || k < D)) wait_lg<NR - 1 - need + (BAR ? 0 : (k < NR ? k : NR))>();
    s1_mm<NC, NH, k>(c, acc, hacc);
    if constexpr (k >= D && k - D < NR) s1_rd<NC, k - D>(n, xo, xh, wa);
  });
}

template <int OFF> DEVI void dsw64(int addr, u32x2 d) { asm volatile("ds_write_b64 %0, %1 offset:%2" ::"v"(addr), "v"(d), "i"(OFF) : "memory"); }
template <int OFF> DEVI void ds64(u32x2& d, int addr) { asm volatile("ds_read_b64 %0, %1 offset:%2" : "=v"(d) : "v"(addr), "i"(OFF)); }

template <int NC, bool att>
DEVI void s1_compute(lptr shm, int w, int lane, int p_, int j_, int hf, char* ws PR_ARGS) {
  constexpr int NH = NC == 4 ? 2 : 3;
  const int mw = w & 1, nw = w >> 1, i = lane & 15, g = lane >> 4;
  f32x4 acc[4][NC], hacc[NH];
#pragma unroll
  for (int m = 0; m < 4; m++)
#pragma unroll
    for (int c = 0; c < NC; c++) { acc[m][c] = f32x4{0, 0, 0, 0}; asm volatile("" : "+v"(acc[m][c])); }
#pragma unroll
  for (int c = 0; c < NH; c++) { hacc[c] = f32x4{0, 0, 0, 0}; asm volatile("" : "+v"(hacc[c])); }
  int wb[NC], cb[NC], ct[NC];
#pragma unroll
  for (int c = 0; c < NC; c++) {
    const ColGeo cg = colgeo(att, nw, mw, c, NC == 4);
    wb[c] = (lane >> 2) * 480 + cg.A + cg.B * (lane & 1) + cg.C * ((lane >> 1) & 1);
    cb[c] = LDS_CS + 2 * (cg.A + cg.B * (g & 1) + cg.C * (g >> 1));
    ct[c] = cg.ct;
    asm volatile("" : "+v"(cb[c]), "+v"(wb[c]));
  }
  int xb = XOFF + i * 128 + ((g ^ (i >> 1) ^ (4 * hf)) << 4);
  int sta = LDS_ST + i * 8;
  asm volatile("" : "+v"(xb), "+v"(sta));
  const int own = (1 + 4 * mw) * 2048;
  Frags<NC> fa, fb;
  if (!att) {
    const unsigned one = i == 0 ? 0x3f803f80u : 0u;
    fa.x[4] = u32x4{one, one, one, one}; pin(fa.x[4]); fb.x[4] = fa.x[4];
  }
  int wa[NC];
  STAMPB(12);
  bar_lgkm();
  STAMP(1);
  sfor<2 * NC + 4 + att>([&](auto K) { s1_rd<NC, decltype(K)::value>(fa, xb + own, xb, wb); });
#pragma unroll
  for (int c = 0; c < NC; c++) wa[c] = wb[c] + WSLAB;
  s1_step<NC, NH, true, att>(fa, fb, (xb ^ 64) + own, xb ^ 64, wa, acc, hacc);
  int sl = 0;
#pragma nounroll
  for (int T = 1; T < 10; T++) {
    sl = sl == 2 * SLOT ? 0 : sl + SLOT;
#pragma unroll
    for (int c = 0; c < NC; c++) wa[c] = wb[c] + sl;
    s1_step<NC, NH, true, att>(fb, fa, xb + sl + own, xb + sl, wa, acc, hacc);
#pragma unroll
    for (int c = 0; c < NC; c++) wa[c] = wb[c] + sl + WSLAB;
    s1_step<NC, NH, false, att>(fa, fb, (xb ^ 64) + sl + own, (xb ^ 64) + sl, wa, acc, hacc);
  }
  wait_lg<0>();
  arrive(2);
  sfor<NH>([&](auto K) { s1_mm<NC, NH, 4 * NC + decltype(K)::value>(fb, acc, hacc); });
  u32x2 stu[5];
  u32x4 b1u[NC], csu[NC];
  sfor<4 * NC>([&](auto KK) {
    constexpr int k = decltype(KK)::value;
    s1_mm<NC, NH, k>(fb, acc, hacc);
    if constexpr (k < 4) ds64<(1 + k) * 128>(stu[k], sta + mw * 512);
    if constexpr (k == 4 && att) ds64<0>(stu[4], sta);
    if constexpr (k == 5) {
      dsw_col0(cb[0], hacc[0], cb[1], hacc[1], cb[NH - 1], hacc[NH - 1]);
      if (NC == 4) arrive(8 + nw);
    }
    if constexpr (k >= 6 && k < 6 + NC && !att) ds128<LDS_B1 - LDS_CS>(b1u[k - 6], cb[k - 6]);
  });
  STAMP(2);
  if (NC == 4) await<0, 2>(8 + nw);
#pragma unroll
  for (int c = 0; c < NC; c++) ds128<0>(csu[c], cb[c]);
  asm volatile("s_waitcnt lgkmcnt(0)\n\ts_nop 7" ::: "memory");
  f32x2 st[5];
  f32x4 cs[NC], b1[NC];
#pragma unroll
  for (int m = 0; m < 4 + att; m++) { asm volatile("" : "+v"(stu[m])); st[m] = __builtin_bit_cast(f32x2, stu[m]); }
#pragma unroll
  for (int c = 0; c < NC; c++) {
    pin(csu[c]); cs[c] = __builtin_bit_cast(f32x4, csu[c]);
    if (!att) { pin(b1u[c]); b1[c] = __builtin_bit_cast(f32x4, b1u[c]); }
  }
#pragma unroll
  for (int m = 0; m < 4; m++)
#pragma unroll
    for (int c = 0; c < NC; c++) pin(acc[m][c]);
#pragma unroll
  for (int c = 0; c < NH; c++) pin(hacc[c]);
  STAMPA(12);
  const int l2 = lane_id();
  if (!att) {
    const u32x4 ra = mkr(ws + WS_A2, A2_BYTES);
    const int base = (8 * p_ + 4 * mw) * KS_T * 1024;
    const int o0 = base + (NC == 4 || j_ < 7 || nw == 2 ? h_slot(j_, NC == 4 ? 2 * nw + mw : 2 + nw) * 1024 : 0x70000000);
    const int o1 = base + (NC == 4 ? h_slot(j_, 2 * nw + (mw ^ 1)) * 1024 : j_ < 7 ? h_slot(j_, 6) * 1024 + (nw - 2) * 512 : 0x70000000);
    const float c1 = -2.f * 0.7978845608028654f * 1.4426950408889634f, c2 = c1 * 0.044715f;
    unsigned sp[2];
#pragma unroll
    for (int m = 0; m < 4; m++) {
      f32x4 v[NC], t[NC];
      SB;
#pragma unroll
      for (int c = 0; c < NC; c++) t[c] = cs[c] * st[m][1] + b1[c];
      SB;
#pragma unroll
      for (int c = 0; c < NC; c++) v[c] = acc[m][c] * st[m][0] + t[c];
      SB;
#pragma unroll
      for (int c = 0; c < NC; c++) t[c] = v[c] * v[c];
      SB;
#pragma unroll
      for (int c = 0; c < NC; c++) t[c] = t[c] * c2 + c1;
      SB;
#pragma unroll
      for (int c = 0; c < NC; c++) t[c] = t[c] * v[c];
      SB;
#pragma unroll
      for (int c = 0; c < NC; c++)
#pragma unroll
        for (int e = 0; e < 4; e++) t[c][e] = __builtin_amdgcn_exp2f(t[c][e]);
      SB;
#pragma unroll
      for (int c = 0; c < NC; c++) t[c] = t[c] + 1.f;
      SB;
#pragma unroll
      for (int c = 0; c < NC; c++)
#pragma unroll
        for (int e = 0; e < 4; e++) t[c][e] = __builtin_amdgcn_rcpf(t[c][e]);
      SB;
#pragma unroll
      for (int c = 0; c < NC; c++) v[c] = v[c] * t[c];
      SB;
      unsigned o[NC][2];
#pragma unroll
      for (int c = 0; c < NC; c++) { o[c][0] = pk2(v[c][0], v[c][1]); o[c][1] = pk2(v[c][2], v[c][3]); }
      st_wt(ra, l2 * 16, o0 + m * KS_T * 1024, u32x4{o[0][0], o[0][1], o[1][0], o[1][1]});
      if (NC == 4) st_wt(ra, l2 * 16, o1 + m * KS_T * 1024, u32x4{o[2][0], o[2][1], o[NC - 1][0], o[NC - 1][1]});
      else if (m & 1) {
        auto s0 = __builtin_amdgcn_permlane32_swap(sp[0], o[2][0], false, false);
        auto s1 = __builtin_amdgcn_permlane32_swap(sp[1], o[2][1], false, false);
        st_wt(ra, (l2 >> 5) * (KS_T * 1024) + (l2 & 31) * 16, o1 + (m - 1) * KS_T * 1024, u32x4{s0[0], s1[0], s0[1], s1[1]});
      } else { sp[0] = o[2][0]; sp[1] = o[2][1]; }
    }
  } else {
    const int g2 = l2 >> 4, piece = LDS_TB + ((l2 & 48) + ((l2 & 15) ^ (8 * (g2 >> 1)))) * 8;
    sfor<NC>([&](auto C) {
      constexpr int c = decltype(C)::value;
      const int ah = piece + ct[c] * 4608, ao = ah + (1 + 4 * mw) * 512;
      u32x2 o[5];
      SB;
#pragma unroll
      for (int m = 0; m < (c < NH ? 5 : 4); m++) {
        f32x4 a = m < 4 ? acc[m < 4 ? m : 0][c] : hacc[c < NH ? c : 0];
        f32x4 v = a * st[m][0] + cs[c] * st[m][1];
        o[m] = u32x2{pk2(v[0], v[1]), pk2(v[2], v[3])};
      }
      dsw64<0>(ao, o[0]); dsw64<512>(ao, o[1]); dsw64<1024>(ao, o[2]); dsw64<1536>(ao, o[3]);
      if (c < NH) dsw64<0>(ah, o[4]);
    });
  }
}

template <bool MAX> DEVI float red4(const float (&v)[4]) {
  auto op = [](unsigned a, unsigned b) { float r; if (MAX) asm("v_max_f32 %0, %1, %2\n\ts_nop 1" : "=v"(r) : "v"(a), "v"(b)); else r = asf(a) + asf(b); return r; };
  auto a = __builtin_amdgcn_permlane32_swap(asu(v[0]), asu(v[1]), false, false);
  auto b = __builtin_amdgcn_permlane32_swap(asu(v[2]), asu(v[3]), false, false);
  auto c = __builtin_amdgcn_permlane16_swap(asu(op(a[0], a[1])), asu(op(b[0], b[1])), false, false);
  return op(c[0], c[1]);
}
DEVI void bcast4(float r, float (&v)[4]) {
  auto e = __builtin_amdgcn_permlane16_swap(asu(r), asu(r), false, false);
  unsigned e0 = e[0], e1 = e[1];
  auto a = __builtin_amdgcn_permlane32_swap(e0, e0, false, false);
  auto b = __builtin_amdgcn_permlane32_swap(e1, e1, false, false);
  v[0] = asf(a[0]); v[1] = asf(a[1]); v[2] = asf(b[0]); v[3] = asf(b[1]);
}

DEVI void att_table(lptr shm, int lane) {
  const int i = lane & 15, g = lane >> 4, p = lane & 3;
  const int d = sig(i) - g;
  const unsigned NEG = 0xf149f2cau;
  bool vo[4], vp[4];
#pragma unroll
  for (int r = 0; r < 4; r++) { vo[r] = (unsigned)(d - 4 * r) <= 7u; vp[r] = d + 16 - 4 * r <= 7; }
  auto pm = [](bool lo, bool hi) { return (lo ? 0xffffu : 0u) | (hi ? 0xffff0000u : 0u); };
  LAS u32x4* t = (LAS u32x4*)(shm + LDS_TAB) + lane;
  t[0] = u32x4{vo[0] ? 0u : NEG, vo[1] ? 0u : NEG, vo[2] ? 0u : NEG, vo[3] ? 0u : NEG};
  t[64] = u32x4{pm(vp[2], vp[3]), pm(vo[0], vo[1]), vp[2] ? 0u : NEG, vp[3] ? 0u : NEG};
  t[128] = u32x4{pm(vo[2], vo[3]), (unsigned)(LDS_TB + (g * 16 + (i ^ (8 * (g >> 1)))) * 8), (unsigned)(LDS_TB + (p * 16 + ((lane >> 2) ^ (8 * (p >> 1)))) * 8), 0u};
}
DEVI int sel(u64 mask, int a, int b) { int r; asm("v_cndmask_b32_e64 %0, %2, %1, %3" : "=v"(r) : "v"(a), "v"(b), "s"(mask)); return r; }

DEVI void s1_attention(lptr shm, int w, int p_, int j_, char* ws PR_ARGS) {
  arrive(3);
  const int lane = lane_id();
  const int tau = (w + 1) * 512, taup = w >= 2 ? tau - 1024 : 0, flip = w == 1 ? 16 : 0;
  const LAS u32x4* t = (const LAS u32x4*)(shm + LDS_TAB) + lane;
  const u32x4 t0 = t[0], t2 = t[128];
  u32x4 t1 = t[64];
  if (p_ == 0 && w < 2) { t1[0] = 0u; t1[2] = 0xf149f2cau; t1[3] = 0xf149f2cau; }
  const f32x4 mO = __builtin_bit_cast(f32x4, t0), mP = __builtin_bit_cast(f32x4, t1);
  const unsigned M1 = t1[0], M2 = t1[1], M3 = t2[0];
  const int po = t2[1] + tau, pp = (t2[1] ^ flip) + taup, to = t2[2] + tau, tp = (t2[2] ^ flip) + taup;
  int aq[4], avo[4], avp[4];
#pragma unroll
  for (int h = 0; h < 4; h++) {
    aq[h] = sel(0xffffull << (16 * h), po, LDS_ZERO - 4 * 4608);
    avo[h] = sel(0x1111111111111111ull << h, to, LDS_ZERO - 14 * 4608);
    avp[h] = sel(0x1111111111111111ull << h, tp, LDS_ZERO - 14 * 4608);
    asm volatile("" : "+v"(aq[h]), "+v"(avo[h]), "+v"(avp[h]));
  }
  STAMPA(14);
  await<0, 8>(3);
  STAMPA(15);
  u32x2 qa[4][2], ko[4][2], kp[4][2], va[4][2], vb[4][2];
  f32x4 sp[4], so[4];
  auto rd = [&](auto H) {
    constexpr int h = decltype(H)::value;
    ds64<(5 + h) * 4608>(ko[h][0], po); ds64<9 * 4608>(ko[h][1], po); ds64<h * 4608>(qa[h][0], po); ds64<4 * 4608>(qa[h][1], aq[h]);
    ds64<(5 + h) * 4608>(kp[h][0], pp); ds64<9 * 4608>(kp[h][1], pp);
  };
  auto rv = [&](auto H) {
    constexpr int h = decltype(H)::value;
    dstr<(10 + h) * 4608>(va[h][0], tp); dstr<(10 + h) * 4608>(va[h][1], to); dstr<14 * 4608>(vb[h][0], avp[h]); dstr<14 * 4608>(vb[h][1], avo[h]);
  };
  auto qk = [&](auto H, auto N) {
    constexpr int h = decltype(H)::value;
    asm volatile("s_waitcnt lgkmcnt(%6)" : "+v"(ko[h][0]), "+v"(ko[h][1]), "+v"(qa[h][0]), "+v"(qa[h][1]), "+v"(kp[h][0]), "+v"(kp[h][1]) : "i"(decltype(N)::value));
    so[h] = mfma(cat(ko[h][0], ko[h][1]), cat(qa[h][0], qa[h][1]), mO);
    sp[h] = mfma(cat(kp[h][0], kp[h][1]), cat(qa[h][0], qa[h][1]), mP);
  };
  rd(IC(0)); rd(IC(1));
  qk(IC(0), IC(6)); rd(IC(2));
  qk(IC(1), IC(6)); rd(IC(3));
  qk(IC(2), IC(6)); rv(IC(0)); rv(IC(1));
  qk(IC(3), IC(8)); rv(IC(2));
  asm volatile("s_waitcnt lgkmcnt(8)");
  rv(IC(3));
  const float qs = 0.22360679774997896f * 1.4426950408889634f;
  float ee[4], eo[4], mx[4], l[4];
#pragma unroll
  for (int h = 0; h < 4; h++) { ee[h] = fmaxf(fmaxf(sp[h][2], so[h][0]), so[h][2]); eo[h] = fmaxf(fmaxf(sp[h][3], so[h][1]), so[h][3]); mx[h] = fmaxf(ee[h], eo[h]); }
  bcast4(red4<true>(mx) * -qs, mx);
#pragma unroll
  for (int h = 0; h < 4; h++) {
    f32x2 t = f32x2{ee[h], eo[h]} * qs + mx[h];
    ee[h] = __builtin_amdgcn_exp2f(t[0]); eo[h] = __builtin_amdgcn_exp2f(t[1]); l[h] = ee[h] + eo[h];
  }
  bcast4(__builtin_amdgcn_rcpf(red4<false>(l)), l);
  asm volatile("s_waitcnt lgkmcnt(0)" : "+v"(va[0][0]), "+v"(va[0][1]), "+v"(va[1][0]), "+v"(va[1][1]), "+v"(va[2][0]), "+v"(va[2][1]), "+v"(va[3][0]), "+v"(va[3][1]),
               "+v"(vb[0][0]), "+v"(vb[0][1]), "+v"(vb[1][0]), "+v"(vb[1][1]), "+v"(vb[2][0]), "+v"(vb[2][1]), "+v"(vb[3][0]), "+v"(vb[3][1]));
  f32x4 o4 = {0, 0, 0, 0}, oh[4];
#pragma unroll
  for (int h = 0; h < 4; h++) {
    const unsigned pk = pk2(f32x2{ee[h], eo[h]} * l[h]);
    u32x4 pf = {0u, pk & M1, pk & M2, pk & M3};
    o4 = mfma(cat(vb[h][0], vb[h][1]), pf, o4);
    oh[h] = mfma(cat(va[h][0], va[h][1]), pf, f32x4{0, 0, 0, 0});
  }
  const u32x4 ra = mkr(ws + WS_A2, A2_BYTES);
  const int base = ((8 * p_ + w) * KS_T + 54 + 5 * (j_ >> 1)) * 1024;
#pragma unroll
  for (int hp = 0; hp < 2; hp++)
    st_wt(ra, lane * 16, base + ((j_ & 1) * 3 + hp) * 1024,
          u32x4{pk2(oh[2 * hp][0], oh[2 * hp][1]), pk2(oh[2 * hp][2], oh[2 * hp][3]), pk2(oh[2 * hp + 1][0], oh[2 * hp + 1][1]), pk2(oh[2 * hp + 1][2], oh[2 * hp + 1][3])});
  unsigned a0 = pk2(o4[0], o4[1]), a1 = pk2(o4[2], o4[3]);
  auto s0 = __builtin_amdgcn_permlane32_swap(a0, a0, false, false);
  auto s1 = __builtin_amdgcn_permlane32_swap(a1, a1, false, false);
  st_wt(ra, lane < 32 ? lane * 16 : 0x70000000, base + 2 * 1024 + (j_ & 1) * 512, u32x4{s0[0], s1[0], s0[1], s1[1]});
  arrive(4);
}

DEVI int s2_perm(int s, int rot) { return s < 8 ? s ^ rot : (s < 10 || s >= 18) ? s ^ (rot & 1) : ((s - 10) ^ rot) + 10; }
DEVI int fin_off(int G0, int n_, int r0, int lane) {
  const int frow = r0 + (lane >> 3), fG = G0 + (frow >> 4);
  return (32 * (fG >> 1) + 2 * sig(frow & 15) + (fG & 1)) * 1280 + n_ * 128 + (lane & 7) * 16;
}
constexpr int s2_slot(int r) { return r < 18 ? (r % 9) * 16384 : 147456 + (r - 18) * 4096; }
DEVI void watch(char* ws, int Ga, int Gb, int n_, int kind, int word, u64 fv) {
  const int lane = lane_id();
  const int off = WS_BOX + ((((lane & 8) ? Gb : Ga) >> 3) * 10 + n_) * 256 + (8 * kind + (lane & 7)) * 8;
  unsigned n = SPIN_CAP;
  u32x2 d;
  asm volatile(".Lg%=:\n\tbuffer_load_dwordx2 %0, %2, %3, 0 offen sc1\n\ts_sub_u32 %1, %1, 1\n\ts_waitcnt vmcnt(0)\n\ts_cbranch_scc1 .Lh%=\n\t"
               "v_cmp_ne_u64 vcc, %4, %0\n\ts_cbranch_vccnz .Lg%=\n.Lh%=:\n\tds_write_b32 %5, %6"
               : "=&v"(d), "+&s"(n) : "v"(off), "s"(mkr(ws, WS_A2)), "s"(fv), "v"(LDS_CNT + 4 * word), "v"(1) : "memory", "scc", "vcc");
}

template <int NS, int MT>
DEVI void s2_compute(int w, int kq, int G0, int n_, int rot, char* ws, u64 fv PR_ARGS) {
  constexpr int AD = (MT == 3 ? 3 : 4) + (NS == 20);
  const int mh = w >> 2, lane = lane_id();
  const u32x4 ra = mkr(ws + WS_A2, A2_BYTES);
  int av = lane * 16;
  pin(av);
  const int ao = ((G0 + MT * mh) * KS_T + (NS == 20 ? 54 : 18 * (kq - 1))) * 1024;
  int bs[4];
#pragma unroll
  for (int c = 0; c < 4; c++) bs[c] = kq * 4096 + (lane >> 2) * 128 + ((c ^ ((lane >> 3) & 3)) << 5) + 8 * (lane & 3);
  f32x4 acc[MT][4];
#pragma unroll
  for (int m = 0; m < MT; m++)
#pragma unroll
    for (int c = 0; c < 4; c++) { acc[m][c] = f32x4{0, 0, 0, 0}; pin(acc[m][c]); }
  u32x4 a[AD][MT];
  u32x2 b[3][4][2];
  int ca = LDS_CONS + kq * 8 + mh * 4, pa = LDS_PROD + kq * 4;
  pin(ca); pin(pa);
  unsigned pv, lv;
  auto rdb = [&](auto R, auto K) {
    constexpr int r = decltype(R)::value, k = decltype(K)::value;
    constexpr int off = s2_slot(r), nxt = s2_slot(r + 1) / 65536 - off / 65536;
    if constexpr (r < 3) dstr<off % 65536 + (k & 1) * 2048>(b[r % 3][k >> 1][k & 1], bs[k >> 1]);
    else redstr<off % 65536 + (k & 1) * 2048>(b[r % 3][k >> 1][k & 1], bs[k >> 1]);
    if constexpr (nxt != 0 && (k & 1)) vinc(bs[k >> 1], nxt * 65536);
  };
  auto lda = [&](auto S, int m) {
    constexpr int s = decltype(S)::value;
    const int o = ao + m * KS_T * 1024 + (s2_perm(s, rot) << 10);
    if constexpr (s < AD) bload_sc1(a[s % AD][m], ra, av, o); else reload_sc1(a[s % AD][m], ra, av, o);
  };
  auto ldb = [&]() {
    peek(pv, pa); wait_lg<0>(); require(pv, pa, 1);
    sfor<3>([&](auto R) { sfor<8>([&](auto K) { rdb(R, K); }); wait_lg<(decltype(R)::value < 2 ? 4 : 0)>(); });
    post(ca, 1);
  };
  constexpr bool BFIRST = NS == 20 || MT == 3;
  if (BFIRST) ldb();
  STAMP(6);
  if (NS == 20) watch(ws, G0, G0 + 2 * MT - 1, n_, w == 0 ? 0 : 2, w == 0 ? 14 : 5, fv);
  await<0, 1>(NS == 20 ? 14 : 15);
  STAMP(7);
  sfor<AD - 1>([&](auto S) {
#pragma unroll
    for (int m = 0; m < MT; m++) lda(S, m);
  });
  if (!BFIRST) ldb();
  sfor<NS - 3>([&](auto S) {
    constexpr int s = decltype(S)::value;
    constexpr bool LA = s + AD - 1 < NS;
    constexpr int later = NS - 1 - s < AD - 2 ? NS - 1 - s : AD - 2;
    if constexpr (s == 3) require(pv, pa, 2);
    if constexpr (s == 7) require(pv, pa, 3);
    if constexpr (s == 10) require(pv, pa, 4);
    if constexpr (s == 13) require(pv, pa, 5);
    if constexpr (s == 2 || s == 6 || s == 9 || s == 12) peek(pv, pa);
    if constexpr (NS == 18 && s == 11 - AD) require(lv, LDS_CNT + 4 * 5, 1);
    if constexpr (NS == 18 && s == 10 - AD) peek(lv, LDS_CNT + 4 * 5);
    if constexpr (s == NS - 4) peek(lv, ca ^ 4);
    sfor<MT>([&](auto M) {
      constexpr int m = decltype(M)::value;
      if constexpr (LA) lda(IC(s + AD - 1), m);
      wait_vm<MT * later + MT - 1 - m + (LA ? m + 1 : 0)>();
      if constexpr (m == 1 && s >= 2) wait_lg<4>();
      sfor<4>([&](auto C) {
        constexpr int c = decltype(C)::value;
        mfma_a(acc[m][c], cat(b[s % 3][c][0], b[s % 3][c][1]), a[s % AD][m]);
        if constexpr (s >= 1 && m < 2) rdb(IC(s + 2), IC(4 * m + c));
      });
    });
    if constexpr (s == 3 || s == 6 || s == 9 || s == 12) post(ca, s);
  });
  wait_lg<4>();
  sfor<8>([&](auto K) { rdb(IC(NS - 1), K); });
  if constexpr (AD == 3) {
#pragma unroll
    for (int m = 0; m < MT; m++) lda(IC(NS - 1), m);
  }
  int da[4];
#pragma unroll
  for (int c = 0; c < 4; c++) da[c] = mh * 49152 + kq * 4096 + (av & 0xf0) * 16 + ((((4 * c) << 4) + (av >> 8 << 4)) ^ (av & 0x70));
  require(lv, ca ^ 4, mh ? 12 : 9);
  sfor<MT>([&](auto M) {
    constexpr int m = decltype(M)::value;
    sfor<3>([&](auto U) {
      constexpr int u = decltype(U)::value, s = NS - 3 + u;
      if constexpr (m == 0) wait_vm<MT * (2 - u) + MT - 1>();
      if constexpr (m > 0 && u == 2) wait_vm<MT - 1 - m>();
      if constexpr (m == 0 && u == 1) wait_lg<8>();
      if constexpr (m == 0 && u == 2) wait_lg<0>();
      sfor<4>([&](auto C) {
        constexpr int c = decltype(C)::value;
        mfma_a(acc[m][c], cat(b[s % 3][c][0], b[s % 3][c][1]), a[s % AD][m]);
        if constexpr (m > 0 && u > 0 && (c & 1)) dsw128<(m - 1) * 16384>(da[2 * (u - 1) + (c >> 1)], __builtin_bit_cast(u32x4, acc[m - 1][2 * (u - 1) + (c >> 1)]));
      });
    });
  });
  STAMP(9);
  asm volatile("s_nop 4");
#pragma unroll
  for (int c = 0; c < 4; c++) dsw128<(MT - 1) * 16384>(da[c], __builtin_bit_cast(u32x4, acc[MT - 1][c]));
  arrive((NS == 20 ? 28 : 12) + mh);
}

typedef __attribute__((ext_vector_type(8))) u64 u64x8;
__global__ __launch_bounds__(768, 3) void fused_block(const void*, const void*, const void*, const void*, const void*, const void*, const void*,
                                                      void*, void*) {
  u64x8 ka; u64 ka8, pc;
  asm volatile("s_load_dwordx16 %0, %3, 0x0\n\ts_load_dwordx2 %1, %3, 0x40\n\ts_getpc_b64 %2"
               : "=&s"(ka), "=&s"(ka8), "=&s"(pc) : "s"(__builtin_amdgcn_kernarg_segment_ptr()));
  extern __shared__ __attribute__((aligned(16))) char shm_[];
  lptr shm = (lptr)shm_;
  const int tid = threadIdx.x, lane = tid & 63, w = __builtin_amdgcn_readfirstlane(tid >> 6);
  const int xcd = blockIdx.x & 7, idx = blockIdx.x >> 3;
  const int p_ = (xcd & 1) * 8 + (idx & 7), sub = idx >> 3;
  const bool att = !(sub & 1);
  const int j_ = (xcd >> 1) * 2 + (sub >> 1);
  const int uidx = (idx & 7) + 8 * (idx >> 4);
  const int n_ = (xcd & 1) * 5 + (int)(((att ? 0x4444433333221100ull : 0x4433222211110000ull) >> (4 * uidx)) & 15);
  const int G0 = 32 * (xcd >> 1) + 2 * (int)(((att ? 0xeca86eca86ecececull : 0x3030963096309630ull) >> (4 * uidx)) & 15);
  const int rot = (int)(((att ? 0x6547610321322110ull : 0x5403103203213210ull) >> (4 * uidx)) & ROTM);
  const int kq = (w + ((w >> 2) & 1)) & 3;
  STAMP(0);
  if (w == 7) {
    touch(mkr((const void*)(pc & ~255ull), CODE_BYTES), (idx + 32 * lane) * 128);
  }
  const int hf = idx & 1, tx = ((idx >> 1) ^ sub) & 1;
  const int rid = (att ? 0 : 8) + j_;
  if (w >= 8) __builtin_amdgcn_s_setprio(3);
  const int lw = w & 3;
  const int RS = att ? 3840 : 3456;
  const int r8 = lane >> 3, cp = lane & 7, t0 = p_ * 128;
  int xo[2], hv, wv;
#pragma unroll
  for (int e = 0; e < 2; e++)
    xo[e] = (t0 + 32 * lw + 2 * (4 * (r8 & 3) + 2 * e + (r8 >> 2))) * 1280 + ((cp ^ (4 * e + (r8 >> 1))) << 4);
  {
    const int hi = lane >= 30, ch = lane - 30 * hi, lim = att ? 63 : j_ < 7 ? 27 : 19;
    wv = hi * RS + ((ch >= 10) + (ch >= 20)) * (att ? 1120 : 0) + j_ * (att ? 160 : 448) + min(ch, lim) * 16;
    const int tok = max(t0 - 16 + 8 * (r8 & 1) + 2 * lw + ((r8 & 3) < 2), 0);
    hv = (tok * 1280 + ((cp ^ ((2 * lw + ((r8 & 3) >> 1)) & 7)) << 4));
  }
  pin(xo[0]); pin(xo[1]); pin(wv); pin(hv);
  asm volatile("s_waitcnt lgkmcnt(0)" : "+s"(ka), "+s"(ka8));
  const char *x = (const char*)ka[0], *wqkv = (const char*)ka[1], *wo = (const char*)ka[2], *w1 = (const char*)ka[3], *b1 = (const char*)ka[4],
             *w2 = (const char*)ka[5], *b2 = (const char*)ka[6];
  char *out = (char*)ka[7], *ws = (char*)ka8;
  int fu;
  if (__builtin_expect(w < 8, 0)) {
    if (tid < 24) *(LAS u32x4*)(shm + (tid >> 3) * SLOT + XOFF + (tid & 7) * 16) = u32x4{0x3f803f80u, 0x3f803f80u, 0x3f803f80u, 0x3f803f80u};
    if (tid >= 448 && tid < 480) *(LAS unsigned*)(shm + LDS_CNT + 4 * (tid - 448)) = 0u;
    if (w == 5 && att) att_table(shm, lane);
    const bool light = att ? (w >= 6) : (w >= 4);
    if (att) {
      if (light) s1_compute<3, true>(shm, w, lane, p_, j_, hf, ws PR_PASS);
      else s1_compute<4, true>(shm, w, lane, p_, j_, hf, ws PR_PASS);
    } else {
      if (light) s1_compute<3, false>(shm, w, lane, p_, j_, hf, ws PR_PASS);
      else s1_compute<4, false>(shm, w, lane, p_, j_, hf, ws PR_PASS);
    }
    STAMP(3);
    if (att) s1_attention(shm, w, p_, j_, ws PR_PASS);
    STAMP(4);
    const unsigned tag = __builtin_amdgcn_readfirstlane(*(LAS unsigned*)(shm + LDS_CNT + 4));
    GAS u64* flags = (GAS u64*)(ws + WS_FLAG);
    const u64 fv = flagval(tag);
    {
      const int lane = lane_id(), kind = att ? 0 : 1 + (w >> 2);
      const int dst = lane == 10 ? WS_FLAG + (p_ * 16 + rid) * 8 : WS_BOX + (p_ * 10 + lane) * 256 + (8 * kind + j_) * 8;
      publish(LDS_CNT + (kind == 1 ? 120 : 0), att ? 7u : 3u, mkr(ws, WS_A2), dst, u32x2{(unsigned)fv, (unsigned)(fv >> 32)});
    }
    STAMP(5);
    if (w == 0) watch(ws, G0, G0 + (att ? 3 : 5), n_, 1, 15, fv);
    if (kq == 0) {
      if (att) s2_compute<20, 2>(w, kq, G0, n_, rot, ws, fv PR_PASS);
      else s2_compute<20, 3>(w, kq, G0, n_, rot, ws, fv PR_PASS);
    } else {
      if (att) s2_compute<18, 2>(w, kq, G0, n_, rot, ws, fv PR_PASS);
      else s2_compute<18, 3>(w, kq, G0, n_, rot, ws, fv PR_PASS);
    }
    if (blockIdx.x == 0 && w == 3) {
      const int lane = lane_id();
#pragma nounroll
      for (unsigned spins = 0; spins < SPIN_CAP; spins++) {
        bool ok = true;
#pragma unroll
        for (int k = 0; k < 4; k++) ok &= __hip_atomic_load(flags + lane + 64 * k, __ATOMIC_RELAXED, __HIP_MEMORY_SCOPE_AGENT) == fv;
        if (__all(ok)) break;
        __builtin_amdgcn_s_sleep(8);
      }
      if (lane == 0) __hip_atomic_store((GAS unsigned*)ws, tag, __ATOMIC_RELAXED, __HIP_MEMORY_SCOPE_AGENT);
    }
    if (att || w == 0 || w > 2) { STAMP(11); FINISH(); return; }
    fu = w + 3;
  } else {
    auto ktile = [&](int T) { return T < KLIM ? T ^ tx : T; };
    const u32x4 rx = mkr(x, S_ * D_ * 2), rw1 = mkr(att ? wqkv : w1, D_ * RS);
    const int wrow = (16 * (lw & 1) + 4 * (lw >> 1)) * RS;
    const u64 hmask = lw == 0 ? 0xffffff00ull : 0xffffffffull;
    auto ldx = [&](int T) { const int o = ktile(T) * 128; dma4<1024>(rx, ~0ull, (T % 3) * SLOT + XOFF + 2048 + 4 * lw * 1024, xo[0], xo[1], o, o, o + 1280, o + 1280); };
    auto ldw = [&](int T, int h) {
      const int so = (64 * ktile(T) + 32 * (h ^ hf)) * RS + wrow;
      dma4<960>(rw1, 0x0fffffffffffffffull, (T % 3) * SLOT + h * WSLAB + 4 * lw * 960, wv, wv, so, so + 2 * RS, so + 8 * RS, so + 10 * RS);
    };
    auto ldh = [&](int T) { if (att) dma1(rx, hmask, (T % 3) * SLOT + XOFF + lw * 512, hv, ktile(T) * 128); };
    const int attf = att;
    STAMP(12);
    ldx(0); ldw(0, 0); ldh(0); ldw(0, 1);
    STAMP(13);
    bar_vm<4>();
    STAMP(14);
    ldh(1); ldx(1);
    bar_vm1<4>(attf);
    ldw(1, 0); ldw(1, 1);
    auto warm1 = [&]() {
      const int wv2 = blockIdx.x * 4 + lw, l = min(lane, 35), l6 = (l * 43) >> 8;
      const int ox = min((wv2 - 608) * 6 + l6, S_ - 1) * 1280 + 512 + (l - 6 * l6) * 128;
      const int ow = (wv2 < 320 ? 256 * 3840 + wv2 * 4608 : 256 * 3456 + (wv2 - 320) * 4608) + l * 128;
      u64 pa = (u64)wqkv, pb = (u64)w1, pc2 = (u64)x;
      spin64(pa); spin64(pb); spin64(pc2);
      touch(mkr((const void*)(wv2 < 320 ? pa : wv2 < 608 ? pb : pc2), wv2 < 320 ? D_ * 3840 : wv2 < 608 ? D_ * 3456 : S_ * 1280), ow + ((ox - ow) & -(int)(wv2 >= 608)));
    };
    auto helpq = [&](int r0) {
      const int l = min(lane, 26), a = (l * 57) >> 9, k = l - 9 * a, sec = (k * 86) >> 8;
      if (!att) touch(mkr(wqkv, D_ * 3840), (r0 + (uidx * 4 + lw) * 3 + a) * 3840 + sec * 1280 + (((320 * (xcd >> 1)) >> 7) + k - 3 * sec) * 128);
    };
    auto helpm = [&](int r0) {
      const int l = min(lane, 20), a = (l * 37) >> 8, k = l - 7 * a;
      if (att) touch(mkr(w1, D_ * 3456), (r0 + (uidx * 4 + lw) * 3 + a) * 3456 + min(7 * (xcd >> 1) + k, 26) * 128);
    };
    ldh(2); ldx(2);
    if (WARM1 == 1) warm1();
    bar_vm1<4 + (WARM1 == 1)>(attf);
    const int srow = p_ * 128 + 8 * rid + 2 * lw + (lane >> 5);
    u32x2 xr[5];
    unsigned ep, b1v;
    setup_loads(xr, ep, b1v, rx, srow * 1280 + (lane & 31) * 8, mkr(ws, 4), mkr(b1, F_ * 2), (j_ * 224 + 64 * lw + lane) * 2);
    touch_sc1(mkr(ws + WS_BOX, lw == 0 ? 160 * 256 : 0), (((G0 >> 3) + (lane & 1)) * 10 + n_) * 256 + (lane & 2) * 64);
    ldw(2, 0);
    if (WARM1 == 2) warm1();
    ldw(2, 1); ldh(3); ldx(3); ldw(3, 0);
    bar_vm1<8>(attf);
    pin(ep); pin(b1v);
#pragma unroll
    for (int k = 0; k < 5; k++) pin(xr[k]);
    const unsigned tag = __builtin_amdgcn_readfirstlane(ep) + 1u;
    ldw(3, 1); ldh(4); ldx(4);
    if (HELPQ) helpq(448);
    if (HELPM) helpm(448);
    ldw(4, 0); ldw(4, 1);
    {
      float s = 0;
#pragma unroll
      for (int k = 0; k < 5; k++) s += (blo(xr[k][0]) + bhi(xr[k][0])) + (blo(xr[k][1]) + bhi(xr[k][1]));
      const float m = half_sum(s) * (1.f / D_);
      float q = 0;
#pragma unroll
      for (int k = 0; k < 10; k++) { const unsigned u = xr[k >> 1][k & 1]; const float a = blo(u) - m, b = bhi(u) - m; q = __builtin_fmaf(a, a, q); q = __builtin_fmaf(b, b, q); }
      const float rs = __builtin_amdgcn_rsqf(half_sum(q) * (1.f / D_) + 1e-5f);
      st_wt(mkr(ws + WS_STAT, S_ * 16), (lane & 31) == 0 ? srow * 16 : 0x70000000, 0, u32x4{asu(rs), tag, asu(-m * rs), tag});
      *(LAS float*)(shm + LDS_B1 + (64 * lw + lane) * 4) = blo(b1v);
      *(LAS unsigned*)(shm + LDS_CNT + 4) = tag;
    }
    if (HELPM) { if (att) bar_vm<15>(); else bar_vm<13>(); } else if (HELPQ) bar_vm<14>(); else bar_vm1<13>(attf);
    const u32x4 rw = mkr(kq == 0 ? wo : w2, kq == 0 ? D_ * 1280 : F_ * 1280);
    const int cbyte = n_ * 128 + ((cp ^ ((lane >> 4) << 1)) << 4);
    const int v1 = r8 * 1280 + cbyte, v2 = kq == 0 ? (20 * (r8 >> 2) + (r8 & 3)) * 1280 + cbyte : v1;
    int gtab;
    {
      const int so = s2_perm(lane, rot), P = (so * 13) >> 6, kp = so - 5 * P;
      const int r0 = 160 * P + (kp == 2 ? 16 : 40 * (kp - (kp > 2)));
      const int t = 3 * so + kq - 1, u = t - 32;
      const int r1 = 32 * (t < 32 ? 7 * (t >> 2) + (t & 3) : 4 * ((u * 43) >> 7) + 4 + u);
      gtab = kq == 0 ? r0 * 1280 + (kp == 2) : r1 * 1280;
      pin(gtab);
    }
    const int sbn = 1280 * (kq == 0 ? 20 : 16);
    auto geom = [&](int s, int& rb, int& sa, int& sb, int& v) {
      const int e = __builtin_amdgcn_readlane(gtab, s), f = e & 1;
      v = v1 + ((v2 - v1) & -f);
      rb = e - f;
      sa = f ? 1280 * 80 : 1280 * 8;
      sb = f ? 1280 * 40 : sbn;
    };
    u32x4 pre[9][4];
    auto ldr = [&](auto R) {
      constexpr int r = decltype(R)::value;
      int rb, sa, sb, v; geom(9 + r, rb, sa, sb, v);
      bload(pre[r][0], rw, v, rb); bload(pre[r][1], rw, v, rb + sa); bload(pre[r][2], rw, v, rb + sb); bload(pre[r][3], rw, v, rb + sa + sb);
    };
    auto ldl = [&](auto S) {
      constexpr int s = decltype(S)::value;
      int rb, sa, sb, v; geom(s, rb, sa, sb, v);
      dma4<1024>(rw, ~0ull, s < 9 ? s * 16384 + kq * 4096 : 147456 + (s - 18) * 4096, v, v, rb, rb + sa, rb + sb, rb + sa + sb);
    };
    const u32x4 rst = mkr(ws + WS_STAT, S_ * 16);
    const int spos = 36 * lw + (lane < 36 ? lane : 35), sv = row_token(p_, spos) * 16;
    u32x4 sg;
    auto warmh = [&]() {
      if (WATT ? att : !att) {
        const int wv2 = ((blockIdx.x >> 7) * 64 + (blockIdx.x & 63)) * 4 + lw;
        u64 pa = (u64)w2, pb = (u64)wo;
        spin64(pa); spin64(pb);
        touch(mkr((const void*)(wv2 < 360 ? pa : pb), wv2 < 360 ? F_ * 1280 : D_ * 1280), min((wv2 < 360 ? wv2 : wv2 - 360) * 48 + min(lane, 47), wv2 < 360 ? F_ * 10 - 1 : D_ * 10 - 1) * 128);
      }
    };
    sfor<5>([&](auto TT) {
      constexpr int T = decltype(TT)::value + 5;
      if (T == 9) bload_sc1(sg, rst, sv, 0);
      if (T == 6) { STAMP(15); }
      ldh(T); ldx(T); ldw(T, 0);
      if (T == WATT) warmh();
      ldw(T, 1);
      if (T == 9 && WPOS == 0) warmh();
      if (T == WATT) { if (att) bar_vm<14>(); else bar_vm<12>(); }
      else if (T < 9 || WPOS) bar_vm1<12>(attf); else bar_vm<13>();
    });
    STAMP(1);
    asm volatile("" : "+v"(sg));
#pragma nounroll
    for (unsigned spins = 0; !__all(sg[1] == tag && sg[3] == tag) && spins < SPIN_CAP; spins++) {
      __builtin_amdgcn_s_sleep(2);
      bload_sc1(sg, rst, sv, 0);
      asm volatile("s_waitcnt vmcnt(0)" : "+v"(sg) :: "memory");
    }
    *(LAS u32x2*)(shm + LDS_ST + spos * 8) = u32x2{sg[0], sg[2]};
    if (att) { ldl(IC(4)); wait_vm<4>(); } else { warmh(); wait_vm<1>(); }
    STAMP(2);
    bar_lgkm();
    __builtin_amdgcn_s_setprio(0);
    STAMP(3);
    const int pa = LDS_PROD + kq * 4, wa = kq * 4096 + lane * 16;
    if (!att) { if (WPOS && !WATT) ldl(IC(4)); ldl(IC(5)); ldl(IC(6)); ldl(IC(7)); }
    ldr(IC(1)); ldr(IC(2)); ldr(IC(3)); ldr(IC(4));
    STAMP(4);
    ldl(IC(3));
    await<1, 8>(2);
    ldl(IC(2)); ldl(IC(1)); ldl(IC(0));
    if (!att) { ldl(IC(8)); if (kq == 0) { ldl(IC(18)); ldl(IC(19)); } }
    ldr(IC(0)); wait_vm<4>();
    post(pa, att ? 1 : 2);
    STAMP(5);
    if (!att) __builtin_amdgcn_s_setprio(3);
    ldr(IC(5)); ldr(IC(6)); ldr(IC(7)); ldr(IC(8));
    if (att) {
      await<1, 8>(4);
      __builtin_amdgcn_s_setprio(3);
      ldl(IC(5)); ldl(IC(6)); ldl(IC(7)); ldl(IC(8));
      if (kq == 0) { ldl(IC(18)); ldl(IC(19)); }
    }
    if (lw == 3) touch(mkr(b2, D_ * 2), n_ * 128);
    wait_vm<0>();
    post(pa, 2);
#define PUT(r, off) dsw128<off>(wa + (r / 3) * 49152, pre[r][0]); dsw128<off + 1024>(wa + (r / 3) * 49152, pre[r][1]); dsw128<off + 2048>(wa + (r / 3) * 49152, pre[r][2]); dsw128<off + 3072>(wa + (r / 3) * 49152, pre[r][3]);
    await_pair(LDS_CONS + kq * 8, 1);
    PUT(0, 0) PUT(1, 16384) PUT(2, 32768)
    post(pa, 3);
    __builtin_amdgcn_s_setprio(0);
    STAMP(6);
    await_pair(LDS_CONS + kq * 8, 3);
    PUT(3, 0) PUT(4, 16384) PUT(5, 32768)
    post(pa, 4);
    await_pair(LDS_CONS + kq * 8, 6);
    PUT(6, 0) PUT(7, 16384) PUT(8, 32768)
    post(pa, 5);
    fu = lw;
  }
  {
    const int lane = lane_id();
    const u32x4 bv = *(const GAS u32x4*)(b2 + n_ * 128 + (lane & 7) * 16);
    int foff[2], pr0[2];
    u32x4 xv[2];
    f32x2 gt[2][4];
#pragma unroll
    for (int hh = 0; hh < 2; hh++) {
      const int frow = 48 * hh + 8 * fu + (lane >> 3);
      foff[hh] = fin_off(G0 + (att ? 2 : 3) * hh, n_, 8 * fu, lane);
      xv[hh] = *(const GAS u32x4*)((const char*)x + foff[hh]);
      pr0[hh] = (frow >> 4) * 16384 + (frow & 15) * 256 + (((2 * (lane & 7)) ^ (frow & 7)) << 4);
      pin(pr0[hh]);
    }
#pragma unroll
    for (int hh = 0; hh < 2; hh++) {
      await<1, 3>(12 + hh);
      f32x4 y[2];
#pragma unroll
      for (int e = 0; e < 2; e++) {
        lptr pr = shm + (pr0[hh] ^ (16 * e));
        y[e] = *(LAS f32x4*)(pr + 4096) + *(LAS f32x4*)(pr + 8192) + *(LAS f32x4*)(pr + 12288);
      }
#pragma unroll
      for (int k = 0; k < 4; k++) {
        f32x2 yy = f32x2{y[k >> 1][2 * (k & 1)], y[k >> 1][2 * (k & 1) + 1]} + f32x2{blo(bv[k]), bhi(bv[k])};
        f32x2 t = yy * -1.4426950408889634f;
        f32x2 e = f32x2{__builtin_amdgcn_exp2f(t[0]), __builtin_amdgcn_exp2f(t[1])} + 1.f;
        gt[hh][k] = yy * f32x2{__builtin_amdgcn_rcpf(e[0]), __builtin_amdgcn_rcpf(e[1])};
        pin(gt[hh][k]);
      }
    }
    STAMP(10);
#pragma unroll
    for (int hh = 0; hh < 2; hh++) {
      await<0, 1>(28 + hh);
      u32x4 o;
#pragma unroll
      for (int e = 0; e < 2; e++) {
        const f32x4 s = *(LAS f32x4*)(shm + (pr0[hh] ^ (16 * e)));
        o[2 * e] = pk2(f32x2{s[0], s[1]} * gt[hh][2 * e] + f32x2{blo(xv[hh][2 * e]), bhi(xv[hh][2 * e])});
        o[2 * e + 1] = pk2(f32x2{s[2], s[3]} * gt[hh][2 * e + 1] + f32x2{blo(xv[hh][2 * e + 1]), bhi(xv[hh][2 * e + 1])});
      }
      __builtin_nontemporal_store(o, (GAS u32x4*)(out + foff[hh]));
    }
  }
  STAMP(11);
  FINISH();
}

extern "C" void kernel_launch(void* const* d_in, const int* in_sizes, int n_in, void* d_out, int out_size, void* d_ws,
                              size_t ws_size, hipStream_t stream) {
  (void)hipFuncSetAttribute((const void*)fused_block, hipFuncAttributeMaxDynamicSharedMemorySize, LDS_TOTAL);
  fused_block<<<256, 768, LDS_TOTAL, stream>>>(d_in[0], d_in[1], d_in[2], d_in[4], d_in[5], d_in[6],
                                               d_in[7], d_out, d_ws);
}
```

```cpp
#include <hip/hip_runtime.h>
#include <hip/hip_bf16.h>
#include <type_traits>
#include <utility>

typedef __attribute__((ext_vector_type(8))) __bf16 bf16x8;
typedef __attribute__((ext_vector_type(2))) __bf16 bf16x2;
typedef __attribute__((ext_vector_type(4))) float f32x4;
typedef __attribute__((ext_vector_type(2))) float f32x2;
typedef __attribute__((ext_vector_type(4))) unsigned u32x4;
typedef __attribute__((ext_vector_type(2))) unsigned u32x2;
#define LAS __attribute__((address_space(3)))
#define GAS __attribute__((address_space(1)))
#define DEVI __device__ __forceinline__
typedef LAS char* lptr;
typedef unsigned long long u64;
#define SB __builtin_amdgcn_sched_barrier(0)
#define IC(n) std::integral_constant<int, n>{}

constexpr int S_ = 2048, D_ = 640, F_ = 1728;
constexpr int KS_T = 74;
constexpr int WSLAB = 15360;
constexpr int XOFF = 2 * WSLAB;
constexpr int SLOT = 49152;
constexpr int LDS_TB = 86016;
constexpr int LDS_ST = 155648;
constexpr int LDS_CS = LDS_ST + 1152;
constexpr int LDS_B1 = LDS_CS + 1024;
constexpr int LDS_CNT = LDS_B1 + 1024;
constexpr int LDS_ZERO = LDS_CNT + 24;
constexpr int LDS_CONS = LDS_CNT + 64;
constexpr int LDS_PROD = LDS_CONS + 32;
constexpr int LDS_TAB = LDS_CNT + 128;
constexpr int LDS_TOTAL = 163840;
constexpr int WS_FLAG = 4096, WS_STAT = 8192, WS_BOX = 65536, WS_A2 = 1 << 20;
constexpr int A2_BYTES = 128 * KS_T * 1024;
constexpr int CODE_BYTES = 49152;
constexpr unsigned SPIN_CAP = 1u << 20;

DEVI void bar_lgkm() { asm volatile("s_waitcnt lgkmcnt(0)\n\ts_barrier" ::: "memory"); }
template <int N> DEVI void bar_vm() { asm volatile("s_waitcnt vmcnt(%0)\n\ts_barrier" ::"i"(N) : "memory"); }
template <int N> DEVI void wait_vm() { asm volatile("s_waitcnt vmcnt(%0)" ::"i"(N) : "memory"); }
DEVI unsigned pk2(float a, float b) { f32x2 v = {a, b}; return __builtin_bit_cast(unsigned, __builtin_convertvector(v, bf16x2)); }
DEVI unsigned pk2(f32x2 v) { return __builtin_bit_cast(unsigned, __builtin_convertvector(v, bf16x2)); }
DEVI float asf(unsigned u) { return __builtin_bit_cast(float, u); }
DEVI unsigned asu(float f) { return __builtin_bit_cast(unsigned, f); }
DEVI float blo(unsigned u) { return __builtin_bit_cast(float, u << 16); }
DEVI float bhi(unsigned u) { return __builtin_bit_cast(float, u & 0xffff0000u); }
DEVI f32x4 mfma(u32x4 a, u32x4 b, f32x4 c) {
  return __builtin_amdgcn_mfma_f32_16x16x32_bf16(__builtin_bit_cast(bf16x8, a), __builtin_bit_cast(bf16x8, b), c, 0, 0, 0);
}
DEVI u32x4 cat(u32x2 a, u32x2 b) { return __builtin_shufflevector(a, b, 0, 1, 2, 3); }
DEVI void mfma_a(f32x4& c, u32x4 a, u32x4 b) { asm volatile("v_mfma_f32_16x16x32_bf16 %0, %1, %2, %0" : "+v"(c) : "v"(a), "v"(b)); }
template <int OFF> DEVI void ds128(u32x4& d, int addr) { asm volatile("ds_read_b128 %0, %1 offset:%2" : "=v"(d) : "v"(addr), "i"(OFF)); }
template <int OFF> DEVI void dstr(u32x2& d, int addr) { asm volatile("ds_read_b64_tr_b16 %0, %1 offset:%2" : "=v"(d) : "v"(addr), "i"(OFF)); }
template <int OFF> DEVI void dsw128(int addr, u32x4 d) { asm volatile("ds_write_b128 %0, %1 offset:%2" ::"v"(addr), "v"(d), "i"(OFF) : "memory"); }
DEVI u32x4 mkr(const void* p, unsigned bytes) {
  u64 a = (u64)p;
  return u32x4{(unsigned)a, (unsigned)(a >> 32) & 0xffffu, bytes, 0x00020000u};
}
DEVI void st_wt(u32x4 r, int voff, int soff, u32x4 v) {
  asm volatile("buffer_store_dwordx4 %0, %1, %2, %3 offen sc1\n\ts_nop 1" ::"v"(v), "v"(voff), "s"(r), "s"(soff) : "memory");
}
template <int LSTEP>
DEVI void dma4(u32x4 r, u64 mask, int l, int va, int vb, int s0, int s1, int s2, int s3) {
  asm volatile(
      "s_mov_b64 exec, %0\n\t"
      "s_mov_b32 m0, %1\n\ts_nop 0\n\tbuffer_load_dwordx4 %2, %4, %5 offen lds\n\t"
      "s_add_u32 m0, %1, %9\n\ts_nop 0\n\tbuffer_load_dwordx4 %3, %4, %6 offen lds\n\t"
      "s_add_u32 m0, %1, %10\n\ts_nop 0\n\tbuffer_load_dwordx4 %2, %4, %7 offen lds\n\t"
      "s_add_u32 m0, %1, %11\n\ts_nop 0\n\tbuffer_load_dwordx4 %3, %4, %8 offen lds\n\t"
      "s_mov_b64 exec, -1"
      :: "s"(mask), "s"(l), "v"(va), "v"(vb), "s"(r), "s"(s0), "s"(s1), "s"(s2), "s"(s3), "i"(LSTEP), "i"(2 * LSTEP), "i"(3 * LSTEP)
      : "memory", "scc");
}
DEVI void dma1(u32x4 r, u64 mask, int l, int v, int s0) {
  asm volatile("s_mov_b64 exec, %0\n\ts_mov_b32 m0, %1\n\ts_nop 0\n\tbuffer_load_dwordx4 %2, %3, %4 offen lds\n\ts_mov_b64 exec, -1"
               :: "s"(mask), "s"(l), "v"(v), "s"(r), "s"(s0) : "memory");
}
DEVI void setup_loads(u32x2 (&xr)[5], unsigned& ep, unsigned& bv, u32x4 rx, int vx, u32x4 re, u32x4 rb, int vb) {
  asm volatile("buffer_load_dwordx2 %0, %7, %8, 0 offen\n\tbuffer_load_dwordx2 %1, %7, %8, 0 offen offset:256\n\tbuffer_load_dwordx2 %2, %7, %8, 0 offen offset:512\n\t"
               "buffer_load_dwordx2 %3, %7, %8, 0 offen offset:768\n\tbuffer_load_dwordx2 %4, %7, %8, 0 offen offset:1024\n\t"
               "buffer_load_dword %5, off, %9, 0 sc1\n\tbuffer_load_ushort %6, %11, %10, 0 offen"
               : "=&v"(xr[0]), "=&v"(xr[1]), "=&v"(xr[2]), "=&v"(xr[3]), "=&v"(xr[4]), "=&v"(ep), "=&v"(bv)
               : "v"(vx), "s"(rx), "s"(re), "s"(rb), "v"(vb) : "memory");
}
template <int N>
DEVI void bar_vm1(int flag) {
  asm volatile("s_waitcnt vmcnt(%2)\n\ts_cmp_lg_u32 %0, 0\n\ts_cbranch_scc1 .La%=\n\ts_waitcnt vmcnt(%1)\n.La%=:\n\ts_barrier" ::"s"(flag), "i"(N), "i"(N + 1) : "memory", "scc");
}
template <class T> DEVI void pin(T& v) { asm volatile("" : "+v"(v)); }
DEVI void dsw_col0(int a0, f32x4 d0, int a1, f32x4 d1, int a2, f32x4 d2) {
  asm volatile("s_mov_b64 exec, %6\n\tds_write_b128 %0, %1\n\tds_write_b128 %2, %3\n\tds_write_b128 %4, %5\n\ts_mov_b64 exec, -1"
               ::"v"(a0), "v"(d0), "v"(a1), "v"(d1), "v"(a2), "v"(d2), "s"(0x0001000100010001ull) : "memory");
}
DEVI void reload_sc1(u32x4& d, u32x4 r, int v, int s0) { asm volatile("buffer_load_dwordx4 %0, %1, %2, %3 offen sc1" : "+v"(d) : "v"(v), "s"(r), "s"(s0) : "memory"); }
template <int OFF> DEVI void redstr(u32x2& d, int addr) { asm volatile("ds_read_b64_tr_b16 %0, %1 offset:%2" : "+v"(d) : "v"(addr), "i"(OFF)); }
DEVI void vinc(int& d, int s) { asm volatile("v_add_u32 %0, %1, %0" : "+v"(d) : "s"(s)); }
template <class F, int... I> DEVI void sfor_(F f, std::integer_sequence<int, I...>) { (f(std::integral_constant<int, I>{}), ...); }
template <int N, class F> DEVI void sfor(F f) { sfor_(f, std::make_integer_sequence<int, N>{}); }
DEVI void touch_sc1(u32x4 r, int v) {
  asm volatile("s_mov_b32 m0, %2\n\ts_nop 0\n\tbuffer_load_dword %0, %1, 0 offen sc1 lds" ::"v"(v), "s"(r), "i"(LDS_TOTAL - 256) : "memory");
}
DEVI void touch(u32x4 r, int v) {
  asm volatile("s_mov_b32 m0, %2\n\ts_nop 0\n\tbuffer_load_dword %0, %1, 0 offen lds" ::"v"(v), "s"(r), "i"(LDS_TOTAL - 256) : "memory");
}
DEVI void bload(u32x4& d, u32x4 r, int v, int s0) { asm volatile("buffer_load_dwordx4 %0, %1, %2, %3 offen" : "=v"(d) : "v"(v), "s"(r), "s"(s0)); }
DEVI void bload_sc1(u32x4& d, u32x4 r, int v, int s0) { asm volatile("buffer_load_dwordx4 %0, %1, %2, %3 offen sc1" : "=v"(d) : "v"(v), "s"(r), "s"(s0) : "memory"); }
DEVI void arrive(int k) {
  asm volatile("s_mov_b64 exec, 1\n\tds_add_u32 %0, %1\n\ts_mov_b64 exec, -1" ::"v"(LDS_CNT + 4 * k), "v"(1) : "memory");
}
template <int SLEEP, unsigned N>
DEVI void await(int k) {
  unsigned v, t;
  if (SLEEP)
    asm volatile("s_branch .Ly%=\n.Lx%=:\n\ts_sleep 1\n.Ly%=:\n\tds_read_b32 %0, %2\n\ts_waitcnt lgkmcnt(0)\n\tv_readfirstlane_b32 %1, %0\n\ts_cmp_ge_u32 %1, %3\n\ts_cbranch_scc0 .Lx%="
                 : "=&v"(v), "=&s"(t) : "v"(LDS_CNT + 4 * k), "i"(N) : "memory", "scc");
  else
    asm volatile(".Lx%=:\n\tds_read_b32 %0, %2\n\ts_waitcnt lgkmcnt(0)\n\tv_readfirstlane_b32 %1, %0\n\ts_cmp_ge_u32 %1, %3\n\ts_cbranch_scc0 .Lx%="
                 : "=&v"(v), "=&s"(t) : "v"(LDS_CNT + 4 * k), "i"(N) : "memory", "scc");
}
DEVI void publish(int cnt, unsigned last, u32x4 r, int voff, u32x2 fv) {
  unsigned v, t;
  asm volatile("s_waitcnt vmcnt(0)\n\ts_mov_b64 exec, 1\n\tds_add_rtn_u32 %0, %2, %3\n\ts_waitcnt lgkmcnt(0)\n\tv_readfirstlane_b32 %1, %0\n\t"
               "s_cmp_lg_u32 %1, %4\n\ts_cbranch_scc1 .Lp%=\n\ts_mov_b64 exec, 0x7ff\n\tbuffer_store_dwordx2 %7, %6, %5, 0 offen sc1\n.Lp%=:\n\ts_mov_b64 exec, -1"
               : "=&v"(v), "=&s"(t) : "v"(cnt), "v"(1), "s"(last), "s"(r), "v"(voff), "v"(fv) : "memory", "scc");
}
DEVI void post(int addr, int val) { asm volatile("ds_write_b32 %0, %1" ::"v"(addr), "v"(val) : "memory"); }
DEVI void peek(unsigned& v, int addr) { asm volatile("ds_read_b32 %0, %1" : "=v"(v) : "v"(addr) : "memory"); }
DEVI void require(unsigned v, int addr, unsigned n) {
  unsigned t;
  asm volatile("v_readfirstlane_b32 %1, %0\n\ts_cmp_ge_u32 %1, %3\n\ts_cbranch_scc1 .Lq%=\n"
               ".Lw%=:\n\tds_read_b32 %0, %2\n\ts_waitcnt lgkmcnt(0)\n\tv_readfirstlane_b32 %1, %0\n\ts_cmp_ge_u32 %1, %3\n\ts_cbranch_scc0 .Lw%=\n.Lq%=:"
               : "+&v"(v), "=&s"(t) : "v"(addr), "s"(n) : "memory", "scc");
}
DEVI void await_pair(int addr, unsigned n) {
  for (;;) {
    u32x2 v;
    asm volatile("ds_read_b64 %0, %1\n\ts_waitcnt lgkmcnt(0)" : "=v"(v) : "v"(addr) : "memory");
    const unsigned a = v[0], b = v[1];
    if (__builtin_amdgcn_readfirstlane(a < b ? a : b) >= n) break;
    __builtin_amdgcn_s_sleep(1);
  }
}
DEVI int lane_id() { int l; asm volatile("v_mbcnt_lo_u32_b32 %0, -1, 0\n\tv_mbcnt_hi_u32_b32 %0, -1, %0" : "=v"(l)); return l; }
DEVI int sig(int i) { return 4 * (i & 3) + (i >> 2); }
DEVI u64 flagval(unsigned tag) { return ((u64)(tag ^ 0x5bd1e995u) << 32) | tag; }

DEVI float xadd16(float v) { auto r = __builtin_amdgcn_permlane16_swap(asu(v), asu(v), false, false); return asf(r[0]) + asf(r[1]); }
template <int CTRL> DEVI float dpp(float v) { return asf(__builtin_amdgcn_mov_dpp(asu(v), CTRL, 0xf, 0xf, true)); }
DEVI float half_sum(float v) {
  v += dpp<0xb1>(v); v += dpp<0x4e>(v); v += dpp<0x141>(v); v += dpp<0x140>(v);
  return xadd16(v);
}

DEVI int h_slot(int j, int l) { const int t = l < 4 ? 4 * j + l : 28 + 3 * j + l, d = (t * 43) >> 7; return 18 * (t - 3 * d) + d; }

DEVI int row_token(int p_, int pos) {
  const int tau = pos >> 4, i = pos & 15;
  int tok = tau == 0 ? p_ * 128 - 16 + 8 * (i & 1) + 2 * (i >> 2) + ((i & 3) < 2) : p_ * 128 + 32 * ((tau - 1) >> 1) + 2 * sig(i) + ((tau - 1) & 1);
  return tok < 0 ? 0 : tok;
}

struct ColGeo { int A, B, C, ct; };
DEVI ColGeo colgeo(bool att, int nw, int mw, int c, bool heavy) {
  const int cc = heavy ? (c ^ (2 * mw)) : c;
  const int ct = 4 * nw + cc, sec = (ct * 13) >> 6, hh = ct - 5 * sec;
  const bool q4 = hh == 4, sp = !heavy && c == 2;
  ColGeo r;
  r.A = att ? sec * 160 + (q4 ? 32 : 40 * hh) : heavy ? 128 * nw + 32 * cc : (sp ? 352 + 16 * nw : 128 + 64 * nw + 32 * c);
  r.B = att && q4 ? 40 : 8;
  r.C = att ? (q4 ? 80 : 16) : (sp ? 32 : 16);
  r.ct = ct;
  return r;
}

template <int NC>
struct Frags { u32x4 x[5]; u32x2 w[NC][2]; };

template <int N> DEVI void wait_lg() { asm volatile("s_waitcnt lgkmcnt(%0)" ::"i"(N) : "memory"); }

template <int NC, int K>
DEVI void s1_rd(Frags<NC>& n, int xo, int xh, const int (&wa)[NC]) {
  if constexpr (K == 0) ds128<0>(n.x[0], xo);
  else if constexpr (K <= 2 * NC) { if ((K - 1) & 1) dstr<7680>(n.w[(K - 1) >> 1][1], wa[(K - 1) >> 1]); else dstr<0>(n.w[(K - 1) >> 1][0], wa[(K - 1) >> 1]); }
  else if constexpr (K < 2 * NC + 4) ds128<(K - 2 * NC) * 2048>(n.x[K - 2 * NC], xo);
  else if constexpr (K == 2 * NC + 4) ds128<0>(n.x[4], xh);
}
template <int NC, int NH, int K>
DEVI void s1_mm(const Frags<NC>& c, f32x4 (&acc)[4][NC], f32x4 (&hacc)[NH]) {
  if constexpr (K < 4 * NC) mfma_a(acc[K / NC][K % NC], cat(c.w[K % NC][0], c.w[K % NC][1]), c.x[K / NC]);
  else mfma_a(hacc[K - 4 * NC], cat(c.w[K - 4 * NC][0], c.w[K - 4 * NC][1]), c.x[4]);
}
template <int NC, int NH, bool BAR, bool HX>
DEVI void s1_step(const Frags<NC>& c, Frags<NC>& n, int xo, int xh, const int (&wa)[NC], f32x4 (&acc)[4][NC], f32x4 (&hacc)[NH]) {
  constexpr int NM = 4 * NC + NH, NR = 2 * NC + 4 + HX, D = BAR ? NM - NR : 0;
  sfor<NM>([&](auto KK) {
    constexpr int k = decltype(KK)::value;
    constexpr int need = k < NC ? 2 * k + 2 : (k % NC == 0 && k < 4 * NC + HX) ? 2 * NC + k / NC : -1;
    if constexpr (BAR && k == D) bar_lgkm();
    if constexpr (need >= 0 && (!BAR || k < D)) wait_lg<NR - 1 - need + (BAR ? 0 : (k < NR ? k : NR))>();
    s1_mm<NC, NH, k>(c, acc, hacc);
    if constexpr (k >= D && k - D < NR) s1_rd<NC, k - D>(n, xo, xh, wa);
  });
}

template <int OFF> DEVI void dsw64(int addr, u32x2 d) { asm volatile("ds_write_b64 %0, %1 offset:%2" ::"v"(addr), "v"(d), "i"(OFF) : "memory"); }
template <int OFF> DEVI void ds64(u32x2& d, int addr) { asm volatile("ds_read_b64 %0, %1 offset:%2" : "=v"(d) : "v"(addr), "i"(OFF)); }

template <int NC, bool att>
DEVI void s1_compute(lptr shm, int w, int lane, int p_, int j_, int hf, char* ws) {
  constexpr int NH = NC == 4 ? 2 : 3;
  const int mw = w & 1, nw = w >> 1, i = lane & 15, g = lane >> 4;
  f32x4 acc[4][NC], hacc[NH];
#pragma unroll
  for (int m = 0; m < 4; m++)
#pragma unroll
    for (int c = 0; c < NC; c++) { acc[m][c] = f32x4{0, 0, 0, 0}; asm volatile("" : "+v"(acc[m][c])); }
#pragma unroll
  for (int c = 0; c < NH; c++) { hacc[c] = f32x4{0, 0, 0, 0}; asm volatile("" : "+v"(hacc[c])); }
  int wb[NC], cb[NC], ct[NC];
#pragma unroll
  for (int c = 0; c < NC; c++) {
    const ColGeo cg = colgeo(att, nw, mw, c, NC == 4);
    wb[c] = (lane >> 2) * 480 + cg.A + cg.B * (lane & 1) + cg.C * ((lane >> 1) & 1);
    cb[c] = LDS_CS + 2 * (cg.A + cg.B * (g & 1) + cg.C * (g >> 1));
    ct[c] = cg.ct;
    asm volatile("" : "+v"(cb[c]), "+v"(wb[c]));
  }
  int xb = XOFF + i * 128 + ((g ^ (i >> 1) ^ (4 * hf)) << 4);
  int sta = LDS_ST + i * 8;
  asm volatile("" : "+v"(xb), "+v"(sta));
  const int own = (1 + 4 * mw) * 2048;
  Frags<NC> fa, fb;
  if (!att) {
    const unsigned one = i == 0 ? 0x3f803f80u : 0u;
    fa.x[4] = u32x4{one, one, one, one}; pin(fa.x[4]); fb.x[4] = fa.x[4];
  }
  int wa[NC];
  bar_lgkm();
  sfor<2 * NC + 4 + att>([&](auto K) { s1_rd<NC, decltype(K)::value>(fa, xb + own, xb, wb); });
#pragma unroll
  for (int c = 0; c < NC; c++) wa[c] = wb[c] + WSLAB;
  s1_step<NC, NH, true, att>(fa, fb, (xb ^ 64) + own, xb ^ 64, wa, acc, hacc);
  int sl = 0;
#pragma nounroll
  for (int T = 1; T < 10; T++) {
    sl = sl == 2 * SLOT ? 0 : sl + SLOT;
#pragma unroll
    for (int c = 0; c < NC; c++) wa[c] = wb[c] + sl;
    s1_step<NC, NH, true, att>(fb, fa, xb + sl + own, xb + sl, wa, acc, hacc);
#pragma unroll
    for (int c = 0; c < NC; c++) wa[c] = wb[c] + sl + WSLAB;
    s1_step<NC, NH, false, att>(fa, fb, (xb ^ 64) + sl + own, (xb ^ 64) + sl, wa, acc, hacc);
  }
  wait_lg<0>();
  arrive(2);
  sfor<NH>([&](auto K) { s1_mm<NC, NH, 4 * NC + decltype(K)::value>(fb, acc, hacc); });
  u32x2 stu[5];
  u32x4 b1u[NC], csu[NC];
  sfor<4 * NC>([&](auto KK) {
    constexpr int k = decltype(KK)::value;
    s1_mm<NC, NH, k>(fb, acc, hacc);
    if constexpr (k < 4) ds64<(1 + k) * 128>(stu[k], sta + mw * 512);
    if constexpr (k == 4 && att) ds64<0>(stu[4], sta);
    if constexpr (k == 5) {
      dsw_col0(cb[0], hacc[0], cb[1], hacc[1], cb[NH - 1], hacc[NH - 1]);
      if (NC == 4) arrive(8 + nw);
    }
    if constexpr (k >= 6 && k < 6 + NC && !att) ds128<LDS_B1 - LDS_CS>(b1u[k - 6], cb[k - 6]);
  });
  if (NC == 4) await<0, 2>(8 + nw);
#pragma unroll
  for (int c = 0; c < NC; c++) ds128<0>(csu[c], cb[c]);
  asm volatile("s_waitcnt lgkmcnt(0)\n\ts_nop 7" ::: "memory");
  f32x2 st[5];
  f32x4 cs[NC], b1[NC];
#pragma unroll
  for (int m = 0; m < 4 + att; m++) { asm volatile("" : "+v"(stu[m])); st[m] = __builtin_bit_cast(f32x2, stu[m]); }
#pragma unroll
  for (int c = 0; c < NC; c++) {
    pin(csu[c]); cs[c] = __builtin_bit_cast(f32x4, csu[c]);
    if (!att) { pin(b1u[c]); b1[c] = __builtin_bit_cast(f32x4, b1u[c]); }
  }
#pragma unroll
  for (int m = 0; m < 4; m++)
#pragma unroll
    for (int c = 0; c < NC; c++) pin(acc[m][c]);
#pragma unroll
  for (int c = 0; c < NH; c++) pin(hacc[c]);
  const int l2 = lane_id();
  if (!att) {
    const u32x4 ra = mkr(ws + WS_A2, A2_BYTES);
    const int base = (8 * p_ + 4 * mw) * KS_T * 1024;
    const int o0 = base + (NC == 4 || j_ < 7 || nw == 2 ? h_slot(j_, NC == 4 ? 2 * nw + mw : 2 + nw) * 1024 : 0x70000000);
    const int o1 = base + (NC == 4 ? h_slot(j_, 2 * nw + (mw ^ 1)) * 1024 : j_ < 7 ? h_slot(j_, 6) * 1024 + (nw - 2) * 512 : 0x70000000);
    const float c1 = -2.f * 0.7978845608028654f * 1.4426950408889634f, c2 = c1 * 0.044715f;
    unsigned sp[2];
#pragma unroll
    for (int m = 0; m < 4; m++) {
      f32x4 v[NC], t[NC];
      SB;
#pragma unroll
      for (int c = 0; c < NC; c++) t[c] = cs[c] * st[m][1] + b1[c];
      SB;
#pragma unroll
      for (int c = 0; c < NC; c++) v[c] = acc[m][c] * st[m][0] + t[c];
      SB;
#pragma unroll
      for (int c = 0; c < NC; c++) t[c] = v[c] * v[c];
      SB;
#pragma unroll
      for (int c = 0; c < NC; c++) t[c] = t[c] * c2 + c1;
      SB;
#pragma unroll
      for (int c = 0; c < NC; c++) t[c] = t[c] * v[c];
      SB;
#pragma unroll
      for (int c = 0; c < NC; c++)
#pragma unroll
        for (int e = 0; e < 4; e++) t[c][e] = __builtin_amdgcn_exp2f(t[c][e]);
      SB;
#pragma unroll
      for (int c = 0; c < NC; c++) t[c] = t[c] + 1.f;
      SB;
#pragma unroll
      for (int c = 0; c < NC; c++)
#pragma unroll
        for (int e = 0; e < 4; e++) t[c][e] = __builtin_amdgcn_rcpf(t[c][e]);
      SB;
#pragma unroll
      for (int c = 0; c < NC; c++) v[c] = v[c] * t[c];
      SB;
      unsigned o[NC][2];
#pragma unroll
      for (int c = 0; c < NC; c++) { o[c][0] = pk2(v[c][0], v[c][1]); o[c][1] = pk2(v[c][2], v[c][3]); }
      st_wt(ra, l2 * 16, o0 + m * KS_T * 1024, u32x4{o[0][0], o[0][1], o[1][0], o[1][1]});
      if (NC == 4) st_wt(ra, l2 * 16, o1 + m * KS_T * 1024, u32x4{o[2][0], o[2][1], o[NC - 1][0], o[NC - 1][1]});
      else if (m & 1) {
        auto s0 = __builtin_amdgcn_permlane32_swap(sp[0], o[2][0], false, false);
        auto s1 = __builtin_amdgcn_permlane32_swap(sp[1], o[2][1], false, false);
        st_wt(ra, (l2 >> 5) * (KS_T * 1024) + (l2 & 31) * 16, o1 + (m - 1) * KS_T * 1024, u32x4{s0[0], s1[0], s0[1], s1[1]});
      } else { sp[0] = o[2][0]; sp[1] = o[2][1]; }
    }
  } else {
    const int g2 = l2 >> 4, piece = LDS_TB + ((l2 & 48) + ((l2 & 15) ^ (8 * (g2 >> 1)))) * 8;
    sfor<NC>([&](auto C) {
      constexpr int c = decltype(C)::value;
      const int ah = piece + ct[c] * 4608, ao = ah + (1 + 4 * mw) * 512;
      u32x2 o[5];
      SB;
#pragma unroll
      for (int m = 0; m < (c < NH ? 5 : 4); m++) {
        f32x4 a = m < 4 ? acc[m < 4 ? m : 0][c] : hacc[c < NH ? c : 0];
        f32x4 v = a * st[m][0] + cs[c] * st[m][1];
        o[m] = u32x2{pk2(v[0], v[1]), pk2(v[2], v[3])};
      }
      dsw64<0>(ao, o[0]); dsw64<512>(ao, o[1]); dsw64<1024>(ao, o[2]); dsw64<1536>(ao, o[3]);
      if (c < NH) dsw64<0>(ah, o[4]);
    });
  }
}

template <bool MAX> DEVI float red4(const float (&v)[4]) {
  auto op = [](unsigned a, unsigned b) { float r; if (MAX) asm("v_max_f32 %0, %1, %2\n\ts_nop 1" : "=v"(r) : "v"(a), "v"(b)); else r = asf(a) + asf(b); return r; };
  auto a = __builtin_amdgcn_permlane32_swap(asu(v[0]), asu(v[1]), false, false);
  auto b = __builtin_amdgcn_permlane32_swap(asu(v[2]), asu(v[3]), false, false);
  auto c = __builtin_amdgcn_permlane16_swap(asu(op(a[0], a[1])), asu(op(b[0], b[1])), false, false);
  return op(c[0], c[1]);
}
DEVI void bcast4(float r, float (&v)[4]) {
  auto e = __builtin_amdgcn_permlane16_swap(asu(r), asu(r), false, false);
  unsigned e0 = e[0], e1 = e[1];
  auto a = __builtin_amdgcn_permlane32_swap(e0, e0, false, false);
  auto b = __builtin_amdgcn_permlane32_swap(e1, e1, false, false);
  v[0] = asf(a[0]); v[1] = asf(a[1]); v[2] = asf(b[0]); v[3] = asf(b[1]);
}

DEVI void att_table(lptr shm, int lane) {
  const int i = lane & 15, g = lane >> 4, p = lane & 3;
  const int d = sig(i) - g;
  const unsigned NEG = 0xf149f2cau;
  bool vo[4], vp[4];
#pragma unroll
  for (int r = 0; r < 4; r++) { vo[r] = (unsigned)(d - 4 * r) <= 7u; vp[r] = d + 16 - 4 * r <= 7; }
  auto pm = [](bool lo, bool hi) { return (lo ? 0xffffu : 0u) | (hi ? 0xffff0000u : 0u); };
  LAS u32x4* t = (LAS u32x4*)(shm + LDS_TAB) + lane;
  t[0] = u32x4{vo[0] ? 0u : NEG, vo[1] ? 0u : NEG, vo[2] ? 0u : NEG, vo[3] ? 0u : NEG};
  t[64] = u32x4{pm(vp[2], vp[3]), pm(vo[0], vo[1]), vp[2] ? 0u : NEG, vp[3] ? 0u : NEG};
  t[128] = u32x4{pm(vo[2], vo[3]), (unsigned)(LDS_TB + (g * 16 + (i ^ (8 * (g >> 1)))) * 8), (unsigned)(LDS_TB + (p * 16 + ((lane >> 2) ^ (8 * (p >> 1)))) * 8), 0u};
}
DEVI int sel(u64 mask, int a, int b) { int r; asm("v_cndmask_b32_e64 %0, %2, %1, %3" : "=v"(r) : "v"(a), "v"(b), "s"(mask)); return r; }

DEVI void s1_attention(lptr shm, int w, int p_, int j_, char* ws) {
  arrive(3);
  const int lane = lane_id();
  const int tau = (w + 1) * 512, taup = w >= 2 ? tau - 1024 : 0, flip = w == 1 ? 16 : 0;
  const LAS u32x4* t = (const LAS u32x4*)(shm + LDS_TAB) + lane;
  const u32x4 t0 = t[0], t2 = t[128];
  u32x4 t1 = t[64];
  if (p_ == 0 && w < 2) { t1[0] = 0u; t1[2] = 0xf149f2cau; t1[3] = 0xf149f2cau; }
  const f32x4 mO = __builtin_bit_cast(f32x4, t0), mP = __builtin_bit_cast(f32x4, t1);
  const unsigned M1 = t1[0], M2 = t1[1], M3 = t2[0];
  const int po = t2[1] + tau, pp = (t2[1] ^ flip) + taup, to = t2[2] + tau, tp = (t2[2] ^ flip) + taup;
  int aq[4], avo[4], avp[4];
#pragma unroll
  for (int h = 0; h < 4; h++) {
    aq[h] = sel(0xffffull << (16 * h), po, LDS_ZERO - 4 * 4608);
    avo[h] = sel(0x1111111111111111ull << h, to, LDS_ZERO - 14 * 4608);
    avp[h] = sel(0x1111111111111111ull << h, tp, LDS_ZERO - 14 * 4608);
    asm volatile("" : "+v"(aq[h]), "+v"(avo[h]), "+v"(avp[h]));
  }
  await<0, 8>(3);
  u32x2 qa[4][2], ko[4][2], kp[4][2], va[4][2], vb[4][2];
  f32x4 sp[4], so[4];
  auto rd = [&](auto H) {
    constexpr int h = decltype(H)::value;
    ds64<(5 + h) * 4608>(ko[h][0], po); ds64<9 * 4608>(ko[h][1], po); ds64<h * 4608>(qa[h][0], po); ds64<4 * 4608>(qa[h][1], aq[h]);
    ds64<(5 + h) * 4608>(kp[h][0], pp); ds64<9 * 4608>(kp[h][1], pp);
  };
  auto rv = [&](auto H) {
    constexpr int h = decltype(H)::value;
    dstr<(10 + h) * 4608>(va[h][0], tp); dstr<(10 + h) * 4608>(va[h][1], to); dstr<14 * 4608>(vb[h][0], avp[h]); dstr<14 * 4608>(vb[h][1], avo[h]);
  };
  auto qk = [&](auto H, auto N) {
    constexpr int h = decltype(H)::value;
    asm volatile("s_waitcnt lgkmcnt(%6)" : "+v"(ko[h][0]), "+v"(ko[h][1]), "+v"(qa[h][0]), "+v"(qa[h][1]), "+v"(kp[h][0]), "+v"(kp[h][1]) : "i"(decltype(N)::value));
    so[h] = mfma(cat(ko[h][0], ko[h][1]), cat(qa[h][0], qa[h][1]), mO);
    sp[h] = mfma(cat(kp[h][0], kp[h][1]), cat(qa[h][0], qa[h][1]), mP);
  };
  rd(IC(0)); rd(IC(1));
  qk(IC(0), IC(6)); rd(IC(2));
  qk(IC(1), IC(6)); rd(IC(3));
  qk(IC(2), IC(6)); rv(IC(0)); rv(IC(1));
  qk(IC(3), IC(8)); rv(IC(2));
  asm volatile("s_waitcnt lgkmcnt(8)");
  rv(IC(3));
  const float qs = 0.22360679774997896f * 1.4426950408889634f;
  float ee[4], eo[4], mx[4], l[4];
#pragma unroll
  for (int h = 0; h < 4; h++) { ee[h] = fmaxf(fmaxf(sp[h][2], so[h][0]), so[h][2]); eo[h] = fmaxf(fmaxf(sp[h][3], so[h][1]), so[h][3]); mx[h] = fmaxf(ee[h], eo[h]); }
  bcast4(red4<true>(mx) * -qs, mx);
#pragma unroll
  for (int h = 0; h < 4; h++) {
    f32x2 t = f32x2{ee[h], eo[h]} * qs + mx[h];
    ee[h] = __builtin_amdgcn_exp2f(t[0]); eo[h] = __builtin_amdgcn_exp2f(t[1]); l[h] = ee[h] + eo[h];
  }
  bcast4(__builtin_amdgcn_rcpf(red4<false>(l)), l);
  asm volatile("s_waitcnt lgkmcnt(0)" : "+v"(va[0][0]), "+v"(va[0][1]), "+v"(va[1][0]), "+v"(va[1][1]), "+v"(va[2][0]), "+v"(va[2][1]), "+v"(va[3][0]), "+v"(va[3][1]),
               "+v"(vb[0][0]), "+v"(vb[0][1]), "+v"(vb[1][0]), "+v"(vb[1][1]), "+v"(vb[2][0]), "+v"(vb[2][1]), "+v"(vb[3][0]), "+v"(vb[3][1]));
  f32x4 o4 = {0, 0, 0, 0}, oh[4];
#pragma unroll
  for (int h = 0; h < 4; h++) {
    const unsigned pk = pk2(f32x2{ee[h], eo[h]} * l[h]);
    u32x4 pf = {0u, pk & M1, pk & M2, pk & M3};
    o4 = mfma(cat(vb[h][0], vb[h][1]), pf, o4);
    oh[h] = mfma(cat(va[h][0], va[h][1]), pf, f32x4{0, 0, 0, 0});
  }
  const u32x4 ra = mkr(ws + WS_A2, A2_BYTES);
  const int base = ((8 * p_ + w) * KS_T + 54 + 5 * (j_ >> 1)) * 1024;
#pragma unroll
  for (int hp = 0; hp < 2; hp++)
    st_wt(ra, lane * 16, base + ((j_ & 1) * 3 + hp) * 1024,
          u32x4{pk2(oh[2 * hp][0], oh[2 * hp][1]), pk2(oh[2 * hp][2], oh[2 * hp][3]), pk2(oh[2 * hp + 1][0], oh[2 * hp + 1][1]), pk2(oh[2 * hp + 1][2], oh[2 * hp + 1][3])});
  unsigned a0 = pk2(o4[0], o4[1]), a1 = pk2(o4[2], o4[3]);
  auto s0 = __builtin_amdgcn_permlane32_swap(a0, a0, false, false);
  auto s1 = __builtin_amdgcn_permlane32_swap(a1, a1, false, false);
  st_wt(ra, lane < 32 ? lane * 16 : 0x70000000, base + 2 * 1024 + (j_ & 1) * 512, u32x4{s0[0], s1[0], s0[1], s1[1]});
  arrive(4);
}

DEVI int s2_perm(int s, int rot) { return s < 8 ? s ^ rot : (s < 10 || s >= 18) ? s ^ (rot & 1) : ((s - 10) ^ rot) + 10; }
DEVI int fin_off(int G0, int n_, int r0, int lane) {
  const int frow = r0 + (lane >> 3), fG = G0 + (frow >> 4);
  return (32 * (fG >> 1) + 2 * sig(frow & 15) + (fG & 1)) * 1280 + n_ * 128 + (lane & 7) * 16;
}
constexpr int s2_slot(int r) { return r < 18 ? (r % 9) * 16384 : 147456 + (r - 18) * 4096; }
DEVI void watch(char* ws, int Ga, int Gb, int n_, int kind, int word, u64 fv) {
  const int lane = lane_id();
  const int off = WS_BOX + ((((lane & 8) ? Gb : Ga) >> 3) * 10 + n_) * 256 + (8 * kind + (lane & 7)) * 8;
  unsigned n = SPIN_CAP;
  u32x2 d;
  asm volatile(".Lg%=:\n\tbuffer_load_dwordx2 %0, %2, %3, 0 offen sc1\n\ts_sub_u32 %1, %1, 1\n\ts_waitcnt vmcnt(0)\n\ts_cbranch_scc1 .Lh%=\n\t"
               "v_cmp_ne_u64 vcc, %4, %0\n\ts_cbranch_vccnz .Lg%=\n.Lh%=:\n\tds_write_b32 %5, %6"
               : "=&v"(d), "+&s"(n) : "v"(off), "s"(mkr(ws, WS_A2)), "s"(fv), "v"(LDS_CNT + 4 * word), "v"(1) : "memory", "scc", "vcc");
}

template <int NS, int MT>
DEVI void s2_compute(int w, int kq, int G0, int n_, int rot, char* ws, u64 fv) {
  constexpr int AD = (MT == 3 ? 3 : 4) + (NS == 20);
  const int mh = w >> 2, lane = lane_id();
  const u32x4 ra = mkr(ws + WS_A2, A2_BYTES);
  int av = lane * 16;
  pin(av);
  const int ao = ((G0 + MT * mh) * KS_T + (NS == 20 ? 54 : 18 * (kq - 1))) * 1024;
  int bs[4];
#pragma unroll
  for (int c = 0; c < 4; c++) bs[c] = kq * 4096 + (lane >> 2) * 128 + ((c ^ ((lane >> 3) & 3)) << 5) + 8 * (lane & 3);
  f32x4 acc[MT][4];
#pragma unroll
  for (int m = 0; m < MT; m++)
#pragma unroll
    for (int c = 0; c < 4; c++) { acc[m][c] = f32x4{0, 0, 0, 0}; pin(acc[m][c]); }
  u32x4 a[AD][MT];
  u32x2 b[3][4][2];
  int ca = LDS_CONS + kq * 8 + mh * 4, pa = LDS_PROD + kq * 4;
  pin(ca); pin(pa);
  unsigned pv, lv;
  auto rdb = [&](auto R, auto K) {
    constexpr int r = decltype(R)::value, k = decltype(K)::value;
    constexpr int off = s2_slot(r), nxt = s2_slot(r + 1) / 65536 - off / 65536;
    if constexpr (r < 3) dstr<off % 65536 + (k & 1) * 2048>(b[r % 3][k >> 1][k & 1], bs[k >> 1]);
    else redstr<off % 65536 + (k & 1) * 2048>(b[r % 3][k >> 1][k & 1], bs[k >> 1]);
    if constexpr (nxt != 0 && (k & 1)) vinc(bs[k >> 1], nxt * 65536);
  };
  auto lda = [&](auto S, int m) {
    constexpr int s = decltype(S)::value;
    const int o = ao + m * KS_T * 1024 + (s2_perm(s, rot) << 10);
    if constexpr (s < AD) bload_sc1(a[s % AD][m], ra, av, o); else reload_sc1(a[s % AD][m], ra, av, o);
  };
  auto ldb = [&]() {
    peek(pv, pa); wait_lg<0>(); require(pv, pa, 1);
    sfor<3>([&](auto R) { sfor<8>([&](auto K) { rdb(R, K); }); wait_lg<(decltype(R)::value < 2 ? 4 : 0)>(); });
    post(ca, 1);
  };
  constexpr bool BFIRST = NS == 20 || MT == 3;
  if (BFIRST) ldb();
  if (NS == 20) watch(ws, G0, G0 + 2 * MT - 1, n_, w == 0 ? 0 : 2, w == 0 ? 14 : 5, fv);
  await<0, 1>(NS == 20 ? 14 : 15);
  sfor<AD - 1>([&](auto S) {
#pragma unroll
    for (int m = 0; m < MT; m++) lda(S, m);
  });
  if (!BFIRST) ldb();
  sfor<NS - 3>([&](auto S) {
    constexpr int s = decltype(S)::value;
    constexpr bool LA = s + AD - 1 < NS;
    constexpr int later = NS - 1 - s < AD - 2 ? NS - 1 - s : AD - 2;
    if constexpr (s == 3) require(pv, pa, 2);
    if constexpr (s == 7) require(pv, pa, 3);
    if constexpr (s == 10) require(pv, pa, 4);
    if constexpr (s == 13) require(pv, pa, 5);
    if constexpr (s == 2 || s == 6 || s == 9 || s == 12) peek(pv, pa);
    if constexpr (NS == 18 && s == 11 - AD) require(lv, LDS_CNT + 4 * 5, 1);
    if constexpr (NS == 18 && s == 10 - AD) peek(lv, LDS_CNT + 4 * 5);
    if constexpr (s == NS - 4) peek(lv, ca ^ 4);
    sfor<MT>([&](auto M) {
      constexpr int m = decltype(M)::value;
      if constexpr (LA) lda(IC(s + AD - 1), m);
      wait_vm<MT * later + MT - 1 - m + (LA ? m + 1 : 0)>();
      if constexpr (m == 1 && s >= 2) wait_lg<4>();
      sfor<4>([&](auto C) {
        constexpr int c = decltype(C)::value;
        mfma_a(acc[m][c], cat(b[s % 3][c][0], b[s % 3][c][1]), a[s % AD][m]);
        if constexpr (s >= 1 && m < 2) rdb(IC(s + 2), IC(4 * m + c));
      });
    });
    if constexpr (s == 3 || s == 6 || s == 9 || s == 12) post(ca, s);
  });
  wait_lg<4>();
  sfor<8>([&](auto K) { rdb(IC(NS - 1), K); });
  if constexpr (AD == 3) {
#pragma unroll
    for (int m = 0; m < MT; m++) lda(IC(NS - 1), m);
  }
  int da[4];
#pragma unroll
  for (int c = 0; c < 4; c++) da[c] = mh * 49152 + kq * 4096 + (av & 0xf0) * 16 + ((((4 * c) << 4) + (av >> 8 << 4)) ^ (av & 0x70));
  require(lv, ca ^ 4, mh ? 12 : 9);
  sfor<MT>([&](auto M) {
    constexpr int m = decltype(M)::value;
    sfor<3>([&](auto U) {
      constexpr int u = decltype(U)::value, s = NS - 3 + u;
      if constexpr (m == 0) wait_vm<MT * (2 - u) + MT - 1>();
      if constexpr (m > 0 && u == 2) wait_vm<MT - 1 - m>();
      if constexpr (m == 0 && u == 1) wait_lg<8>();
      if constexpr (m == 0 && u == 2) wait_lg<0>();
      sfor<4>([&](auto C) {
        constexpr int c = decltype(C)::value;
        mfma_a(acc[m][c], cat(b[s % 3][c][0], b[s % 3][c][1]), a[s % AD][m]);
        if constexpr (m > 0 && u > 0 && (c & 1)) dsw128<(m - 1) * 16384>(da[2 * (u - 1) + (c >> 1)], __builtin_bit_cast(u32x4, acc[m - 1][2 * (u - 1) + (c >> 1)]));
      });
    });
  });
  asm volatile("s_nop 4");
#pragma unroll
  for (int c = 0; c < 4; c++) dsw128<(MT - 1) * 16384>(da[c], __builtin_bit_cast(u32x4, acc[MT - 1][c]));
  arrive((NS == 20 ? 28 : 12) + mh);
}

typedef __attribute__((ext_vector_type(8))) u64 u64x8;
__global__ __launch_bounds__(768, 3) void fused_block(const void*, const void*, const void*, const void*, const void*, const void*, const void*,
                                                      void*, void*) {
  u64x8 ka; u64 ka8, pc;
  asm volatile("s_load_dwordx16 %0, %3, 0x0\n\ts_load_dwordx2 %1, %3, 0x40\n\ts_getpc_b64 %2"
               : "=&s"(ka), "=&s"(ka8), "=&s"(pc) : "s"(__builtin_amdgcn_kernarg_segment_ptr()));
  extern __shared__ __attribute__((aligned(16))) char shm_[];
  lptr shm = (lptr)shm_;
  const int tid = threadIdx.x, lane = tid & 63, w = __builtin_amdgcn_readfirstlane(tid >> 6);
  const int xcd = blockIdx.x & 7, idx = blockIdx.x >> 3;
  const int p_ = (xcd & 1) * 8 + (idx & 7), sub = idx >> 3;
  const bool att = !(sub & 1);
  const int j_ = (xcd >> 1) * 2 + (sub >> 1);
  const int uidx = (idx & 7) + 8 * (idx >> 4);
  const int n_ = (xcd & 1) * 5 + (int)(((att ? 0x4444433333221100ull : 0x4433222211110000ull) >> (4 * uidx)) & 15);
  const int G0 = 32 * (xcd >> 1) + 2 * (int)(((att ? 0xeca86eca86ecececull : 0x3030963096309630ull) >> (4 * uidx)) & 15);
  const int rot = (int)(((att ? 0x6547610321322110ull : 0x5403103203213210ull) >> (4 * uidx)) & 15);
  const int kq = (w + ((w >> 2) & 1)) & 3;
  if (w == 7) {
    touch(mkr((const void*)(pc & ~255ull), CODE_BYTES), (idx + 32 * lane) * 128);
  }
  const int hf = idx & 1, tx = ((idx >> 1) ^ sub) & 1;
  const int rid = (att ? 0 : 8) + j_;
  if (w >= 8) __builtin_amdgcn_s_setprio(3);
  const int lw = w & 3;
  const int RS = att ? 3840 : 3456;
  const int r8 = lane >> 3, cp = lane & 7, t0 = p_ * 128;
  int xo[2], hv, wv;
#pragma unroll
  for (int e = 0; e < 2; e++)
    xo[e] = (t0 + 32 * lw + 2 * (4 * (r8 & 3) + 2 * e + (r8 >> 2))) * 1280 + ((cp ^ (4 * e + (r8 >> 1))) << 4);
  {
    const int hi = lane >= 30, ch = lane - 30 * hi, lim = att ? 63 : j_ < 7 ? 27 : 19;
    wv = hi * RS + ((ch >= 10) + (ch >= 20)) * (att ? 1120 : 0) + j_ * (att ? 160 : 448) + min(ch, lim) * 16;
    const int tok = max(t0 - 16 + 8 * (r8 & 1) + 2 * lw + ((r8 & 3) < 2), 0);
    hv = (tok * 1280 + ((cp ^ ((2 * lw + ((r8 & 3) >> 1)) & 7)) << 4));
  }
  pin(xo[0]); pin(xo[1]); pin(wv); pin(hv);
  asm volatile("s_waitcnt lgkmcnt(0)" : "+s"(ka), "+s"(ka8));
  const char *x = (const char*)ka[0], *wqkv = (const char*)ka[1], *wo = (const char*)ka[2], *w1 = (const char*)ka[3], *b1 = (const char*)ka[4],
             *w2 = (const char*)ka[5], *b2 = (const char*)ka[6];
  char *out = (char*)ka[7], *ws = (char*)ka8;
  int fu;
  if (__builtin_expect(w < 8, 0)) {
    if (tid < 24) *(LAS u32x4*)(shm + (tid >> 3) * SLOT + XOFF + (tid & 7) * 16) = u32x4{0x3f803f80u, 0x3f803f80u, 0x3f803f80u, 0x3f803f80u};
    if (tid >= 448 && tid < 480) *(LAS unsigned*)(shm + LDS_CNT + 4 * (tid - 448)) = 0u;
    if (w == 5 && att) att_table(shm, lane);
    const bool light = att ? (w >= 6) : (w >= 4);
    if (att) {
      if (light) s1_compute<3, true>(shm, w, lane, p_, j_, hf, ws);
      else s1_compute<4, true>(shm, w, lane, p_, j_, hf, ws);
    } else {
      if (light) s1_compute<3, false>(shm, w, lane, p_, j_, hf, ws);
      else s1_compute<4, false>(shm, w, lane, p_, j_, hf, ws);
    }
    if (att) s1_attention(shm, w, p_, j_, ws);
    const unsigned tag = __builtin_amdgcn_readfirstlane(*(LAS unsigned*)(shm + LDS_CNT + 4));
    GAS u64* flags = (GAS u64*)(ws + WS_FLAG);
    const u64 fv = flagval(tag);
    {
      const int lane = lane_id(), kind = att ? 0 : 1 + (w >> 2);
      const int dst = lane == 10 ? WS_FLAG + (p_ * 16 + rid) * 8 : WS_BOX + (p_ * 10 + lane) * 256 + (8 * kind + j_) * 8;
      publish(LDS_CNT + (kind == 1 ? 120 : 0), att ? 7u : 3u, mkr(ws, WS_A2), dst, u32x2{(unsigned)fv, (unsigned)(fv >> 32)});
    }
    if (w == 0) watch(ws, G0, G0 + (att ? 3 : 5), n_, 1, 15, fv);
    if (kq == 0) {
      if (att) s2_compute<20, 2>(w, kq, G0, n_, rot, ws, fv);
      else s2_compute<20, 3>(w, kq, G0, n_, rot, ws, fv);
    } else {
      if (att) s2_compute<18, 2>(w, kq, G0, n_, rot, ws, fv);
      else s2_compute<18, 3>(w, kq, G0, n_, rot, ws, fv);
    }
    if (blockIdx.x == 0 && w == 3) {
      const int lane = lane_id();
#pragma nounroll
      for (unsigned spins = 0; spins < SPIN_CAP; spins++) {
        bool ok = true;
#pragma unroll
        for (int k = 0; k < 4; k++) ok &= __hip_atomic_load(flags + lane + 64 * k, __ATOMIC_RELAXED, __HIP_MEMORY_SCOPE_AGENT) == fv;
        if (__all(ok)) break;
        __builtin_amdgcn_s_sleep(8);
      }
      if (lane == 0) __hip_atomic_store((GAS unsigned*)ws, tag, __ATOMIC_RELAXED, __HIP_MEMORY_SCOPE_AGENT);
    }
    if (att || w == 0 || w > 2) return;
    fu = w + 3;
  } else {
    auto ktile = [&](int T) { return T ^ tx; };
    const u32x4 rx = mkr(x, S_ * D_ * 2), rw1 = mkr(att ? wqkv : w1, D_ * RS);
    const int wrow = (16 * (lw & 1) + 4 * (lw >> 1)) * RS;
    const u64 hmask = lw == 0 ? 0xffffff00ull : 0xffffffffull;
    auto ldx = [&](int T) { const int o = ktile(T) * 128; dma4<1024>(rx, ~0ull, (T % 3) * SLOT + XOFF + 2048 + 4 * lw * 1024, xo[0], xo[1], o, o, o + 1280, o + 1280); };
    auto ldw = [&](int T, int h) {
      const int so = (64 * ktile(T) + 32 * (h ^ hf)) * RS + wrow;
      dma4<960>(rw1, 0x0fffffffffffffffull, (T % 3) * SLOT + h * WSLAB + 4 * lw * 960, wv, wv, so, so + 2 * RS, so + 8 * RS, so + 10 * RS);
    };
    auto ldh = [&](int T) { if (att) dma1(rx, hmask, (T % 3) * SLOT + XOFF + lw * 512, hv, ktile(T) * 128); };
    const int attf = att;
    ldx(0); ldw(0, 0); ldh(0); ldw(0, 1);
    bar_vm<4>();
    ldh(1); ldx(1);
    bar_vm1<4>(attf);
    ldw(1, 0); ldw(1, 1);
    ldh(2); ldx(2);
    bar_vm1<4>(attf);
    const int srow = p_ * 128 + 8 * rid + 2 * lw + (lane >> 5);
    u32x2 xr[5];
    unsigned ep, b1v;
    setup_loads(xr, ep, b1v, rx, srow * 1280 + (lane & 31) * 8, mkr(ws, 4), mkr(b1, F_ * 2), (j_ * 224 + 64 * lw + lane) * 2);
    touch_sc1(mkr(ws + WS_BOX, lw == 0 ? 160 * 256 : 0), (((G0 >> 3) + (lane & 1)) * 10 + n_) * 256 + (lane & 2) * 64);
    ldw(2, 0); ldw(2, 1); ldh(3); ldx(3); ldw(3, 0);
    bar_vm1<8>(attf);
    pin(ep); pin(b1v);
#pragma unroll
    for (int k = 0; k < 5; k++) pin(xr[k]);
    const unsigned tag = __builtin_amdgcn_readfirstlane(ep) + 1u;
    ldw(3, 1); ldh(4); ldx(4); ldw(4, 0); ldw(4, 1);
    {
      float s = 0;
#pragma unroll
      for (int k = 0; k < 5; k++) s += (blo(xr[k][0]) + bhi(xr[k][0])) + (blo(xr[k][1]) + bhi(xr[k][1]));
      const float m = half_sum(s) * (1.f / D_);
      float q = 0;
#pragma unroll
      for (int k = 0; k < 10; k++) { const unsigned u = xr[k >> 1][k & 1]; const float a = blo(u) - m, b = bhi(u) - m; q = __builtin_fmaf(a, a, q); q = __builtin_fmaf(b, b, q); }
      const float rs = __builtin_amdgcn_rsqf(half_sum(q) * (1.f / D_) + 1e-5f);
      st_wt(mkr(ws + WS_STAT, S_ * 16), (lane & 31) == 0 ? srow * 16 : 0x70000000, 0, u32x4{asu(rs), tag, asu(-m * rs), tag});
      *(LAS float*)(shm + LDS_B1 + (64 * lw + lane) * 4) = blo(b1v);
      *(LAS unsigned*)(shm + LDS_CNT + 4) = tag;
    }
    bar_vm1<13>(attf);
    const u32x4 rw = mkr(kq == 0 ? wo : w2, kq == 0 ? D_ * 1280 : F_ * 1280);
    const int cbyte = n_ * 128 + ((cp ^ ((lane >> 4) << 1)) << 4);
    const int v1 = r8 * 1280 + cbyte, v2 = kq == 0 ? (20 * (r8 >> 2) + (r8 & 3)) * 1280 + cbyte : v1;
    int gtab;
    {
      const int so = s2_perm(lane, rot), P = (so * 13) >> 6, kp = so - 5 * P;
      const int r0 = 160 * P + (kp == 2 ? 16 : 40 * (kp - (kp > 2)));
      const int t = 3 * so + kq - 1, u = t - 32;
      const int r1 = 32 * (t < 32 ? 7 * (t >> 2) + (t & 3) : 4 * ((u * 43) >> 7) + 4 + u);
      gtab = kq == 0 ? r0 * 1280 + (kp == 2) : r1 * 1280;
      pin(gtab);
    }
    const int sbn = 1280 * (kq == 0 ? 20 : 16);
    auto geom = [&](int s, int& rb, int& sa, int& sb, int& v) {
      const int e = __builtin_amdgcn_readlane(gtab, s), f = e & 1;
      v = v1 + ((v2 - v1) & -f);
      rb = e - f;
      sa = f ? 1280 * 80 : 1280 * 8;
      sb = f ? 1280 * 40 : sbn;
    };
    u32x4 pre[9][4];
    auto ldr = [&](auto R) {
      constexpr int r = decltype(R)::value;
      int rb, sa, sb, v; geom(9 + r, rb, sa, sb, v);
      bload(pre[r][0], rw, v, rb); bload(pre[r][1], rw, v, rb + sa); bload(pre[r][2], rw, v, rb + sb); bload(pre[r][3], rw, v, rb + sa + sb);
    };
    auto ldl = [&](auto S) {
      constexpr int s = decltype(S)::value;
      int rb, sa, sb, v; geom(s, rb, sa, sb, v);
      dma4<1024>(rw, ~0ull, s < 9 ? s * 16384 + kq * 4096 : 147456 + (s - 18) * 4096, v, v, rb, rb + sa, rb + sb, rb + sa + sb);
    };
    const u32x4 rst = mkr(ws + WS_STAT, S_ * 16);
    const int spos = 36 * lw + (lane < 36 ? lane : 35), sv = row_token(p_, spos) * 16;
    u32x4 sg;
    sfor<5>([&](auto TT) {
      constexpr int T = decltype(TT)::value + 5;
      if (T == 9) bload_sc1(sg, rst, sv, 0);
      ldh(T); ldx(T); ldw(T, 0); ldw(T, 1);
      bar_vm1<12>(attf);
    });
    asm volatile("" : "+v"(sg));
#pragma nounroll
    for (unsigned spins = 0; !__all(sg[1] == tag && sg[3] == tag) && spins < SPIN_CAP; spins++) {
      __builtin_amdgcn_s_sleep(2);
      bload_sc1(sg, rst, sv, 0);
      asm volatile("s_waitcnt vmcnt(0)" : "+v"(sg) :: "memory");
    }
    *(LAS u32x2*)(shm + LDS_ST + spos * 8) = u32x2{sg[0], sg[2]};
    if (att) {
      ldl(IC(4));
      wait_vm<4>();
    } else {
      const int wv2 = ((blockIdx.x >> 7) * 64 + (blockIdx.x & 63)) * 4 + lw;
      touch(mkr(wv2 < 360 ? w2 : wo, wv2 < 360 ? F_ * 1280 : D_ * 1280), min((wv2 < 360 ? wv2 : wv2 - 360) * 48 + min(lane, 47), wv2 < 360 ? F_ * 10 - 1 : D_ * 10 - 1) * 128);
      wait_vm<1>();
    }
    bar_lgkm();
    __builtin_amdgcn_s_setprio(0);
    const int pa = LDS_PROD + kq * 4, wa = kq * 4096 + lane * 16;
    if (!att) { ldl(IC(4)); ldl(IC(5)); ldl(IC(6)); ldl(IC(7)); }
    ldr(IC(1)); ldr(IC(2)); ldr(IC(3)); ldr(IC(4));
    ldl(IC(3));
    await<1, 8>(2);
    wait_vm<32>();
    ldl(IC(2)); ldl(IC(1)); ldl(IC(0));
    if (!att) { ldl(IC(8)); if (kq == 0) { ldl(IC(18)); ldl(IC(19)); } }
    ldr(IC(0)); wait_vm<4>();
    post(pa, att ? 1 : 2);
    if (!att) __builtin_amdgcn_s_setprio(3);
    ldr(IC(5)); ldr(IC(6)); ldr(IC(7)); ldr(IC(8));
    if (att) {
      await<1, 8>(4);
      __builtin_amdgcn_s_setprio(3);
      ldl(IC(5)); ldl(IC(6)); ldl(IC(7)); ldl(IC(8));
      if (kq == 0) { ldl(IC(18)); ldl(IC(19)); }
    }
    if (lw == 3) touch(mkr(b2, D_ * 2), n_ * 128);
    wait_vm<0>();
    post(pa, 2);
#define PUT(r, off) dsw128<off>(wa + (r / 3) * 49152, pre[r][0]); dsw128<off + 1024>(wa + (r / 3) * 49152, pre[r][1]); dsw128<off + 2048>(wa + (r / 3) * 49152, pre[r][2]); dsw128<off + 3072>(wa + (r / 3) * 49152, pre[r][3]);
    await_pair(LDS_CONS + kq * 8, 1);
    PUT(0, 0) PUT(1, 16384) PUT(2, 32768)
    post(pa, 3);
    __builtin_amdgcn_s_setprio(0);
    await_pair(LDS_CONS + kq * 8, 3);
    PUT(3, 0) PUT(4, 16384) PUT(5, 32768)
    post(pa, 4);
    await_pair(LDS_CONS + kq * 8, 6);
    PUT(6, 0) PUT(7, 16384) PUT(8, 32768)
    post(pa, 5);
    fu = lw;
  }
  {
    const int lane = lane_id();
    const u32x4 bv = *(const GAS u32x4*)(b2 + n_ * 128 + (lane & 7) * 16);
    int foff[2], pr0[2];
    u32x4 xv[2];
    f32x2 gt[2][4];
#pragma unroll
    for (int hh = 0; hh < 2; hh++) {
      const int frow = 48 * hh + 8 * fu + (lane >> 3);
      foff[hh] = fin_off(G0 + (att ? 2 : 3) * hh, n_, 8 * fu, lane);
      xv[hh] = *(const GAS u32x4*)((const char*)x + foff[hh]);
      pr0[hh] = (frow >> 4) * 16384 + (frow & 15) * 256 + (((2 * (lane & 7)) ^ (frow & 7)) << 4);
      pin(pr0[hh]);
    }
#pragma unroll
    for (int hh = 0; hh < 2; hh++) {
      await<1, 3>(12 + hh);
      f32x4 y[2];
#pragma unroll
      for (int e = 0; e < 2; e++) {
        lptr pr = shm + (pr0[hh] ^ (16 * e));
        y[e] = *(LAS f32x4*)(pr + 4096) + *(LAS f32x4*)(pr + 8192) + *(LAS f32x4*)(pr + 12288);
      }
#pragma unroll
      for (int k = 0; k < 4; k++) {
        f32x2 yy = f32x2{y[k >> 1][2 * (k & 1)], y[k >> 1][2 * (k & 1) + 1]} + f32x2{blo(bv[k]), bhi(bv[k])};
        f32x2 t = yy * -1.4426950408889634f;
        f32x2 e = f32x2{__builtin_amdgcn_exp2f(t[0]), __builtin_amdgcn_exp2f(t[1])} + 1.f;
        gt[hh][k] = yy * f32x2{__builtin_amdgcn_rcpf(e[0]), __builtin_amdgcn_rcpf(e[1])};
        pin(gt[hh][k]);
      }
    }
#pragma unroll
    for (int hh = 0; hh < 2; hh++) {
      await<0, 1>(28 + hh);
      u32x4 o;
#pragma unroll
      for (int e = 0; e < 2; e++) {
        const f32x4 s = *(LAS f32x4*)(shm + (pr0[hh] ^ (16 * e)));
        o[2 * e] = pk2(f32x2{s[0], s[1]} * gt[hh][2 * e] + f32x2{blo(xv[hh][2 * e]), bhi(xv[hh][2 * e])});
        o[2 * e + 1] = pk2(f32x2{s[2], s[3]} * gt[hh][2 * e + 1] + f32x2{blo(xv[hh][2 * e + 1]), bhi(xv[hh][2 * e + 1])});
      }
      __builtin_nontemporal_store(o, (GAS u32x4*)(out + foff[hh]));
    }
  }
}

extern "C" void kernel_launch(void* const* d_in, const int* in_sizes, int n_in, void* d_out, int out_size, void* d_ws,
                              size_t ws_size, hipStream_t stream) {
  (void)hipFuncSetAttribute((const void*)fused_block, hipFuncAttributeMaxDynamicSharedMemorySize, LDS_TOTAL);
  fused_block<<<256, 768, LDS_TOTAL, stream>>>(d_in[0], d_in[1], d_in[2], d_in[4], d_in[5], d_in[6],
                                               d_in[7], d_out, d_ws);
}
```
